```python
import torch

def _softmax(x, axis=-1):
    return torch.softmax(x, dim=axis)
def _gelu(x):
    return torch.nn.functional.gelu(x, approximate="tanh")
def _silu(x):
    return torch.nn.functional.silu(x)
def _sigmoid(x):
    return torch.sigmoid(x)
def _layernorm(x, axis=-1, eps=1e-5):
    m = x.mean(dim=axis, keepdim=True)
    v = x.var(dim=axis, keepdim=True, unbiased=False)
    return (x - m) / torch.sqrt(v + eps)
def _scatter_add(tgt, idx, src, axis=0):
    return tgt.clone().index_add_(axis, idx, src)
def _segment_sum(src, seg, n):
    out = torch.zeros((n,) + src.shape[1:], dtype=src.dtype)
    return out.index_add_(0, seg, src)
def _pad(x, pw):
    flat = tuple(v for pair in reversed(pw) for v in pair)
    return torch.nn.functional.pad(x, flat)

kernel_name = 'sequential_preLN__sliding_block__glu_gated_swiglu__prompt'

def setup_inputs():

    _w = (lambda a: (lambda t: t.to(torch.bfloat16) if t.is_floating_point() else t)(torch.as_tensor(a)))
    def _A_setup():
        B, S, D = 1, 2048, 512
        return {
            "x": _w((lambda *s: torch.randn(*s, dtype=torch.bfloat16) * 0.05)(B, S, D)),
            "wqkv": _w((lambda *s: torch.randn(*s, dtype=torch.bfloat16) * 0.05)(D, 3 * D)),
            "wo": _w((lambda *s: torch.randn(*s, dtype=torch.bfloat16) * 0.05)(D, D)),
        }
    _A_in = _A_setup()

    def _B_setup():
        B, S, D, F = 1, 2048, 512, 1792
        return {
            "x": _w((lambda *s: torch.randn(*s, dtype=torch.bfloat16) * 0.05)(B, S, D)),
            "w1": _w((lambda *s: torch.randn(*s, dtype=torch.bfloat16) * 0.8)(D, F) * (2.0 / D) ** 0.5),
            "w3": _w((lambda *s: torch.randn(*s, dtype=torch.bfloat16) * 0.8)(D, F) * (2.0 / D) ** 0.5),
            "w2": _w((lambda *s: torch.randn(*s, dtype=torch.bfloat16) * 0.8)(F, D) * (2.0 / F) ** 0.5),
        }
    _B_in = _B_setup()

    return {
        "A_x": _A_in["x"],
        "A_wqkv": _A_in["wqkv"],
        "A_wo": _A_in["wo"],
        "B_x": _B_in["x"],
        "B_w1": _B_in["w1"],
        "B_w3": _B_in["w3"],
        "B_w2": _B_in["w2"],
    }

def reference(A_x, A_wqkv, A_wo, B_x, B_w1, B_w3, B_w2):
    def _A_ref(x, wqkv, wo):
        B, S, D = x.shape
        H = 32; Dh = D // H; W = 256
        qkv = (lambda x, *d: x.permute(*d))((x @ wqkv).reshape(B, S, 3, H, Dh), 2, 0, 3, 1, 4)
        q, k, v = qkv[0], qkv[1], qkv[2]
        scores = torch.einsum("bhqd,bhkd->bhqk", q, k) * (Dh ** -0.5)
        idx = torch.arange(S)
        mask = (idx[:, None] - idx[None, :] >= 0) & (idx[:, None] - idx[None, :] < W)
        scores = torch.where(mask[None, None], scores, -1e9)
        p = _softmax(scores, axis=-1)
        o = torch.einsum("bhqk,bhkd->bhqd", p, v)
        return (lambda x, *d: x.permute(*d))(o, 0, 2, 1, 3).reshape(B, S, D) @ wo

    def _B_ref(x, w1, w3, w2):
        gate = torch.einsum("bsd,df->bsf", x, w1)
        up = torch.einsum("bsd,df->bsf", x, w3)
        h = _silu(gate) * up
        out = torch.einsum("bsf,fd->bsd", h, w2)
        return out

    x = A_x
    h = x + _A_ref(_layernorm(x, axis=-1), A_wqkv, A_wo)
    h2 = h + _B_ref(_layernorm(h, axis=-1), B_w1, B_w3, B_w2)
    return h2
```

```cpp
#include <hip/hip_runtime.h>
#include <hip/hip_bf16.h>
#include <stdint.h>

typedef __attribute__((ext_vector_type(8))) __bf16 bf16x8;
typedef __attribute__((ext_vector_type(4))) __bf16 bf16x4;
typedef __attribute__((ext_vector_type(2))) __bf16 bf16x2;
typedef __attribute__((ext_vector_type(4))) float f32x4;
typedef __attribute__((ext_vector_type(16))) float f32x16;
typedef __attribute__((ext_vector_type(4))) unsigned int u32x4;
typedef __attribute__((ext_vector_type(2))) unsigned int u32x2;
typedef __bf16 bf16;

#define LDSP(T) __attribute__((address_space(3))) T*
#define GLBP(T) __attribute__((address_space(1))) T*

constexpr int S_ = 2048, D_ = 512, H_ = 32, F_ = 1792;

enum { MODE_QKV = 0, MODE_WO = 1, MODE_GU = 2, MODE_DOWN = 3 };

__device__ __forceinline__ float bfr(float x) { return (float)(bf16)x; }

template <int N>
__device__ __forceinline__ void wait_vm() {
  asm volatile("s_waitcnt vmcnt(%0)" ::"n"(N) : "memory");
}

struct GemmArgs {
  const bf16* A;
  const bf16* B0;
  const bf16* B1;
  const bf16* R;
  bf16* O0;
  bf16* O1;
};

template <int MODE, int BM, int NB, int KTOT, int BK, int NST, int WM, int WN, int WK, int NTN>
__global__ __launch_bounds__(512, 2) void gemm_kernel(GemmArgs g) {
  constexpr int MI = BM / 16 / WM;
  constexpr int NI = NB / WN;
  constexpr int NK = KTOT / BK;
  constexpr int CPR = BK / 8;
  constexpr int UA = BM * CPR;
  constexpr int UB = BK * NB * 2;
  constexpr int NA = UA / 512;
  constexpr int NBF = UB / 512;
  constexpr int RB = (UB % 512) / 8;
  constexpr int NBI = NBF + (RB ? 1 : 0);
  constexpr int LPC = NA + NBI;
  constexpr int STAGE = (UA + UB) * 16;
  constexpr int LDB = (MODE == MODE_QKV) ? 3 * D_ : (MODE == MODE_GU ? F_ : D_);
  constexpr bool LN = (MODE == MODE_QKV || MODE == MODE_GU);
  constexpr int KS = BK / 32;
  static_assert(UA % 512 == 0, "A units");
  static_assert(WM * WN * WK == 8, "waves");
  static_assert(!(LN && WK > 1), "LN with k split unsupported");

  extern __shared__ __attribute__((aligned(16))) char smem[];

  const int tid = threadIdx.x;
  const int lane = tid & 63;
  const int wid = __builtin_amdgcn_readfirstlane(tid >> 6);
  const int wk = wid % WK;
  const int wn = (wid / WK) % WN;
  const int wm = wid / (WK * WN);
  const int l15 = lane & 15, gq = lane >> 4;

  constexpr int NTM = S_ / BM;
  constexpr int PER_X = NTM / 8;
  const int xg = blockIdx.x & 7, idx = blockIdx.x >> 3;
  const int pm = xg * PER_X + idx / NTN;
  const int pn = idx % NTN;
  const int row0 = pm * BM;
  constexpr int NBH = (MODE == MODE_GU) ? NB / 2 : NB;
  const int col0 = pn * NBH * 16;

  unsigned oa[NA];
#pragma unroll
  for (int i = 0; i < NA; ++i) {
    int u = i * 512 + tid;
    int r = u / CPR, c = u % CPR;
    int sw = (CPR == 8) ? ((r >> 1) & 7) : (r & 15);
    oa[i] = (unsigned)((r * KTOT + ((c ^ sw) * 8)) * 2);
  }
  const char* baseA = (const char*)(g.A + (size_t)row0 * KTOT);
  constexpr int UREG = BK * NBH * 2;
  unsigned ob[NBI];
  const char* baseB[NBI];
#pragma unroll
  for (int i = 0; i < NBI; ++i) {
    int uw = (i < NBF) ? (i * 512 + wid * 64) : (NBF * 512 + wid * RB);
    int u = uw + ((i < NBF) ? lane : (lane < RB ? lane : 0));
    int m = uw / UREG;
    u -= m * UREG;
    int c2 = u & 1, q = (u >> 1) & 3, g1 = (u >> 3) & 1;
    int rest = u >> 4;
    int nb = rest % NBH;
    rest /= NBH;
    int gp = rest & 1, t = (rest >> 1) & 1, kk = rest >> 2;
    int k = 32 * kk + 16 * gp + 8 * g1 + 4 * t + q;
    ob[i] = (unsigned)((k * LDB + nb * 16 + c2 * 8) * 2);
    baseB[i] = (const char*)(((MODE == MODE_GU && m) ? g.B1 : g.B0) + col0);
  }

  auto issue = [&](int kc) {
    char* sb = smem + (kc % NST) * STAGE;
    const char* ba = baseA + (size_t)kc * BK * 2;
#pragma unroll
    for (int i = 0; i < NA; ++i) {
      __builtin_amdgcn_global_load_lds((GLBP(const void))(ba + oa[i]),
                                       (LDSP(void))(sb + (i * 512 + wid * 64) * 16), 16, 0, 0);
    }
#pragma unroll
    for (int i = 0; i < NBF; ++i) {
      const char* bb = baseB[i] + (size_t)kc * BK * LDB * 2;
      __builtin_amdgcn_global_load_lds((GLBP(const void))(bb + ob[i]),
                                       (LDSP(void))(sb + UA * 16 + (i * 512 + wid * 64) * 16), 16, 0, 0);
    }
    if (RB) {
      const char* bb = baseB[NBI - 1] + (size_t)kc * BK * LDB * 2;
      if (lane < RB)
        __builtin_amdgcn_global_load_lds((GLBP(const void))(bb + ob[NBI - 1]),
                                         (LDSP(void))(sb + UA * 16 + (NBF * 512 + wid * RB) * 16), 16, 0, 0);
    }
  };

  u32x2 res[MI][NI];
  if (MODE == MODE_WO || MODE == MODE_DOWN) {
    if (wk == 0) {
#pragma unroll
      for (int mi = 0; mi < MI; ++mi)
#pragma unroll
        for (int ni = 0; ni < NI; ++ni) {
          int tok = row0 + (wm * MI + mi) * 16 + l15;
          int n = col0 + (wn * NI + ni) * 16 + gq * 4;
          res[mi][ni] = *(const u32x2*)(g.R + (size_t)tok * D_ + n);
        }
    }
  }

#pragma unroll
  for (int s = 0; s < NST; ++s)
    if (s < NK) issue(s);

  f32x4 acc[MI][NI];
#pragma unroll
  for (int mi = 0; mi < MI; ++mi)
#pragma unroll
    for (int ni = 0; ni < NI; ++ni) acc[mi][ni] = f32x4{0.f, 0.f, 0.f, 0.f};
  float s1[MI], s2[MI], cs[NI];
#pragma unroll
  for (int mi = 0; mi < MI; ++mi) { s1[mi] = 0.f; s2[mi] = 0.f; }
#pragma unroll
  for (int ni = 0; ni < NI; ++ni) cs[ni] = 0.f;

  const int asw = (CPR == 8) ? (l15 >> 1) : l15;
  const int a_row_off = (wm * MI * 16 + l15) * (CPR * 16);
  const int b_lane_off = UA * 16 + (gq >> 1) * NBH * 256 + (gq & 1) * 128 + l15 * 8 +
                         wn * ((MODE == MODE_GU) ? NI / 2 : NI) * 256;

  const unsigned lds0 = (unsigned)(uintptr_t)(LDSP(char))smem;
  constexpr int KSW = KS / WK;
  constexpr int NT = NK * KSW;
  unsigned a_addr[KSW];
#pragma unroll
  for (int ks = 0; ks < KSW; ++ks) a_addr[ks] = lds0 + a_row_off + (((4 * (ks * WK + wk) + gq) ^ asw) * 16);
  const unsigned b_addr = lds0 + b_lane_off + wk * (2 * 2 * NBH * 256);

  bf16x8 af[2][MI];
  bf16x4 bl[2][NI], bh[2][NI];

  auto rd = [&](int t, int set) {
    const int kc = t / KSW, ks = t % KSW;
    const unsigned so = (kc % NST) * STAGE;
    const unsigned aa = a_addr[ks] + so;
    const unsigned bb = b_addr + so;
#pragma unroll
    for (int ni = 0; ni < NI; ++ni) {
      int nb, mreg = 0;
      if (MODE == MODE_GU) { mreg = (ni < NI / 2) ? 0 : 1; nb = (ni % (NI / 2)); }
      else nb = ni;
      const int off = mreg * UREG * 16 + ((ks * WK * 2) * 2 * NBH + nb) * 256;
      asm volatile("ds_read_b64_tr_b16 %0, %1 offset:%2" : "=v"(bl[set][ni]) : "v"(bb), "i"(off));
      asm volatile("ds_read_b64_tr_b16 %0, %1 offset:%2" : "=v"(bh[set][ni]) : "v"(bb), "i"(off + 2 * NBH * 256));
      if (ni < MI)
        asm volatile("ds_read_b128 %0, %1 offset:%2" : "=v"(af[set][ni]) : "v"(aa), "i"(ni * 16 * (CPR * 16)));
    }
#pragma unroll
    for (int mi = NI; mi < MI; ++mi)
      asm volatile("ds_read_b128 %0, %1 offset:%2" : "=v"(af[set][mi]) : "v"(aa), "i"(mi * 16 * (CPR * 16)));
  };
  auto wt = [&](int set) {
    asm volatile("s_waitcnt lgkmcnt(0)" ::: "memory");
#pragma unroll
    for (int mi = 0; mi < MI; ++mi) asm volatile("" : "+v"(af[set][mi]));
#pragma unroll
    for (int ni = 0; ni < NI; ++ni) asm volatile("" : "+v"(bl[set][ni]), "+v"(bh[set][ni]));
  };

  {
    constexpr int inflight = ((NST < NK) ? NST : NK) - 1;
    wait_vm<inflight * LPC>();
    __builtin_amdgcn_s_barrier();
    rd(0, 0);
    wt(0);
  }
#pragma unroll
  for (int t = 0; t < NT; ++t) {
    const int kc = t / KSW, ks = t % KSW;
    const int cur = t & 1, nxt = cur ^ 1;
    if (ks == KSW - 1 && kc + 1 < NK) {
      int last = (kc + NST - 1 < NK - 1) ? (kc + NST - 1) : (NK - 1);
      int n = (last - (kc + 1)) * LPC;
      switch (n) {
#define WC(X) case X: wait_vm<X>(); break;
        WC(0) WC(1) WC(2) WC(3) WC(4) WC(5) WC(6) WC(7) WC(8) WC(9) WC(10) WC(11) WC(12) WC(13) WC(14) WC(15)
        WC(16) WC(17) WC(18) WC(19) WC(20) WC(21) WC(22) WC(23) WC(24) WC(25) WC(26) WC(27) WC(28) WC(29) WC(30)
        WC(31) WC(32) WC(33) WC(34) WC(35) WC(36) WC(37) WC(38) WC(39) WC(40)
#undef WC
        default: wait_vm<0>(); break;
      }
      __builtin_amdgcn_s_barrier();
    }
    if (t + 1 < NT) rd(t + 1, nxt);
    if (ks == KSW - 1 && kc + NST < NK) issue(kc + NST);
    __builtin_amdgcn_sched_barrier(0);
    {
      bf16x8 b[NI];
#pragma unroll
      for (int ni = 0; ni < NI; ++ni) b[ni] = __builtin_shufflevector(bl[cur][ni], bh[cur][ni], 0, 1, 2, 3, 4, 5, 6, 7);
#pragma unroll
      for (int mi = 0; mi < MI; ++mi)
#pragma unroll
        for (int ni = 0; ni < NI; ++ni)
          acc[mi][ni] = __builtin_amdgcn_mfma_f32_16x16x32_bf16(b[ni], af[cur][mi], acc[mi][ni], 0, 0, 0);
      if (LN) {
        const bf16x2 ones = {(bf16)1.0f, (bf16)1.0f};
#pragma unroll
        for (int mi = 0; mi < MI; ++mi) {
#pragma unroll
          for (int j = 0; j < 4; ++j) {
            bf16x2 p = {af[cur][mi][2 * j], af[cur][mi][2 * j + 1]};
            s1[mi] = __builtin_amdgcn_fdot2_f32_bf16(p, ones, s1[mi], false);
            s2[mi] = __builtin_amdgcn_fdot2_f32_bf16(p, p, s2[mi], false);
          }
          asm volatile("" : "+v"(s1[mi]), "+v"(s2[mi]));
        }
#pragma unroll
        for (int ni = 0; ni < NI; ++ni) {
#pragma unroll
          for (int j = 0; j < 4; ++j) {
            bf16x2 p = {b[ni][2 * j], b[ni][2 * j + 1]};
            cs[ni] = __builtin_amdgcn_fdot2_f32_bf16(p, ones, cs[ni], false);
          }
          asm volatile("" : "+v"(cs[ni]));
        }
      }
    }
    __builtin_amdgcn_sched_barrier(0);
    if (t + 1 < NT) wt(nxt);
  }

  if (WK > 1) {
    __builtin_amdgcn_s_barrier();
    float* red = (float*)smem;
    const int slot = (wm * WN + wn) * (MI * NI * 256);
    if (wk != 0) {
#pragma unroll
      for (int mi = 0; mi < MI; ++mi)
#pragma unroll
        for (int ni = 0; ni < NI; ++ni)
          *(f32x4*)(red + (wk - 1) * (WM * WN * MI * NI * 256) + slot + (mi * NI + ni) * 256 + lane * 4) = acc[mi][ni];
    }
    __syncthreads();
    if (wk != 0) return;
#pragma unroll
    for (int w = 1; w < WK; ++w)
#pragma unroll
      for (int mi = 0; mi < MI; ++mi)
#pragma unroll
        for (int ni = 0; ni < NI; ++ni) {
          f32x4 v = *(const f32x4*)(red + (w - 1) * (WM * WN * MI * NI * 256) + slot + (mi * NI + ni) * 256 + lane * 4);
          acc[mi][ni] += v;
        }
  }

  float mean_b[MI], rstd[MI];
  float csr[NI][4];
  if (LN) {
#pragma unroll
    for (int mi = 0; mi < MI; ++mi) {
      float a1 = s1[mi], a2 = s2[mi];
      a1 += __shfl_xor(a1, 16); a1 += __shfl_xor(a1, 32);
      a2 += __shfl_xor(a2, 16); a2 += __shfl_xor(a2, 32);
      float m = a1 * (1.0f / D_);
      float var = fmaxf(a2 * (1.0f / D_) - m * m, 0.f);
      float vb = bfr(var);
      float sd = bfr(__builtin_sqrtf(bfr(vb + 1e-5f)));
      mean_b[mi] = bfr(m);
      rstd[mi] = 1.0f / sd;
    }
#pragma unroll
    for (int ni = 0; ni < NI; ++ni) {
      float c = cs[ni];
      c += __shfl_xor(c, 16); c += __shfl_xor(c, 32);
#pragma unroll
      for (int r = 0; r < 4; ++r) csr[ni][r] = __shfl(c, gq * 4 + r);
    }
  }

  if (MODE == MODE_QKV) {
#pragma unroll
    for (int mi = 0; mi < MI; ++mi)
#pragma unroll
      for (int ni = 0; ni < NI; ++ni) {
        int f = pn * NB + wn * NI + ni;
        int sec = f >> 5, h = f & 31;
        int tok = row0 + (wm * MI + mi) * 16 + l15;
        bf16x4 o;
#pragma unroll
        for (int r = 0; r < 4; ++r) o[r] = (bf16)((acc[mi][ni][r] - mean_b[mi] * csr[ni][r]) * rstd[mi]);
        if (sec < 2) {
          *(bf16x4*)(g.O0 + ((size_t)(sec * H_ + h) * S_ + tok) * 16 + gq * 4) = o;
        } else {
          int kap = tok & 15;
          int pos = ((tok >> 4) & 1) * 16 + ((kap >> 2) & 1) * 8 + (kap >> 3) * 4 + (kap & 3);
          bf16* dst = g.O1 + ((size_t)(h * (S_ / 32) + (tok >> 5)) * 16 + gq * 4) * 32 + pos;
#pragma unroll
          for (int r = 0; r < 4; ++r) dst[r * 32] = o[r];
        }
      }
    if (blockIdx.x == 0 && tid < 8) { g.O1[(size_t)H_ * S_ * 16 + tid] = (bf16)1.0f; }
  } else if (MODE == MODE_WO || MODE == MODE_DOWN) {
#pragma unroll
    for (int mi = 0; mi < MI; ++mi)
#pragma unroll
      for (int ni = 0; ni < NI; ++ni) {
        int tok = row0 + (wm * MI + mi) * 16 + l15;
        int n = col0 + (wn * NI + ni) * 16 + gq * 4;
        bf16x4 rr = __builtin_bit_cast(bf16x4, res[mi][ni]);
        bf16x4 o;
#pragma unroll
        for (int r = 0; r < 4; ++r) o[r] = (bf16)((float)rr[r] + bfr(acc[mi][ni][r]));
        *(bf16x4*)(g.O0 + (size_t)tok * D_ + n) = o;
      }
  } else {
    constexpr int NH = NI / 2;
#pragma unroll
    for (int mi = 0; mi < MI; ++mi)
#pragma unroll
      for (int ni = 0; ni < NH; ++ni) {
        int tok = row0 + (wm * MI + mi) * 16 + l15;
        int n = col0 + (wn * NH + ni) * 16 + gq * 4;
        bf16x4 o;
#pragma unroll
        for (int r = 0; r < 4; ++r) {
          float gt = bfr((acc[mi][ni][r] - mean_b[mi] * csr[ni][r]) * rstd[mi]);
          float up = bfr((acc[mi][ni + NH][r] - mean_b[mi] * csr[ni + NH][r]) * rstd[mi]);
          float sl = bfr(gt / (1.0f + __expf(-gt)));
          o[r] = (bf16)(sl * up);
        }
        *(bf16x4*)(g.O0 + (size_t)tok * F_ + n) = o;
      }
  }
}

__global__ __launch_bounds__(256, 2) void attn_kernel(const bf16* __restrict__ QK, const bf16* __restrict__ Vt,
                                                      bf16* __restrict__ O) {
  const int lane = threadIdx.x & 63;
  const int wid = __builtin_amdgcn_readfirstlane(threadIdx.x >> 6);
  const int xg = blockIdx.x & 7, idx = blockIdx.x >> 3;
  const int h = idx & 31;
  const int qt = xg * 8 + (idx >> 5) * 4 + wid;
  const int q0 = qt * 32;
  const int c = lane & 31, hh = lane >> 5;

  const bf16* Qh = QK + (size_t)h * S_ * 16;
  const bf16* Kh = QK + (size_t)(H_ + h) * S_ * 16;
  const bf16* Vh = Vt + (size_t)h * S_ * 16;
  const bf16* ones = Vt + (size_t)H_ * S_ * 16;

  bf16x8 qf = *(const bf16x8*)(Qh + (size_t)(q0 + c) * 16 + hh * 8);
  bf16x8 kf[9];
#pragma unroll
  for (int t = 0; t < 9; ++t) {
    int kt = qt - 8 + t;
    if (kt < 0) kt = 0;
    kf[t] = *(const bf16x8*)(Kh + (size_t)(kt * 32 + c) * 16 + hh * 8);
  }
  f32x16 s[9];
  const f32x16 zero16 = {};
#pragma unroll
  for (int t = 0; t < 9; ++t) s[t] = __builtin_amdgcn_mfma_f32_32x32x16_bf16(kf[t], qf, zero16, 0, 0, 0);

  bf16x8 vf[9][2];
#pragma unroll
  for (int t = 0; t < 9; ++t) {
    int kt = qt - 8 + t;
    if (kt < 0) kt = 0;
#pragma unroll
    for (int ss = 0; ss < 2; ++ss) {
      const bf16* p = (c < 16) ? (Vh + ((size_t)kt * 16 + c) * 32 + ss * 16 + hh * 8) : ones;
      vf[t][ss] = *(const bf16x8*)p;
    }
  }

  const float NEG = -1e30f;
#pragma unroll
  for (int r = 0; r < 16; ++r) {
    int kr = (r & 3) + 8 * (r >> 2) + 4 * hh;
    s[0][r] = (kr > c) ? s[0][r] : NEG;
    s[8][r] = (kr <= c) ? s[8][r] : NEG;
  }
  if (qt < 8) {
#pragma unroll
    for (int t = 0; t < 8; ++t)
      if (qt - 8 + t < 0) {
#pragma unroll
        for (int r = 0; r < 16; ++r) s[t][r] = NEG;
      }
  }
  float m = NEG;
#pragma unroll
  for (int t = 0; t < 9; ++t)
#pragma unroll
    for (int r = 0; r < 16; ++r) m = fmaxf(m, s[t][r]);
  m = fmaxf(m, __shfl_xor(m, 32));
  const float cscale = 0.25f * 1.4426950408889634f;
  const float mc = m * cscale;

  f32x16 o = {};
#pragma unroll
  for (int t = 0; t < 9; ++t) {
#pragma unroll
    for (int ss = 0; ss < 2; ++ss) {
      bf16x8 pf;
#pragma unroll
      for (int j = 0; j < 8; ++j) pf[j] = (bf16)__builtin_amdgcn_exp2f(__builtin_fmaf(s[t][ss * 8 + j], cscale, -mc));
      o = __builtin_amdgcn_mfma_f32_32x32x16_bf16(vf[t][ss], pf, o, 0, 0, 0);
    }
  }
  const float inv = 1.0f / o[8];
  bf16x4 o0, o1;
#pragma unroll
  for (int r = 0; r < 4; ++r) { o0[r] = (bf16)(o[r] * inv); o1[r] = (bf16)(o[4 + r] * inv); }
  bf16* dst = O + (size_t)(q0 + c) * D_ + h * 16 + hh * 4;
  *(bf16x4*)dst = o0;
  *(bf16x4*)(dst + 8) = o1;
}

extern "C" void kernel_launch(void* const* d_in, const int* in_sizes, int n_in, void* d_out, int out_size, void* d_ws,
                              size_t ws_size, hipStream_t stream) {
  const bf16* x = (const bf16*)d_in[0];
  const bf16* wqkv = (const bf16*)d_in[1];
  const bf16* wo = (const bf16*)d_in[2];
  const bf16* w1 = (const bf16*)d_in[4];
  const bf16* w3 = (const bf16*)d_in[5];
  const bf16* w2 = (const bf16*)d_in[6];
  bf16* out = (bf16*)d_out;

  char* ws = (char*)d_ws;
  bf16* QK = (bf16*)(ws);
  bf16* Vt = (bf16*)(ws + (4u << 20));
  bf16* Oa = (bf16*)(ws + (7u << 20));
  bf16* Hh = (bf16*)(ws + (9u << 20));
  bf16* Hm = (bf16*)(ws + (11u << 20));

  {
    auto k = gemm_kernel<MODE_QKV, 128, 6, 512, 64, 5, 4, 2, 1, 16>;
    constexpr int lds = 5 * (128 * 8 + 64 * 6 * 2) * 16;
    (void)hipFuncSetAttribute((const void*)k, hipFuncAttributeMaxDynamicSharedMemorySize, lds);
    GemmArgs a{x, wqkv, nullptr, nullptr, QK, Vt};
    k<<<256, 512, lds, stream>>>(a);
  }
  attn_kernel<<<512, 256, 0, stream>>>(QK, Vt, Oa);
  {
    auto k = gemm_kernel<MODE_WO, 64, 4, 512, 128, 4, 2, 2, 2, 8>;
    constexpr int lds = 4 * (64 * 16 + 128 * 4 * 2) * 16;
    (void)hipFuncSetAttribute((const void*)k, hipFuncAttributeMaxDynamicSharedMemorySize, lds);
    GemmArgs a{Oa, wo, nullptr, x, Hh, nullptr};
    k<<<256, 512, lds, stream>>>(a);
  }
  {
    auto k = gemm_kernel<MODE_GU, 128, 16, 512, 64, 3, 2, 4, 1, 14>;
    constexpr int lds = 3 * (128 * 8 + 64 * 16 * 2) * 16;
    (void)hipFuncSetAttribute((const void*)k, hipFuncAttributeMaxDynamicSharedMemorySize, lds);
    GemmArgs a{Hh, w1, w3, nullptr, Hm, nullptr};
    k<<<224, 512, lds, stream>>>(a);
  }
  {
    auto k = gemm_kernel<MODE_DOWN, 64, 4, 1792, 128, 4, 2, 2, 2, 8>;
    constexpr int lds = 4 * (64 * 16 + 128 * 4 * 2) * 16;
    (void)hipFuncSetAttribute((const void*)k, hipFuncAttributeMaxDynamicSharedMemorySize, lds);
    GemmArgs a{Hm, w2, nullptr, Hh, out, nullptr};
    k<<<256, 512, lds, stream>>>(a);
  }
}
```

```cpp
#include <hip/hip_runtime.h>
#include <hip/hip_bf16.h>
#include <stdint.h>

typedef __attribute__((ext_vector_type(8))) __bf16 bf16x8;
typedef __attribute__((ext_vector_type(4))) __bf16 bf16x4;
typedef __attribute__((ext_vector_type(2))) __bf16 bf16x2;
typedef __attribute__((ext_vector_type(4))) float f32x4;
typedef __attribute__((ext_vector_type(16))) float f32x16;
typedef __attribute__((ext_vector_type(4))) unsigned int u32x4;
typedef __attribute__((ext_vector_type(2))) unsigned int u32x2;
typedef __bf16 bf16;

#define LDSP(T) __attribute__((address_space(3))) T*
#define GLBP(T) __attribute__((address_space(1))) T*

constexpr int S_ = 2048, D_ = 512, H_ = 32, F_ = 1792;

enum { MODE_QKV = 0, MODE_WO = 1, MODE_GU = 2, MODE_DOWN = 3 };

__device__ __forceinline__ float bfr(float x) { return (float)(bf16)x; }

template <int N>
__device__ __forceinline__ void wait_vm() {
  asm volatile("s_waitcnt vmcnt(%0)" ::"n"(N) : "memory");
}

struct GemmArgs {
  const bf16* A;
  const bf16* B0;
  const bf16* B1;
  const bf16* R;
  bf16* O0;
  bf16* O1;
};

template <int MODE, int BM, int NB, int KTOT, int BK, int NST, int WM, int WN, int WK, int NTN>
__global__ __launch_bounds__(512, 2) void gemm_kernel(GemmArgs g) {
  constexpr int MI = BM / 16 / WM;
  constexpr int NI = NB / WN;
  constexpr int NK = KTOT / BK;
  constexpr int CPR = BK / 8;
  constexpr int UA = BM * CPR;
  constexpr int UB = BK * NB * 2;
  constexpr int NA = UA / 512;
  constexpr int NBF = UB / 512;
  constexpr int RB = (UB % 512) / 8;
  constexpr int NBI = NBF + (RB ? 1 : 0);
  constexpr int LPC = NA + NBI;
  constexpr int STAGE = (UA + UB) * 16;
  constexpr int LDB = (MODE == MODE_QKV) ? 3 * D_ : (MODE == MODE_GU ? F_ : D_);
  constexpr bool LN = (MODE == MODE_QKV || MODE == MODE_GU);
  constexpr int KS = BK / 32;
  static_assert(UA % 512 == 0, "A units");
  static_assert(WM * WN * WK == 8, "waves");
  static_assert(!(LN && WK > 1), "LN with k split unsupported");

  extern __shared__ __attribute__((aligned(16))) char smem[];

  const int tid = threadIdx.x;
  const int lane = tid & 63;
  const int wid = __builtin_amdgcn_readfirstlane(tid >> 6);
  const int wk = wid % WK;
  const int wn = (wid / WK) % WN;
  const int wm = wid / (WK * WN);
  const int l15 = lane & 15, gq = lane >> 4;

  constexpr int NTM = S_ / BM;
  constexpr int PER_X = NTM / 8;
  const int xg = blockIdx.x & 7, idx = blockIdx.x >> 3;
  const int pm = xg * PER_X + idx / NTN;
  const int pn = idx % NTN;
  const int row0 = pm * BM;
  constexpr int NBH = (MODE == MODE_GU) ? NB / 2 : NB;
  const int col0 = pn * NBH * 16;

  unsigned oa[NA];
#pragma unroll
  for (int i = 0; i < NA; ++i) {
    int u = i * 512 + tid;
    int r = u / CPR, c = u % CPR;
    int sw = (CPR == 8) ? ((r >> 1) & 7) : (r & 15);
    oa[i] = (unsigned)((r * KTOT + ((c ^ sw) * 8)) * 2);
  }
  const char* baseA = (const char*)(g.A + (size_t)row0 * KTOT);
  constexpr int UREG = BK * NBH * 2;
  constexpr int CPB = NBH * 2;
  auto fsw = [](int k) -> int {
    if (NBH == 8) return (k & 3) | (((k >> 3) & 1) << 2);
    if (NBH == 4) return ((k >> 1) & 1) | (((k >> 3) & 1) << 1);
    return (k >> 3) & 1;
  };
  static_assert(NBH == 8 || NBH == 4 || NBH == 6, "NBH");
  unsigned ob[NBI];
  const char* baseB[NBI];
#pragma unroll
  for (int i = 0; i < NBI; ++i) {
    int uw = (i < NBF) ? (i * 512 + wid * 64) : (NBF * 512 + wid * RB);
    int u = uw + ((i < NBF) ? lane : (lane < RB ? lane : 0));
    int m = uw / UREG;
    u -= m * UREG;
    int k = u / CPB, cp = u % CPB;
    int nb = (cp >> 1) ^ fsw(k);
    ob[i] = (unsigned)((k * LDB + nb * 16 + (cp & 1) * 8) * 2);
    baseB[i] = (const char*)(((MODE == MODE_GU && m) ? g.B1 : g.B0) + col0);
  }

  const int rot = idx % NK;
  auto issue = [&](int kc0) {
    char* sb = smem + (kc0 % NST) * STAGE;
    int kc = kc0 + rot;
    if (kc >= NK) kc -= NK;
    const char* ba = baseA + (size_t)kc * BK * 2;
#pragma unroll
    for (int i = 0; i < NA; ++i) {
      __builtin_amdgcn_global_load_lds((GLBP(const void))(ba + oa[i]),
                                       (LDSP(void))(sb + (i * 512 + wid * 64) * 16), 16, 0, 0);
    }
#pragma unroll
    for (int i = 0; i < NBF; ++i) {
      const char* bb = baseB[i] + (size_t)kc * BK * LDB * 2;
      __builtin_amdgcn_global_load_lds((GLBP(const void))(bb + ob[i]),
                                       (LDSP(void))(sb + UA * 16 + (i * 512 + wid * 64) * 16), 16, 0, 0);
    }
    if (RB) {
      const char* bb = baseB[NBI - 1] + (size_t)kc * BK * LDB * 2;
      if (lane < RB)
        __builtin_amdgcn_global_load_lds((GLBP(const void))(bb + ob[NBI - 1]),
                                         (LDSP(void))(sb + UA * 16 + (NBF * 512 + wid * RB) * 16), 16, 0, 0);
    }
  };

  u32x2 res[MI][NI];
  if (MODE == MODE_WO || MODE == MODE_DOWN) {
    if (wk == 0) {
#pragma unroll
      for (int mi = 0; mi < MI; ++mi)
#pragma unroll
        for (int ni = 0; ni < NI; ++ni) {
          int tok = row0 + (wm * MI + mi) * 16 + l15;
          int n = col0 + (wn * NI + ni) * 16 + gq * 4;
          res[mi][ni] = *(const u32x2*)(g.R + (size_t)tok * D_ + n);
        }
    }
  }

#pragma unroll
  for (int s = 0; s < NST; ++s)
    if (s < NK) issue(s);

  f32x4 acc[MI][NI];
#pragma unroll
  for (int mi = 0; mi < MI; ++mi)
#pragma unroll
    for (int ni = 0; ni < NI; ++ni) acc[mi][ni] = f32x4{0.f, 0.f, 0.f, 0.f};
  float s1[MI], s2[MI], cs[NI];
#pragma unroll
  for (int mi = 0; mi < MI; ++mi) { s1[mi] = 0.f; s2[mi] = 0.f; }
#pragma unroll
  for (int ni = 0; ni < NI; ++ni) cs[ni] = 0.f;

  const int asw = (CPR == 8) ? (l15 >> 1) : l15;
  const int a_row_off = (wm * MI * 16 + l15) * (CPR * 16);
  const unsigned lds0 = (unsigned)(uintptr_t)(LDSP(char))smem;
  constexpr int KSW = KS / WK;
  constexpr int NT = NK * KSW;
  unsigned a_addr[KSW];
#pragma unroll
  for (int ks = 0; ks < KSW; ++ks) a_addr[ks] = lds0 + a_row_off + (((4 * (ks * WK + wk) + gq) ^ asw) * 16);
  constexpr int NBW = (MODE == MODE_GU) ? NI / 2 : NI;
  unsigned b_addr[NBW];
  {
    const int kl = wk * 32 + (gq >> 1) * 16 + (gq & 1) * 8 + (l15 >> 2);
    const int f = fsw(kl);
#pragma unroll
    for (int j = 0; j < NBW; ++j)
      b_addr[j] = lds0 + UA * 16 + kl * (CPB * 16) + (((wn * NBW + j) ^ f) * 32) + (l15 & 3) * 8;
  }

  bf16x8 af[2][MI];
  bf16x4 bl[2][NI], bh[2][NI];

  auto rd = [&](int t, int set) {
    const int kc = t / KSW, ks = t % KSW;
    const unsigned so = (kc % NST) * STAGE;
    const unsigned aa = a_addr[ks] + so;
#pragma unroll
    for (int ni = 0; ni < NI; ++ni) {
      const int mreg = (MODE == MODE_GU && ni >= NBW) ? 1 : 0;
      const unsigned bb = b_addr[ni % NBW] + so;
      const int off = mreg * UREG * 16 + (ks * WK * 32) * (CPB * 16);
      asm volatile("ds_read_b64_tr_b16 %0, %1 offset:%2" : "=v"(bl[set][ni]) : "v"(bb), "i"(off));
      asm volatile("ds_read_b64_tr_b16 %0, %1 offset:%2" : "=v"(bh[set][ni]) : "v"(bb), "i"(off + 4 * CPB * 16));
      if (ni < MI)
        asm volatile("ds_read_b128 %0, %1 offset:%2" : "=v"(af[set][ni]) : "v"(aa), "i"(ni * 16 * (CPR * 16)));
    }
#pragma unroll
    for (int mi = NI; mi < MI; ++mi)
      asm volatile("ds_read_b128 %0, %1 offset:%2" : "=v"(af[set][mi]) : "v"(aa), "i"(mi * 16 * (CPR * 16)));
  };
  auto wt = [&](int set) {
    asm volatile("s_waitcnt lgkmcnt(0)" ::: "memory");
#pragma unroll
    for (int mi = 0; mi < MI; ++mi) asm volatile("" : "+v"(af[set][mi]));
#pragma unroll
    for (int ni = 0; ni < NI; ++ni) asm volatile("" : "+v"(bl[set][ni]), "+v"(bh[set][ni]));
  };

  {
    constexpr int inflight = ((NST < NK) ? NST : NK) - 1;
    wait_vm<inflight * LPC>();
    __builtin_amdgcn_s_barrier();
    rd(0, 0);
    wt(0);
  }
#pragma unroll
  for (int t = 0; t < NT; ++t) {
    const int kc = t / KSW, ks = t % KSW;
    const int cur = t & 1, nxt = cur ^ 1;
    if (ks == KSW - 1 && kc + 1 < NK) {
      int last = (kc + NST - 1 < NK - 1) ? (kc + NST - 1) : (NK - 1);
      int n = (last - (kc + 1)) * LPC;
      switch (n) {
#define WC(X) case X: wait_vm<X>(); break;
        WC(0) WC(1) WC(2) WC(3) WC(4) WC(5) WC(6) WC(7) WC(8) WC(9) WC(10) WC(11) WC(12) WC(13) WC(14) WC(15)
        WC(16) WC(17) WC(18) WC(19) WC(20) WC(21) WC(22) WC(23) WC(24) WC(25) WC(26) WC(27) WC(28) WC(29) WC(30)
        WC(31) WC(32) WC(33) WC(34) WC(35) WC(36) WC(37) WC(38) WC(39) WC(40)
#undef WC
        default: wait_vm<0>(); break;
      }
      __builtin_amdgcn_s_barrier();
    }
    if (t + 1 < NT) rd(t + 1, nxt);
    if (ks == KSW - 1 && kc + NST < NK) issue(kc + NST);
    __builtin_amdgcn_sched_barrier(0);
    {
      bf16x8 b[NI];
#pragma unroll
      for (int ni = 0; ni < NI; ++ni) b[ni] = __builtin_shufflevector(bl[cur][ni], bh[cur][ni], 0, 1, 2, 3, 4, 5, 6, 7);
#pragma unroll
      for (int mi = 0; mi < MI; ++mi)
#pragma unroll
        for (int ni = 0; ni < NI; ++ni)
          acc[mi][ni] = __builtin_amdgcn_mfma_f32_16x16x32_bf16(b[ni], af[cur][mi], acc[mi][ni], 0, 0, 0);
      if (LN) {
        const bf16x2 ones = {(bf16)1.0f, (bf16)1.0f};
#pragma unroll
        for (int mi = 0; mi < MI; ++mi) {
#pragma unroll
          for (int j = 0; j < 4; ++j) {
            bf16x2 p = {af[cur][mi][2 * j], af[cur][mi][2 * j + 1]};
            s1[mi] = __builtin_amdgcn_fdot2_f32_bf16(p, ones, s1[mi], false);
            s2[mi] = __builtin_amdgcn_fdot2_f32_bf16(p, p, s2[mi], false);
          }
          asm volatile("" : "+v"(s1[mi]), "+v"(s2[mi]));
        }
#pragma unroll
        for (int ni = 0; ni < NI; ++ni) {
#pragma unroll
          for (int j = 0; j < 4; ++j) {
            bf16x2 p = {b[ni][2 * j], b[ni][2 * j + 1]};
            cs[ni] = __builtin_amdgcn_fdot2_f32_bf16(p, ones, cs[ni], false);
          }
          asm volatile("" : "+v"(cs[ni]));
        }
      }
    }
    __builtin_amdgcn_sched_barrier(0);
    if (t + 1 < NT) wt(nxt);
  }

  if (WK > 1) {
    __builtin_amdgcn_s_barrier();
    float* red = (float*)smem;
    const int slot = (wm * WN + wn) * (MI * NI * 256);
    if (wk != 0) {
#pragma unroll
      for (int mi = 0; mi < MI; ++mi)
#pragma unroll
        for (int ni = 0; ni < NI; ++ni)
          *(f32x4*)(red + (wk - 1) * (WM * WN * MI * NI * 256) + slot + (mi * NI + ni) * 256 + lane * 4) = acc[mi][ni];
    }
    __syncthreads();
    if (wk != 0) return;
#pragma unroll
    for (int w = 1; w < WK; ++w)
#pragma unroll
      for (int mi = 0; mi < MI; ++mi)
#pragma unroll
        for (int ni = 0; ni < NI; ++ni) {
          f32x4 v = *(const f32x4*)(red + (w - 1) * (WM * WN * MI * NI * 256) + slot + (mi * NI + ni) * 256 + lane * 4);
          acc[mi][ni] += v;
        }
  }

  float mean_b[MI], rstd[MI];
  float csr[NI][4];
  if (LN) {
#pragma unroll
    for (int mi = 0; mi < MI; ++mi) {
      float a1 = s1[mi], a2 = s2[mi];
      a1 += __shfl_xor(a1, 16); a1 += __shfl_xor(a1, 32);
      a2 += __shfl_xor(a2, 16); a2 += __shfl_xor(a2, 32);
      float m = a1 * (1.0f / D_);
      float var = fmaxf(a2 * (1.0f / D_) - m * m, 0.f);
      float vb = bfr(var);
      float sd = bfr(__builtin_sqrtf(bfr(vb + 1e-5f)));
      mean_b[mi] = bfr(m);
      rstd[mi] = 1.0f / sd;
    }
#pragma unroll
    for (int ni = 0; ni < NI; ++ni) {
      float c = cs[ni];
      c += __shfl_xor(c, 16); c += __shfl_xor(c, 32);
#pragma unroll
      for (int r = 0; r < 4; ++r) csr[ni][r] = __shfl(c, gq * 4 + r);
    }
  }

  if (MODE == MODE_QKV) {
#pragma unroll
    for (int mi = 0; mi < MI; ++mi)
#pragma unroll
      for (int ni = 0; ni < NI; ++ni) {
        int f = pn * NB + wn * NI + ni;
        int sec = f >> 5, h = f & 31;
        int tok = row0 + (wm * MI + mi) * 16 + l15;
        bf16x4 o;
#pragma unroll
        for (int r = 0; r < 4; ++r) o[r] = (bf16)((acc[mi][ni][r] - mean_b[mi] * csr[ni][r]) * rstd[mi]);
        if (sec < 2) {
          *(bf16x4*)(g.O0 + ((size_t)(sec * H_ + h) * S_ + tok) * 16 + gq * 4) = o;
        } else {
          int kap = tok & 15;
          int pos = ((tok >> 4) & 1) * 16 + ((kap >> 2) & 1) * 8 + (kap >> 3) * 4 + (kap & 3);
          bf16* dst = g.O1 + ((size_t)(h * (S_ / 32) + (tok >> 5)) * 16 + gq * 4) * 32 + pos;
#pragma unroll
          for (int r = 0; r < 4; ++r) dst[r * 32] = o[r];
        }
      }
    if (blockIdx.x == 0 && tid < 8) { g.O1[(size_t)H_ * S_ * 16 + tid] = (bf16)1.0f; }
  } else if (MODE == MODE_WO || MODE == MODE_DOWN) {
#pragma unroll
    for (int mi = 0; mi < MI; ++mi)
#pragma unroll
      for (int ni = 0; ni < NI; ++ni) {
        int tok = row0 + (wm * MI + mi) * 16 + l15;
        int n = col0 + (wn * NI + ni) * 16 + gq * 4;
        bf16x4 rr = __builtin_bit_cast(bf16x4, res[mi][ni]);
        bf16x4 o;
#pragma unroll
        for (int r = 0; r < 4; ++r) o[r] = (bf16)((float)rr[r] + bfr(acc[mi][ni][r]));
        *(bf16x4*)(g.O0 + (size_t)tok * D_ + n) = o;
      }
  } else {
    constexpr int NH = NI / 2;
#pragma unroll
    for (int mi = 0; mi < MI; ++mi)
#pragma unroll
      for (int ni = 0; ni < NH; ++ni) {
        int tok = row0 + (wm * MI + mi) * 16 + l15;
        int n = col0 + (wn * NH + ni) * 16 + gq * 4;
        bf16x4 o;
#pragma unroll
        for (int r = 0; r < 4; ++r) {
          float gt = bfr((acc[mi][ni][r] - mean_b[mi] * csr[ni][r]) * rstd[mi]);
          float up = bfr((acc[mi][ni + NH][r] - mean_b[mi] * csr[ni + NH][r]) * rstd[mi]);
          float sl = bfr(gt / (1.0f + __expf(-gt)));
          o[r] = (bf16)(sl * up);
        }
        *(bf16x4*)(g.O0 + (size_t)tok * F_ + n) = o;
      }
  }
}

__global__ __launch_bounds__(256, 2) void attn_kernel(const bf16* __restrict__ QK, const bf16* __restrict__ Vt,
                                                      bf16* __restrict__ O) {
  const int lane = threadIdx.x & 63;
  const int wid = __builtin_amdgcn_readfirstlane(threadIdx.x >> 6);
  const int xg = blockIdx.x & 7, idx = blockIdx.x >> 3;
  const int h = idx & 31;
  const int qt = xg * 8 + (idx >> 5) * 4 + wid;
  const int q0 = qt * 32;
  const int c = lane & 31, hh = lane >> 5;

  const bf16* Qh = QK + (size_t)h * S_ * 16;
  const bf16* Kh = QK + (size_t)(H_ + h) * S_ * 16;
  const bf16* Vh = Vt + (size_t)h * S_ * 16;
  const bf16* ones = Vt + (size_t)H_ * S_ * 16;

  bf16x8 qf = *(const bf16x8*)(Qh + (size_t)(q0 + c) * 16 + hh * 8);
  bf16x8 kf[9];
#pragma unroll
  for (int t = 0; t < 9; ++t) {
    int kt = qt - 8 + t;
    if (kt < 0) kt = 0;
    kf[t] = *(const bf16x8*)(Kh + (size_t)(kt * 32 + c) * 16 + hh * 8);
  }
  f32x16 s[9];
  const f32x16 zero16 = {};
#pragma unroll
  for (int t = 0; t < 9; ++t) s[t] = __builtin_amdgcn_mfma_f32_32x32x16_bf16(kf[t], qf, zero16, 0, 0, 0);

  bf16x8 vf[9][2];
#pragma unroll
  for (int t = 0; t < 9; ++t) {
    int kt = qt - 8 + t;
    if (kt < 0) kt = 0;
#pragma unroll
    for (int ss = 0; ss < 2; ++ss) {
      const bf16* p = (c < 16) ? (Vh + ((size_t)kt * 16 + c) * 32 + ss * 16 + hh * 8) : ones;
      vf[t][ss] = *(const bf16x8*)p;
    }
  }

  const float NEG = -1e30f;
#pragma unroll
  for (int r = 0; r < 16; ++r) {
    int kr = (r & 3) + 8 * (r >> 2) + 4 * hh;
    s[0][r] = (kr > c) ? s[0][r] : NEG;
    s[8][r] = (kr <= c) ? s[8][r] : NEG;
  }
  if (qt < 8) {
#pragma unroll
    for (int t = 0; t < 8; ++t)
      if (qt - 8 + t < 0) {
#pragma unroll
        for (int r = 0; r < 16; ++r) s[t][r] = NEG;
      }
  }
  float m = NEG;
#pragma unroll
  for (int t = 0; t < 9; ++t)
#pragma unroll
    for (int r = 0; r < 16; ++r) m = fmaxf(m, s[t][r]);
  m = fmaxf(m, __shfl_xor(m, 32));
  const float cscale = 0.25f * 1.4426950408889634f;
  const float mc = m * cscale;

  f32x16 o = {};
#pragma unroll
  for (int t = 0; t < 9; ++t) {
#pragma unroll
    for (int ss = 0; ss < 2; ++ss) {
      bf16x8 pf;
#pragma unroll
      for (int j = 0; j < 8; ++j) pf[j] = (bf16)__builtin_amdgcn_exp2f(__builtin_fmaf(s[t][ss * 8 + j], cscale, -mc));
      o = __builtin_amdgcn_mfma_f32_32x32x16_bf16(vf[t][ss], pf, o, 0, 0, 0);
    }
  }
  const float inv = 1.0f / o[8];
  bf16x4 o0, o1;
#pragma unroll
  for (int r = 0; r < 4; ++r) { o0[r] = (bf16)(o[r] * inv); o1[r] = (bf16)(o[4 + r] * inv); }
  bf16* dst = O + (size_t)(q0 + c) * D_ + h * 16 + hh * 4;
  *(bf16x4*)dst = o0;
  *(bf16x4*)(dst + 8) = o1;
}

extern "C" void kernel_launch(void* const* d_in, const int* in_sizes, int n_in, void* d_out, int out_size, void* d_ws,
                              size_t ws_size, hipStream_t stream) {
  const bf16* x = (const bf16*)d_in[0];
  const bf16* wqkv = (const bf16*)d_in[1];
  const bf16* wo = (const bf16*)d_in[2];
  const bf16* w1 = (const bf16*)d_in[4];
  const bf16* w3 = (const bf16*)d_in[5];
  const bf16* w2 = (const bf16*)d_in[6];
  bf16* out = (bf16*)d_out;

  char* ws = (char*)d_ws;
  bf16* QK = (bf16*)(ws);
  bf16* Vt = (bf16*)(ws + (4u << 20));
  bf16* Oa = (bf16*)(ws + (7u << 20));
  bf16* Hh = (bf16*)(ws + (9u << 20));
  bf16* Hm = (bf16*)(ws + (11u << 20));

  {
    auto k = gemm_kernel<MODE_QKV, 128, 6, 512, 64, 5, 4, 2, 1, 16>;
    constexpr int lds = 5 * (128 * 8 + 64 * 6 * 2) * 16;
    (void)hipFuncSetAttribute((const void*)k, hipFuncAttributeMaxDynamicSharedMemorySize, lds);
    GemmArgs a{x, wqkv, nullptr, nullptr, QK, Vt};
    k<<<256, 512, lds, stream>>>(a);
  }
  attn_kernel<<<512, 256, 0, stream>>>(QK, Vt, Oa);
  {
    auto k = gemm_kernel<MODE_WO, 64, 4, 512, 128, 4, 2, 2, 2, 8>;
    constexpr int lds = 4 * (64 * 16 + 128 * 4 * 2) * 16;
    (void)hipFuncSetAttribute((const void*)k, hipFuncAttributeMaxDynamicSharedMemorySize, lds);
    GemmArgs a{Oa, wo, nullptr, x, Hh, nullptr};
    k<<<256, 512, lds, stream>>>(a);
  }
  {
    auto k = gemm_kernel<MODE_GU, 128, 16, 512, 64, 3, 2, 4, 1, 14>;
    constexpr int lds = 3 * (128 * 8 + 64 * 16 * 2) * 16;
    (void)hipFuncSetAttribute((const void*)k, hipFuncAttributeMaxDynamicSharedMemorySize, lds);
    GemmArgs a{Hh, w1, w3, nullptr, Hm, nullptr};
    k<<<224, 512, lds, stream>>>(a);
  }
  {
    auto k = gemm_kernel<MODE_DOWN, 64, 4, 1792, 128, 4, 2, 2, 2, 8>;
    constexpr int lds = 4 * (64 * 16 + 128 * 4 * 2) * 16;
    (void)hipFuncSetAttribute((const void*)k, hipFuncAttributeMaxDynamicSharedMemorySize, lds);
    GemmArgs a{Hm, w2, nullptr, Hh, out, nullptr};
    k<<<256, 512, lds, stream>>>(a);
  }
}
```

```cpp
#include <hip/hip_runtime.h>
#include <stdint.h>

typedef __attribute__((ext_vector_type(8))) __bf16 bf16x8;
typedef __attribute__((ext_vector_type(4))) __bf16 bf16x4;
typedef __attribute__((ext_vector_type(2))) __bf16 bf16x2;
typedef __attribute__((ext_vector_type(2))) float f32x2;
typedef __attribute__((ext_vector_type(4))) float f32x4;
typedef __attribute__((ext_vector_type(16))) float f32x16;
typedef __attribute__((ext_vector_type(4))) unsigned int u32x4;
typedef __attribute__((ext_vector_type(2))) unsigned int u32x2;
typedef __bf16 bf16;

#define LDSP(T) __attribute__((address_space(3))) T*
#define DEV __device__ __forceinline__

constexpr int S_ = 2048, D_ = 512, H_ = 32, F_ = 1792;

DEV float bfr(float x) { return (float)(bf16)x; }

template <int N>
DEV void wait_vm() {
  asm volatile("s_waitcnt vmcnt(%0)" ::"n"(N) : "memory");
}
DEV void wait_vm_n(int n) {
  switch (n) {
#define WC(X) case X: wait_vm<X>(); break;
    WC(0) WC(1) WC(2) WC(3) WC(4) WC(5) WC(6) WC(7) WC(8) WC(9) WC(10) WC(11) WC(12) WC(13) WC(14) WC(15)
    WC(16) WC(17) WC(18) WC(19) WC(20) WC(21) WC(22) WC(23) WC(24)
#undef WC
    default: wait_vm<0>(); break;
  }
}

DEV void glds(unsigned voff, const void* sbase, unsigned ldsaddr) {
  asm volatile("s_mov_b32 m0, %2\n\ts_nop 0\n\tglobal_load_lds_dwordx4 %0, %1" ::"v"(voff), "s"(sbase), "s"(ldsaddr)
               : "memory");
}
#define DSR128(dst, addr, off) asm volatile("ds_read_b128 %0, %1 offset:%2" : "=v"(dst) : "v"(addr), "i"(off))
#define DSRTR(dst, addr, off) asm volatile("ds_read_b64_tr_b16 %0, %1 offset:%2" : "=v"(dst) : "v"(addr), "i"(off))
#define PIN(x) asm volatile("" : "+v"(x))
#define GLD4(dst, voff, sbase, off) \
  asm volatile("global_load_dwordx4 %0, %1, %2 offset:%3" : "=v"(dst) : "v"(voff), "s"(sbase), "i"(off))
#define GLD2(dst, voff, sbase, off) \
  asm volatile("global_load_dwordx2 %0, %1, %2 offset:%3" : "=v"(dst) : "v"(voff), "s"(sbase), "i"(off))
#define GLD1(dst, voff, sbase, off) \
  asm volatile("global_load_dword %0, %1, %2 offset:%3" : "=v"(dst) : "v"(voff), "s"(sbase), "i"(off))

#define TSTAMP(m, i) do { } while (0)

DEV bf16x8 cat8(bf16x4 a, bf16x4 b) { return __builtin_shufflevector(a, b, 0, 1, 2, 3, 4, 5, 6, 7); }
DEV f32x4 mfma16(bf16x8 a, bf16x8 b, f32x4 c) { return __builtin_amdgcn_mfma_f32_16x16x32_bf16(a, b, c, 0, 0, 0); }
DEV f32x16 mfma32(bf16x8 a, bf16x8 b, f32x16 c) { return __builtin_amdgcn_mfma_f32_32x32x16_bf16(a, b, c, 0, 0, 0); }

DEV f32x2 ln_stats(float sum, float sq) {
  float m = sum * (1.0f / D_);
  float var = fmaxf(sq * (1.0f / D_) - m * m, 0.f);
  float vb = bfr(var);
  float sd = bfr(__builtin_sqrtf(bfr(vb + 1e-5f)));
  f32x2 r;
  r[0] = bfr(m);
  r[1] = 1.0f / sd;
  return r;
}

__global__ __launch_bounds__(512, 2) void k1_qkv(const bf16* __restrict__ x, const bf16* __restrict__ w,
                                                 bf16* __restrict__ Qb, bf16* __restrict__ Kb,
                                                 bf16* __restrict__ Vb) {
  constexpr int NST = 4, STAGE = 32768, NK = 8, LPC = 4, NT = 16;
  extern __shared__ __attribute__((aligned(16))) char smem[];
  TSTAMP(0, 0);
  const int tid = threadIdx.x, lane = tid & 63;
  const int wid = __builtin_amdgcn_readfirstlane(tid >> 6);
  const int wm = wid & 3, wn = wid >> 2;
  const int l15 = lane & 15, gq = lane >> 4;
  const int xg = blockIdx.x & 7, idx = blockIdx.x >> 3;
  const int pm = xg * 2 + (idx >> 4), pn = idx & 15;
  const unsigned lds0 = (unsigned)(uintptr_t)(LDSP(char))smem;

  const unsigned voffA = (unsigned)((wid * 8 + (lane >> 3)) * 1024 + (((lane & 7) ^ ((wid & 1) * 4 + gq)) * 16));
  const int pB = (l15 >> 1) ^ (gq | (((wid >> 1) & 1) << 2));
  const bool validB = pB < 6;
  const unsigned voffB =
      (unsigned)((wid * 4 + gq) * 3072 + ((pB >> 1) * 512 + (pB & 1) * 16 + (lane & 1) * 8) * 2);
  const char* baseA = (const char*)x + (size_t)pm * (128 * 1024);
  const char* baseB = (const char*)w + pn * 64;
  const unsigned sbw = lds0 + wid * 1024;
  auto issue = [&](int kc) {
    const unsigned sb = sbw + (kc % NST) * STAGE;
    glds(voffA, baseA + kc * 128, sb);
    glds(voffA, baseA + kc * 128 + 65536, sb + 8192);
    if (validB) {
      glds(voffB, baseB + kc * (64 * 3072), sb + 16384);
      glds(voffB, baseB + kc * (64 * 3072) + 32 * 3072, sb + 16384 + 8192);
    }
  };
#pragma unroll
  for (int s = 0; s < NST; ++s) issue(s);
  TSTAMP(0, 1);

  unsigned a_addr[2][2], b_addr[3];
#pragma unroll
  for (int mi = 0; mi < 2; ++mi)
#pragma unroll
    for (int ks = 0; ks < 2; ++ks)
      a_addr[mi][ks] = lds0 + (wm * 32 + ((mi ^ wn) * 16) + l15) * 128 + (((4 * ks + gq) ^ (l15 >> 1)) * 16);
  {
    const int kl = (gq >> 1) * 16 + (gq & 1) * 8 + (l15 >> 2);
    const int fr = (l15 >> 2) | ((gq & 1) << 2);
#pragma unroll
    for (int j = 0; j < 3; ++j) b_addr[j] = lds0 + 16384 + kl * 256 + (((2 * j + wn) ^ fr) * 32) + (l15 & 3) * 8;
  }

  f32x4 acc[2][3], s1, s2, cs[3];
  const f32x4 z4 = {0.f, 0.f, 0.f, 0.f};
#pragma unroll
  for (int j = 0; j < 3; ++j) { acc[0][j] = z4; acc[1][j] = z4; cs[j] = z4; }
  s1 = z4; s2 = z4;
  bf16x8 af[2][2];
  bf16x4 bl[2][3], bh[2][3];
  const bf16 one = (bf16)1.0f;
  const bf16x8 ones = {one, one, one, one, one, one, one, one};

  auto rd = [&](int t, int set) {
    const int kc = t >> 1, ks = t & 1, st = kc % NST;
    const unsigned hi = (st >= 2) ? 65536u : 0u;
    const int so = (st & 1) * STAGE;
#pragma unroll
    for (int j = 0; j < 3; ++j) {
      DSRTR(bl[set][j], b_addr[j] + hi, so + ks * 8192);
      DSRTR(bh[set][j], b_addr[j] + hi, so + ks * 8192 + 1024);
      if (j < 2) DSR128(af[set][j], a_addr[j][ks] + hi, so);
    }
  };
  auto wt = [&](int set) {
    asm volatile("s_waitcnt lgkmcnt(0)" ::: "memory");
    PIN(af[set][0]); PIN(af[set][1]);
#pragma unroll
    for (int j = 0; j < 3; ++j) { PIN(bl[set][j]); PIN(bh[set][j]); }
  };

  wait_vm<(NST - 1) * LPC>();
  __builtin_amdgcn_s_barrier();
  TSTAMP(0, 2);
  rd(0, 0);
  wt(0);
#pragma unroll
  for (int t = 0; t < NT; ++t) {
    const int kc = t >> 1, ks = t & 1, cur = t & 1, nxt = cur ^ 1;
    if (ks == 1 && kc + 1 < NK) {
      const int last = (kc + NST - 1 < NK - 1) ? (kc + NST - 1) : (NK - 1);
      wait_vm_n((last - (kc + 1)) * LPC);
      __builtin_amdgcn_s_barrier();
      TSTAMP(0, 3 + kc);
    }
    if (t + 1 < NT) rd(t + 1, nxt);
    if (ks == 1 && kc + NST < NK) issue(kc + NST);
    __builtin_amdgcn_sched_barrier(0);
    {
      const bf16x8 bq = cat8(bl[cur][0], bh[cur][0]), bk = cat8(bl[cur][1], bh[cur][1]),
                   bv = cat8(bl[cur][2], bh[cur][2]);
#pragma unroll
      for (int mi = 0; mi < 2; ++mi) {
        acc[mi][0] = mfma16(bq, af[cur][mi], acc[mi][0]);
        acc[mi][1] = mfma16(bk, af[cur][mi], acc[mi][1]);
        acc[mi][2] = mfma16(af[cur][mi], bv, acc[mi][2]);
      }
      s1 = mfma16(ones, af[cur][0], s1);
      s2 = mfma16(af[cur][0], af[cur][0], s2);
      if ((t & 3) == wm) {
        cs[0] = mfma16(bq, ones, cs[0]);
        cs[1] = mfma16(bk, ones, cs[1]);
        cs[2] = mfma16(bv, ones, cs[2]);
      }
    }
    __builtin_amdgcn_sched_barrier(0);
    if (t + 1 < NT) wt(nxt);
  }
  TSTAMP(0, 20);

  float* st_ = (float*)(smem + NST * STAGE);
  float* csum = st_ + 256;
  {
    const float sq = (l15 & 2) ? ((l15 & 1) ? s2[3] : s2[2]) : ((l15 & 1) ? s2[1] : s2[0]);
    const f32x2 ms = ln_stats(s1[0], sq);
    if (gq == (l15 >> 2)) *(f32x2*)(st_ + (wm * 32 + wn * 16 + l15) * 2) = ms;
    if (l15 == 0) {
#pragma unroll
      for (int j = 0; j < 3; ++j) *(f32x4*)(csum + wm * 96 + (2 * j + wn) * 16 + gq * 4) = cs[j];
    }
  }
  __syncthreads();
  TSTAMP(0, 21);
  const int h = pn * 2 + wn;
  const int tile = pm * 4 + wm;
  f32x4 c4[2];
  float cv = 0.f;
#pragma unroll
  for (int j = 0; j < 2; ++j) {
    c4[j] = z4;
#pragma unroll
    for (int p = 0; p < 4; ++p) c4[j] += *(const f32x4*)(csum + p * 96 + (2 * j + wn) * 16 + gq * 4);
  }
#pragma unroll
  for (int p = 0; p < 4; ++p) cv += csum[p * 96 + (4 + wn) * 16 + l15];
  const float qscale = 0.25f * 1.4426950408889634f;
#pragma unroll
  for (int mi = 0; mi < 2; ++mi) {
    const int tf = mi ^ wn;
    const f32x2 ms = *(const f32x2*)(st_ + (wm * 32 + tf * 16 + l15) * 2);
#pragma unroll
    for (int j = 0; j < 2; ++j) {
      const float rs = j == 0 ? ms[1] * qscale : ms[1];
      bf16x4 o;
#pragma unroll
      for (int r = 0; r < 4; ++r) o[r] = (bf16)((acc[mi][j][r] - ms[0] * c4[j][r]) * rs);
      bf16* dst = (j == 0 ? Qb : Kb) + ((size_t)(h * 64 + tile) * 2 + (gq >> 1)) * 256 + (tf * 16 + l15) * 8 + (gq & 1) * 4;
      *(bf16x4*)dst = o;
    }
    {
      const f32x4 m01 = *(const f32x4*)(st_ + (wm * 32 + tf * 16 + gq * 4) * 2);
      const f32x4 m23 = *(const f32x4*)(st_ + (wm * 32 + tf * 16 + gq * 4 + 2) * 2);
      bf16x4 o;
      o[0] = (bf16)((acc[mi][2][0] - m01[0] * cv) * m01[1]);
      o[1] = (bf16)((acc[mi][2][1] - m01[2] * cv) * m01[3]);
      o[2] = (bf16)((acc[mi][2][2] - m23[0] * cv) * m23[1]);
      o[3] = (bf16)((acc[mi][2][3] - m23[2] * cv) * m23[3]);
      bf16* dst = Vb + (((size_t)(h * 64 + tile) * 2 + tf) * 2 + (gq & 1)) * 128 + l15 * 8 + (gq >> 1) * 4;
      *(bf16x4*)dst = o;
    }
  }
  TSTAMP(0, 22);
}

template <bool FULL>
DEV void attn_body(unsigned kaddr, unsigned vaddr, bf16x8 qf, int wid, int lane, bf16* dst) {
  const int c = lane & 31, hh = lane >> 5;
  const float NEG = -1e30f;
  f32x16 cm0, cm8, z16;
#pragma unroll
  for (int r = 0; r < 16; ++r) {
    const int kr = (r & 3) + 8 * (r >> 2) + 4 * hh;
    cm0[r] = (kr > c) ? 0.f : NEG;
    cm8[r] = (kr <= c) ? 0.f : NEG;
    z16[r] = 0.f;
  }
  bf16x8 kf[9];
#pragma unroll
  for (int t = 0; t < 9; ++t)
    if (FULL || wid + t >= 8) DSR128(kf[t], kaddr, t * 1024);
  asm volatile("s_waitcnt lgkmcnt(0)" ::: "memory");
#pragma unroll
  for (int t = 0; t < 9; ++t) PIN(kf[t]);
  float m = NEG;
#pragma unroll
  for (int t = 0; t < 9; ++t)
    if (FULL || wid + t >= 8) {
      const f32x16 s = mfma32(kf[t], qf, t == 0 ? cm0 : (t == 8 ? cm8 : z16));
#pragma unroll
      for (int r = 0; r < 16; r += 2) m = fmaxf(fmaxf(m, s[r]), s[r + 1]);
    }
  m = fmaxf(m, __shfl_xor(m, 32));
  f32x16 nm;
#pragma unroll
  for (int r = 0; r < 16; ++r) { nm[r] = -m; cm0[r] -= m; cm8[r] -= m; }
  f32x16 o = z16;
#pragma unroll
  for (int t = 0; t < 9; ++t)
    if (FULL || wid + t >= 8) {
      bf16x8 vf0, vf1;
      DSR128(vf0, vaddr, t * 2048);
      DSR128(vf1, vaddr, t * 2048 + 1024);
      const f32x16 s = mfma32(kf[t], qf, t == 0 ? cm0 : (t == 8 ? cm8 : nm));
      bf16x8 p0, p1;
#pragma unroll
      for (int j = 0; j < 8; ++j) {
        p0[j] = (bf16)__builtin_amdgcn_exp2f(s[j]);
        p1[j] = (bf16)__builtin_amdgcn_exp2f(s[8 + j]);
      }
      asm volatile("s_waitcnt lgkmcnt(0)" ::: "memory");
      PIN(vf0); PIN(vf1);
      o = mfma32(vf0, p0, o);
      o = mfma32(vf1, p1, o);
    }
  const float inv = 1.0f / o[8];
  bf16x4 o0, o1;
#pragma unroll
  for (int r = 0; r < 4; ++r) { o0[r] = (bf16)(o[r] * inv); o1[r] = (bf16)(o[4 + r] * inv); }
  *(bf16x4*)dst = o0;
  *(bf16x4*)(dst + 8) = o1;
}

__global__ __launch_bounds__(512, 2) void k2_attn(const bf16* __restrict__ Qb, const bf16* __restrict__ Kb,
                                                  const bf16* __restrict__ Vb, bf16* __restrict__ O,
                                                  const bf16* __restrict__ wo, const bf16* __restrict__ w1,
                                                  const bf16* __restrict__ w3, const bf16* __restrict__ w2) {
  extern __shared__ __attribute__((aligned(16))) char smem[];
  const int tid = threadIdx.x, lane = tid & 63;
  const int wid = __builtin_amdgcn_readfirstlane(tid >> 6);
  const int xg = blockIdx.x & 7, h = blockIdx.x >> 3;
  const unsigned lds0 = (unsigned)(uintptr_t)(LDSP(char))smem;
  const int t0 = xg * 8 - 8;
  const unsigned l16 = lane * 16;

  if (xg > 0 || wid >= 4) {
    const char* kb = (const char*)Kb + (size_t)(h * 64 + t0 + 2 * wid) * 1024;
    glds(l16, kb, lds0 + wid * 2048);
    glds(l16, kb + 1024, lds0 + wid * 2048 + 1024);
    if (!(lane & 16)) {
      const char* vb = (const char*)Vb + (size_t)(h * 64 + t0 + 2 * wid) * 1024;
      const unsigned voffV = (unsigned)(((lane >> 5) * 16 + (lane & 15)) * 16);
#pragma unroll
      for (int i = 0; i < 4; ++i) glds(voffV, vb + i * 512, lds0 + 16384 + wid * 4096 + i * 1024);
    }
  }
  bf16x8 qf;
  GLD4(qf, l16, (const char*)Qb + (size_t)(h * 64 + xg * 8 + wid) * 1024, 0);
  {
    const unsigned sc = lds0 + 49152 + wid * 3072;
    if (wid < 7) {
      const size_t o = (size_t)blockIdx.x * 7168 + wid * 1024;
      glds(l16, (const char*)w1 + o, sc);
      glds(l16, (const char*)w3 + o, sc + 1024);
      glds(l16, (const char*)w2 + o, sc + 2048);
    } else {
      const size_t o = (size_t)blockIdx.x * 2048;
      glds(l16, (const char*)wo + o, sc);
      glds(l16, (const char*)wo + o + 1024, sc + 1024);
      glds(l16, (const char*)wo + o + 1024, sc + 2048);
    }
  }
  {
    const u32x4 o4 = {0x3f803f80u, 0x3f803f80u, 0x3f803f80u, 0x3f803f80u};
    char* p = smem + 16384 + (tid >> 4) * 1024 + ((tid >> 3) & 1) * 512 + 256 + (tid & 7) * 32;
    *(u32x4*)p = o4;
    *(u32x4*)(p + 16) = o4;
  }
  asm volatile("s_waitcnt vmcnt(3) lgkmcnt(0)" ::: "memory");
  PIN(qf);
  __builtin_amdgcn_s_barrier();

  const unsigned kaddr = lds0 + wid * 1024 + l16;
  const unsigned vaddr = lds0 + 16384 + wid * 2048 + l16;
  bf16* dst = O + (size_t)((xg * 8 + wid) * 32 + (lane & 31)) * D_ + h * 16 + (lane >> 5) * 4;
  if (xg > 0) attn_body<true>(kaddr, vaddr, qf, wid, lane, dst);
  else attn_body<false>(kaddr, vaddr, qf, wid, lane, dst);
  wait_vm<0>();
}

template <int MODE, int KTOT>
__global__ __launch_bounds__(512, 2) void k35_gemm(const bf16* __restrict__ A, const bf16* __restrict__ B,
                                                   const bf16* __restrict__ R, bf16* __restrict__ O,
                                                   float* __restrict__ RS, const bf16* __restrict__ w1,
                                                   const bf16* __restrict__ w3, float* __restrict__ CS) {
  constexpr int NST = 4, STAGE = 32768, NK = KTOT / 128, LPC = 4, NT = NK * 4;
  extern __shared__ __attribute__((aligned(16))) char smem[];
  TSTAMP(MODE == 0 ? 2 : 4, 0);
  const int tid = threadIdx.x, lane = tid & 63;
  const int wid = __builtin_amdgcn_readfirstlane(tid >> 6);
  const int wm = wid & 3, wn = wid >> 2;
  const int l15 = lane & 15, gq = lane >> 4;
  const int xg = blockIdx.x & 7, idx = blockIdx.x >> 3;
  const int pm = xg * 4 + (idx >> 3), pn = idx & 7;
  const unsigned lds0 = (unsigned)(uintptr_t)(LDSP(char))smem;

  u32x4 cw0, cw1;
  if (MODE == 0) {
    const int ct = blockIdx.x & 31, kslab = blockIdx.x >> 5;
    const char* wb = (const char*)(ct < 16 ? w1 : w3) + (size_t)(kslab * 64 + wid * 8) * (F_ * 2) + (ct & 15) * 224;
    const unsigned vo = (unsigned)(gq * 2 * (F_ * 2) + l15 * 16);
    if (l15 < 14) {
      GLD4(cw0, vo, wb, 0);
      GLD4(cw1, vo, wb, F_ * 2);
    }
  }
  u32x2 res[2];
  const int tok = pm * 64 + wm * 16 + l15;
  const int ncol = pn * 64 + wn * 32 + gq * 4;
  {
    const unsigned vo = (unsigned)((tok * D_ + ncol) * 2);
    GLD2(res[0], vo, R, 0);
    GLD2(res[1], vo, R, 32);
  }

  const int ra = wid * 4 + gq;
  const unsigned voffA = (unsigned)(ra * (KTOT * 2) + ((l15 ^ (ra & 15)) * 16));
  const int pB = ((lane & 7) >> 1) ^ (((lane >> 4) & 1) | ((wid & 1) << 1));
  const unsigned voffB = (unsigned)((wid * 8 + (lane >> 3)) * 1024 + pB * 32 + (lane & 1) * 16);
  const char* baseA = (const char*)A + (size_t)pm * 64 * (KTOT * 2);
  const char* baseB = (const char*)B + pn * 128;
  const unsigned sbw = lds0 + wid * 1024;
  auto issue = [&](int kc) {
    const unsigned sb = sbw + (kc % NST) * STAGE;
    glds(voffA, baseA + kc * 256, sb);
    glds(voffA, baseA + kc * 256 + 32 * (KTOT * 2), sb + 8192);
    glds(voffB, baseB + (size_t)kc * (128 * 1024), sb + 16384);
    glds(voffB, baseB + (size_t)kc * (128 * 1024) + 65536, sb + 16384 + 8192);
  };
#pragma unroll
  for (int s = 0; s < NST; ++s) issue(s);
  TSTAMP(MODE == 0 ? 2 : 4, 1);

  unsigned a_addr[4], b_addr[2];
#pragma unroll
  for (int ks = 0; ks < 4; ++ks) a_addr[ks] = lds0 + (wm * 16 + l15) * 256 + (((4 * ks + gq) ^ l15) * 16);
  {
    const int kl = (gq >> 1) * 16 + (gq & 1) * 8 + (l15 >> 2);
    const int fr = ((l15 >> 3) & 1) | ((gq & 1) << 1);
#pragma unroll
    for (int j = 0; j < 2; ++j) b_addr[j] = lds0 + 16384 + kl * 128 + (((2 * wn + j) ^ fr) * 32) + (l15 & 3) * 8;
  }

  float* cpart = (float*)(smem + NST * STAGE);
  if (MODE == 0) {
    wait_vm<NST * LPC + 2>();
    PIN(cw0); PIN(cw1);
    float cs[8];
#pragma unroll
    for (int j = 0; j < 4; ++j) {
      cs[2 * j] = __builtin_bit_cast(float, cw0[j] << 16) + __builtin_bit_cast(float, cw1[j] << 16);
      cs[2 * j + 1] = __builtin_bit_cast(float, cw0[j] & 0xffff0000u) + __builtin_bit_cast(float, cw1[j] & 0xffff0000u);
    }
#pragma unroll
    for (int j = 0; j < 8; ++j) {
      cs[j] += __shfl_xor(cs[j], 16);
      cs[j] += __shfl_xor(cs[j], 32);
    }
    if (lane < 14) {
      *(f32x4*)(cpart + wid * 112 + lane * 8) = f32x4{cs[0], cs[1], cs[2], cs[3]};
      *(f32x4*)(cpart + wid * 112 + lane * 8 + 4) = f32x4{cs[4], cs[5], cs[6], cs[7]};
    }
  }

  f32x4 acc[2];
  const f32x4 z4 = {0.f, 0.f, 0.f, 0.f};
  acc[0] = z4; acc[1] = z4;
  bf16x8 af[2];
  bf16x4 bl[2][2], bh[2][2];

  auto rd = [&](int t, int set) {
    const int kc = t >> 2, ks = t & 3, st = kc % NST;
    const unsigned hi = (st >= 2) ? 65536u : 0u;
    const int so = (st & 1) * STAGE;
    DSRTR(bl[set][0], b_addr[0] + hi, so + ks * 4096);
    DSRTR(bh[set][0], b_addr[0] + hi, so + ks * 4096 + 512);
    DSR128(af[set], a_addr[ks] + hi, so);
    DSRTR(bl[set][1], b_addr[1] + hi, so + ks * 4096);
    DSRTR(bh[set][1], b_addr[1] + hi, so + ks * 4096 + 512);
  };
  auto wt = [&](int set) {
    asm volatile("s_waitcnt lgkmcnt(0)" ::: "memory");
    PIN(af[set]);
    PIN(bl[set][0]); PIN(bh[set][0]); PIN(bl[set][1]); PIN(bh[set][1]);
  };

  asm volatile("s_waitcnt vmcnt(%0) lgkmcnt(0)" ::"n"((NST - 1) * LPC) : "memory");
  __builtin_amdgcn_s_barrier();
  TSTAMP(MODE == 0 ? 2 : 4, 2);
  rd(0, 0);
  wt(0);
#pragma unroll
  for (int t = 0; t < NT; ++t) {
    const int kc = t >> 2, ks = t & 3, cur = t & 1, nxt = cur ^ 1;
    if (ks == 3 && kc + 1 < NK) {
      const int last = (kc + NST - 1 < NK - 1) ? (kc + NST - 1) : (NK - 1);
      wait_vm_n((last - (kc + 1)) * LPC);
      __builtin_amdgcn_s_barrier();
      TSTAMP(MODE == 0 ? 2 : 4, 3 + kc);
    }
    if (t + 1 < NT) rd(t + 1, nxt);
    if (ks == 3 && kc + NST < NK) issue(kc + NST);
    __builtin_amdgcn_sched_barrier(0);
    acc[0] = mfma16(cat8(bl[cur][0], bh[cur][0]), af[cur], acc[0]);
    acc[1] = mfma16(cat8(bl[cur][1], bh[cur][1]), af[cur], acc[1]);
    __builtin_amdgcn_sched_barrier(0);
    if (t + 1 < NT) wt(nxt);
  }
  TSTAMP(MODE == 0 ? 2 : 4, 20);
  PIN(res[0]); PIN(res[1]);
  float s = 0.f, q = 0.f;
#pragma unroll
  for (int j = 0; j < 2; ++j) {
    const bf16x4 rr = __builtin_bit_cast(bf16x4, res[j]);
    bf16x4 o;
#pragma unroll
    for (int r = 0; r < 4; ++r) {
      o[r] = (bf16)((float)rr[r] + bfr(acc[j][r]));
      const float v = (float)o[r];
      s += v;
      q += v * v;
    }
    *(bf16x4*)(O + (size_t)tok * D_ + ncol + j * 16) = o;
  }
  if (MODE == 0) {
    s += __shfl_xor(s, 16); s += __shfl_xor(s, 32);
    q += __shfl_xor(q, 16); q += __shfl_xor(q, 32);
    if (gq == 0) *(f32x2*)(RS + ((size_t)tok * 16 + pn * 2 + wn) * 2) = f32x2{s, q};
    if (tid < 112) {
      float c = 0.f;
#pragma unroll
      for (int p = 0; p < 8; ++p) c += cpart[p * 112 + tid];
      const int ct = blockIdx.x & 31, kslab = blockIdx.x >> 5;
      CS[kslab * (2 * F_) + ct * 112 + tid] = c;
    }
  }
  TSTAMP(MODE == 0 ? 2 : 4, 22);
}

template <int NP, int P0>
DEV void gu_body(char* smem, unsigned lds0, int wm, int lane, unsigned voffA, unsigned voffB, bool validB,
                 const char* baseA, const char* baseB1, const char* baseB3, unsigned sbw, bf16* __restrict__ dst0) {
  constexpr int NST = 3, STAGE = 49152, NK = 8, LPC = 6, NT = 16;
  const int l15 = lane & 15, gq = lane >> 4;
  auto issue = [&](int kc) {
    const unsigned sb = sbw + (kc % NST) * STAGE;
    glds(voffA, baseA + kc * 128, sb);
    glds(voffA, baseA + kc * 128 + 65536, sb + 8192);
    if (validB) {
      glds(voffB, baseB1 + kc * (64 * 3584), sb + 16384);
      glds(voffB, baseB1 + kc * (64 * 3584) + 32 * 3584, sb + 16384 + 8192);
      glds(voffB, baseB3 + kc * (64 * 3584), sb + 32768);
      glds(voffB, baseB3 + kc * (64 * 3584) + 32 * 3584, sb + 32768 + 8192);
    }
  };
  unsigned a_addr[2], b_addr[NP];
#pragma unroll
  for (int ks = 0; ks < 2; ++ks) a_addr[ks] = lds0 + (wm * 32 + l15) * 128 + (((4 * ks + gq) ^ (l15 >> 1)) * 16);
  {
    const int kl = (gq >> 1) * 16 + (gq & 1) * 8 + (l15 >> 2);
    const int fr = (l15 >> 2) | ((gq & 1) << 2);
#pragma unroll
    for (int j = 0; j < NP; ++j) b_addr[j] = lds0 + 16384 + kl * 256 + (((P0 + j) ^ fr) * 32) + (l15 & 3) * 8;
  }
  f32x4 ag[2][NP], au[2][NP];
  const f32x4 z4 = {0.f, 0.f, 0.f, 0.f};
#pragma unroll
  for (int j = 0; j < NP; ++j) { ag[0][j] = z4; ag[1][j] = z4; au[0][j] = z4; au[1][j] = z4; }
  bf16x8 af[2][2];
  bf16x4 gl[2][NP], gh[2][NP], ul[2][NP], uh[2][NP];
  auto rd = [&](int t, int set) {
    const int kc = t >> 1, ks = t & 1, st = kc % NST;
    const unsigned so = st * STAGE;
    DSR128(af[set][0], a_addr[ks] + so, 0);
    DSR128(af[set][1], a_addr[ks] + so, 2048);
#pragma unroll
    for (int j = 0; j < NP; ++j) {
      DSRTR(gl[set][j], b_addr[j] + so, ks * 8192);
      DSRTR(gh[set][j], b_addr[j] + so, ks * 8192 + 1024);
      DSRTR(ul[set][j], b_addr[j] + so, 16384 + ks * 8192);
      DSRTR(uh[set][j], b_addr[j] + so, 16384 + ks * 8192 + 1024);
    }
  };
  auto wt = [&](int set) {
    asm volatile("s_waitcnt lgkmcnt(0)" ::: "memory");
    PIN(af[set][0]); PIN(af[set][1]);
#pragma unroll
    for (int j = 0; j < NP; ++j) { PIN(gl[set][j]); PIN(gh[set][j]); PIN(ul[set][j]); PIN(uh[set][j]); }
  };
  rd(0, 0);
  wt(0);
#pragma unroll
  for (int t = 0; t < NT; ++t) {
    const int kc = t >> 1, ks = t & 1, cur = t & 1, nxt = cur ^ 1;
    if (ks == 1 && kc + 1 < NK) {
      const int last = (kc + NST - 1 < NK - 1) ? (kc + NST - 1) : (NK - 1);
      wait_vm_n((last - (kc + 1)) * LPC);
      __builtin_amdgcn_s_barrier();
      TSTAMP(3, 3 + kc);
    }
    if (t + 1 < NT) rd(t + 1, nxt);
    if (ks == 1 && kc + NST < NK) issue(kc + NST);
    __builtin_amdgcn_sched_barrier(0);
#pragma unroll
    for (int j = 0; j < NP; ++j) {
      const bf16x8 bg = cat8(gl[cur][j], gh[cur][j]), bu = cat8(ul[cur][j], uh[cur][j]);
      ag[0][j] = mfma16(bg, af[cur][0], ag[0][j]);
      ag[1][j] = mfma16(bg, af[cur][1], ag[1][j]);
      au[0][j] = mfma16(bu, af[cur][0], au[0][j]);
      au[1][j] = mfma16(bu, af[cur][1], au[1][j]);
    }
    __builtin_amdgcn_sched_barrier(0);
    if (t + 1 < NT) wt(nxt);
  }
  TSTAMP(3, 20);
  const float* st_ = (const float*)(smem + NST * STAGE);
  const float* csum = st_ + 256;
#pragma unroll
  for (int mi = 0; mi < 2; ++mi) {
    const f32x2 ms = *(const f32x2*)(st_ + (wm * 32 + mi * 16 + l15) * 2);
#pragma unroll
    for (int j = 0; j < NP; ++j) {
      const f32x4 cg = *(const f32x4*)(csum + (P0 + j) * 16 + gq * 4);
      const f32x4 cu = *(const f32x4*)(csum + 112 + (P0 + j) * 16 + gq * 4);
      bf16x4 o;
#pragma unroll
      for (int r = 0; r < 4; ++r) {
        const float g = (ag[mi][j][r] - ms[0] * cg[r]) * ms[1];
        const float u = (au[mi][j][r] - ms[0] * cu[r]) * ms[1];
        const float e = __builtin_amdgcn_exp2f(g * -1.4426950408889634f);
        o[r] = (bf16)(g * u * __builtin_amdgcn_rcpf(1.0f + e));
      }
      *(bf16x4*)(dst0 + (size_t)mi * 16 * F_ + (P0 + j) * 16) = o;
    }
  }
}

__global__ __launch_bounds__(512, 2) void k4_gu(const bf16* __restrict__ Hh, const bf16* __restrict__ w1,
                                                const bf16* __restrict__ w3, const float* __restrict__ RS,
                                                const float* __restrict__ CS, bf16* __restrict__ Hm) {
  constexpr int NST = 3, STAGE = 49152, LPC = 6;
  extern __shared__ __attribute__((aligned(16))) char smem[];
  TSTAMP(3, 0);
  const int tid = threadIdx.x, lane = tid & 63;
  const int wid = __builtin_amdgcn_readfirstlane(tid >> 6);
  const int wm = wid & 3, wn = wid >> 2;
  const int l15 = lane & 15, gq = lane >> 4;
  const int xg = blockIdx.x & 7, idx = blockIdx.x >> 3;
  const int pm = xg * 2 + (idx >> 4), pn = idx & 15;
  const unsigned lds0 = (unsigned)(uintptr_t)(LDSP(char))smem;

  const unsigned voffA = (unsigned)((wid * 8 + (lane >> 3)) * 1024 + (((lane & 7) ^ ((wid & 1) * 4 + gq)) * 16));
  const int pB = (l15 >> 1) ^ (gq | (((wid >> 1) & 1) << 2));
  const bool validB = pB < 7;
  const unsigned voffB = (unsigned)((wid * 4 + gq) * 3584 + (pB * 16 + (lane & 1) * 8) * 2);
  const char* baseA = (const char*)Hh + (size_t)pm * (128 * 1024);
  const char* baseB1 = (const char*)w1 + pn * 224;
  const char* baseB3 = (const char*)w3 + pn * 224;
  const unsigned sbw = lds0 + wid * 1024;
  auto issue_all = [&]() {
#pragma unroll
    for (int kc = 0; kc < NST; ++kc) {
      const unsigned sb = sbw + kc * STAGE;
      glds(voffA, baseA + kc * 128, sb);
      glds(voffA, baseA + kc * 128 + 65536, sb + 8192);
      if (validB) {
        glds(voffB, baseB1 + kc * (64 * 3584), sb + 16384);
        glds(voffB, baseB1 + kc * (64 * 3584) + 32 * 3584, sb + 16384 + 8192);
        glds(voffB, baseB3 + kc * (64 * 3584), sb + 32768);
        glds(voffB, baseB3 + kc * (64 * 3584) + 32 * 3584, sb + 32768 + 8192);
      }
    }
  };
  {
    float* st_ = (float*)(smem + NST * STAGE);
    float* csum = st_ + 256;
    if (wid < 2) {
      f32x4 sv[8];
      const char* rb = (const char*)RS + (size_t)(pm * 128) * 128;
      const unsigned vo = (unsigned)(tid * 128);
#pragma unroll
      for (int i = 0; i < 8; ++i) GLD4(sv[i], vo, rb, i * 16);
      issue_all();
      wait_vm<NST * LPC>();
#pragma unroll
      for (int i = 0; i < 8; ++i) PIN(sv[i]);
      f32x4 a = sv[0];
#pragma unroll
      for (int i = 1; i < 8; ++i) a += sv[i];
      *(f32x2*)(st_ + tid * 2) = ln_stats(a[0] + a[2], a[1] + a[3]);
    } else if (wid < 6) {
      float sc[8];
      int c = tid - 128;
      const bool ok = c < 224;
      if (!ok) c = 0;
      const unsigned vo = (unsigned)(((c < 112 ? 0 : F_ - 112) + pn * 112 + c) * 4);
#pragma unroll
      for (int i = 0; i < 8; ++i) GLD1(sc[i], vo, (const char*)CS + i * (2 * F_ * 4), 0);
      issue_all();
      wait_vm<NST * LPC>();
#pragma unroll
      for (int i = 0; i < 8; ++i) PIN(sc[i]);
      float a = sc[0];
#pragma unroll
      for (int i = 1; i < 8; ++i) a += sc[i];
      if (ok) csum[c] = a;
    } else {
      issue_all();
    }
  }
  TSTAMP(3, 1);
  asm volatile("s_waitcnt vmcnt(%0) lgkmcnt(0)" ::"n"((NST - 1) * LPC) : "memory");
  __builtin_amdgcn_s_barrier();
  TSTAMP(3, 2);
  bf16* dst0 = Hm + (size_t)(pm * 128 + wm * 32 + l15) * F_ + pn * 112 + gq * 4;
  if (wn == 0) gu_body<4, 0>(smem, lds0, wm, lane, voffA, voffB, validB, baseA, baseB1, baseB3, sbw, dst0);
  else gu_body<3, 4>(smem, lds0, wm, lane, voffA, voffB, validB, baseA, baseB1, baseB3, sbw, dst0);
  TSTAMP(3, 22);
}

extern "C" void kernel_launch(void* const* d_in, const int* in_sizes, int n_in, void* d_out, int out_size, void* d_ws,
                              size_t ws_size, hipStream_t stream) {
  const bf16* x = (const bf16*)d_in[0];
  const bf16* wqkv = (const bf16*)d_in[1];
  const bf16* wo = (const bf16*)d_in[2];
  const bf16* w1 = (const bf16*)d_in[4];
  const bf16* w3 = (const bf16*)d_in[5];
  const bf16* w2 = (const bf16*)d_in[6];
  bf16* out = (bf16*)d_out;

  char* ws = (char*)d_ws;
  bf16* Qb = (bf16*)(ws);
  bf16* Kb = (bf16*)(ws + (2u << 20));
  bf16* Vb = (bf16*)(ws + (4u << 20));
  bf16* Oa = (bf16*)(ws + (7u << 20));
  bf16* Hh = (bf16*)(ws + (9u << 20));
  bf16* Hm = (bf16*)(ws + (11u << 20));
  float* RS = (float*)(ws + (18u << 20));
  float* CS = (float*)(ws + (19u << 20));

  {
    constexpr int lds = 4 * 32768 + 1024 + 4 * 96 * 4;
    (void)hipFuncSetAttribute((const void*)k1_qkv, hipFuncAttributeMaxDynamicSharedMemorySize, lds);
    k1_qkv<<<256, 512, lds, stream>>>(x, wqkv, Qb, Kb, Vb);
  }
  {
    constexpr int lds = 49152 + 8 * 3072;
    (void)hipFuncSetAttribute((const void*)k2_attn, hipFuncAttributeMaxDynamicSharedMemorySize, lds);
    k2_attn<<<256, 512, lds, stream>>>(Qb, Kb, Vb, Oa, wo, w1, w3, w2);
  }
  {
    auto k = k35_gemm<0, 512>;
    constexpr int lds = 4 * 32768 + 8 * 112 * 4;
    (void)hipFuncSetAttribute((const void*)k, hipFuncAttributeMaxDynamicSharedMemorySize, lds);
    k<<<256, 512, lds, stream>>>(Oa, wo, x, Hh, RS, w1, w3, CS);
  }
  {
    constexpr int lds = 3 * 49152 + 1024 + 224 * 4;
    (void)hipFuncSetAttribute((const void*)k4_gu, hipFuncAttributeMaxDynamicSharedMemorySize, lds);
    k4_gu<<<256, 512, lds, stream>>>(Hh, w1, w3, RS, CS, Hm);
  }
  {
    auto k = k35_gemm<1, 1792>;
    constexpr int lds = 4 * 32768;
    (void)hipFuncSetAttribute((const void*)k, hipFuncAttributeMaxDynamicSharedMemorySize, lds);
    k<<<256, 512, lds, stream>>>(Hm, w2, Hh, out, nullptr, nullptr, nullptr, nullptr);
  }
}
```

```cpp
#include <hip/hip_runtime.h>
#include <stdint.h>

typedef __attribute__((ext_vector_type(8))) __bf16 bf16x8;
typedef __attribute__((ext_vector_type(4))) __bf16 bf16x4;
typedef __attribute__((ext_vector_type(2))) __bf16 bf16x2;
typedef __attribute__((ext_vector_type(2))) float f32x2;
typedef __attribute__((ext_vector_type(4))) float f32x4;
typedef __attribute__((ext_vector_type(16))) float f32x16;
typedef __attribute__((ext_vector_type(4))) unsigned int u32x4;
typedef __attribute__((ext_vector_type(2))) unsigned int u32x2;
typedef __bf16 bf16;

#define LDSP(T) __attribute__((address_space(3))) T*
#define DEV __device__ __forceinline__

constexpr int S_ = 2048, D_ = 512, H_ = 32, F_ = 1792;

DEV float bfr(float x) { return (float)(bf16)x; }

template <int N>
DEV void wait_vm() {
  asm volatile("s_waitcnt vmcnt(%0)" ::"n"(N) : "memory");
}
DEV void wait_vm_n(int n) {
  switch (n) {
#define WC(X) case X: wait_vm<X>(); break;
    WC(0) WC(1) WC(2) WC(3) WC(4) WC(5) WC(6) WC(7) WC(8) WC(9) WC(10) WC(11) WC(12) WC(13) WC(14) WC(15)
    WC(16) WC(17) WC(18) WC(19) WC(20) WC(21) WC(22) WC(23) WC(24)
#undef WC
    default: wait_vm<0>(); break;
  }
}

DEV void glds(unsigned voff, const void* sbase, unsigned ldsaddr) {
  asm volatile("s_mov_b32 m0, %2\n\ts_nop 0\n\tglobal_load_lds_dwordx4 %0, %1" ::"v"(voff), "s"(sbase), "s"(ldsaddr)
               : "memory");
}
#define DSR128(dst, addr, off) asm volatile("ds_read_b128 %0, %1 offset:%2" : "=v"(dst) : "v"(addr), "i"(off))
#define DSRTR(dst, addr, off) asm volatile("ds_read_b64_tr_b16 %0, %1 offset:%2" : "=v"(dst) : "v"(addr), "i"(off))
#define PIN(x) asm volatile("" : "+v"(x))
#define GLD4(dst, voff, sbase, off) \
  asm volatile("global_load_dwordx4 %0, %1, %2 offset:%3" : "=v"(dst) : "v"(voff), "s"(sbase), "i"(off))
#define GLD2(dst, voff, sbase, off) \
  asm volatile("global_load_dwordx2 %0, %1, %2 offset:%3" : "=v"(dst) : "v"(voff), "s"(sbase), "i"(off))
#define GLD1(dst, voff, sbase, off) \
  asm volatile("global_load_dword %0, %1, %2 offset:%3" : "=v"(dst) : "v"(voff), "s"(sbase), "i"(off))

#define TDECL
#define TSTAMP(m, i) do { } while (0)
#define TFLUSH(m) do { } while (0)
#define TARG
#define TPASS

DEV bf16x8 cat8(bf16x4 a, bf16x4 b) { return __builtin_shufflevector(a, b, 0, 1, 2, 3, 4, 5, 6, 7); }
DEV f32x4 mfma16(bf16x8 a, bf16x8 b, f32x4 c) { return __builtin_amdgcn_mfma_f32_16x16x32_bf16(a, b, c, 0, 0, 0); }
DEV f32x16 mfma32(bf16x8 a, bf16x8 b, f32x16 c) { return __builtin_amdgcn_mfma_f32_32x32x16_bf16(a, b, c, 0, 0, 0); }

DEV f32x2 ln_stats(float sum, float sq) {
  float m = sum * (1.0f / D_);
  float var = fmaxf(sq * (1.0f / D_) - m * m, 0.f);
  float vb = bfr(var);
  float sd = bfr(__builtin_sqrtf(bfr(vb + 1e-5f)));
  f32x2 r;
  r[0] = bfr(m);
  r[1] = 1.0f / sd;
  return r;
}

__global__ __launch_bounds__(512, 2) void k1_qkv(const bf16* __restrict__ x, const bf16* __restrict__ w,
                                                 bf16* __restrict__ Qb, bf16* __restrict__ Kb,
                                                 bf16* __restrict__ Vb) {
  constexpr int NST = 4, STAGE = 32768, NK = 8, LPC = 4, NT = 16;
  extern __shared__ __attribute__((aligned(16))) char smem[];
  TDECL
  TSTAMP(0, 0);
  const int tid = threadIdx.x, lane = tid & 63;
  const int wid = __builtin_amdgcn_readfirstlane(tid >> 6);
  const int wm = wid & 3, wn = wid >> 2;
  const int l15 = lane & 15, gq = lane >> 4;
  const int xg = blockIdx.x & 7, idx = blockIdx.x >> 3;
  const int pm = xg * 2 + (idx >> 4), pn = idx & 15;
  const unsigned lds0 = (unsigned)(uintptr_t)(LDSP(char))smem;

  const unsigned voffA = (unsigned)((wid * 8 + (lane >> 3)) * 1024 + (((lane & 7) ^ ((wid & 1) * 4 + gq)) * 16));
  const int pB = (l15 >> 1) ^ (gq | (((wid >> 1) & 1) << 2));
  const bool validB = pB < 6;
  const unsigned voffB =
      (unsigned)((wid * 4 + gq) * 3072 + ((pB >> 1) * 512 + (pB & 1) * 16 + (lane & 1) * 8) * 2);
  const char* baseA = (const char*)x + (size_t)pm * (128 * 1024);
  const char* baseB = (const char*)w + pn * 64;
  const unsigned sbw = lds0 + wid * 1024;
  auto issue = [&](int kc) {
    const unsigned sb = sbw + (kc % NST) * STAGE;
    glds(voffA, baseA + kc * 128, sb);
    glds(voffA, baseA + kc * 128 + 65536, sb + 8192);
    if (validB) {
      glds(voffB, baseB + kc * (64 * 3072), sb + 16384);
      glds(voffB, baseB + kc * (64 * 3072) + 32 * 3072, sb + 16384 + 8192);
    }
  };
#pragma unroll
  for (int s = 0; s < NST; ++s) issue(s);
  TSTAMP(0, 1);

  unsigned a_addr[2][2], b_addr[3];
#pragma unroll
  for (int mi = 0; mi < 2; ++mi)
#pragma unroll
    for (int ks = 0; ks < 2; ++ks)
      a_addr[mi][ks] = lds0 + (wm * 32 + ((mi ^ wn) * 16) + l15) * 128 + (((4 * ks + gq) ^ (l15 >> 1)) * 16);
  {
    const int kl = (gq >> 1) * 16 + (gq & 1) * 8 + (l15 >> 2);
    const int fr = (l15 >> 2) | ((gq & 1) << 2);
#pragma unroll
    for (int j = 0; j < 3; ++j) b_addr[j] = lds0 + 16384 + kl * 256 + (((2 * j + wn) ^ fr) * 32) + (l15 & 3) * 8;
  }

  f32x4 acc[2][3], s1, s2, cs[3];
  const f32x4 z4 = {0.f, 0.f, 0.f, 0.f};
#pragma unroll
  for (int j = 0; j < 3; ++j) { acc[0][j] = z4; acc[1][j] = z4; cs[j] = z4; }
  s1 = z4; s2 = z4;
  bf16x8 af[2][2];
  bf16x4 bl[2][3], bh[2][3];
  const bf16 one = (bf16)1.0f;
  const bf16x8 ones = {one, one, one, one, one, one, one, one};

  auto rd = [&](int t, int set) {
    const int kc = t >> 1, ks = t & 1, st = kc % NST;
    const unsigned hi = (st >= 2) ? 65536u : 0u;
    const int so = (st & 1) * STAGE;
#pragma unroll
    for (int j = 0; j < 3; ++j) {
      DSRTR(bl[set][j], b_addr[j] + hi, so + ks * 8192);
      DSRTR(bh[set][j], b_addr[j] + hi, so + ks * 8192 + 1024);
      if (j < 2) DSR128(af[set][j], a_addr[j][ks] + hi, so);
    }
  };
  auto wt = [&](int set) {
    asm volatile("s_waitcnt lgkmcnt(0)" ::: "memory");
    PIN(af[set][0]); PIN(af[set][1]);
#pragma unroll
    for (int j = 0; j < 3; ++j) { PIN(bl[set][j]); PIN(bh[set][j]); }
  };

  wait_vm<(NST - 1) * LPC>();
  __builtin_amdgcn_s_barrier();
  TSTAMP(0, 2);
  rd(0, 0);
  wt(0);
#pragma unroll
  for (int t = 0; t < NT; ++t) {
    const int kc = t >> 1, ks = t & 1, cur = t & 1, nxt = cur ^ 1;
    if (ks == 1 && kc + 1 < NK) {
      const int last = (kc + NST - 1 < NK - 1) ? (kc + NST - 1) : (NK - 1);
      wait_vm_n((last - (kc + 1)) * LPC);
      __builtin_amdgcn_s_barrier();
      TSTAMP(0, 3 + kc);
    }
    if (t + 1 < NT) rd(t + 1, nxt);
    if (ks == 1 && kc + NST < NK) issue(kc + NST);
    __builtin_amdgcn_sched_barrier(0);
    {
      const bf16x8 bq = cat8(bl[cur][0], bh[cur][0]), bk = cat8(bl[cur][1], bh[cur][1]),
                   bv = cat8(bl[cur][2], bh[cur][2]);
#pragma unroll
      for (int mi = 0; mi < 2; ++mi) {
        acc[mi][0] = mfma16(bq, af[cur][mi], acc[mi][0]);
        acc[mi][1] = mfma16(bk, af[cur][mi], acc[mi][1]);
        acc[mi][2] = mfma16(af[cur][mi], bv, acc[mi][2]);
      }
      s1 = mfma16(ones, af[cur][0], s1);
      s2 = mfma16(af[cur][0], af[cur][0], s2);
      if ((t & 3) == wm) {
        cs[0] = mfma16(bq, ones, cs[0]);
        cs[1] = mfma16(bk, ones, cs[1]);
        cs[2] = mfma16(bv, ones, cs[2]);
      }
    }
    __builtin_amdgcn_sched_barrier(0);
    if (t + 1 < NT) wt(nxt);
  }
  TSTAMP(0, 20);

  float* st_ = (float*)(smem + NST * STAGE);
  float* csum = st_ + 256;
  {
    const float sq = (l15 & 2) ? ((l15 & 1) ? s2[3] : s2[2]) : ((l15 & 1) ? s2[1] : s2[0]);
    const f32x2 ms = ln_stats(s1[0], sq);
    if (gq == (l15 >> 2)) *(f32x2*)(st_ + (wm * 32 + wn * 16 + l15) * 2) = ms;
    if (l15 == 0) {
#pragma unroll
      for (int j = 0; j < 3; ++j) *(f32x4*)(csum + wm * 96 + (2 * j + wn) * 16 + gq * 4) = cs[j];
    }
  }
  __syncthreads();
  TSTAMP(0, 21);
  const int h = pn * 2 + wn;
  const int tile = pm * 4 + wm;
  f32x4 c4[2];
  float cv = 0.f;
#pragma unroll
  for (int j = 0; j < 2; ++j) {
    c4[j] = z4;
#pragma unroll
    for (int p = 0; p < 4; ++p) c4[j] += *(const f32x4*)(csum + p * 96 + (2 * j + wn) * 16 + gq * 4);
  }
#pragma unroll
  for (int p = 0; p < 4; ++p) cv += csum[p * 96 + (4 + wn) * 16 + l15];
  const float qscale = 0.25f * 1.4426950408889634f;
#pragma unroll
  for (int mi = 0; mi < 2; ++mi) {
    const int tf = mi ^ wn;
    const f32x2 ms = *(const f32x2*)(st_ + (wm * 32 + tf * 16 + l15) * 2);
#pragma unroll
    for (int j = 0; j < 2; ++j) {
      const float rs = j == 0 ? ms[1] * qscale : ms[1];
      bf16x4 o;
#pragma unroll
      for (int r = 0; r < 4; ++r) o[r] = (bf16)((acc[mi][j][r] - ms[0] * c4[j][r]) * rs);
      bf16* dst = (j == 0 ? Qb : Kb) + ((size_t)(h * 64 + tile) * 2 + (gq >> 1)) * 256 + (tf * 16 + l15) * 8 + (gq & 1) * 4;
      *(bf16x4*)dst = o;
    }
    {
      const f32x4 m01 = *(const f32x4*)(st_ + (wm * 32 + tf * 16 + gq * 4) * 2);
      const f32x4 m23 = *(const f32x4*)(st_ + (wm * 32 + tf * 16 + gq * 4 + 2) * 2);
      bf16x4 o;
      o[0] = (bf16)((acc[mi][2][0] - m01[0] * cv) * m01[1]);
      o[1] = (bf16)((acc[mi][2][1] - m01[2] * cv) * m01[3]);
      o[2] = (bf16)((acc[mi][2][2] - m23[0] * cv) * m23[1]);
      o[3] = (bf16)((acc[mi][2][3] - m23[2] * cv) * m23[3]);
      bf16* dst = Vb + (((size_t)(h * 64 + tile) * 2 + tf) * 2 + (gq & 1)) * 128 + l15 * 8 + (gq >> 1) * 4;
      *(bf16x4*)dst = o;
    }
  }
  TSTAMP(0, 22);
  TFLUSH(0);
}

template <bool FULL>
DEV void attn_body(unsigned kaddr, unsigned vaddr, bf16x8 qf, int wid, int lane, bf16* dst) {
  const int c = lane & 31, hh = lane >> 5;
  const float NEG = -1e30f;
  f32x16 cm0, cm8, z16;
#pragma unroll
  for (int r = 0; r < 16; ++r) {
    const int kr = (r & 3) + 8 * (r >> 2) + 4 * hh;
    cm0[r] = (kr > c) ? 0.f : NEG;
    cm8[r] = (kr <= c) ? 0.f : NEG;
    z16[r] = 0.f;
  }
  bf16x8 kf[9];
#pragma unroll
  for (int t = 0; t < 9; ++t)
    if (FULL || wid + t >= 8) DSR128(kf[t], kaddr, t * 1024);
  asm volatile("s_waitcnt lgkmcnt(0)" ::: "memory");
#pragma unroll
  for (int t = 0; t < 9; ++t) PIN(kf[t]);
  float m = NEG;
#pragma unroll
  for (int t = 0; t < 9; ++t)
    if (FULL || wid + t >= 8) {
      const f32x16 s = mfma32(kf[t], qf, t == 0 ? cm0 : (t == 8 ? cm8 : z16));
#pragma unroll
      for (int r = 0; r < 16; r += 2) m = fmaxf(fmaxf(m, s[r]), s[r + 1]);
    }
  m = fmaxf(m, __shfl_xor(m, 32));
  f32x16 nm;
#pragma unroll
  for (int r = 0; r < 16; ++r) { nm[r] = -m; cm0[r] -= m; cm8[r] -= m; }
  f32x16 o = z16;
#pragma unroll
  for (int t = 0; t < 9; ++t)
    if (FULL || wid + t >= 8) {
      bf16x8 vf0, vf1;
      DSR128(vf0, vaddr, t * 2048);
      DSR128(vf1, vaddr, t * 2048 + 1024);
      const f32x16 s = mfma32(kf[t], qf, t == 0 ? cm0 : (t == 8 ? cm8 : nm));
      bf16x8 p0, p1;
#pragma unroll
      for (int j = 0; j < 8; ++j) {
        p0[j] = (bf16)__builtin_amdgcn_exp2f(s[j]);
        p1[j] = (bf16)__builtin_amdgcn_exp2f(s[8 + j]);
      }
      asm volatile("s_waitcnt lgkmcnt(0)" ::: "memory");
      PIN(vf0); PIN(vf1);
      o = mfma32(vf0, p0, o);
      o = mfma32(vf1, p1, o);
    }
  const float inv = 1.0f / o[8];
  bf16x4 o0, o1;
#pragma unroll
  for (int r = 0; r < 4; ++r) { o0[r] = (bf16)(o[r] * inv); o1[r] = (bf16)(o[4 + r] * inv); }
  *(bf16x4*)dst = o0;
  *(bf16x4*)(dst + 8) = o1;
}

__global__ __launch_bounds__(512, 2) void k2_attn(const bf16* __restrict__ Qb, const bf16* __restrict__ Kb,
                                                  const bf16* __restrict__ Vb, bf16* __restrict__ O,
                                                  const bf16* __restrict__ wo, const bf16* __restrict__ w1,
                                                  const bf16* __restrict__ w3, const bf16* __restrict__ w2) {
  extern __shared__ __attribute__((aligned(16))) char smem[];
  const int tid = threadIdx.x, lane = tid & 63;
  const int wid = __builtin_amdgcn_readfirstlane(tid >> 6);
  const int xg = blockIdx.x & 7, h = blockIdx.x >> 3;
  const unsigned lds0 = (unsigned)(uintptr_t)(LDSP(char))smem;
  const int t0 = xg * 8 - 8;
  const unsigned l16 = lane * 16;

  if (xg > 0 || wid >= 4) {
    const char* kb = (const char*)Kb + (size_t)(h * 64 + t0 + 2 * wid) * 1024;
    glds(l16, kb, lds0 + wid * 2048);
    glds(l16, kb + 1024, lds0 + wid * 2048 + 1024);
    if (!(lane & 16)) {
      const char* vb = (const char*)Vb + (size_t)(h * 64 + t0 + 2 * wid) * 1024;
      const unsigned voffV = (unsigned)(((lane >> 5) * 16 + (lane & 15)) * 16);
#pragma unroll
      for (int i = 0; i < 4; ++i) glds(voffV, vb + i * 512, lds0 + 16384 + wid * 4096 + i * 1024);
    }
  }
  bf16x8 qf;
  GLD4(qf, l16, (const char*)Qb + (size_t)(h * 64 + xg * 8 + wid) * 1024, 0);
  {
    const unsigned sc = lds0 + 49152 + wid * 3072;
    if (wid < 7) {
      const size_t o = (size_t)blockIdx.x * 7168 + wid * 1024;
      glds(l16, (const char*)w1 + o, sc);
      glds(l16, (const char*)w3 + o, sc + 1024);
      glds(l16, (const char*)w2 + o, sc + 2048);
    } else {
      const size_t o = (size_t)blockIdx.x * 2048;
      glds(l16, (const char*)wo + o, sc);
      glds(l16, (const char*)wo + o + 1024, sc + 1024);
      glds(l16, (const char*)wo + o + 1024, sc + 2048);
    }
  }
  {
    const u32x4 o4 = {0x3f803f80u, 0x3f803f80u, 0x3f803f80u, 0x3f803f80u};
    char* p = smem + 16384 + (tid >> 4) * 1024 + ((tid >> 3) & 1) * 512 + 256 + (tid & 7) * 32;
    *(u32x4*)p = o4;
    *(u32x4*)(p + 16) = o4;
  }
  asm volatile("s_waitcnt vmcnt(3) lgkmcnt(0)" ::: "memory");
  PIN(qf);
  __builtin_amdgcn_s_barrier();

  const unsigned kaddr = lds0 + wid * 1024 + l16;
  const unsigned vaddr = lds0 + 16384 + wid * 2048 + l16;
  bf16* dst = O + (size_t)((xg * 8 + wid) * 32 + (lane & 31)) * D_ + h * 16 + (lane >> 5) * 4;
  if (xg > 0) attn_body<true>(kaddr, vaddr, qf, wid, lane, dst);
  else attn_body<false>(kaddr, vaddr, qf, wid, lane, dst);
  wait_vm<0>();
}

template <int MODE, int KTOT>
__global__ __launch_bounds__(512, 2) void k35_gemm(const bf16* __restrict__ A, const bf16* __restrict__ B,
                                                   const bf16* __restrict__ R, bf16* __restrict__ O,
                                                   float* __restrict__ RS, const bf16* __restrict__ w1,
                                                   const bf16* __restrict__ w3, float* __restrict__ CS) {
  constexpr int NST = 4, STAGE = 32768, NK = KTOT / 128, LPC = 4, NT = NK * 4;
  extern __shared__ __attribute__((aligned(16))) char smem[];
  TDECL
  TSTAMP(MODE == 0 ? 2 : 4, 0);
  const int tid = threadIdx.x, lane = tid & 63;
  const int wid = __builtin_amdgcn_readfirstlane(tid >> 6);
  const int wm = wid & 3, wn = wid >> 2;
  const int l15 = lane & 15, gq = lane >> 4;
  const int xg = blockIdx.x & 7, idx = blockIdx.x >> 3;
  const int pm = xg * 4 + (idx >> 3), pn = idx & 7;
  const unsigned lds0 = (unsigned)(uintptr_t)(LDSP(char))smem;

  u32x4 cw0, cw1;
  if (MODE == 0) {
    const int ct = blockIdx.x & 31, kslab = blockIdx.x >> 5;
    const char* wb = (const char*)(ct < 16 ? w1 : w3) + (size_t)(kslab * 64 + wid * 8) * (F_ * 2) + (ct & 15) * 224;
    const unsigned vo = (unsigned)(gq * 2 * (F_ * 2) + l15 * 16);
    if (l15 < 14) {
      GLD4(cw0, vo, wb, 0);
      GLD4(cw1, vo, wb, F_ * 2);
    }
  }
  u32x2 res[2];
  const int tok = pm * 64 + wm * 16 + l15;
  const int ncol = pn * 64 + wn * 32 + gq * 4;
  {
    const unsigned vo = (unsigned)((tok * D_ + ncol) * 2);
    GLD2(res[0], vo, R, 0);
    GLD2(res[1], vo, R, 32);
  }

  const int ra = wid * 4 + gq;
  const unsigned voffA = (unsigned)(ra * (KTOT * 2) + ((l15 ^ (ra & 15)) * 16));
  const int pB = ((lane & 7) >> 1) ^ (((lane >> 4) & 1) | ((wid & 1) << 1));
  const unsigned voffB = (unsigned)((wid * 8 + (lane >> 3)) * 1024 + pB * 32 + (lane & 1) * 16);
  const char* baseA = (const char*)A + (size_t)pm * 64 * (KTOT * 2);
  const char* baseB = (const char*)B + pn * 128;
  const unsigned sbw = lds0 + wid * 1024;
  auto issue = [&](int kc) {
    const unsigned sb = sbw + (kc % NST) * STAGE;
    glds(voffA, baseA + kc * 256, sb);
    glds(voffA, baseA + kc * 256 + 32 * (KTOT * 2), sb + 8192);
    glds(voffB, baseB + (size_t)kc * (128 * 1024), sb + 16384);
    glds(voffB, baseB + (size_t)kc * (128 * 1024) + 65536, sb + 16384 + 8192);
  };
#pragma unroll
  for (int s = 0; s < NST; ++s) issue(s);
  TSTAMP(MODE == 0 ? 2 : 4, 1);

  unsigned a_addr[4], b_addr[2];
#pragma unroll
  for (int ks = 0; ks < 4; ++ks) a_addr[ks] = lds0 + (wm * 16 + l15) * 256 + (((4 * ks + gq) ^ l15) * 16);
  {
    const int kl = (gq >> 1) * 16 + (gq & 1) * 8 + (l15 >> 2);
    const int fr = ((l15 >> 3) & 1) | ((gq & 1) << 1);
#pragma unroll
    for (int j = 0; j < 2; ++j) b_addr[j] = lds0 + 16384 + kl * 128 + (((2 * wn + j) ^ fr) * 32) + (l15 & 3) * 8;
  }

  float* cpart = (float*)(smem + NST * STAGE);
  if (MODE == 0) {
    wait_vm<NST * LPC + 2>();
    PIN(cw0); PIN(cw1);
    float cs[8];
#pragma unroll
    for (int j = 0; j < 4; ++j) {
      cs[2 * j] = __builtin_bit_cast(float, cw0[j] << 16) + __builtin_bit_cast(float, cw1[j] << 16);
      cs[2 * j + 1] = __builtin_bit_cast(float, cw0[j] & 0xffff0000u) + __builtin_bit_cast(float, cw1[j] & 0xffff0000u);
    }
#pragma unroll
    for (int j = 0; j < 8; ++j) {
      cs[j] += __shfl_xor(cs[j], 16);
      cs[j] += __shfl_xor(cs[j], 32);
    }
    if (lane < 14) {
      *(f32x4*)(cpart + wid * 112 + lane * 8) = f32x4{cs[0], cs[1], cs[2], cs[3]};
      *(f32x4*)(cpart + wid * 112 + lane * 8 + 4) = f32x4{cs[4], cs[5], cs[6], cs[7]};
    }
  }

  f32x4 acc[2];
  const f32x4 z4 = {0.f, 0.f, 0.f, 0.f};
  acc[0] = z4; acc[1] = z4;
  bf16x8 af[2];
  bf16x4 bl[2][2], bh[2][2];

  auto rd = [&](int t, int set) {
    const int kc = t >> 2, ks = t & 3, st = kc % NST;
    const unsigned hi = (st >= 2) ? 65536u : 0u;
    const int so = (st & 1) * STAGE;
    DSRTR(bl[set][0], b_addr[0] + hi, so + ks * 4096);
    DSRTR(bh[set][0], b_addr[0] + hi, so + ks * 4096 + 512);
    DSR128(af[set], a_addr[ks] + hi, so);
    DSRTR(bl[set][1], b_addr[1] + hi, so + ks * 4096);
    DSRTR(bh[set][1], b_addr[1] + hi, so + ks * 4096 + 512);
  };
  auto wt = [&](int set) {
    asm volatile("s_waitcnt lgkmcnt(0)" ::: "memory");
    PIN(af[set]);
    PIN(bl[set][0]); PIN(bh[set][0]); PIN(bl[set][1]); PIN(bh[set][1]);
  };

  asm volatile("s_waitcnt vmcnt(%0) lgkmcnt(0)" ::"n"((NST - 1) * LPC) : "memory");
  __builtin_amdgcn_s_barrier();
  TSTAMP(MODE == 0 ? 2 : 4, 2);
  rd(0, 0);
  wt(0);
#pragma unroll
  for (int t = 0; t < NT; ++t) {
    const int kc = t >> 2, ks = t & 3, cur = t & 1, nxt = cur ^ 1;
    if (ks == 3 && kc + 1 < NK) {
      const int last = (kc + NST - 1 < NK - 1) ? (kc + NST - 1) : (NK - 1);
      wait_vm_n((last - (kc + 1)) * LPC);
      __builtin_amdgcn_s_barrier();
      TSTAMP(MODE == 0 ? 2 : 4, 3 + kc);
    }
    if (t + 1 < NT) rd(t + 1, nxt);
    if (ks == 3 && kc + NST < NK) issue(kc + NST);
    __builtin_amdgcn_sched_barrier(0);
    acc[0] = mfma16(cat8(bl[cur][0], bh[cur][0]), af[cur], acc[0]);
    acc[1] = mfma16(cat8(bl[cur][1], bh[cur][1]), af[cur], acc[1]);
    __builtin_amdgcn_sched_barrier(0);
    if (t + 1 < NT) wt(nxt);
  }
  TSTAMP(MODE == 0 ? 2 : 4, 20);
  PIN(res[0]); PIN(res[1]);
  float s = 0.f, q = 0.f;
#pragma unroll
  for (int j = 0; j < 2; ++j) {
    const bf16x4 rr = __builtin_bit_cast(bf16x4, res[j]);
    bf16x4 o;
#pragma unroll
    for (int r = 0; r < 4; ++r) {
      o[r] = (bf16)((float)rr[r] + bfr(acc[j][r]));
      const float v = (float)o[r];
      s += v;
      q += v * v;
    }
    *(bf16x4*)(O + (size_t)tok * D_ + ncol + j * 16) = o;
  }
  if (MODE == 0) {
    s += __shfl_xor(s, 16); s += __shfl_xor(s, 32);
    q += __shfl_xor(q, 16); q += __shfl_xor(q, 32);
    if (gq == 0) *(f32x2*)(RS + ((size_t)tok * 16 + pn * 2 + wn) * 2) = f32x2{s, q};
    if (tid < 112) {
      float c = 0.f;
#pragma unroll
      for (int p = 0; p < 8; ++p) c += cpart[p * 112 + tid];
      const int ct = blockIdx.x & 31, kslab = blockIdx.x >> 5;
      CS[kslab * (2 * F_) + ct * 112 + tid] = c;
    }
  }
  TSTAMP(MODE == 0 ? 2 : 4, 22);
  TFLUSH(MODE == 0 ? 2 : 4);
}

template <int NP, int P0>
DEV void gu_body(char* smem, unsigned lds0, int wm, int lane, unsigned voffA, unsigned voffB, bool validB,
                 const char* baseA, const char* baseB1, const char* baseB3, unsigned sbw, bf16* __restrict__ dst0 TARG) {
  constexpr int NST = 3, STAGE = 49152, NK = 8, LPC = 6, NT = 16;
  const int l15 = lane & 15, gq = lane >> 4;
  auto issue = [&](int kc) {
    const unsigned sb = sbw + (kc % NST) * STAGE;
    glds(voffA, baseA + kc * 128, sb);
    glds(voffA, baseA + kc * 128 + 65536, sb + 8192);
    if (validB) {
      glds(voffB, baseB1 + kc * (64 * 3584), sb + 16384);
      glds(voffB, baseB1 + kc * (64 * 3584) + 32 * 3584, sb + 16384 + 8192);
      glds(voffB, baseB3 + kc * (64 * 3584), sb + 32768);
      glds(voffB, baseB3 + kc * (64 * 3584) + 32 * 3584, sb + 32768 + 8192);
    }
  };
  unsigned a_addr[2], b_addr[NP];
#pragma unroll
  for (int ks = 0; ks < 2; ++ks) a_addr[ks] = lds0 + (wm * 32 + l15) * 128 + (((4 * ks + gq) ^ (l15 >> 1)) * 16);
  {
    const int kl = (gq >> 1) * 16 + (gq & 1) * 8 + (l15 >> 2);
    const int fr = (l15 >> 2) | ((gq & 1) << 2);
#pragma unroll
    for (int j = 0; j < NP; ++j) b_addr[j] = lds0 + 16384 + kl * 256 + (((P0 + j) ^ fr) * 32) + (l15 & 3) * 8;
  }
  f32x4 ag[2][NP], au[2][NP];
  const f32x4 z4 = {0.f, 0.f, 0.f, 0.f};
#pragma unroll
  for (int j = 0; j < NP; ++j) { ag[0][j] = z4; ag[1][j] = z4; au[0][j] = z4; au[1][j] = z4; }
  bf16x8 af[2][2];
  bf16x4 gl[2][NP], gh[2][NP], ul[2][NP], uh[2][NP];
  auto rd = [&](int t, int set) {
    const int kc = t >> 1, ks = t & 1, st = kc % NST;
    const unsigned so = st * STAGE;
    DSR128(af[set][0], a_addr[ks] + so, 0);
    DSR128(af[set][1], a_addr[ks] + so, 2048);
#pragma unroll
    for (int j = 0; j < NP; ++j) {
      DSRTR(gl[set][j], b_addr[j] + so, ks * 8192);
      DSRTR(gh[set][j], b_addr[j] + so, ks * 8192 + 1024);
      DSRTR(ul[set][j], b_addr[j] + so, 16384 + ks * 8192);
      DSRTR(uh[set][j], b_addr[j] + so, 16384 + ks * 8192 + 1024);
    }
  };
  auto wt = [&](int set) {
    asm volatile("s_waitcnt lgkmcnt(0)" ::: "memory");
    PIN(af[set][0]); PIN(af[set][1]);
#pragma unroll
    for (int j = 0; j < NP; ++j) { PIN(gl[set][j]); PIN(gh[set][j]); PIN(ul[set][j]); PIN(uh[set][j]); }
  };
  rd(0, 0);
  wt(0);
#pragma unroll
  for (int t = 0; t < NT; ++t) {
    const int kc = t >> 1, ks = t & 1, cur = t & 1, nxt = cur ^ 1;
    if (ks == 1 && kc + 1 < NK) {
      const int last = (kc + NST - 1 < NK - 1) ? (kc + NST - 1) : (NK - 1);
      wait_vm_n((last - (kc + 1)) * LPC);
      __builtin_amdgcn_s_barrier();
      TSTAMP(3, 3 + kc);
    }
    if (t + 1 < NT) rd(t + 1, nxt);
    if (ks == 1 && kc + NST < NK) issue(kc + NST);
    __builtin_amdgcn_sched_barrier(0);
#pragma unroll
    for (int j = 0; j < NP; ++j) {
      const bf16x8 bg = cat8(gl[cur][j], gh[cur][j]), bu = cat8(ul[cur][j], uh[cur][j]);
      ag[0][j] = mfma16(bg, af[cur][0], ag[0][j]);
      ag[1][j] = mfma16(bg, af[cur][1], ag[1][j]);
      au[0][j] = mfma16(bu, af[cur][0], au[0][j]);
      au[1][j] = mfma16(bu, af[cur][1], au[1][j]);
    }
    __builtin_amdgcn_sched_barrier(0);
    if (t + 1 < NT) wt(nxt);
  }
  TSTAMP(3, 20);
  const float* st_ = (const float*)(smem + NST * STAGE);
  const float* csum = st_ + 256;
#pragma unroll
  for (int mi = 0; mi < 2; ++mi) {
    const f32x2 ms = *(const f32x2*)(st_ + (wm * 32 + mi * 16 + l15) * 2);
#pragma unroll
    for (int j = 0; j < NP; ++j) {
      const f32x4 cg = *(const f32x4*)(csum + (P0 + j) * 16 + gq * 4);
      const f32x4 cu = *(const f32x4*)(csum + 112 + (P0 + j) * 16 + gq * 4);
      bf16x4 o;
#pragma unroll
      for (int r = 0; r < 4; ++r) {
        const float g = (ag[mi][j][r] - ms[0] * cg[r]) * ms[1];
        const float u = (au[mi][j][r] - ms[0] * cu[r]) * ms[1];
        const float e = __builtin_amdgcn_exp2f(g * -1.4426950408889634f);
        o[r] = (bf16)(g * u * __builtin_amdgcn_rcpf(1.0f + e));
      }
      *(bf16x4*)(dst0 + (size_t)mi * 16 * F_ + (P0 + j) * 16) = o;
    }
  }
}

__global__ __launch_bounds__(512, 2) void k4_gu(const bf16* __restrict__ Hh, const bf16* __restrict__ w1,
                                                const bf16* __restrict__ w3, const float* __restrict__ RS,
                                                const float* __restrict__ CS, bf16* __restrict__ Hm) {
  constexpr int NST = 3, STAGE = 49152, LPC = 6;
  extern __shared__ __attribute__((aligned(16))) char smem[];
  TDECL
  TSTAMP(3, 0);
  const int tid = threadIdx.x, lane = tid & 63;
  const int wid = __builtin_amdgcn_readfirstlane(tid >> 6);
  const int wm = wid & 3, wn = wid >> 2;
  const int l15 = lane & 15, gq = lane >> 4;
  const int xg = blockIdx.x & 7, idx = blockIdx.x >> 3;
  const int pm = xg * 2 + (idx >> 4), pn = idx & 15;
  const unsigned lds0 = (unsigned)(uintptr_t)(LDSP(char))smem;

  const unsigned voffA = (unsigned)((wid * 8 + (lane >> 3)) * 1024 + (((lane & 7) ^ ((wid & 1) * 4 + gq)) * 16));
  const int pB = (l15 >> 1) ^ (gq | (((wid >> 1) & 1) << 2));
  const bool validB = pB < 7;
  const unsigned voffB = (unsigned)((wid * 4 + gq) * 3584 + (pB * 16 + (lane & 1) * 8) * 2);
  const char* baseA = (const char*)Hh + (size_t)pm * (128 * 1024);
  const char* baseB1 = (const char*)w1 + pn * 224;
  const char* baseB3 = (const char*)w3 + pn * 224;
  const unsigned sbw = lds0 + wid * 1024;
  auto issue_all = [&]() {
#pragma unroll
    for (int kc = 0; kc < NST; ++kc) {
      const unsigned sb = sbw + kc * STAGE;
      glds(voffA, baseA + kc * 128, sb);
      glds(voffA, baseA + kc * 128 + 65536, sb + 8192);
      if (validB) {
        glds(voffB, baseB1 + kc * (64 * 3584), sb + 16384);
        glds(voffB, baseB1 + kc * (64 * 3584) + 32 * 3584, sb + 16384 + 8192);
        glds(voffB, baseB3 + kc * (64 * 3584), sb + 32768);
        glds(voffB, baseB3 + kc * (64 * 3584) + 32 * 3584, sb + 32768 + 8192);
      }
    }
  };
  {
    float* st_ = (float*)(smem + NST * STAGE);
    float* csum = st_ + 256;
    if (wid < 2) {
      f32x4 sv[8];
      const char* rb = (const char*)RS + (size_t)(pm * 128) * 128;
      const unsigned vo = (unsigned)(tid * 128);
#pragma unroll
      for (int i = 0; i < 8; ++i) GLD4(sv[i], vo, rb, i * 16);
      issue_all();
      wait_vm<NST * LPC>();
#pragma unroll
      for (int i = 0; i < 8; ++i) PIN(sv[i]);
      f32x4 a = sv[0];
#pragma unroll
      for (int i = 1; i < 8; ++i) a += sv[i];
      *(f32x2*)(st_ + tid * 2) = ln_stats(a[0] + a[2], a[1] + a[3]);
    } else if (wid < 6) {
      float sc[8];
      int c = tid - 128;
      const bool ok = c < 224;
      if (!ok) c = 0;
      const unsigned vo = (unsigned)(((c < 112 ? 0 : F_ - 112) + pn * 112 + c) * 4);
#pragma unroll
      for (int i = 0; i < 8; ++i) GLD1(sc[i], vo, (const char*)CS + i * (2 * F_ * 4), 0);
      issue_all();
      wait_vm<NST * LPC>();
#pragma unroll
      for (int i = 0; i < 8; ++i) PIN(sc[i]);
      float a = sc[0];
#pragma unroll
      for (int i = 1; i < 8; ++i) a += sc[i];
      if (ok) csum[c] = a;
    } else {
      issue_all();
    }
  }
  TSTAMP(3, 1);
  asm volatile("s_waitcnt vmcnt(%0) lgkmcnt(0)" ::"n"((NST - 1) * LPC) : "memory");
  __builtin_amdgcn_s_barrier();
  TSTAMP(3, 2);
  bf16* dst0 = Hm + (size_t)(pm * 128 + wm * 32 + l15) * F_ + pn * 112 + gq * 4;
  if (wn == 0) gu_body<4, 0>(smem, lds0, wm, lane, voffA, voffB, validB, baseA, baseB1, baseB3, sbw, dst0 TPASS);
  else gu_body<3, 4>(smem, lds0, wm, lane, voffA, voffB, validB, baseA, baseB1, baseB3, sbw, dst0 TPASS);
  TSTAMP(3, 22);
  TFLUSH(3);
}

extern "C" void kernel_launch(void* const* d_in, const int* in_sizes, int n_in, void* d_out, int out_size, void* d_ws,
                              size_t ws_size, hipStream_t stream) {
  const bf16* x = (const bf16*)d_in[0];
  const bf16* wqkv = (const bf16*)d_in[1];
  const bf16* wo = (const bf16*)d_in[2];
  const bf16* w1 = (const bf16*)d_in[4];
  const bf16* w3 = (const bf16*)d_in[5];
  const bf16* w2 = (const bf16*)d_in[6];
  bf16* out = (bf16*)d_out;

  char* ws = (char*)d_ws;
  bf16* Qb = (bf16*)(ws);
  bf16* Kb = (bf16*)(ws + (2u << 20));
  bf16* Vb = (bf16*)(ws + (4u << 20));
  bf16* Oa = (bf16*)(ws + (7u << 20));
  bf16* Hh = (bf16*)(ws + (9u << 20));
  bf16* Hm = (bf16*)(ws + (11u << 20));
  float* RS = (float*)(ws + (18u << 20));
  float* CS = (float*)(ws + (19u << 20));

  {
    constexpr int lds = 4 * 32768 + 1024 + 4 * 96 * 4;
    (void)hipFuncSetAttribute((const void*)k1_qkv, hipFuncAttributeMaxDynamicSharedMemorySize, lds);
    k1_qkv<<<256, 512, lds, stream>>>(x, wqkv, Qb, Kb, Vb);
  }
  {
    constexpr int lds = 49152 + 8 * 3072;
    (void)hipFuncSetAttribute((const void*)k2_attn, hipFuncAttributeMaxDynamicSharedMemorySize, lds);
    k2_attn<<<256, 512, lds, stream>>>(Qb, Kb, Vb, Oa, wo, w1, w3, w2);
  }
  {
    auto k = k35_gemm<0, 512>;
    constexpr int lds = 4 * 32768 + 8 * 112 * 4;
    (void)hipFuncSetAttribute((const void*)k, hipFuncAttributeMaxDynamicSharedMemorySize, lds);
    k<<<256, 512, lds, stream>>>(Oa, wo, x, Hh, RS, w1, w3, CS);
  }
  {
    constexpr int lds = 3 * 49152 + 1024 + 224 * 4;
    (void)hipFuncSetAttribute((const void*)k4_gu, hipFuncAttributeMaxDynamicSharedMemorySize, lds);
    k4_gu<<<256, 512, lds, stream>>>(Hh, w1, w3, RS, CS, Hm);
  }
  {
    auto k = k35_gemm<1, 1792>;
    constexpr int lds = 4 * 32768;
    (void)hipFuncSetAttribute((const void*)k, hipFuncAttributeMaxDynamicSharedMemorySize, lds);
    k<<<256, 512, lds, stream>>>(Hm, w2, Hh, out, nullptr, nullptr, nullptr, nullptr);
  }
}
```

```cpp
#include <hip/hip_runtime.h>
#include <stdint.h>

typedef __attribute__((ext_vector_type(8))) __bf16 bf16x8;
typedef __attribute__((ext_vector_type(4))) __bf16 bf16x4;
typedef __attribute__((ext_vector_type(2))) __bf16 bf16x2;
typedef __attribute__((ext_vector_type(2))) float f32x2;
typedef __attribute__((ext_vector_type(4))) float f32x4;
typedef __attribute__((ext_vector_type(16))) float f32x16;
typedef __attribute__((ext_vector_type(4))) unsigned int u32x4;
typedef __attribute__((ext_vector_type(2))) unsigned int u32x2;
typedef __bf16 bf16;

#define LDSP(T) __attribute__((address_space(3))) T*
#define DEV __device__ __forceinline__

constexpr int S_ = 2048, D_ = 512, H_ = 32, F_ = 1792;

DEV float bfr(float x) { return (float)(bf16)x; }

template <int N>
DEV void wait_vm() {
  asm volatile("s_waitcnt vmcnt(%0)" ::"n"(N) : "memory");
}
DEV void wait_vm_n(int n) {
  switch (n) {
#define WC(X) case X: wait_vm<X>(); break;
    WC(0) WC(1) WC(2) WC(3) WC(4) WC(5) WC(6) WC(7) WC(8) WC(9) WC(10) WC(11) WC(12) WC(13) WC(14) WC(15)
    WC(16) WC(17) WC(18) WC(19) WC(20) WC(21) WC(22) WC(23) WC(24)
#undef WC
    default: wait_vm<0>(); break;
  }
}

DEV void glds(unsigned voff, const void* sbase, unsigned ldsaddr) {
  ldsaddr = __builtin_amdgcn_readfirstlane(ldsaddr);
  asm volatile("s_mov_b32 m0, %2\n\ts_nop 0\n\tglobal_load_lds_dwordx4 %0, %1" ::"v"(voff), "s"(sbase), "s"(ldsaddr)
               : "memory");
}
#define DSR128(dst, addr, off) asm volatile("ds_read_b128 %0, %1 offset:%2" : "=v"(dst) : "v"(addr), "i"(off))
#define DSRTR(dst, addr, off) asm volatile("ds_read_b64_tr_b16 %0, %1 offset:%2" : "=v"(dst) : "v"(addr), "i"(off))
#define PIN(x) asm volatile("" : "+v"(x))
#define GLD4(dst, voff, sbase, off) \
  asm volatile("global_load_dwordx4 %0, %1, %2 offset:%3" : "=v"(dst) : "v"(voff), "s"(sbase), "i"(off))
#define GLD2(dst, voff, sbase, off) \
  asm volatile("global_load_dwordx2 %0, %1, %2 offset:%3" : "=v"(dst) : "v"(voff), "s"(sbase), "i"(off))
#define GLD1(dst, voff, sbase, off) \
  asm volatile("global_load_dword %0, %1, %2 offset:%3" : "=v"(dst) : "v"(voff), "s"(sbase), "i"(off))
#define BAR() __builtin_amdgcn_s_barrier()
#define SCHED() __builtin_amdgcn_sched_barrier(0)

#define TDECL
#define TSTAMP(m, i) do { } while (0)
#define TFLUSH(m) do { } while (0)
#define TARG
#define TPASS

DEV bf16x8 cat8(bf16x4 a, bf16x4 b) { return __builtin_shufflevector(a, b, 0, 1, 2, 3, 4, 5, 6, 7); }
DEV f32x4 mfma16(bf16x8 a, bf16x8 b, f32x4 c) { return __builtin_amdgcn_mfma_f32_16x16x32_bf16(a, b, c, 0, 0, 0); }
DEV f32x16 mfma32(bf16x8 a, bf16x8 b, f32x16 c) { return __builtin_amdgcn_mfma_f32_32x32x16_bf16(a, b, c, 0, 0, 0); }

DEV f32x2 ln_stats(float sum, float sq) {
  float m = sum * (1.0f / D_);
  float var = fmaxf(sq * (1.0f / D_) - m * m, 0.f);
  float vb = bfr(var);
  float sd = bfr(__builtin_sqrtf(bfr(vb + 1e-5f)));
  f32x2 r;
  r[0] = bfr(m);
  r[1] = 1.0f / sd;
  return r;
}

__global__ __launch_bounds__(512, 2) void k1_qkv(const bf16* __restrict__ x, const bf16* __restrict__ w,
                                                 bf16* __restrict__ QKV) {
  constexpr int NST = 4, STAGE = 28672, NK = 8, LPC = 4, NT = 16;
  extern __shared__ __attribute__((aligned(16))) char smem[];
  TDECL
  TSTAMP(0, 0);
  const int tid = threadIdx.x, lane = tid & 63;
  const int wid = __builtin_amdgcn_readfirstlane(tid >> 6);
  const int wm = wid & 3, wn = wid >> 2;
  const int l15 = lane & 15, gq = lane >> 4;
  const int xg = blockIdx.x & 7, idx = blockIdx.x >> 3;
  const int pm = xg * 2 + (idx >> 4), pn = idx & 15;
  const unsigned lds0 = (unsigned)(uintptr_t)(LDSP(char))smem;

  const unsigned voffA = (unsigned)((wid * 8 + (lane >> 3)) * 1024 + (((lane & 7) ^ ((wid & 1) * 4 + gq)) * 16));
  const int r12 = (lane * 43) >> 9, c12 = lane - r12 * 12;
  const bool validB = lane < 48;
  const unsigned voffB = (unsigned)((wid * 8 + r12) * 3072 + (((c12 >> 1) ^ (wid & 1)) * 16 + (c12 & 1) * 8) * 2);
  const char* baseA = (const char*)x + (size_t)pm * (128 * 1024);
  const char* baseB = (const char*)w + pn * 192;
  auto issue = [&](int kc) {
    const unsigned sb = lds0 + (kc % NST) * STAGE;
    glds(voffA, baseA + kc * 128, sb + wid * 1024);
    glds(voffA, baseA + kc * 128 + 65536, sb + 8192 + wid * 1024);
    if (validB) {
      glds(voffB, baseB + kc * (64 * 3072), sb + 16384 + wid * 1536);
      glds(voffB, baseB + kc * (64 * 3072) + 4 * 3072, sb + 16384 + wid * 1536 + 768);
    }
  };
  issue(0);
  BAR();
  issue(1);
  BAR();
  issue(2);
  TSTAMP(0, 1);

  unsigned a_addr[2][2], b_addr[3];
#pragma unroll
  for (int mi = 0; mi < 2; ++mi)
#pragma unroll
    for (int ks = 0; ks < 2; ++ks)
      a_addr[mi][ks] = lds0 + (wm * 32 + ((mi ^ wn) * 16) + l15) * 128 + (((4 * ks + gq) ^ (l15 >> 1)) * 16);
  {
    const int kl = gq * 8 + (l15 >> 2);
#pragma unroll
    for (int j = 0; j < 3; ++j)
      b_addr[j] = lds0 + 16384 + kl * 192 + (((3 * wn + j) ^ (gq & 1)) * 32) + (l15 & 3) * 8;
  }

  f32x4 acc[2][3], s1, s2, cs[3];
  const f32x4 z4 = {0.f, 0.f, 0.f, 0.f};
#pragma unroll
  for (int j = 0; j < 3; ++j) { acc[0][j] = z4; acc[1][j] = z4; cs[j] = z4; }
  s1 = z4; s2 = z4;
  bf16x8 af[2][2];
  bf16x4 bl[2][3], bh[2][3];
  const bf16 one = (bf16)1.0f;
  const bf16x8 ones = {one, one, one, one, one, one, one, one};

  auto rd = [&](int t, int set) {
    const int kc = t >> 1, ks = t & 1, st = kc % NST;
    const unsigned hi = (st >= 2) ? 2u * STAGE : 0u;
    const int so = (st & 1) * STAGE;
#pragma unroll
    for (int j = 0; j < 3; ++j) {
      DSRTR(bl[set][j], b_addr[j] + hi, so + ks * 6144);
      DSRTR(bh[set][j], b_addr[j] + hi, so + ks * 6144 + 768);
      if (j < 2) DSR128(af[set][j], a_addr[j][ks] + hi, so);
    }
  };
  auto wt = [&](int set) {
    asm volatile("s_waitcnt lgkmcnt(0)" ::: "memory");
    PIN(af[set][0]); PIN(af[set][1]);
#pragma unroll
    for (int j = 0; j < 3; ++j) { PIN(bl[set][j]); PIN(bh[set][j]); }
  };

  wait_vm<2 * LPC>();
  BAR();
  TSTAMP(0, 2);
  rd(0, 0);
  issue(NST - 1);
  wt(0);
#pragma unroll
  for (int t = 0; t < NT; ++t) {
    const int kc = t >> 1, ks = t & 1, cur = t & 1, nxt = cur ^ 1;
    if (ks == 1 && kc + 1 < NK) {
      const int last = (kc + NST - 1 < NK - 1) ? (kc + NST - 1) : (NK - 1);
      wait_vm_n((last - (kc + 1)) * LPC);
      BAR();
      TSTAMP(0, 3 + kc);
    }
    if (t + 1 < NT) rd(t + 1, nxt);
    if (ks == 1 && kc + NST < NK) issue(kc + NST);
    SCHED();
    {
      const bf16x8 b0 = cat8(bl[cur][0], bh[cur][0]), b1 = cat8(bl[cur][1], bh[cur][1]),
                   b2 = cat8(bl[cur][2], bh[cur][2]);
#pragma unroll
      for (int mi = 0; mi < 2; ++mi) {
        acc[mi][0] = mfma16(b0, af[cur][mi], acc[mi][0]);
        acc[mi][1] = mfma16(b1, af[cur][mi], acc[mi][1]);
        acc[mi][2] = mfma16(b2, af[cur][mi], acc[mi][2]);
      }
      s1 = mfma16(ones, af[cur][0], s1);
      s2 = mfma16(af[cur][0], af[cur][0], s2);
      if ((t & 3) == wm) {
        cs[0] = mfma16(b0, ones, cs[0]);
        cs[1] = mfma16(b1, ones, cs[1]);
        cs[2] = mfma16(b2, ones, cs[2]);
      }
    }
    SCHED();
    if (t + 1 < NT) wt(nxt);
  }
  TSTAMP(0, 20);

  float* st_ = (float*)(smem + NST * STAGE);
  float* csum = st_ + 256;
  {
    const float sq = (l15 & 2) ? ((l15 & 1) ? s2[3] : s2[2]) : ((l15 & 1) ? s2[1] : s2[0]);
    const f32x2 ms = ln_stats(s1[0], sq);
    if (gq == (l15 >> 2)) *(f32x2*)(st_ + (wm * 32 + wn * 16 + l15) * 2) = ms;
    if (l15 == 0) {
#pragma unroll
      for (int j = 0; j < 3; ++j) *(f32x4*)(csum + wm * 96 + (3 * wn + j) * 16 + gq * 4) = cs[j];
    }
  }
  __syncthreads();
  TSTAMP(0, 21);
  const int tile = pm * 4 + wm;
  const float qscale = 0.25f * 1.4426950408889634f;
  f32x2 ms[2];
#pragma unroll
  for (int mi = 0; mi < 2; ++mi) ms[mi] = *(const f32x2*)(st_ + (wm * 32 + (mi ^ wn) * 16 + l15) * 2);
  const int loq = (gq >> 1) * 256 + l15 * 8 + (gq & 1) * 4, lov = l15 * 16 + gq * 4;
#pragma unroll
  for (int j = 0; j < 3; ++j) {
    f32x4 c4 = z4;
#pragma unroll
    for (int p = 0; p < 4; ++p) c4 += *(const f32x4*)(csum + p * 96 + (3 * wn + j) * 16 + gq * 4);
    const int blk = pn * 6 + wn * 3 + j;
    const int typ = blk >> 5, h = blk & 31;
    const float sc = typ == 0 ? qscale : 1.0f;
    bf16* db = QKV + ((size_t)typ << 20) + (size_t)(h * 64 + tile) * 512 + (typ == 2 ? lov : loq);
    const int tfs = typ == 2 ? 256 : 128;
#pragma unroll
    for (int mi = 0; mi < 2; ++mi) {
      const float rs = ms[mi][1] * sc;
      bf16x4 o;
#pragma unroll
      for (int r = 0; r < 4; ++r) o[r] = (bf16)((acc[mi][j][r] - ms[mi][0] * c4[r]) * rs);
      *(bf16x4*)(db + (mi ^ wn) * tfs) = o;
    }
  }
  TSTAMP(0, 22);
  TFLUSH(0);
}

template <bool FULL, int NPF>
DEV void attn_body(unsigned kaddr, unsigned vaddr, bf16x8 qf, f32x16 cm0, f32x16 cm8, int wid, bf16* dst TARG) {
  const float NEG = -1e30f;
  f32x16 z16;
#pragma unroll
  for (int r = 0; r < 16; ++r) z16[r] = 0.f;
  bf16x8 kf[9];
#pragma unroll
  for (int t = 0; t < 9; ++t)
    if (FULL || wid + t >= 8) DSR128(kf[t], kaddr, t * 1024);
  asm volatile("s_waitcnt lgkmcnt(0)" ::: "memory");
#pragma unroll
  for (int t = 0; t < 9; ++t) PIN(kf[t]);
  TSTAMP(1, 3);
  float m0 = NEG, m1 = NEG;
#pragma unroll
  for (int t = 0; t < 9; ++t)
    if (FULL || wid + t >= 8) {
      const f32x16 s = mfma32(kf[t], qf, t == 0 ? cm0 : (t == 8 ? cm8 : z16));
#pragma unroll
      for (int r = 0; r < 16; r += 4) {
        m0 = fmaxf(fmaxf(m0, s[r]), s[r + 1]);
        m1 = fmaxf(fmaxf(m1, s[r + 2]), s[r + 3]);
      }
    }
  float m = fmaxf(m0, m1);
  m = fmaxf(m, __shfl_xor(m, 32));
  f32x16 nm;
#pragma unroll
  for (int r = 0; r < 16; ++r) { nm[r] = -m; cm0[r] -= m; cm8[r] -= m; }
  TSTAMP(1, 4);
  wait_vm<NPF>();
  BAR();
  TSTAMP(1, 5);
  f32x16 oa = z16, ob = z16;
  f32x16 sc_, sn_;
  bf16x4 v0l[2], v0h[2], v1l[2], v1h[2];
  auto vrd = [&](int t, int b) {
    DSRTR(v0l[b], vaddr, t * 1024);
    DSRTR(v0h[b], vaddr, t * 1024 + 256);
    DSRTR(v1l[b], vaddr, t * 1024 + 512);
    DSRTR(v1h[b], vaddr, t * 1024 + 768);
  };
  if (FULL) { sc_ = mfma32(kf[0], qf, cm0); vrd(0, 0); }
  else sc_ = z16;
  sn_ = z16;
#pragma unroll
  for (int t = 0; t < 9; ++t) {
    const int b = t & 1;
    const bool cur_ok = FULL || wid + t >= 8, nxt_ok = t < 8 && (FULL || wid + t + 1 >= 8);
    if (cur_ok) {
      asm volatile("s_waitcnt lgkmcnt(0)" ::: "memory");
      PIN(v0l[b]); PIN(v0h[b]); PIN(v1l[b]); PIN(v1h[b]);
    }
    if (nxt_ok) {
      vrd(t + 1, b ^ 1);
      sn_ = mfma32(kf[t + 1], qf, t + 1 == 8 ? cm8 : nm);
    }
    SCHED();
    if (cur_ok) {
      bf16x8 p0, p1;
#pragma unroll
      for (int j = 0; j < 8; ++j) p0[j] = (bf16)__builtin_amdgcn_exp2f(sc_[j]);
      SCHED();
      oa = mfma32(cat8(v0l[b], v0h[b]), p0, oa);
      SCHED();
#pragma unroll
      for (int j = 0; j < 8; ++j) p1[j] = (bf16)__builtin_amdgcn_exp2f(sc_[8 + j]);
      SCHED();
      ob = mfma32(cat8(v1l[b], v1h[b]), p1, ob);
      SCHED();
    }
    sc_ = sn_;
  }
  TSTAMP(1, 6);
  const float inv = 1.0f / (oa[8] + ob[8]);
  bf16x4 o0, o1;
#pragma unroll
  for (int r = 0; r < 4; ++r) {
    o0[r] = (bf16)((oa[r] + ob[r]) * inv);
    o1[r] = (bf16)((oa[4 + r] + ob[4 + r]) * inv);
  }
  *(bf16x4*)dst = o0;
  *(bf16x4*)(dst + 8) = o1;
}

__global__ __launch_bounds__(512, 2) void k2_attn(const bf16* __restrict__ QKV, bf16* __restrict__ O,
                                                  const bf16* __restrict__ wo, const bf16* __restrict__ w1,
                                                  const bf16* __restrict__ w3, const bf16* __restrict__ w2,
                                                  bf16* __restrict__ Wt, float* __restrict__ CS) {
  constexpr int LV = 16384, LONE = 32768, LSJ = 43008, LPFX = 57344;
  extern __shared__ __attribute__((aligned(16))) char smem[];
  TDECL
  TSTAMP(1, 0);
  const int tid = threadIdx.x, lane = tid & 63;
  const int wid = __builtin_amdgcn_readfirstlane(tid >> 6);
  const int l15 = lane & 15, gq = lane >> 4;
  const int xg = blockIdx.x & 7, h = blockIdx.x >> 3;
  const unsigned lds0 = (unsigned)(uintptr_t)(LDSP(char))smem;
  const int t0 = xg * 8 - 8;
  const unsigned l16 = lane * 16;
  const bool ld = xg > 0 || wid >= 4;

  const char* kb = (const char*)QKV + (2u << 20) + (ptrdiff_t)(h * 64 + t0 + 2 * wid) * 1024;
  if (ld) {
    glds(l16, kb, lds0 + wid * 2048);
    glds(l16, kb + 1024, lds0 + wid * 2048 + 1024);
  }
  bf16x8 qf;
  GLD4(qf, l16, (const char*)QKV + (size_t)(h * 64 + xg * 8 + wid) * 1024, 0);
  if (ld) {
    glds(l16, kb + (2u << 20), lds0 + LV + wid * 2048);
    glds(l16, kb + (2u << 20) + 1024, lds0 + LV + wid * 2048 + 1024);
  }
  {
    const u32x4 o4 = {0x3f803f80u, 0x3f803f80u, 0x3f803f80u, 0x3f803f80u};
    *(u32x4*)(smem + LONE + tid * 16) = o4;
    if (tid < 128) *(u32x4*)(smem + LONE + 8192 + tid * 16) = o4;
  }
  f32x16 cm0, cm8;
  {
    const int c = lane & 31, hh = lane >> 5;
#pragma unroll
    for (int r = 0; r < 16; ++r) {
      const int kr = (r & 3) + 8 * (r >> 2) + 4 * hh;
      cm0[r] = (kr > c) ? 0.f : -1e30f;
      cm8[r] = (kr <= c) ? 0.f : -1e30f;
    }
    PIN(cm0); PIN(cm8);
  }
  TSTAMP(1, 1);
  if (ld) asm volatile("s_waitcnt vmcnt(2) lgkmcnt(0)" ::: "memory");
  else asm volatile("s_waitcnt vmcnt(0) lgkmcnt(0)" ::: "memory");
  PIN(qf);
  BAR();
  TSTAMP(1, 2);
  const int skc = blockIdx.x >> 5, smt = (blockIdx.x >> 4) & 1, spn = blockIdx.x & 15;
  {
    const unsigned sc = lds0 + LPFX + wid * 3072;
    if (wid < 7) {
      const char* sb = (const char*)(smt ? w3 : w1) + (size_t)(skc * 64) * (F_ * 2) + (spn * 112 + wid * 16) * 2;
      const unsigned vo = (unsigned)((lane >> 1) * (F_ * 2) + (lane & 1) * 16);
      glds(vo, sb, lds0 + LSJ + wid * 2048);
      glds(vo, sb + 32 * (F_ * 2), lds0 + LSJ + wid * 2048 + 1024);
      glds(l16, (const char*)w2 + (size_t)blockIdx.x * 7168 + wid * 1024, sc);
    } else {
      const size_t o = (size_t)blockIdx.x * 2048;
      glds(l16, (const char*)wo + o, sc);
      glds(l16, (const char*)wo + o + 1024, sc + 1024);
      glds(l16, (const char*)wo + o + 1024, sc + 2048);
    }
  }

  const unsigned kaddr = lds0 + wid * 1024 + l16;
  const unsigned vlane = (unsigned)(((lane >> 5) * 4 + (l15 >> 2)) * 32 + (l15 & 3) * 8);
  const unsigned vaddr = (lane & 16) ? (lds0 + LONE + (vlane ^ 128u)) : (lds0 + LV + wid * 1024 + vlane);
  bf16* dst = O + (size_t)((xg * 8 + wid) * 32 + (lane & 31)) * D_ + h * 16 + (lane >> 5) * 4;
  if (xg > 0) attn_body<true, 3>(kaddr, vaddr, qf, cm0, cm8, wid, dst TPASS);
  else attn_body<false, 3>(kaddr, vaddr, qf, cm0, cm8, wid, dst TPASS);
  TSTAMP(1, 20);
  if (wid < 7) {
    wait_vm<3>();
    const unsigned ra = lds0 + LSJ + wid * 2048 + (gq * 8 + (l15 >> 2)) * 32 + (l15 & 3) * 8;
    bf16x4 f[4];
    DSRTR(f[0], ra, 0);
    DSRTR(f[1], ra, 128);
    DSRTR(f[2], ra, 1024);
    DSRTR(f[3], ra, 1024 + 128);
    asm volatile("s_waitcnt lgkmcnt(0)" ::: "memory");
    float c = 0.f;
#pragma unroll
    for (int i = 0; i < 4; ++i) {
      PIN(f[i]);
#pragma unroll
      for (int e = 0; e < 4; ++e) c += (float)f[i][e];
    }
    char* wd = (char*)Wt + (size_t)(((spn * 8 + skc) * 4 + smt) * 7 + wid) * 1024 + l16;
    *(bf16x8*)wd = cat8(f[0], f[1]);
    *(bf16x8*)(wd + 14 * 1024) = cat8(f[2], f[3]);
    c += __shfl_xor(c, 16);
    c += __shfl_xor(c, 32);
    if (lane < 16) CS[skc * (2 * F_) + smt * F_ + spn * 112 + wid * 16 + lane] = c;
  }
  TSTAMP(1, 22);
  wait_vm<0>();
  TFLUSH(1);
}

template <int MODE, int KTOT>
__global__ __launch_bounds__(512, 2) void k35_gemm(const bf16* __restrict__ A, const bf16* __restrict__ B,
                                                   const bf16* __restrict__ R, bf16* __restrict__ O,
                                                   float* __restrict__ RS) {
  constexpr int NST = 4, STAGE = 32768, NK = KTOT / 128, LPC = 4, NT = NK * 4;
  extern __shared__ __attribute__((aligned(16))) char smem[];
  TDECL
  TSTAMP(MODE == 0 ? 2 : 4, 0);
  const int tid = threadIdx.x, lane = tid & 63;
  const int wid = __builtin_amdgcn_readfirstlane(tid >> 6);
  const int wm = wid & 3, wn = wid >> 2;
  const int l15 = lane & 15, gq = lane >> 4;
  const int xg = blockIdx.x & 7, idx = blockIdx.x >> 3;
  const int pm = xg * 4 + (idx >> 3), pn = idx & 7;
  const unsigned lds0 = (unsigned)(uintptr_t)(LDSP(char))smem;

  u32x2 res[2];
  const int tok = pm * 64 + wm * 16 + l15;
  const int ncol = pn * 64 + wn * 32 + gq * 4;
  {
    const unsigned vo = (unsigned)((tok * D_ + ncol) * 2);
    GLD2(res[0], vo, R, 0);
    GLD2(res[1], vo, R, 32);
  }

  const int ra = wid * 4 + gq;
  const unsigned voffA = (unsigned)(ra * (KTOT * 2) + ((l15 ^ (ra & 15)) * 16));
  const int pB = ((lane & 7) >> 1) ^ (((lane >> 4) & 1) | ((wid & 1) << 1));
  const unsigned voffB = (unsigned)((wid * 8 + (lane >> 3)) * 1024 + pB * 32 + (lane & 1) * 16);
  const char* baseA = (const char*)A + (size_t)pm * 64 * (KTOT * 2);
  const char* baseB = (const char*)B + pn * 128;
  const unsigned sbw = lds0 + wid * 1024;
  auto issue = [&](int kc) {
    const unsigned sb = sbw + (kc % NST) * STAGE;
    glds(voffA, baseA + kc * 256, sb);
    glds(voffA, baseA + kc * 256 + 32 * (KTOT * 2), sb + 8192);
    glds(voffB, baseB + (size_t)kc * (128 * 1024), sb + 16384);
    glds(voffB, baseB + (size_t)kc * (128 * 1024) + 65536, sb + 16384 + 8192);
  };
  issue(0);
  BAR();
  issue(1);
  BAR();
  issue(2);
  TSTAMP(MODE == 0 ? 2 : 4, 1);

  unsigned a_addr[4], b_addr[2];
#pragma unroll
  for (int ks = 0; ks < 4; ++ks) a_addr[ks] = lds0 + (wm * 16 + l15) * 256 + (((4 * ks + gq) ^ l15) * 16);
  {
    const int kl = gq * 8 + (l15 >> 2);
    const int fr = ((l15 >> 3) & 1) | ((gq & 1) << 1);
#pragma unroll
    for (int j = 0; j < 2; ++j) b_addr[j] = lds0 + 16384 + kl * 128 + (((2 * wn + j) ^ fr) * 32) + (l15 & 3) * 8;
  }

  f32x4 acc[2];
  const f32x4 z4 = {0.f, 0.f, 0.f, 0.f};
  acc[0] = z4; acc[1] = z4;
  bf16x8 af[2];
  bf16x4 bl[2][2], bh[2][2];

  auto rd = [&](int t, int set) {
    const int kc = t >> 2, ks = t & 3, st = kc % NST;
    const unsigned hi = (st >= 2) ? 65536u : 0u;
    const int so = (st & 1) * STAGE;
    DSRTR(bl[set][0], b_addr[0] + hi, so + ks * 4096);
    DSRTR(bh[set][0], b_addr[0] + hi, so + ks * 4096 + 512);
    DSR128(af[set], a_addr[ks] + hi, so);
    DSRTR(bl[set][1], b_addr[1] + hi, so + ks * 4096);
    DSRTR(bh[set][1], b_addr[1] + hi, so + ks * 4096 + 512);
  };
  auto wt = [&](int set) {
    asm volatile("s_waitcnt lgkmcnt(0)" ::: "memory");
    PIN(af[set]);
    PIN(bl[set][0]); PIN(bh[set][0]); PIN(bl[set][1]); PIN(bh[set][1]);
  };

  wait_vm<2 * LPC>();
  BAR();
  TSTAMP(MODE == 0 ? 2 : 4, 2);
  rd(0, 0);
  issue(NST - 1);
  wt(0);
#pragma unroll
  for (int t = 0; t < NT; ++t) {
    const int kc = t >> 2, ks = t & 3, cur = t & 1, nxt = cur ^ 1;
    if (ks == 3 && kc + 1 < NK) {
      const int last = (kc + NST - 1 < NK - 1) ? (kc + NST - 1) : (NK - 1);
      wait_vm_n((last - (kc + 1)) * LPC);
      BAR();
      TSTAMP(MODE == 0 ? 2 : 4, 3 + kc);
    }
    if (t + 1 < NT) rd(t + 1, nxt);
    if (ks == 3 && kc + NST < NK) issue(kc + NST);
    SCHED();
    acc[0] = mfma16(cat8(bl[cur][0], bh[cur][0]), af[cur], acc[0]);
    acc[1] = mfma16(cat8(bl[cur][1], bh[cur][1]), af[cur], acc[1]);
    SCHED();
    if (t + 1 < NT) wt(nxt);
  }
  TSTAMP(MODE == 0 ? 2 : 4, 20);
  PIN(res[0]); PIN(res[1]);
  float s = 0.f, q = 0.f;
#pragma unroll
  for (int j = 0; j < 2; ++j) {
    const bf16x4 rr = __builtin_bit_cast(bf16x4, res[j]);
    bf16x4 o;
#pragma unroll
    for (int r = 0; r < 4; ++r) {
      o[r] = (bf16)((float)rr[r] + bfr(acc[j][r]));
      const float v = (float)o[r];
      s += v;
      q += v * v;
    }
    *(bf16x4*)(O + (size_t)tok * D_ + ncol + j * 16) = o;
  }
  if (MODE == 0) {
    s += __shfl_xor(s, 16); s += __shfl_xor(s, 32);
    q += __shfl_xor(q, 16); q += __shfl_xor(q, 32);
    if (gq == 0) *(f32x2*)(RS + ((size_t)tok * 16 + pn * 2 + wn) * 2) = f32x2{s, q};
  }
  TSTAMP(MODE == 0 ? 2 : 4, 22);
  TFLUSH(MODE == 0 ? 2 : 4);
}

template <int NP, int P0>
DEV void gu_body(char* smem, unsigned lds0, int tid, int wid, int pm, int pn, const bf16* __restrict__ Hh,
                 const bf16* __restrict__ Wt, const float* __restrict__ RS, const float* __restrict__ CS,
                 bf16* __restrict__ Hm TARG) {
  constexpr int NST = 3, STAGE = 45056, NK = 8, LPC = 2 + NP, NT = 16;
  const int lane = tid & 63, wm = wid & 3;
  const int l15 = lane & 15, gq = lane >> 4;
  const unsigned l16 = lane * 16;
  const unsigned voffA = (unsigned)((wid * 8 + (lane >> 3)) * 1024 + (((lane & 7) ^ ((wid & 1) * 4 + gq)) * 16));
  const char* baseA = (const char*)Hh + (size_t)pm * (128 * 1024);
  const int pc0 = (P0 == 0 ? 0 : 16) + wm * NP;
  const char* baseW = (const char*)Wt + (size_t)pn * (8 * 28672) + pc0 * 1024;
  auto issue = [&](int kc) {
    const unsigned sb = lds0 + (kc % NST) * STAGE;
    glds(voffA, baseA + kc * 128, sb + wid * 1024);
    glds(voffA, baseA + kc * 128 + 65536, sb + 8192 + wid * 1024);
#pragma unroll
    for (int i = 0; i < NP; ++i) glds(l16, baseW + kc * 28672 + i * 1024, sb + 16384 + (pc0 + i) * 1024);
  };
  float* st_ = (float*)(smem + NST * STAGE);
  float* csum = st_ + 256;
  if (P0 == 0) {
    if (wid < 2) {
      f32x4 sv[8];
      const char* rb = (const char*)RS + (size_t)(pm * 128) * 128;
      const unsigned vo = (unsigned)(tid * 128);
#pragma unroll
      for (int i = 0; i < 8; ++i) GLD4(sv[i], vo, rb, i * 16);
      issue(0);
      BAR();
      issue(1);
      wait_vm<2 * LPC>();
#pragma unroll
      for (int i = 0; i < 8; ++i) PIN(sv[i]);
      f32x4 a = sv[0];
#pragma unroll
      for (int i = 1; i < 8; ++i) a += sv[i];
      *(f32x2*)(st_ + tid * 2) = ln_stats(a[0] + a[2], a[1] + a[3]);
    } else {
      issue(0);
      BAR();
      issue(1);
    }
  } else {
    float sc[8];
    int c = tid - 256;
    const bool ok = c < 224;
    if (!ok) c = 0;
    const unsigned vo = (unsigned)(((c < 112 ? 0 : F_ - 112) + pn * 112 + c) * 4);
#pragma unroll
    for (int i = 0; i < 8; ++i) GLD1(sc[i], vo, (const char*)CS + i * (2 * F_ * 4), 0);
    issue(0);
    BAR();
    issue(1);
    wait_vm<2 * LPC>();
#pragma unroll
    for (int i = 0; i < 8; ++i) PIN(sc[i]);
    float a = sc[0];
#pragma unroll
    for (int i = 1; i < 8; ++i) a += sc[i];
    if (ok) csum[c] = a;
  }
  TSTAMP(3, 1);

  unsigned a_addr[2];
#pragma unroll
  for (int ks = 0; ks < 2; ++ks) a_addr[ks] = lds0 + (wm * 32 + l15) * 128 + (((4 * ks + gq) ^ (l15 >> 1)) * 16);
  const unsigned b_addr = lds0 + 16384 + P0 * 1024 + l16;
  f32x4 ag[2][NP], au[2][NP];
  const f32x4 z4 = {0.f, 0.f, 0.f, 0.f};
#pragma unroll
  for (int j = 0; j < NP; ++j) { ag[0][j] = z4; ag[1][j] = z4; au[0][j] = z4; au[1][j] = z4; }
  bf16x8 af[2][2], bg[2][NP], bu[2][NP];
  auto rd = [&](int t, int set) {
    const int kc = t >> 1, ks = t & 1, st = kc % NST;
    const unsigned so = st * STAGE;
    DSR128(af[set][0], a_addr[ks] + so, 0);
    DSR128(af[set][1], a_addr[ks] + so, 2048);
#pragma unroll
    for (int j = 0; j < NP; ++j) {
      DSR128(bg[set][j], b_addr + so, (ks * 14 + j) * 1024);
      DSR128(bu[set][j], b_addr + so, (ks * 14 + 7 + j) * 1024);
    }
  };
  auto wt = [&](int set) {
    asm volatile("s_waitcnt lgkmcnt(0)" ::: "memory");
    PIN(af[set][0]); PIN(af[set][1]);
#pragma unroll
    for (int j = 0; j < NP; ++j) { PIN(bg[set][j]); PIN(bu[set][j]); }
  };
  asm volatile("s_waitcnt vmcnt(%0) lgkmcnt(0)" ::"n"(LPC) : "memory");
  BAR();
  TSTAMP(3, 2);
  rd(0, 0);
  issue(NST - 1);
  wt(0);
#pragma unroll
  for (int t = 0; t < NT; ++t) {
    const int kc = t >> 1, ks = t & 1, cur = t & 1, nxt = cur ^ 1;
    if (ks == 1 && kc + 1 < NK) {
      const int last = (kc + NST - 1 < NK - 1) ? (kc + NST - 1) : (NK - 1);
      wait_vm_n((last - (kc + 1)) * LPC);
      BAR();
      TSTAMP(3, 3 + kc);
    }
    if (t + 1 < NT) rd(t + 1, nxt);
    if (ks == 1 && kc + NST < NK) issue(kc + NST);
    SCHED();
#pragma unroll
    for (int j = 0; j < NP; ++j) {
      ag[0][j] = mfma16(bg[cur][j], af[cur][0], ag[0][j]);
      ag[1][j] = mfma16(bg[cur][j], af[cur][1], ag[1][j]);
      au[0][j] = mfma16(bu[cur][j], af[cur][0], au[0][j]);
      au[1][j] = mfma16(bu[cur][j], af[cur][1], au[1][j]);
    }
    SCHED();
    if (t + 1 < NT) wt(nxt);
  }
  TSTAMP(3, 20);
  bf16* dst0 = Hm + (size_t)(pm * 128 + wm * 32 + l15) * F_ + pn * 112 + gq * 4;
#pragma unroll
  for (int mi = 0; mi < 2; ++mi) {
    const f32x2 ms = *(const f32x2*)(st_ + (wm * 32 + mi * 16 + l15) * 2);
#pragma unroll
    for (int j = 0; j < NP; ++j) {
      const f32x4 cg = *(const f32x4*)(csum + (P0 + j) * 16 + gq * 4);
      const f32x4 cu = *(const f32x4*)(csum + 112 + (P0 + j) * 16 + gq * 4);
      bf16x4 o;
#pragma unroll
      for (int r = 0; r < 4; ++r) {
        const float g = (ag[mi][j][r] - ms[0] * cg[r]) * ms[1];
        const float u = (au[mi][j][r] - ms[0] * cu[r]) * ms[1];
        const float e = __builtin_amdgcn_exp2f(g * -1.4426950408889634f);
        o[r] = (bf16)(g * u * __builtin_amdgcn_rcpf(1.0f + e));
      }
      *(bf16x4*)(dst0 + (size_t)mi * 16 * F_ + (P0 + j) * 16) = o;
    }
  }
}

__global__ __launch_bounds__(512, 2) void k4_gu(const bf16* __restrict__ Hh, const bf16* __restrict__ Wt,
                                                const float* __restrict__ RS, const float* __restrict__ CS,
                                                bf16* __restrict__ Hm) {
  extern __shared__ __attribute__((aligned(16))) char smem[];
  TDECL
  TSTAMP(3, 0);
  const int tid = threadIdx.x;
  const int wid = __builtin_amdgcn_readfirstlane(tid >> 6);
  const int xg = blockIdx.x & 7, idx = blockIdx.x >> 3;
  const int pm = xg * 2 + (idx >> 4), pn = idx & 15;
  const unsigned lds0 = (unsigned)(uintptr_t)(LDSP(char))smem;
  if (wid < 4) gu_body<4, 0>(smem, lds0, tid, wid, pm, pn, Hh, Wt, RS, CS, Hm TPASS);
  else gu_body<3, 4>(smem, lds0, tid, wid, pm, pn, Hh, Wt, RS, CS, Hm TPASS);
  TSTAMP(3, 22);
  TFLUSH(3);
}

extern "C" void kernel_launch(void* const* d_in, const int* in_sizes, int n_in, void* d_out, int out_size, void* d_ws,
                              size_t ws_size, hipStream_t stream) {
  const bf16* x = (const bf16*)d_in[0];
  const bf16* wqkv = (const bf16*)d_in[1];
  const bf16* wo = (const bf16*)d_in[2];
  const bf16* w1 = (const bf16*)d_in[4];
  const bf16* w3 = (const bf16*)d_in[5];
  const bf16* w2 = (const bf16*)d_in[6];
  bf16* out = (bf16*)d_out;

  char* ws = (char*)d_ws;
  bf16* QKV = (bf16*)(ws);
  bf16* Oa = (bf16*)(ws + (7u << 20));
  bf16* Hh = (bf16*)(ws + (9u << 20));
  bf16* Hm = (bf16*)(ws + (11u << 20));
  float* RS = (float*)(ws + (18u << 20));
  float* CS = (float*)(ws + (19u << 20));
  bf16* Wt = (bf16*)(ws + (20u << 20));

  {
    constexpr int lds = 4 * 28672 + 1024 + 4 * 96 * 4;
    (void)hipFuncSetAttribute((const void*)k1_qkv, hipFuncAttributeMaxDynamicSharedMemorySize, lds);
    k1_qkv<<<256, 512, lds, stream>>>(x, wqkv, QKV);
  }
  {
    constexpr int lds = 57344 + 8 * 3072;
    (void)hipFuncSetAttribute((const void*)k2_attn, hipFuncAttributeMaxDynamicSharedMemorySize, lds);
    k2_attn<<<256, 512, lds, stream>>>(QKV, Oa, wo, w1, w3, w2, Wt, CS);
  }
  {
    auto k = k35_gemm<0, 512>;
    constexpr int lds = 4 * 32768;
    (void)hipFuncSetAttribute((const void*)k, hipFuncAttributeMaxDynamicSharedMemorySize, lds);
    k<<<256, 512, lds, stream>>>(Oa, wo, x, Hh, RS);
  }
  {
    constexpr int lds = 3 * 45056 + 1024 + 224 * 4;
    (void)hipFuncSetAttribute((const void*)k4_gu, hipFuncAttributeMaxDynamicSharedMemorySize, lds);
    k4_gu<<<256, 512, lds, stream>>>(Hh, Wt, RS, CS, Hm);
  }
  {
    auto k = k35_gemm<1, 1792>;
    constexpr int lds = 4 * 32768;
    (void)hipFuncSetAttribute((const void*)k, hipFuncAttributeMaxDynamicSharedMemorySize, lds);
    k<<<256, 512, lds, stream>>>(Hm, w2, Hh, out, nullptr);
  }
}
```

```cpp
#include <hip/hip_runtime.h>
#include <stdint.h>

typedef __attribute__((ext_vector_type(8))) __bf16 bf16x8;
typedef __attribute__((ext_vector_type(4))) __bf16 bf16x4;
typedef __attribute__((ext_vector_type(2))) __bf16 bf16x2;
typedef __attribute__((ext_vector_type(2))) float f32x2;
typedef __attribute__((ext_vector_type(4))) float f32x4;
typedef __attribute__((ext_vector_type(16))) float f32x16;
typedef __attribute__((ext_vector_type(4))) unsigned int u32x4;
typedef __attribute__((ext_vector_type(2))) unsigned int u32x2;
typedef __bf16 bf16;

#define LDSP(T) __attribute__((address_space(3))) T*
#define DEV __device__ __forceinline__

constexpr int S_ = 2048, D_ = 512, H_ = 32, F_ = 1792;

DEV float bfr(float x) { return (float)(bf16)x; }

template <int N>
DEV void wait_vm() {
  asm volatile("s_waitcnt vmcnt(%0)" ::"n"(N) : "memory");
}
DEV void wait_vm_n(int n) {
  switch (n) {
#define WC(X) case X: wait_vm<X>(); break;
    WC(0) WC(1) WC(2) WC(3) WC(4) WC(5) WC(6) WC(7) WC(8) WC(9) WC(10) WC(11) WC(12) WC(13) WC(14) WC(15)
    WC(16) WC(17) WC(18) WC(19) WC(20) WC(21) WC(22) WC(23) WC(24)
#undef WC
    default: wait_vm<0>(); break;
  }
}

DEV void glds(unsigned voff, const void* sbase, unsigned ldsaddr) {
  ldsaddr = __builtin_amdgcn_readfirstlane(ldsaddr);
  asm volatile("s_mov_b32 m0, %2\n\ts_nop 0\n\tglobal_load_lds_dwordx4 %0, %1" ::"v"(voff), "s"(sbase), "s"(ldsaddr)
               : "memory");
}
#define DSR128(dst, addr, off) asm volatile("ds_read_b128 %0, %1 offset:%2" : "=v"(dst) : "v"(addr), "i"(off))
#define DSRTR(dst, addr, off) asm volatile("ds_read_b64_tr_b16 %0, %1 offset:%2" : "=v"(dst) : "v"(addr), "i"(off))
#define PIN(x) asm volatile("" : "+v"(x))
#define GLD4(dst, voff, sbase, off) \
  asm volatile("global_load_dwordx4 %0, %1, %2 offset:%3" : "=v"(dst) : "v"(voff), "s"(sbase), "i"(off))
#define GLD2(dst, voff, sbase, off) \
  asm volatile("global_load_dwordx2 %0, %1, %2 offset:%3" : "=v"(dst) : "v"(voff), "s"(sbase), "i"(off))
#define GLD1(dst, voff, sbase, off) \
  asm volatile("global_load_dword %0, %1, %2 offset:%3" : "=v"(dst) : "v"(voff), "s"(sbase), "i"(off))
#define BAR() __builtin_amdgcn_s_barrier()
#define SCHED() __builtin_amdgcn_sched_barrier(0)

#define TDECL
#define TSTAMP(m, i) do { } while (0)
#define TFLUSH(m) do { } while (0)
#define TARG
#define TPASS

DEV bf16x8 cat8(bf16x4 a, bf16x4 b) { return __builtin_shufflevector(a, b, 0, 1, 2, 3, 4, 5, 6, 7); }
DEV f32x4 mfma16(bf16x8 a, bf16x8 b, f32x4 c) { return __builtin_amdgcn_mfma_f32_16x16x32_bf16(a, b, c, 0, 0, 0); }
DEV f32x16 mfma32(bf16x8 a, bf16x8 b, f32x16 c) { return __builtin_amdgcn_mfma_f32_32x32x16_bf16(a, b, c, 0, 0, 0); }

DEV f32x2 ln_stats(float sum, float sq) {
  float m = sum * (1.0f / D_);
  float var = fmaxf(sq * (1.0f / D_) - m * m, 0.f);
  float vb = bfr(var);
  float sd = bfr(__builtin_sqrtf(bfr(vb + 1e-5f)));
  f32x2 r;
  r[0] = bfr(m);
  r[1] = 1.0f / sd;
  return r;
}

__global__ __launch_bounds__(512, 2) void k1_qkv(const bf16* __restrict__ x, const bf16* __restrict__ w,
                                                 bf16* __restrict__ QKV) {
  constexpr int NST = 4, STAGE = 28672, NK = 8, LPC = 4, NT = 16;
  extern __shared__ __attribute__((aligned(16))) char smem[];
  TDECL
  TSTAMP(0, 0);
  const int tid = threadIdx.x, lane = tid & 63;
  const int wid = __builtin_amdgcn_readfirstlane(tid >> 6);
  const int wm = wid & 3, wn = wid >> 2;
  const int l15 = lane & 15, gq = lane >> 4;
  const int xg = blockIdx.x & 7, idx = blockIdx.x >> 3;
  const int pm = (xg & 3) * 4 + (idx & 3), pn = (xg >> 2) * 8 + (idx >> 2);
  const unsigned lds0 = (unsigned)(uintptr_t)(LDSP(char))smem;

  const unsigned voffA = (unsigned)((wid * 8 + (lane >> 3)) * 1024 + (((lane & 7) ^ ((wid & 1) * 4 + gq)) * 16));
  const int r12 = (lane * 43) >> 9, c12 = lane - r12 * 12;
  const bool validB = lane < 48;
  const unsigned voffB = (unsigned)((wid * 8 + r12) * 3072 + (((c12 >> 1) ^ (wid & 1)) * 16 + (c12 & 1) * 8) * 2);
  const char* baseA = (const char*)x + (size_t)pm * (128 * 1024);
  const char* baseB = (const char*)w + pn * 192;
  auto part = [&](int kc, int p) {
    const unsigned sb = lds0 + (kc % NST) * STAGE;
    if (p == 0) {
      glds(voffA, baseA + kc * 128, sb + wid * 1024);
      glds(voffA, baseA + kc * 128 + 65536, sb + 8192 + wid * 1024);
    } else if (validB) {
      glds(voffB, baseB + kc * (64 * 3072), sb + 16384 + wid * 1536);
      glds(voffB, baseB + kc * (64 * 3072) + 4 * 3072, sb + 16384 + wid * 1536 + 768);
    }
  };
  auto issue = [&](int kc) { part(kc, 0); part(kc, 1); };
  issue(0);
  BAR();
  issue(1);
  BAR();
  issue(2);
  TSTAMP(0, 1);

  unsigned a_addr[2][2], b_addr[3];
#pragma unroll
  for (int mi = 0; mi < 2; ++mi)
#pragma unroll
    for (int ks = 0; ks < 2; ++ks)
      a_addr[mi][ks] = lds0 + (wm * 32 + ((mi ^ wn) * 16) + l15) * 128 + (((4 * ks + gq) ^ (l15 >> 1)) * 16);
  {
    const int kl = gq * 8 + (l15 >> 2);
#pragma unroll
    for (int j = 0; j < 3; ++j)
      b_addr[j] = lds0 + 16384 + kl * 192 + (((3 * wn + j) ^ (gq & 1)) * 32) + (l15 & 3) * 8;
  }

  f32x4 acc[2][3], s1, s2, cs[3];
  const f32x4 z4 = {0.f, 0.f, 0.f, 0.f};
#pragma unroll
  for (int j = 0; j < 3; ++j) { acc[0][j] = z4; acc[1][j] = z4; cs[j] = z4; }
  s1 = z4; s2 = z4;
  bf16x8 af[2][2];
  bf16x4 bl[2][3], bh[2][3];
  const bf16 one = (bf16)1.0f;
  const bf16x8 ones = {one, one, one, one, one, one, one, one};

  auto rd = [&](int t, int set) {
    const int kc = t >> 1, ks = t & 1, st = kc % NST;
    const unsigned hi = (st >= 2) ? 2u * STAGE : 0u;
    const int so = (st & 1) * STAGE;
#pragma unroll
    for (int j = 0; j < 3; ++j) {
      DSRTR(bl[set][j], b_addr[j] + hi, so + ks * 6144);
      DSRTR(bh[set][j], b_addr[j] + hi, so + ks * 6144 + 768);
      if (j < 2) DSR128(af[set][j], a_addr[j][ks] + hi, so);
    }
  };
  auto wt = [&](int set) {
    asm volatile("s_waitcnt lgkmcnt(0)" ::: "memory");
    PIN(af[set][0]); PIN(af[set][1]);
#pragma unroll
    for (int j = 0; j < 3; ++j) { PIN(bl[set][j]); PIN(bh[set][j]); }
  };

  wait_vm<2 * LPC>();
  BAR();
  TSTAMP(0, 2);
  rd(0, 0);
  part(NST - 1, 0);
  wt(0);
#pragma unroll
  for (int t = 0; t < NT; ++t) {
    const int kc = t >> 1, ks = t & 1, cur = t & 1, nxt = cur ^ 1;
    if (ks == 1 && kc + 1 < NK) {
      const int last = (kc + NST - 1 < NK - 1) ? (kc + NST - 1) : (NK - 1);
      wait_vm_n((last - (kc + 1)) * LPC);
      BAR();
      TSTAMP(0, 3 + kc);
    }
    if (t + 1 < NT) rd(t + 1, nxt);
    if ((t + 1) / 2 + NST - 1 < NK) part((t + 1) / 2 + NST - 1, (t + 1) & 1);
    SCHED();
    {
      const bf16x8 b0 = cat8(bl[cur][0], bh[cur][0]), b1 = cat8(bl[cur][1], bh[cur][1]),
                   b2 = cat8(bl[cur][2], bh[cur][2]);
#pragma unroll
      for (int mi = 0; mi < 2; ++mi) {
        acc[mi][0] = mfma16(b0, af[cur][mi], acc[mi][0]);
        acc[mi][1] = mfma16(b1, af[cur][mi], acc[mi][1]);
        acc[mi][2] = mfma16(b2, af[cur][mi], acc[mi][2]);
      }
      s1 = mfma16(ones, af[cur][0], s1);
      s2 = mfma16(af[cur][0], af[cur][0], s2);
      if ((t & 3) == wm) {
        cs[0] = mfma16(b0, ones, cs[0]);
        cs[1] = mfma16(b1, ones, cs[1]);
        cs[2] = mfma16(b2, ones, cs[2]);
      }
    }
    SCHED();
    if (t + 1 < NT) wt(nxt);
  }
  TSTAMP(0, 20);

  float* st_ = (float*)(smem + NST * STAGE);
  float* csum = st_ + 256;
  {
    const float sq = (l15 & 2) ? ((l15 & 1) ? s2[3] : s2[2]) : ((l15 & 1) ? s2[1] : s2[0]);
    const f32x2 ms = ln_stats(s1[0], sq);
    if (gq == (l15 >> 2)) *(f32x2*)(st_ + (wm * 32 + wn * 16 + l15) * 2) = ms;
    if (l15 == 0) {
#pragma unroll
      for (int j = 0; j < 3; ++j) *(f32x4*)(csum + wm * 96 + (3 * wn + j) * 16 + gq * 4) = cs[j];
    }
  }
  __syncthreads();
  TSTAMP(0, 21);
  const int tile = pm * 4 + wm;
  const float qscale = 0.25f * 1.4426950408889634f;
  f32x2 ms[2];
#pragma unroll
  for (int mi = 0; mi < 2; ++mi) ms[mi] = *(const f32x2*)(st_ + (wm * 32 + (mi ^ wn) * 16 + l15) * 2);
  const int loq = (gq >> 1) * 256 + l15 * 8 + (gq & 1) * 4, lov = l15 * 16 + gq * 4;
#pragma unroll
  for (int j = 0; j < 3; ++j) {
    f32x4 c4 = z4;
#pragma unroll
    for (int p = 0; p < 4; ++p) c4 += *(const f32x4*)(csum + p * 96 + (3 * wn + j) * 16 + gq * 4);
    const int blk = pn * 6 + wn * 3 + j;
    const int typ = blk >> 5, h = blk & 31;
    const float sc = typ == 0 ? qscale : 1.0f;
    bf16* db = QKV + ((size_t)typ << 20) + (size_t)(h * 64 + tile) * 512 + (typ == 2 ? lov : loq);
    const int tfs = typ == 2 ? 256 : 128;
#pragma unroll
    for (int mi = 0; mi < 2; ++mi) {
      const float rs = ms[mi][1] * sc;
      bf16x4 o;
#pragma unroll
      for (int r = 0; r < 4; ++r) o[r] = (bf16)((acc[mi][j][r] - ms[mi][0] * c4[r]) * rs);
      *(bf16x4*)(db + (mi ^ wn) * tfs) = o;
    }
  }
  TSTAMP(0, 22);
  TFLUSH(0);
}

template <bool FULL, int NPF>
DEV void attn_body(unsigned kaddr, unsigned vaddr, bf16x8 qf, f32x16 cm0, f32x16 cm8, int wid, bf16* dst TARG) {
  const float NEG = -1e30f;
  f32x16 z16;
#pragma unroll
  for (int r = 0; r < 16; ++r) z16[r] = 0.f;
  bf16x8 kf[9];
#pragma unroll
  for (int t = 0; t < 9; ++t)
    if (FULL || wid + t >= 8) DSR128(kf[t], kaddr, t * 1024);
  asm volatile("s_waitcnt lgkmcnt(0)" ::: "memory");
#pragma unroll
  for (int t = 0; t < 9; ++t) PIN(kf[t]);
  TSTAMP(1, 3);
  float m0 = NEG, m1 = NEG;
#pragma unroll
  for (int t = 0; t < 9; ++t)
    if (FULL || wid + t >= 8) {
      const f32x16 s = mfma32(kf[t], qf, t == 0 ? cm0 : (t == 8 ? cm8 : z16));
#pragma unroll
      for (int r = 0; r < 16; r += 4) {
        m0 = fmaxf(fmaxf(m0, s[r]), s[r + 1]);
        m1 = fmaxf(fmaxf(m1, s[r + 2]), s[r + 3]);
      }
    }
  float m = fmaxf(m0, m1);
  m = fmaxf(m, __shfl_xor(m, 32));
  f32x16 nm;
#pragma unroll
  for (int r = 0; r < 16; ++r) { nm[r] = -m; cm0[r] -= m; cm8[r] -= m; }
  TSTAMP(1, 4);
  wait_vm<NPF>();
  BAR();
  TSTAMP(1, 5);
  f32x16 oa = z16, ob = z16;
  f32x16 sc_, sn_;
  bf16x4 v0l[2], v0h[2], v1l[2], v1h[2];
  auto vrd = [&](int t, int b) {
    DSRTR(v0l[b], vaddr, t * 1024);
    DSRTR(v0h[b], vaddr, t * 1024 + 256);
    DSRTR(v1l[b], vaddr, t * 1024 + 512);
    DSRTR(v1h[b], vaddr, t * 1024 + 768);
  };
  if (FULL) { sc_ = mfma32(kf[0], qf, cm0); vrd(0, 0); }
  else sc_ = z16;
  sn_ = z16;
#pragma unroll
  for (int t = 0; t < 9; ++t) {
    const int b = t & 1;
    const bool cur_ok = FULL || wid + t >= 8, nxt_ok = t < 8 && (FULL || wid + t + 1 >= 8);
    if (cur_ok) {
      asm volatile("s_waitcnt lgkmcnt(0)" ::: "memory");
      PIN(v0l[b]); PIN(v0h[b]); PIN(v1l[b]); PIN(v1h[b]);
    }
    if (nxt_ok) {
      vrd(t + 1, b ^ 1);
      sn_ = mfma32(kf[t + 1], qf, t + 1 == 8 ? cm8 : nm);
    }
    SCHED();
    if (cur_ok) {
      bf16x8 p0, p1;
#pragma unroll
      for (int j = 0; j < 8; ++j) p0[j] = (bf16)__builtin_amdgcn_exp2f(sc_[j]);
      SCHED();
      oa = mfma32(cat8(v0l[b], v0h[b]), p0, oa);
      SCHED();
#pragma unroll
      for (int j = 0; j < 8; ++j) p1[j] = (bf16)__builtin_amdgcn_exp2f(sc_[8 + j]);
      SCHED();
      ob = mfma32(cat8(v1l[b], v1h[b]), p1, ob);
      SCHED();
    }
    sc_ = sn_;
  }
  TSTAMP(1, 6);
  const float inv = 1.0f / (oa[8] + ob[8]);
  bf16x4 o0, o1;
#pragma unroll
  for (int r = 0; r < 4; ++r) {
    o0[r] = (bf16)((oa[r] + ob[r]) * inv);
    o1[r] = (bf16)((oa[4 + r] + ob[4 + r]) * inv);
  }
  *(bf16x4*)dst = o0;
  *(bf16x4*)(dst + 8) = o1;
}

__global__ __launch_bounds__(512, 2) void k2_attn(const bf16* __restrict__ QKV, bf16* __restrict__ O,
                                                  const bf16* __restrict__ wo, const bf16* __restrict__ w1,
                                                  const bf16* __restrict__ w3, const bf16* __restrict__ w2,
                                                  bf16* __restrict__ Wt, float* __restrict__ CS) {
  constexpr int LV = 16384, LONE = 32768, LSJ = 43008, LPFX = 57344;
  extern __shared__ __attribute__((aligned(16))) char smem[];
  TDECL
  TSTAMP(1, 0);
  const int tid = threadIdx.x, lane = tid & 63;
  const int wid = __builtin_amdgcn_readfirstlane(tid >> 6);
  const int l15 = lane & 15, gq = lane >> 4;
  const int xg = blockIdx.x >> 5, h = (blockIdx.x & 7) * 4 + ((blockIdx.x >> 3) & 3);
  const unsigned lds0 = (unsigned)(uintptr_t)(LDSP(char))smem;
  const int t0 = xg * 8 - 8;
  const unsigned l16 = lane * 16;
  const bool ld = xg > 0 || wid >= 4;

  const char* kb = (const char*)QKV + (2u << 20) + (ptrdiff_t)(h * 64 + t0 + 2 * wid) * 1024;
  if (ld) {
    glds(l16, kb, lds0 + wid * 2048);
    glds(l16, kb + 1024, lds0 + wid * 2048 + 1024);
  }
  bf16x8 qf;
  GLD4(qf, l16, (const char*)QKV + (size_t)(h * 64 + xg * 8 + wid) * 1024, 0);
  if (ld) {
    glds(l16, kb + (2u << 20), lds0 + LV + wid * 2048);
    glds(l16, kb + (2u << 20) + 1024, lds0 + LV + wid * 2048 + 1024);
  }
  {
    const u32x4 o4 = {0x3f803f80u, 0x3f803f80u, 0x3f803f80u, 0x3f803f80u};
    *(u32x4*)(smem + LONE + tid * 16) = o4;
    if (tid < 128) *(u32x4*)(smem + LONE + 8192 + tid * 16) = o4;
  }
  f32x16 cm0, cm8;
  {
    const int c = lane & 31, hh = lane >> 5;
#pragma unroll
    for (int r = 0; r < 16; ++r) {
      const int kr = (r & 3) + 8 * (r >> 2) + 4 * hh;
      cm0[r] = (kr > c) ? 0.f : -1e30f;
      cm8[r] = (kr <= c) ? 0.f : -1e30f;
    }
    PIN(cm0); PIN(cm8);
  }
  TSTAMP(1, 1);
  if (ld) asm volatile("s_waitcnt vmcnt(2) lgkmcnt(0)" ::: "memory");
  else asm volatile("s_waitcnt vmcnt(0) lgkmcnt(0)" ::: "memory");
  PIN(qf);
  BAR();
  TSTAMP(1, 2);
  const int skc = blockIdx.x >> 5, smt = (blockIdx.x >> 4) & 1, spn = blockIdx.x & 15;
  {
    const unsigned sc = lds0 + LPFX + wid * 3072;
    if (wid < 7) {
      const char* sb = (const char*)(smt ? w3 : w1) + (size_t)(skc * 64) * (F_ * 2) + (spn * 112 + wid * 16) * 2;
      const unsigned vo = (unsigned)((lane >> 1) * (F_ * 2) + (lane & 1) * 16);
      glds(vo, sb, lds0 + LSJ + wid * 2048);
      glds(vo, sb + 32 * (F_ * 2), lds0 + LSJ + wid * 2048 + 1024);
      glds(l16, (const char*)w2 + (size_t)blockIdx.x * 7168 + wid * 1024, sc);
    } else {
      const size_t o = (size_t)blockIdx.x * 2048;
      glds(l16, (const char*)wo + o, sc);
      glds(l16, (const char*)wo + o + 1024, sc + 1024);
      glds(l16, (const char*)wo + o + 1024, sc + 2048);
    }
  }

  const unsigned kaddr = lds0 + wid * 1024 + l16;
  const unsigned vlane = (unsigned)(((lane >> 5) * 4 + (l15 >> 2)) * 32 + (l15 & 3) * 8);
  const unsigned vaddr = (lane & 16) ? (lds0 + LONE + (vlane ^ 128u)) : (lds0 + LV + wid * 1024 + vlane);
  bf16* dst = O + (size_t)((xg * 8 + wid) * 32 + (lane & 31)) * D_ + h * 16 + (lane >> 5) * 4;
  if (xg > 0) attn_body<true, 3>(kaddr, vaddr, qf, cm0, cm8, wid, dst TPASS);
  else attn_body<false, 3>(kaddr, vaddr, qf, cm0, cm8, wid, dst TPASS);
  TSTAMP(1, 20);
  if (wid < 7) {
    wait_vm<3>();
    const unsigned ra = lds0 + LSJ + wid * 2048 + (gq * 8 + (l15 >> 2)) * 32 + (l15 & 3) * 8;
    bf16x4 f[4];
    DSRTR(f[0], ra, 0);
    DSRTR(f[1], ra, 128);
    DSRTR(f[2], ra, 1024);
    DSRTR(f[3], ra, 1024 + 128);
    asm volatile("s_waitcnt lgkmcnt(0)" ::: "memory");
    float c = 0.f;
#pragma unroll
    for (int i = 0; i < 4; ++i) {
      PIN(f[i]);
#pragma unroll
      for (int e = 0; e < 4; ++e) c += (float)f[i][e];
    }
    char* wd = (char*)Wt + (size_t)(((spn * 8 + skc) * 4 + smt) * 7 + wid) * 1024 + l16;
    *(bf16x8*)wd = cat8(f[0], f[1]);
    *(bf16x8*)(wd + 14 * 1024) = cat8(f[2], f[3]);
    c += __shfl_xor(c, 16);
    c += __shfl_xor(c, 32);
    if (lane < 16) CS[skc * (2 * F_) + smt * F_ + spn * 112 + wid * 16 + lane] = c;
  }
  TSTAMP(1, 22);
  wait_vm<0>();
  TFLUSH(1);
}

template <int MODE, int KTOT>
__global__ __launch_bounds__(512, 2) void k35_gemm(const bf16* __restrict__ A, const bf16* __restrict__ B,
                                                   const bf16* __restrict__ R, bf16* __restrict__ O,
                                                   float* __restrict__ RS) {
  constexpr int NST = 4, STAGE = 32768, NK = KTOT / 128, LPC = 4, NT = NK * 4;
  extern __shared__ __attribute__((aligned(16))) char smem[];
  TDECL
  TSTAMP(MODE == 0 ? 2 : 4, 0);
  const int tid = threadIdx.x, lane = tid & 63;
  const int wid = __builtin_amdgcn_readfirstlane(tid >> 6);
  const int wm = wid & 3, wn = wid >> 2;
  const int l15 = lane & 15, gq = lane >> 4;
  const int xg = blockIdx.x & 7, idx = blockIdx.x >> 3;
  const int pm = xg * 4 + (idx >> 3), pn = idx & 7;
  const unsigned lds0 = (unsigned)(uintptr_t)(LDSP(char))smem;

  u32x2 res[2];
  const int tok = pm * 64 + wm * 16 + l15;
  const int ncol = pn * 64 + wn * 32 + gq * 4;
  {
    const unsigned vo = (unsigned)((tok * D_ + ncol) * 2);
    GLD2(res[0], vo, R, 0);
    GLD2(res[1], vo, R, 32);
  }

  const int ra = wid * 4 + gq;
  const unsigned voffA = (unsigned)(ra * (KTOT * 2) + ((l15 ^ (ra & 15)) * 16));
  const int pB = ((lane & 7) >> 1) ^ (((lane >> 4) & 1) | ((wid & 1) << 1));
  const unsigned voffB = (unsigned)((wid * 8 + (lane >> 3)) * 1024 + pB * 32 + (lane & 1) * 16);
  const char* baseA = (const char*)A + (size_t)pm * 64 * (KTOT * 2);
  const char* baseB = (const char*)B + pn * 128;
  const unsigned sbw = lds0 + wid * 1024;
  auto part = [&](int kc, int p) {
    const unsigned sb = sbw + (kc % NST) * STAGE;
    if (p == 0) glds(voffA, baseA + kc * 256, sb);
    if (p == 1) glds(voffA, baseA + kc * 256 + 32 * (KTOT * 2), sb + 8192);
    if (p == 2) glds(voffB, baseB + (size_t)kc * (128 * 1024), sb + 16384);
    if (p == 3) glds(voffB, baseB + (size_t)kc * (128 * 1024) + 65536, sb + 16384 + 8192);
  };
  auto issue = [&](int kc) { part(kc, 0); part(kc, 1); part(kc, 2); part(kc, 3); };
  issue(0);
  BAR();
  issue(1);
  BAR();
  issue(2);
  TSTAMP(MODE == 0 ? 2 : 4, 1);

  unsigned a_addr[4], b_addr[2];
#pragma unroll
  for (int ks = 0; ks < 4; ++ks) a_addr[ks] = lds0 + (wm * 16 + l15) * 256 + (((4 * ks + gq) ^ l15) * 16);
  {
    const int kl = gq * 8 + (l15 >> 2);
    const int fr = ((l15 >> 3) & 1) | ((gq & 1) << 1);
#pragma unroll
    for (int j = 0; j < 2; ++j) b_addr[j] = lds0 + 16384 + kl * 128 + (((2 * wn + j) ^ fr) * 32) + (l15 & 3) * 8;
  }

  f32x4 acc[2];
  const f32x4 z4 = {0.f, 0.f, 0.f, 0.f};
  acc[0] = z4; acc[1] = z4;
  bf16x8 af[2];
  bf16x4 bl[2][2], bh[2][2];

  auto rd = [&](int t, int set) {
    const int kc = t >> 2, ks = t & 3, st = kc % NST;
    const unsigned hi = (st >= 2) ? 65536u : 0u;
    const int so = (st & 1) * STAGE;
    DSRTR(bl[set][0], b_addr[0] + hi, so + ks * 4096);
    DSRTR(bh[set][0], b_addr[0] + hi, so + ks * 4096 + 512);
    DSR128(af[set], a_addr[ks] + hi, so);
    DSRTR(bl[set][1], b_addr[1] + hi, so + ks * 4096);
    DSRTR(bh[set][1], b_addr[1] + hi, so + ks * 4096 + 512);
  };
  auto wt = [&](int set) {
    asm volatile("s_waitcnt lgkmcnt(0)" ::: "memory");
    PIN(af[set]);
    PIN(bl[set][0]); PIN(bh[set][0]); PIN(bl[set][1]); PIN(bh[set][1]);
  };

  wait_vm<2 * LPC>();
  BAR();
  TSTAMP(MODE == 0 ? 2 : 4, 2);
  rd(0, 0);
  part(NST - 1, 0);
  wt(0);
#pragma unroll
  for (int t = 0; t < NT; ++t) {
    const int kc = t >> 2, ks = t & 3, cur = t & 1, nxt = cur ^ 1;
    if (ks == 3 && kc + 1 < NK) {
      const int last = (kc + NST - 1 < NK - 1) ? (kc + NST - 1) : (NK - 1);
      wait_vm_n((last - (kc + 1)) * LPC);
      BAR();
      TSTAMP(MODE == 0 ? 2 : 4, 3 + kc);
    }
    if (t + 1 < NT) rd(t + 1, nxt);
    if ((t + 1) / 4 + NST - 1 < NK) part((t + 1) / 4 + NST - 1, (t + 1) & 3);
    SCHED();
    acc[0] = mfma16(cat8(bl[cur][0], bh[cur][0]), af[cur], acc[0]);
    acc[1] = mfma16(cat8(bl[cur][1], bh[cur][1]), af[cur], acc[1]);
    SCHED();
    if (t + 1 < NT) wt(nxt);
  }
  TSTAMP(MODE == 0 ? 2 : 4, 20);
  PIN(res[0]); PIN(res[1]);
  float s = 0.f, q = 0.f;
#pragma unroll
  for (int j = 0; j < 2; ++j) {
    const bf16x4 rr = __builtin_bit_cast(bf16x4, res[j]);
    bf16x4 o;
#pragma unroll
    for (int r = 0; r < 4; ++r) {
      o[r] = (bf16)((float)rr[r] + bfr(acc[j][r]));
      const float v = (float)o[r];
      s += v;
      q += v * v;
    }
    *(bf16x4*)(O + (size_t)tok * D_ + ncol + j * 16) = o;
  }
  if (MODE == 0) {
    s += __shfl_xor(s, 16); s += __shfl_xor(s, 32);
    q += __shfl_xor(q, 16); q += __shfl_xor(q, 32);
    if (gq == 0) *(f32x2*)(RS + ((size_t)tok * 16 + pn * 2 + wn) * 2) = f32x2{s, q};
  }
  TSTAMP(MODE == 0 ? 2 : 4, 22);
  TFLUSH(MODE == 0 ? 2 : 4);
}

template <int NP, int P0>
DEV void gu_body(char* smem, unsigned lds0, int tid, int wid, int pm, int pn, const bf16* __restrict__ Hh,
                 const bf16* __restrict__ Wt, const float* __restrict__ RS, const float* __restrict__ CS,
                 bf16* __restrict__ Hm TARG) {
  constexpr int NST = 3, STAGE = 45056, NK = 8, LPC = 2 + NP, NT = 16;
  const int lane = tid & 63, wm = wid & 3;
  const int l15 = lane & 15, gq = lane >> 4;
  const unsigned l16 = lane * 16;
  const unsigned voffA = (unsigned)((wid * 8 + (lane >> 3)) * 1024 + (((lane & 7) ^ ((wid & 1) * 4 + gq)) * 16));
  const char* baseA = (const char*)Hh + (size_t)pm * (128 * 1024);
  const int pc0 = (P0 == 0 ? 0 : 16) + wm * NP;
  const char* baseW = (const char*)Wt + (size_t)pn * (8 * 28672) + pc0 * 1024;
  auto piece = [&](int kc, int i) {
    const unsigned sb = lds0 + (kc % NST) * STAGE;
    if (i == 0) glds(voffA, baseA + kc * 128, sb + wid * 1024);
    else if (i == 1) glds(voffA, baseA + kc * 128 + 65536, sb + 8192 + wid * 1024);
    else glds(l16, baseW + kc * 28672 + (i - 2) * 1024, sb + 16384 + (pc0 + i - 2) * 1024);
  };
  auto issue = [&](int kc) {
#pragma unroll
    for (int i = 0; i < LPC; ++i) piece(kc, i);
  };
  float* st_ = (float*)(smem + NST * STAGE);
  float* csum = st_ + 256;
  if (P0 == 0) {
    if (wid < 2) {
      f32x4 sv[8];
      const char* rb = (const char*)RS + (size_t)(pm * 128) * 128;
      const unsigned vo = (unsigned)(tid * 128);
#pragma unroll
      for (int i = 0; i < 8; ++i) GLD4(sv[i], vo, rb, i * 16);
      issue(0);
      BAR();
      issue(1);
      wait_vm<2 * LPC>();
#pragma unroll
      for (int i = 0; i < 8; ++i) PIN(sv[i]);
      f32x4 a = sv[0];
#pragma unroll
      for (int i = 1; i < 8; ++i) a += sv[i];
      *(f32x2*)(st_ + tid * 2) = ln_stats(a[0] + a[2], a[1] + a[3]);
    } else {
      issue(0);
      BAR();
      issue(1);
    }
  } else {
    float sc[8];
    int c = tid - 256;
    const bool ok = c < 224;
    if (!ok) c = 0;
    const unsigned vo = (unsigned)(((c < 112 ? 0 : F_ - 112) + pn * 112 + c) * 4);
#pragma unroll
    for (int i = 0; i < 8; ++i) GLD1(sc[i], vo, (const char*)CS + i * (2 * F_ * 4), 0);
    issue(0);
    BAR();
    issue(1);
    wait_vm<2 * LPC>();
#pragma unroll
    for (int i = 0; i < 8; ++i) PIN(sc[i]);
    float a = sc[0];
#pragma unroll
    for (int i = 1; i < 8; ++i) a += sc[i];
    if (ok) csum[c] = a;
  }
  TSTAMP(3, 1);

  unsigned a_addr[2];
#pragma unroll
  for (int ks = 0; ks < 2; ++ks) a_addr[ks] = lds0 + (wm * 32 + l15) * 128 + (((4 * ks + gq) ^ (l15 >> 1)) * 16);
  const unsigned b_addr = lds0 + 16384 + P0 * 1024 + l16;
  f32x4 ag[2][NP], au[2][NP];
  const f32x4 z4 = {0.f, 0.f, 0.f, 0.f};
#pragma unroll
  for (int j = 0; j < NP; ++j) { ag[0][j] = z4; ag[1][j] = z4; au[0][j] = z4; au[1][j] = z4; }
  bf16x8 af[2][2], bg[2][NP], bu[2][NP];
  auto rd = [&](int t, int set) {
    const int kc = t >> 1, ks = t & 1, st = kc % NST;
    const unsigned so = st * STAGE;
    DSR128(af[set][0], a_addr[ks] + so, 0);
    DSR128(af[set][1], a_addr[ks] + so, 2048);
#pragma unroll
    for (int j = 0; j < NP; ++j) {
      DSR128(bg[set][j], b_addr + so, (ks * 14 + j) * 1024);
      DSR128(bu[set][j], b_addr + so, (ks * 14 + 7 + j) * 1024);
    }
  };
  auto wt = [&](int set) {
    asm volatile("s_waitcnt lgkmcnt(0)" ::: "memory");
    PIN(af[set][0]); PIN(af[set][1]);
#pragma unroll
    for (int j = 0; j < NP; ++j) { PIN(bg[set][j]); PIN(bu[set][j]); }
  };
  asm volatile("s_waitcnt vmcnt(%0) lgkmcnt(0)" ::"n"(LPC) : "memory");
  BAR();
  TSTAMP(3, 2);
  rd(0, 0);
  piece(NST - 1, 0); piece(NST - 1, 1); piece(NST - 1, 2);
  wt(0);
#pragma unroll
  for (int t = 0; t < NT; ++t) {
    const int kc = t >> 1, ks = t & 1, cur = t & 1, nxt = cur ^ 1;
    if (ks == 1 && kc + 1 < NK) {
      const int last = (kc + NST - 1 < NK - 1) ? (kc + NST - 1) : (NK - 1);
      wait_vm_n((last - (kc + 1)) * LPC);
      BAR();
      TSTAMP(3, 3 + kc);
    }
    if (t + 1 < NT) rd(t + 1, nxt);
    const int ic = (t + 1) / 2 + NST - 1, ip = ((t + 1) & 1) * 3;
    SCHED();
#pragma unroll
    for (int j = 0; j < NP; ++j) {
      ag[0][j] = mfma16(bg[cur][j], af[cur][0], ag[0][j]);
      ag[1][j] = mfma16(bg[cur][j], af[cur][1], ag[1][j]);
      au[0][j] = mfma16(bu[cur][j], af[cur][0], au[0][j]);
      au[1][j] = mfma16(bu[cur][j], af[cur][1], au[1][j]);
      SCHED();
      if (j < 3 && ic < NK && ip + j < LPC) piece(ic, ip + j);
      SCHED();
    }
    if (t + 1 < NT) wt(nxt);
  }
  TSTAMP(3, 20);
  bf16* dst0 = Hm + (size_t)(pm * 128 + wm * 32 + l15) * F_ + pn * 112 + gq * 4;
#pragma unroll
  for (int mi = 0; mi < 2; ++mi) {
    const f32x2 ms = *(const f32x2*)(st_ + (wm * 32 + mi * 16 + l15) * 2);
#pragma unroll
    for (int j = 0; j < NP; ++j) {
      const f32x4 cg = *(const f32x4*)(csum + (P0 + j) * 16 + gq * 4);
      const f32x4 cu = *(const f32x4*)(csum + 112 + (P0 + j) * 16 + gq * 4);
      bf16x4 o;
#pragma unroll
      for (int r = 0; r < 4; ++r) {
        const float g = (ag[mi][j][r] - ms[0] * cg[r]) * ms[1];
        const float u = (au[mi][j][r] - ms[0] * cu[r]) * ms[1];
        const float e = __builtin_amdgcn_exp2f(g * -1.4426950408889634f);
        o[r] = (bf16)(g * u * __builtin_amdgcn_rcpf(1.0f + e));
      }
      *(bf16x4*)(dst0 + (size_t)mi * 16 * F_ + (P0 + j) * 16) = o;
    }
  }
}

__global__ __launch_bounds__(512, 2) void k4_gu(const bf16* __restrict__ Hh, const bf16* __restrict__ Wt,
                                                const float* __restrict__ RS, const float* __restrict__ CS,
                                                bf16* __restrict__ Hm) {
  extern __shared__ __attribute__((aligned(16))) char smem[];
  TDECL
  TSTAMP(3, 0);
  const int tid = threadIdx.x;
  const int wid = __builtin_amdgcn_readfirstlane(tid >> 6);
  const int xg = blockIdx.x & 7, idx = blockIdx.x >> 3;
  const int pm = (xg & 1) * 8 + (idx & 7), pn = (xg >> 1) * 4 + (idx >> 3);
  const unsigned lds0 = (unsigned)(uintptr_t)(LDSP(char))smem;
  if (wid < 4) gu_body<4, 0>(smem, lds0, tid, wid, pm, pn, Hh, Wt, RS, CS, Hm TPASS);
  else gu_body<3, 4>(smem, lds0, tid, wid, pm, pn, Hh, Wt, RS, CS, Hm TPASS);
  TSTAMP(3, 22);
  TFLUSH(3);
}

extern "C" void kernel_launch(void* const* d_in, const int* in_sizes, int n_in, void* d_out, int out_size, void* d_ws,
                              size_t ws_size, hipStream_t stream) {
  const bf16* x = (const bf16*)d_in[0];
  const bf16* wqkv = (const bf16*)d_in[1];
  const bf16* wo = (const bf16*)d_in[2];
  const bf16* w1 = (const bf16*)d_in[4];
  const bf16* w3 = (const bf16*)d_in[5];
  const bf16* w2 = (const bf16*)d_in[6];
  bf16* out = (bf16*)d_out;

  char* ws = (char*)d_ws;
  bf16* QKV = (bf16*)(ws);
  bf16* Oa = (bf16*)(ws + (7u << 20));
  bf16* Hh = (bf16*)(ws + (9u << 20));
  bf16* Hm = (bf16*)(ws + (11u << 20));
  float* RS = (float*)(ws + (18u << 20));
  float* CS = (float*)(ws + (19u << 20));
  bf16* Wt = (bf16*)(ws + (20u << 20));

  {
    constexpr int lds = 4 * 28672 + 1024 + 4 * 96 * 4;
    (void)hipFuncSetAttribute((const void*)k1_qkv, hipFuncAttributeMaxDynamicSharedMemorySize, lds);
    k1_qkv<<<256, 512, lds, stream>>>(x, wqkv, QKV);
  }
  {
    constexpr int lds = 57344 + 8 * 3072;
    (void)hipFuncSetAttribute((const void*)k2_attn, hipFuncAttributeMaxDynamicSharedMemorySize, lds);
    k2_attn<<<256, 512, lds, stream>>>(QKV, Oa, wo, w1, w3, w2, Wt, CS);
  }
  {
    auto k = k35_gemm<0, 512>;
    constexpr int lds = 4 * 32768;
    (void)hipFuncSetAttribute((const void*)k, hipFuncAttributeMaxDynamicSharedMemorySize, lds);
    k<<<256, 512, lds, stream>>>(Oa, wo, x, Hh, RS);
  }
  {
    constexpr int lds = 3 * 45056 + 1024 + 224 * 4;
    (void)hipFuncSetAttribute((const void*)k4_gu, hipFuncAttributeMaxDynamicSharedMemorySize, lds);
    k4_gu<<<256, 512, lds, stream>>>(Hh, Wt, RS, CS, Hm);
  }
  {
    auto k = k35_gemm<1, 1792>;
    constexpr int lds = 4 * 32768;
    (void)hipFuncSetAttribute((const void*)k, hipFuncAttributeMaxDynamicSharedMemorySize, lds);
    k<<<256, 512, lds, stream>>>(Hm, w2, Hh, out, nullptr);
  }
}
```

```cpp
#include <hip/hip_runtime.h>
#include <stdint.h>

typedef __attribute__((ext_vector_type(8))) __bf16 bf16x8;
typedef __attribute__((ext_vector_type(4))) __bf16 bf16x4;
typedef __attribute__((ext_vector_type(2))) __bf16 bf16x2;
typedef __attribute__((ext_vector_type(2))) float f32x2;
typedef __attribute__((ext_vector_type(4))) float f32x4;
typedef __attribute__((ext_vector_type(16))) float f32x16;
typedef __attribute__((ext_vector_type(4))) unsigned int u32x4;
typedef __attribute__((ext_vector_type(2))) unsigned int u32x2;
typedef __bf16 bf16;

#define LDSP(T) __attribute__((address_space(3))) T*
#define DEV __device__ __forceinline__

#define K1_NST 4
#define G_NST 4
#define G_R 1
#define ROT1 0
#define ROT3 0
#define ROT4 0
#define ROT5 0
constexpr int S_ = 2048, D_ = 512, H_ = 32, F_ = 1792;

DEV float bfr(float x) { return (float)(bf16)x; }

template <int N>
DEV void wait_vm() {
  asm volatile("s_waitcnt vmcnt(%0)" ::"n"(N) : "memory");
}
DEV void wait_vm_n(int n) {
  switch (n) {
#define WC(X) case X: wait_vm<X>(); break;
    WC(0) WC(1) WC(2) WC(3) WC(4) WC(5) WC(6) WC(7) WC(8) WC(9) WC(10) WC(11) WC(12) WC(13) WC(14) WC(15)
    WC(16) WC(17) WC(18) WC(19) WC(20) WC(21) WC(22) WC(23) WC(24)
#undef WC
    default: wait_vm<0>(); break;
  }
}

DEV void glds(unsigned voff, const void* sbase, unsigned ldsaddr) {
  ldsaddr = __builtin_amdgcn_readfirstlane(ldsaddr);
  asm volatile("s_mov_b32 m0, %2\n\ts_nop 0\n\tglobal_load_lds_dwordx4 %0, %1" ::"v"(voff), "s"(sbase), "s"(ldsaddr)
               : "memory");
}
#define DSR128(dst, addr, off) asm volatile("ds_read_b128 %0, %1 offset:%2" : "=v"(dst) : "v"(addr), "i"(off))
#define DSRTR(dst, addr, off) asm volatile("ds_read_b64_tr_b16 %0, %1 offset:%2" : "=v"(dst) : "v"(addr), "i"(off))
#define PIN(x) asm volatile("" : "+v"(x))
#define GLD4(dst, voff, sbase, off) \
  asm volatile("global_load_dwordx4 %0, %1, %2 offset:%3" : "=v"(dst) : "v"(voff), "s"(sbase), "i"(off))
#define GLD2(dst, voff, sbase, off) \
  asm volatile("global_load_dwordx2 %0, %1, %2 offset:%3" : "=v"(dst) : "v"(voff), "s"(sbase), "i"(off))
#define GLD1(dst, voff, sbase, off) \
  asm volatile("global_load_dword %0, %1, %2 offset:%3" : "=v"(dst) : "v"(voff), "s"(sbase), "i"(off))
#define BAR() __builtin_amdgcn_s_barrier()
#define SCHED() __builtin_amdgcn_sched_barrier(0)

#define TDECL
#define TSTAMP(m, i) do { } while (0)
#define TFLUSH(m) do { } while (0)
#define TARG
#define TPASS

template <int V> struct IC { static constexpr int value = V; };
template <int I, int N, class F>
DEV void static_for(F&& f) {
  if constexpr (I < N) {
    f(IC<I>{});
    static_for<I + 1, N>(f);
  }
}
template <int P, int KS, int NST, int NK, int PRE, int R>
struct Sched {
  static constexpr int avail(int t) {
    int n = ((t + 1) / KS + NST - 1) * P + ((t + 1) % KS + 1) * (P / KS);
    return n > NK * P ? NK * P : n;
  }
  static constexpr int cum(int t) {
    int n = PRE;
    for (int i = -1; i < t; ++i) { int a = avail(i); n = (n + R < a) ? n + R : a; }
    return n;
  }
};

DEV void swap32(unsigned& a, unsigned& b) {
  const auto r = __builtin_amdgcn_permlane32_swap(a, b, false, false);
  a = r[0]; b = r[1];
}
DEV void swap16(unsigned& a, unsigned& b) {
  const auto r = __builtin_amdgcn_permlane16_swap(a, b, false, false);
  a = r[0]; b = r[1];
}
DEV void swap16f(float& a, float& b) {
  unsigned x = __builtin_bit_cast(unsigned, a), y = __builtin_bit_cast(unsigned, b);
  swap16(x, y);
  a = __builtin_bit_cast(float, x); b = __builtin_bit_cast(float, y);
}
DEV unsigned pk2(float a, float b) { return __builtin_bit_cast(unsigned, bf16x2{(bf16)a, (bf16)b}); }
DEV bf16x8 cat8(bf16x4 a, bf16x4 b) { return __builtin_shufflevector(a, b, 0, 1, 2, 3, 4, 5, 6, 7); }
DEV f32x4 mfma16(bf16x8 a, bf16x8 b, f32x4 c) { return __builtin_amdgcn_mfma_f32_16x16x32_bf16(a, b, c, 0, 0, 0); }
DEV f32x16 mfma32(bf16x8 a, bf16x8 b, f32x16 c) { return __builtin_amdgcn_mfma_f32_32x32x16_bf16(a, b, c, 0, 0, 0); }

DEV f32x2 ln_stats(float sum, float sq) {
  float m = sum * (1.0f / D_);
  float var = fmaxf(sq * (1.0f / D_) - m * m, 0.f);
  float vb = bfr(var);
  float sd = bfr(__builtin_sqrtf(bfr(vb + 1e-5f)));
  f32x2 r;
  r[0] = bfr(m);
  r[1] = 1.0f / sd;
  return r;
}

__global__ __launch_bounds__(512, 2) void k1_qkv(const bf16* __restrict__ x, const bf16* __restrict__ w,
                                                 bf16* __restrict__ QKV) {
  constexpr int NST = K1_NST, STAGE = 28672, NK = 8, NT = 16;
  extern __shared__ __attribute__((aligned(16))) char smem[];
  TDECL
  TSTAMP(0, 0);
  const int tid = threadIdx.x, lane = tid & 63;
  const int wid = __builtin_amdgcn_readfirstlane(tid >> 6);
  const int wm = wid & 3, wn = wid >> 2;
  const int l15 = lane & 15, gq = lane >> 4;
  const int xg = blockIdx.x & 7, idx = blockIdx.x >> 3;
  const int pm = (xg & 3) * 4 + (idx & 3), pn = (xg >> 2) * 8 + (idx >> 2);
  const unsigned lds0 = (unsigned)(uintptr_t)(LDSP(char))smem;

  const unsigned voffA = (unsigned)((wid * 8 + (lane >> 3)) * 1024 + (((lane & 7) ^ ((wid & 1) * 4 + gq)) * 16));
  const int r12 = (lane * 43) >> 9, c12 = lane - r12 * 12;
  const bool validB = lane < 48;
  const unsigned voffB = (unsigned)((wid * 8 + r12) * 3072 + (((c12 >> 1) ^ (wid & 1)) * 16 + (c12 & 1) * 8) * 2);
  const char* baseA = (const char*)x + (size_t)pm * (128 * 1024);
  const char* baseB = (const char*)w + pn * 192;
  const int rot = ROT1 * ((idx >> 2) + 2 * (idx & 3));
  auto part = [&](int kc, int p) {
    const unsigned sb = lds0 + (kc % NST) * STAGE;
    const int kk = (kc + rot) & 7;
    if (p == 0) {
      glds(voffA, baseA + kk * 128, sb + wid * 1024);
      glds(voffA, baseA + kk * 128 + 65536, sb + 8192 + wid * 1024);
    } else if (validB) {
      glds(voffB, baseB + kk * (64 * 3072), sb + 16384 + wid * 1536);
      glds(voffB, baseB + kk * (64 * 3072) + 4 * 3072, sb + 16384 + wid * 1536 + 768);
    }
  };
  auto issue = [&](int kc) { part(kc, 0); part(kc, 1); };
  issue(0);
  BAR();
  issue(1);
  BAR();
  issue(2);
  TSTAMP(0, 1);

  unsigned a_addr[2][2], b_addr[3];
#pragma unroll
  for (int mi = 0; mi < 2; ++mi)
#pragma unroll
    for (int ks = 0; ks < 2; ++ks)
      a_addr[mi][ks] = lds0 + (wm * 32 + ((mi ^ wn) * 16) + l15) * 128 + (((4 * ks + gq) ^ (l15 >> 1)) * 16);
  {
    const int kl = gq * 8 + (l15 >> 2);
#pragma unroll
    for (int j = 0; j < 3; ++j)
      b_addr[j] = lds0 + 16384 + kl * 192 + (((3 * wn + j) ^ (gq & 1)) * 32) + (l15 & 3) * 8;
  }

  f32x4 acc[2][3], s1, s2, cs[3];
  const f32x4 z4 = {0.f, 0.f, 0.f, 0.f};
#pragma unroll
  for (int j = 0; j < 3; ++j) { acc[0][j] = z4; acc[1][j] = z4; cs[j] = z4; }
  s1 = z4; s2 = z4;
  bf16x8 af[2][2];
  bf16x4 bl[2][3], bh[2][3];
  const bf16 one = (bf16)1.0f;
  const bf16x8 ones = {one, one, one, one, one, one, one, one};

  auto rd = [&](int t, int set) {
    const int kc = t >> 1, ks = t & 1, st = kc % NST;
    const unsigned hi = (unsigned)((st & ~1) * STAGE);
    const int so = (st & 1) * STAGE;
#pragma unroll
    for (int j = 0; j < 3; ++j) {
      DSRTR(bl[set][j], b_addr[j] + hi, so + ks * 6144);
      DSRTR(bh[set][j], b_addr[j] + hi, so + ks * 6144 + 768);
      if (j < 2) DSR128(af[set][j], a_addr[j][ks] + hi, so);
    }
  };
  auto wt = [&](int set) {
    asm volatile("s_waitcnt lgkmcnt(0)" ::: "memory");
    PIN(af[set][0]); PIN(af[set][1]);
#pragma unroll
    for (int j = 0; j < 3; ++j) { PIN(bl[set][j]); PIN(bh[set][j]); }
  };

  using SC = Sched<2, 2, NST, NK, 6, 2>;
  wait_vm<(SC::cum(-1) - 2) * 2>();
  BAR();
  TSTAMP(0, 2);
  rd(0, 0);
  static_for<SC::cum(-1), SC::cum(0)>([&](auto n) { part(n.value >> 1, n.value & 1); });
  wt(0);
  static_for<0, NT>([&](auto tc) {
    constexpr int t = tc.value, kc = t >> 1, ks = t & 1, cur = t & 1, nxt = cur ^ 1;
    if constexpr (ks == 1 && kc + 1 < NK) {
      static_assert(SC::cum(t) >= (kc + 2) * 2, "schedule");
      wait_vm<(SC::cum(t) - (kc + 2) * 2) * 2>();
      BAR();
      TSTAMP(0, 3 + kc);
    }
    if constexpr (t + 1 < NT) rd(t + 1, nxt);
    static_for<SC::cum(t), SC::cum(t + 1)>([&](auto n) { part(n.value >> 1, n.value & 1); });
    SCHED();
    {
      const bf16x8 b0 = cat8(bl[cur][0], bh[cur][0]), b1 = cat8(bl[cur][1], bh[cur][1]),
                   b2 = cat8(bl[cur][2], bh[cur][2]);
#pragma unroll
      for (int mi = 0; mi < 2; ++mi) {
        acc[mi][0] = mfma16(b0, af[cur][mi], acc[mi][0]);
        acc[mi][1] = mfma16(b1, af[cur][mi], acc[mi][1]);
        acc[mi][2] = mfma16(b2, af[cur][mi], acc[mi][2]);
      }
      s1 = mfma16(ones, af[cur][0], s1);
      s2 = mfma16(af[cur][0], af[cur][0], s2);
      if ((t & 3) == wm) {
        cs[0] = mfma16(b0, ones, cs[0]);
        cs[1] = mfma16(b1, ones, cs[1]);
        cs[2] = mfma16(b2, ones, cs[2]);
      }
    }
    SCHED();
    if constexpr (t + 1 < NT) wt(nxt);
  });
  TSTAMP(0, 20);

  float* st_ = (float*)(smem + NST * STAGE);
  float* csum = st_ + 256;
  {
    const float sq = (l15 & 2) ? ((l15 & 1) ? s2[3] : s2[2]) : ((l15 & 1) ? s2[1] : s2[0]);
    const f32x2 ms = ln_stats(s1[0], sq);
    if (gq == (l15 >> 2)) *(f32x2*)(st_ + (wm * 32 + wn * 16 + l15) * 2) = ms;
    if (l15 == 0) {
#pragma unroll
      for (int j = 0; j < 3; ++j) *(f32x4*)(csum + wm * 96 + (3 * wn + j) * 16 + gq * 4) = cs[j];
    }
  }
  __syncthreads();
  TSTAMP(0, 21);
  const int tile = pm * 4 + wm;
  const float qscale = 0.25f * 1.4426950408889634f;
  f32x2 ms[2];
#pragma unroll
  for (int mi = 0; mi < 2; ++mi) ms[mi] = *(const f32x2*)(st_ + (wm * 32 + (mi ^ wn) * 16 + l15) * 2);
  const int loq = (gq >> 1) * 256 + l15 * 8 + (gq & 1) * 4, lov = l15 * 16 + gq * 4;
#pragma unroll
  for (int j = 0; j < 3; ++j) {
    f32x4 c4 = z4;
#pragma unroll
    for (int p = 0; p < 4; ++p) c4 += *(const f32x4*)(csum + p * 96 + (3 * wn + j) * 16 + gq * 4);
    const int blk = pn * 6 + wn * 3 + j;
    const int typ = blk >> 5, h = blk & 31;
    const float sc = typ == 0 ? qscale : 1.0f;
    bf16* db = QKV + ((size_t)typ << 20) + (size_t)(h * 64 + tile) * 512 + (typ == 2 ? lov : loq);
    const int tfs = typ == 2 ? 256 : 128;
#pragma unroll
    for (int mi = 0; mi < 2; ++mi) {
      const float rs = ms[mi][1] * sc;
      bf16x4 o;
#pragma unroll
      for (int r = 0; r < 4; ++r) o[r] = (bf16)((acc[mi][j][r] - ms[mi][0] * c4[r]) * rs);
      *(bf16x4*)(db + (mi ^ wn) * tfs) = o;
    }
  }
  TSTAMP(0, 22);
  TFLUSH(0);
}

template <bool FULL, int NPF>
DEV void attn_body(unsigned kaddr, unsigned vaddr, bf16x8 qf, f32x16 cm0, f32x16 cm8, int wid, bf16* dst TARG) {
  const float NEG = -1e30f;
  f32x16 z16;
#pragma unroll
  for (int r = 0; r < 16; ++r) z16[r] = 0.f;
  bf16x8 kf[9];
#pragma unroll
  for (int t = 0; t < 9; ++t)
    if (FULL || wid + t >= 8) DSR128(kf[t], kaddr, t * 1024);
  asm volatile("s_waitcnt lgkmcnt(0)" ::: "memory");
#pragma unroll
  for (int t = 0; t < 9; ++t) PIN(kf[t]);
  TSTAMP(1, 3);
  float m0 = NEG, m1 = NEG;
#pragma unroll
  for (int t = 0; t < 9; ++t)
    if (FULL || wid + t >= 8) {
      const f32x16 s = mfma32(kf[t], qf, t == 0 ? cm0 : (t == 8 ? cm8 : z16));
#pragma unroll
      for (int r = 0; r < 16; r += 4) {
        m0 = fmaxf(fmaxf(m0, s[r]), s[r + 1]);
        m1 = fmaxf(fmaxf(m1, s[r + 2]), s[r + 3]);
      }
    }
  float m = fmaxf(m0, m1);
  m = fmaxf(m, __shfl_xor(m, 32));
  f32x16 nm;
#pragma unroll
  for (int r = 0; r < 16; ++r) { nm[r] = -m; cm0[r] -= m; cm8[r] -= m; }
  TSTAMP(1, 4);
  wait_vm<NPF>();
  BAR();
  TSTAMP(1, 5);
  f32x16 oa = z16, ob = z16;
  f32x16 sc_, sn_;
  bf16x4 v0l[2], v0h[2], v1l[2], v1h[2];
  auto vrd = [&](int t, int b) {
    DSRTR(v0l[b], vaddr, t * 1024);
    DSRTR(v0h[b], vaddr, t * 1024 + 256);
    DSRTR(v1l[b], vaddr, t * 1024 + 512);
    DSRTR(v1h[b], vaddr, t * 1024 + 768);
  };
  if (FULL) { sc_ = mfma32(kf[0], qf, cm0); vrd(0, 0); }
  else sc_ = z16;
  sn_ = z16;
#pragma unroll
  for (int t = 0; t < 9; ++t) {
    const int b = t & 1;
    const bool cur_ok = FULL || wid + t >= 8, nxt_ok = t < 8 && (FULL || wid + t + 1 >= 8);
    if (cur_ok) {
      asm volatile("s_waitcnt lgkmcnt(0)" ::: "memory");
      PIN(v0l[b]); PIN(v0h[b]); PIN(v1l[b]); PIN(v1h[b]);
    }
    if (nxt_ok) {
      vrd(t + 1, b ^ 1);
      sn_ = mfma32(kf[t + 1], qf, t + 1 == 8 ? cm8 : nm);
    }
    SCHED();
    if (cur_ok) {
      bf16x8 p0, p1;
#pragma unroll
      for (int j = 0; j < 8; ++j) p0[j] = (bf16)__builtin_amdgcn_exp2f(sc_[j]);
      SCHED();
      oa = mfma32(cat8(v0l[b], v0h[b]), p0, oa);
      SCHED();
#pragma unroll
      for (int j = 0; j < 8; ++j) p1[j] = (bf16)__builtin_amdgcn_exp2f(sc_[8 + j]);
      SCHED();
      ob = mfma32(cat8(v1l[b], v1h[b]), p1, ob);
      SCHED();
    }
    sc_ = sn_;
  }
  TSTAMP(1, 6);
  const float inv = 1.0f / (oa[8] + ob[8]);
  unsigned a0 = pk2((oa[0] + ob[0]) * inv, (oa[1] + ob[1]) * inv), a1 = pk2((oa[2] + ob[2]) * inv, (oa[3] + ob[3]) * inv);
  unsigned b0 = pk2((oa[4] + ob[4]) * inv, (oa[5] + ob[5]) * inv), b1 = pk2((oa[6] + ob[6]) * inv, (oa[7] + ob[7]) * inv);
  swap32(a0, b0);
  swap32(a1, b1);
  *(u32x4*)dst = u32x4{a0, a1, b0, b1};
}

__global__ __launch_bounds__(512, 2) void k2_attn(const bf16* __restrict__ QKV, bf16* __restrict__ O,
                                                  const bf16* __restrict__ wo, const bf16* __restrict__ w1,
                                                  const bf16* __restrict__ w3, const bf16* __restrict__ w2,
                                                  bf16* __restrict__ Wt, float* __restrict__ CS) {
  constexpr int LV = 16384, LONE = 32768, LSJ = 43008, LPFX = 57344;
  extern __shared__ __attribute__((aligned(16))) char smem[];
  TDECL
  TSTAMP(1, 0);
  const int tid = threadIdx.x, lane = tid & 63;
  const int wid = __builtin_amdgcn_readfirstlane(tid >> 6);
  const int l15 = lane & 15, gq = lane >> 4;
  const int xg = blockIdx.x >> 5, h = (blockIdx.x & 7) * 4 + ((blockIdx.x >> 3) & 3);
  const unsigned lds0 = (unsigned)(uintptr_t)(LDSP(char))smem;
  const int t0 = xg * 8 - 8;
  const unsigned l16 = lane * 16;
  const bool ld = xg > 0 || wid >= 4;

  const char* kb = (const char*)QKV + (2u << 20) + (ptrdiff_t)(h * 64 + t0 + 2 * wid) * 1024;
  if (ld) {
    glds(l16, kb, lds0 + wid * 2048);
    glds(l16, kb + 1024, lds0 + wid * 2048 + 1024);
  }
  bf16x8 qf;
  GLD4(qf, l16, (const char*)QKV + (size_t)(h * 64 + xg * 8 + wid) * 1024, 0);
  if (ld) {
    glds(l16, kb + (2u << 20), lds0 + LV + wid * 2048);
    glds(l16, kb + (2u << 20) + 1024, lds0 + LV + wid * 2048 + 1024);
  }
  {
    const u32x4 o4 = {0x3f803f80u, 0x3f803f80u, 0x3f803f80u, 0x3f803f80u};
    *(u32x4*)(smem + LONE + tid * 16) = o4;
    if (tid < 128) *(u32x4*)(smem + LONE + 8192 + tid * 16) = o4;
  }
  f32x16 cm0, cm8;
  {
    const int c = lane & 31, hh = lane >> 5;
#pragma unroll
    for (int r = 0; r < 16; ++r) {
      const int kr = (r & 3) + 8 * (r >> 2) + 4 * hh;
      cm0[r] = (kr > c) ? 0.f : -1e30f;
      cm8[r] = (kr <= c) ? 0.f : -1e30f;
    }
    PIN(cm0); PIN(cm8);
  }
  TSTAMP(1, 1);
  if (ld) asm volatile("s_waitcnt vmcnt(2) lgkmcnt(0)" ::: "memory");
  else asm volatile("s_waitcnt vmcnt(0) lgkmcnt(0)" ::: "memory");
  PIN(qf);
  BAR();
  TSTAMP(1, 2);
  const int skc = blockIdx.x >> 5, smt = (blockIdx.x >> 4) & 1, spn = blockIdx.x & 15;
  {
    const unsigned sc = lds0 + LPFX + wid * 3072;
    if (wid < 7) {
      const char* sb = (const char*)(smt ? w3 : w1) + (size_t)(skc * 64) * (F_ * 2) + (spn * 112 + wid * 16) * 2;
      const unsigned vo = (unsigned)((lane >> 1) * (F_ * 2) + (lane & 1) * 16);
      glds(vo, sb, lds0 + LSJ + wid * 2048);
      glds(vo, sb + 32 * (F_ * 2), lds0 + LSJ + wid * 2048 + 1024);
      glds(l16, (const char*)w2 + (size_t)blockIdx.x * 7168 + wid * 1024, sc);
    } else {
      const size_t o = (size_t)blockIdx.x * 2048;
      glds(l16, (const char*)wo + o, sc);
      glds(l16, (const char*)wo + o + 1024, sc + 1024);
      glds(l16, (const char*)wo + o + 1024, sc + 2048);
    }
  }

  const unsigned kaddr = lds0 + wid * 1024 + l16;
  const unsigned vlane = (unsigned)(((lane >> 5) * 4 + (l15 >> 2)) * 32 + (l15 & 3) * 8);
  const unsigned vaddr = (lane & 16) ? (lds0 + LONE + (vlane ^ 128u)) : (lds0 + LV + wid * 1024 + vlane);
  bf16* dst = O + (size_t)(h * S_ + (xg * 8 + wid) * 32 + (lane & 31)) * 16 + (lane >> 5) * 8;
  if (xg > 0) attn_body<true, 3>(kaddr, vaddr, qf, cm0, cm8, wid, dst TPASS);
  else attn_body<false, 3>(kaddr, vaddr, qf, cm0, cm8, wid, dst TPASS);
  TSTAMP(1, 20);
  if (wid < 7) {
    wait_vm<2>();
    const unsigned ra = lds0 + LSJ + wid * 2048 + (gq * 8 + (l15 >> 2)) * 32 + (l15 & 3) * 8;
    bf16x4 f[4];
    DSRTR(f[0], ra, 0);
    DSRTR(f[1], ra, 128);
    DSRTR(f[2], ra, 1024);
    DSRTR(f[3], ra, 1024 + 128);
    asm volatile("s_waitcnt lgkmcnt(0)" ::: "memory");
    float c = 0.f;
#pragma unroll
    for (int i = 0; i < 4; ++i) {
      PIN(f[i]);
#pragma unroll
      for (int e = 0; e < 4; ++e) c += (float)f[i][e];
    }
    char* wd = (char*)Wt + (size_t)(((spn * 8 + skc) * 4 + smt) * 7 + wid) * 1024 + l16;
    *(bf16x8*)wd = cat8(f[0], f[1]);
    *(bf16x8*)(wd + 14 * 1024) = cat8(f[2], f[3]);
    c += __shfl_xor(c, 16);
    c += __shfl_xor(c, 32);
    if (lane < 16) CS[skc * (2 * F_) + smt * F_ + spn * 112 + wid * 16 + lane] = c;
  }
  TSTAMP(1, 22);
  wait_vm<0>();
  TFLUSH(1);
}

template <int MODE, int KTOT>
__global__ __launch_bounds__(512, 2) void k35_gemm(const bf16* __restrict__ A, const bf16* __restrict__ B,
                                                   const bf16* __restrict__ R, bf16* __restrict__ O,
                                                   float* __restrict__ RS) {
  constexpr int NST = G_NST, STAGE = 32768, NK = KTOT / 128, NT = NK * 4;
  extern __shared__ __attribute__((aligned(16))) char smem[];
  TDECL
  TSTAMP(MODE == 0 ? 2 : 4, 0);
  const int tid = threadIdx.x, lane = tid & 63;
  const int wid = __builtin_amdgcn_readfirstlane(tid >> 6);
  const int wm = wid & 3, wn = wid >> 2;
  const int l15 = lane & 15, gq = lane >> 4;
  const int xg = blockIdx.x & 7, idx = blockIdx.x >> 3;
  const int pm = xg * 4 + (idx >> 3), pn = idx & 7;
  const unsigned lds0 = (unsigned)(uintptr_t)(LDSP(char))smem;

  u32x4 res;
  const int tok = pm * 64 + wm * 16 + l15;
  const int ncol = pn * 64 + wn * 32 + (gq & 1) * 16 + (gq >> 1) * 8;
  GLD4(res, (unsigned)((tok * D_ + ncol) * 2), R, 0);

  const int ra = wid * 4 + gq;
  const int pa = l15 ^ (ra & 15);
  const unsigned voffA = (unsigned)((pa >> 1) * 65536 + ra * 32 + (pa & 1) * 16);
  const int pB = ((lane & 7) >> 1) ^ (((lane >> 4) & 1) | ((wid & 1) << 1));
  const unsigned voffB = (unsigned)((wid * 8 + (lane >> 3)) * 1024 + pB * 32 + (lane & 1) * 16);
  const char* baseA = (const char*)A + pm * 2048;
  const char* baseB = (const char*)B + pn * 128;
  const unsigned sbw = lds0 + wid * 1024;
  const int rot = MODE == 0 ? ROT3 * ((pn + (idx >> 3)) & 3) : ROT5 * ((pn * 2 + (idx >> 3) * 3) % NK);
  auto part = [&](int kc, int p) {
    const unsigned sb = sbw + (kc % NST) * STAGE;
    int kk = kc + rot;
    if (kk >= NK) kk -= NK;
    if (p == 0) glds(voffA, baseA + (size_t)kk * (8 * 65536), sb);
    if (p == 1) glds(voffA, baseA + (size_t)kk * (8 * 65536) + 1024, sb + 8192);
    if (p == 2) glds(voffB, baseB + (size_t)kk * (128 * 1024), sb + 16384);
    if (p == 3) glds(voffB, baseB + (size_t)kk * (128 * 1024) + 65536, sb + 16384 + 8192);
  };
  auto issue = [&](int kc) { part(kc, 0); part(kc, 1); part(kc, 2); part(kc, 3); };
  issue(0);
  BAR();
  issue(1);
  BAR();
  issue(2);
  TSTAMP(MODE == 0 ? 2 : 4, 1);

  unsigned a_addr[4], b_addr[2];
#pragma unroll
  for (int ks = 0; ks < 4; ++ks) a_addr[ks] = lds0 + (wm * 16 + l15) * 256 + (((4 * ks + gq) ^ l15) * 16);
  {
    const int kl = gq * 8 + (l15 >> 2);
    const int fr = ((l15 >> 3) & 1) | ((gq & 1) << 1);
#pragma unroll
    for (int j = 0; j < 2; ++j) b_addr[j] = lds0 + 16384 + kl * 128 + (((2 * wn + j) ^ fr) * 32) + (l15 & 3) * 8;
  }

  f32x4 acc[2];
  const f32x4 z4 = {0.f, 0.f, 0.f, 0.f};
  acc[0] = z4; acc[1] = z4;
  bf16x8 af[2];
  bf16x4 bl[2][2], bh[2][2];

  auto rd = [&](int t, int set) {
    const int kc = t >> 2, ks = t & 3, st = kc % NST;
    const unsigned hi = (unsigned)((st & ~1) * STAGE);
    const int so = (st & 1) * STAGE;
    DSRTR(bl[set][0], b_addr[0] + hi, so + ks * 4096);
    DSRTR(bh[set][0], b_addr[0] + hi, so + ks * 4096 + 512);
    DSR128(af[set], a_addr[ks] + hi, so);
    DSRTR(bl[set][1], b_addr[1] + hi, so + ks * 4096);
    DSRTR(bh[set][1], b_addr[1] + hi, so + ks * 4096 + 512);
  };
  auto wt = [&](int set) {
    asm volatile("s_waitcnt lgkmcnt(0)" ::: "memory");
    PIN(af[set]);
    PIN(bl[set][0]); PIN(bh[set][0]); PIN(bl[set][1]); PIN(bh[set][1]);
  };

  using SC = Sched<4, 4, NST, NK, 12, G_R>;
  wait_vm<SC::cum(-1) - 4>();
  BAR();
  TSTAMP(MODE == 0 ? 2 : 4, 2);
  rd(0, 0);
  static_for<SC::cum(-1), SC::cum(0)>([&](auto n) { part(n.value >> 2, n.value & 3); });
  wt(0);
  static_for<0, NT>([&](auto tc) {
    constexpr int t = tc.value, kc = t >> 2, ks = t & 3, cur = t & 1, nxt = cur ^ 1;
    if constexpr (ks == 3 && kc + 1 < NK) {
      static_assert(SC::cum(t) >= (kc + 2) * 4, "schedule");
      wait_vm<SC::cum(t) - (kc + 2) * 4>();
      BAR();
      TSTAMP(MODE == 0 ? 2 : 4, 3 + kc);
    }
    if constexpr (t + 1 < NT) rd(t + 1, nxt);
    static_for<SC::cum(t), SC::cum(t + 1)>([&](auto n) { part(n.value >> 2, n.value & 3); });
    SCHED();
    acc[0] = mfma16(cat8(bl[cur][0], bh[cur][0]), af[cur], acc[0]);
    acc[1] = mfma16(cat8(bl[cur][1], bh[cur][1]), af[cur], acc[1]);
    SCHED();
    if constexpr (t + 1 < NT) wt(nxt);
  });
  TSTAMP(MODE == 0 ? 2 : 4, 20);
  PIN(res);
  float s = 0.f, q = 0.f;
  {
#pragma unroll
    for (int r = 0; r < 4; ++r) { float a = acc[0][r], b = acc[1][r]; swap16f(a, b); acc[0][r] = a; acc[1][r] = b; }
    const bf16x8 rr = __builtin_bit_cast(bf16x8, res);
    bf16x8 o;
#pragma unroll
    for (int j = 0; j < 2; ++j)
#pragma unroll
      for (int r = 0; r < 4; ++r) {
        o[j * 4 + r] = (bf16)((float)rr[j * 4 + r] + bfr(acc[j][r]));
        const float v = (float)o[j * 4 + r];
        s += v;
        q += v * v;
      }
    *(bf16x8*)(O + (size_t)tok * D_ + ncol) = o;
  }
  if (MODE == 0) {
    s += __shfl_xor(s, 16); s += __shfl_xor(s, 32);
    q += __shfl_xor(q, 16); q += __shfl_xor(q, 32);
    if (gq == 0) *(f32x2*)(RS + ((size_t)tok * 16 + pn * 2 + wn) * 2) = f32x2{s, q};
  }
  TSTAMP(MODE == 0 ? 2 : 4, 22);
  TFLUSH(MODE == 0 ? 2 : 4);
}

template <int NP, int P0>
DEV void gu_body(char* smem, unsigned lds0, int tid, int wid, int pm, int pn, const bf16* __restrict__ Hh,
                 const bf16* __restrict__ Wt, const float* __restrict__ RS, const float* __restrict__ CS,
                 bf16* __restrict__ Hm TARG) {
  constexpr int NST = 3, STAGE = 45056, NK = 8, LPC = 2 + NP, NT = 16;
  const int lane = tid & 63, wm = wid & 3;
  const int l15 = lane & 15, gq = lane >> 4;
  const unsigned l16 = lane * 16;
  const unsigned voffA = (unsigned)((wid * 8 + (lane >> 3)) * 1024 + (((lane & 7) ^ ((wid & 1) * 4 + gq)) * 16));
  const char* baseA = (const char*)Hh + (size_t)pm * (128 * 1024);
  const int pc0 = (P0 == 0 ? 0 : 16) + wm * NP;
  const char* baseW = (const char*)Wt + (size_t)pn * (8 * 28672) + pc0 * 1024;
  const int rot = ROT4 * (pm + 2 * pn);
  auto piece = [&](int kc, int i) {
    const unsigned sb = lds0 + (kc % NST) * STAGE;
    const int kk = (kc + rot) & 7;
    if (i == 0) glds(voffA, baseA + kk * 128, sb + wid * 1024);
    else if (i == 1) glds(voffA, baseA + kk * 128 + 65536, sb + 8192 + wid * 1024);
    else glds(l16, baseW + kk * 28672 + (i - 2) * 1024, sb + 16384 + (pc0 + i - 2) * 1024);
  };
  auto issue = [&](int kc) {
#pragma unroll
    for (int i = 0; i < LPC; ++i) piece(kc, i);
  };
  float* st_ = (float*)(smem + NST * STAGE);
  float* csum = st_ + 256;
  if (P0 == 0) {
    if (wid < 2) {
      f32x4 sv[8];
      const char* rb = (const char*)RS + (size_t)(pm * 128) * 128;
      const unsigned vo = (unsigned)(tid * 128);
#pragma unroll
      for (int i = 0; i < 8; ++i) GLD4(sv[i], vo, rb, i * 16);
      issue(0);
      BAR();
      issue(1);
      wait_vm<2 * LPC>();
#pragma unroll
      for (int i = 0; i < 8; ++i) PIN(sv[i]);
      f32x4 a = sv[0];
#pragma unroll
      for (int i = 1; i < 8; ++i) a += sv[i];
      *(f32x2*)(st_ + tid * 2) = ln_stats(a[0] + a[2], a[1] + a[3]);
    } else {
      issue(0);
      BAR();
      issue(1);
    }
  } else {
    float sc[8];
    int c = tid - 256;
    const bool ok = c < 224;
    if (!ok) c = 0;
    const unsigned vo = (unsigned)(((c < 112 ? 0 : F_ - 112) + pn * 112 + c) * 4);
#pragma unroll
    for (int i = 0; i < 8; ++i) GLD1(sc[i], vo, (const char*)CS + i * (2 * F_ * 4), 0);
    issue(0);
    BAR();
    issue(1);
    wait_vm<2 * LPC>();
#pragma unroll
    for (int i = 0; i < 8; ++i) PIN(sc[i]);
    float a = sc[0];
#pragma unroll
    for (int i = 1; i < 8; ++i) a += sc[i];
    if (ok) csum[c] = a;
  }
  TSTAMP(3, 1);

  unsigned a_addr[2];
#pragma unroll
  for (int ks = 0; ks < 2; ++ks) a_addr[ks] = lds0 + (wm * 32 + l15) * 128 + (((4 * ks + gq) ^ (l15 >> 1)) * 16);
  const unsigned b_addr = lds0 + 16384 + P0 * 1024 + l16;
  f32x4 ag[2][NP], au[2][NP];
  const f32x4 z4 = {0.f, 0.f, 0.f, 0.f};
#pragma unroll
  for (int j = 0; j < NP; ++j) { ag[0][j] = z4; ag[1][j] = z4; au[0][j] = z4; au[1][j] = z4; }
  bf16x8 af[2][2], bg[2][NP], bu[2][NP];
  auto rd = [&](int t, int set) {
    const int kc = t >> 1, ks = t & 1, st = kc % NST;
    const unsigned so = st * STAGE;
    DSR128(af[set][0], a_addr[ks] + so, 0);
    DSR128(af[set][1], a_addr[ks] + so, 2048);
#pragma unroll
    for (int j = 0; j < NP; ++j) {
      DSR128(bg[set][j], b_addr + so, (ks * 14 + j) * 1024);
      DSR128(bu[set][j], b_addr + so, (ks * 14 + 7 + j) * 1024);
    }
  };
  auto wt = [&](int set) {
    asm volatile("s_waitcnt lgkmcnt(0)" ::: "memory");
    PIN(af[set][0]); PIN(af[set][1]);
#pragma unroll
    for (int j = 0; j < NP; ++j) { PIN(bg[set][j]); PIN(bu[set][j]); }
  };
  asm volatile("s_waitcnt vmcnt(%0) lgkmcnt(0)" ::"n"(LPC) : "memory");
  BAR();
  TSTAMP(3, 2);
  rd(0, 0);
  piece(NST - 1, 0); piece(NST - 1, 1); piece(NST - 1, 2);
  wt(0);
#pragma unroll
  for (int t = 0; t < NT; ++t) {
    const int kc = t >> 1, ks = t & 1, cur = t & 1, nxt = cur ^ 1;
    if (ks == 1 && kc + 1 < NK) {
      const int last = (kc + NST - 1 < NK - 1) ? (kc + NST - 1) : (NK - 1);
      wait_vm_n((last - (kc + 1)) * LPC);
      BAR();
      TSTAMP(3, 3 + kc);
    }
    if (t + 1 < NT) rd(t + 1, nxt);
    const int ic = (t + 1) / 2 + NST - 1, ip = ((t + 1) & 1) * 3;
    SCHED();
#pragma unroll
    for (int j = 0; j < NP; ++j) {
      ag[0][j] = mfma16(bg[cur][j], af[cur][0], ag[0][j]);
      ag[1][j] = mfma16(bg[cur][j], af[cur][1], ag[1][j]);
      au[0][j] = mfma16(bu[cur][j], af[cur][0], au[0][j]);
      au[1][j] = mfma16(bu[cur][j], af[cur][1], au[1][j]);
      SCHED();
      if (j < 3 && ic < NK && ip + j < LPC) piece(ic, ip + j);
      SCHED();
    }
    if (t + 1 < NT) wt(nxt);
  }
  TSTAMP(3, 20);
  bf16* dst0 = Hm + ((size_t)(pn * 7 + P0) * S_ + pm * 128 + wm * 32 + l15) * 16;
#pragma unroll
  for (int mi = 0; mi < 2; ++mi) {
    const f32x2 ms = *(const f32x2*)(st_ + (wm * 32 + mi * 16 + l15) * 2);
    const f32x2 nm = {-ms[0], -ms[0]};
    const float le = ms[1] * -1.4426950408889634f, iv = 1.0f / (ms[1] * ms[1]);
    const f32x2 le2 = {le, le}, iv2 = {iv, iv};
    unsigned pk[NP][2];
#pragma unroll
    for (int j = 0; j < NP; ++j) {
      const f32x4 cg = *(const f32x4*)(csum + (P0 + j) * 16 + gq * 4);
      const f32x4 cu = *(const f32x4*)(csum + 112 + (P0 + j) * 16 + gq * 4);
#pragma unroll
      for (int hf = 0; hf < 2; ++hf) {
        const f32x2 g = __builtin_elementwise_fma(f32x2{cg[2 * hf], cg[2 * hf + 1]}, nm,
                                                  f32x2{ag[mi][j][2 * hf], ag[mi][j][2 * hf + 1]});
        const f32x2 u = __builtin_elementwise_fma(f32x2{cu[2 * hf], cu[2 * hf + 1]}, nm,
                                                  f32x2{au[mi][j][2 * hf], au[mi][j][2 * hf + 1]});
        const f32x2 ea = g * le2;
        const f32x2 e = {__builtin_amdgcn_exp2f(ea[0]), __builtin_amdgcn_exp2f(ea[1])};
        const f32x2 d = __builtin_elementwise_fma(e, iv2, iv2);
        const f32x2 rc = {__builtin_amdgcn_rcpf(d[0]), __builtin_amdgcn_rcpf(d[1])};
        const f32x2 o = g * u * rc;
        pk[j][hf] = pk2(o[0], o[1]);
      }
    }
#pragma unroll
    for (int j = 0; j + 1 < NP; j += 2) {
      swap16(pk[j][0], pk[j + 1][0]);
      swap16(pk[j][1], pk[j + 1][1]);
      *(u32x4*)(dst0 + ((size_t)(j + (gq & 1)) * S_ + mi * 16) * 16 + (gq >> 1) * 8) =
          u32x4{pk[j][0], pk[j][1], pk[j + 1][0], pk[j + 1][1]};
    }
    if (NP & 1) *(u32x2*)(dst0 + ((size_t)(NP - 1) * S_ + mi * 16) * 16 + gq * 4) = u32x2{pk[NP - 1][0], pk[NP - 1][1]};
  }
}

__global__ __launch_bounds__(512, 2) void k4_gu(const bf16* __restrict__ Hh, const bf16* __restrict__ Wt,
                                                const float* __restrict__ RS, const float* __restrict__ CS,
                                                bf16* __restrict__ Hm) {
  extern __shared__ __attribute__((aligned(16))) char smem[];
  TDECL
  TSTAMP(3, 0);
  const int tid = threadIdx.x;
  const int wid = __builtin_amdgcn_readfirstlane(tid >> 6);
  const int xg = blockIdx.x & 7, idx = blockIdx.x >> 3;
  const int pm = (xg & 1) * 8 + (idx & 7), pn = (xg >> 1) * 4 + (idx >> 3);
  const unsigned lds0 = (unsigned)(uintptr_t)(LDSP(char))smem;
  if (wid < 4) gu_body<4, 0>(smem, lds0, tid, wid, pm, pn, Hh, Wt, RS, CS, Hm TPASS);
  else gu_body<3, 4>(smem, lds0, tid, wid, pm, pn, Hh, Wt, RS, CS, Hm TPASS);
  TSTAMP(3, 22);
  TFLUSH(3);
}

extern "C" void kernel_launch(void* const* d_in, const int* in_sizes, int n_in, void* d_out, int out_size, void* d_ws,
                              size_t ws_size, hipStream_t stream) {
  const bf16* x = (const bf16*)d_in[0];
  const bf16* wqkv = (const bf16*)d_in[1];
  const bf16* wo = (const bf16*)d_in[2];
  const bf16* w1 = (const bf16*)d_in[4];
  const bf16* w3 = (const bf16*)d_in[5];
  const bf16* w2 = (const bf16*)d_in[6];
  bf16* out = (bf16*)d_out;

  char* ws = (char*)d_ws;
  bf16* QKV = (bf16*)(ws);
  bf16* Oa = (bf16*)(ws + (7u << 20));
  bf16* Hh = (bf16*)(ws + (9u << 20));
  bf16* Hm = (bf16*)(ws + (11u << 20));
  float* RS = (float*)(ws + (18u << 20));
  float* CS = (float*)(ws + (19u << 20));
  bf16* Wt = (bf16*)(ws + (20u << 20));

  {
    constexpr int lds = K1_NST * 28672 + 1024 + 4 * 96 * 4;
    (void)hipFuncSetAttribute((const void*)k1_qkv, hipFuncAttributeMaxDynamicSharedMemorySize, lds);
    k1_qkv<<<256, 512, lds, stream>>>(x, wqkv, QKV);
  }
  {
    constexpr int lds = 57344 + 8 * 3072;
    (void)hipFuncSetAttribute((const void*)k2_attn, hipFuncAttributeMaxDynamicSharedMemorySize, lds);
    k2_attn<<<256, 512, lds, stream>>>(QKV, Oa, wo, w1, w3, w2, Wt, CS);
  }
  {
    auto k = k35_gemm<0, 512>;
    constexpr int lds = G_NST * 32768;
    (void)hipFuncSetAttribute((const void*)k, hipFuncAttributeMaxDynamicSharedMemorySize, lds);
    k<<<256, 512, lds, stream>>>(Oa, wo, x, Hh, RS);
  }
  {
    constexpr int lds = 3 * 45056 + 1024 + 224 * 4;
    (void)hipFuncSetAttribute((const void*)k4_gu, hipFuncAttributeMaxDynamicSharedMemorySize, lds);
    k4_gu<<<256, 512, lds, stream>>>(Hh, Wt, RS, CS, Hm);
  }
  {
    auto k = k35_gemm<1, 1792>;
    constexpr int lds = G_NST * 32768;
    (void)hipFuncSetAttribute((const void*)k, hipFuncAttributeMaxDynamicSharedMemorySize, lds);
    k<<<256, 512, lds, stream>>>(Hm, w2, Hh, out, nullptr);
  }
}
```

```cpp
#include <hip/hip_runtime.h>
#include <stdint.h>

typedef __attribute__((ext_vector_type(8))) __bf16 bf16x8;
typedef __attribute__((ext_vector_type(4))) __bf16 bf16x4;
typedef __attribute__((ext_vector_type(2))) __bf16 bf16x2;
typedef __attribute__((ext_vector_type(2))) float f32x2;
typedef __attribute__((ext_vector_type(4))) float f32x4;
typedef __attribute__((ext_vector_type(16))) float f32x16;
typedef __attribute__((ext_vector_type(4))) unsigned int u32x4;
typedef __attribute__((ext_vector_type(2))) unsigned int u32x2;
typedef __bf16 bf16;

#define LDSP(T) __attribute__((address_space(3))) T*
#define DEV __device__ __forceinline__

#define K1_NST 4
#define G_NST 4
#define G_R 1
#define ROT1 0
#define ROT3 0
#define ROT4 0
#define ROT5 0
constexpr int S_ = 2048, D_ = 512, H_ = 32, F_ = 1792;

DEV float bfr(float x) { return (float)(bf16)x; }

template <int N>
DEV void wait_vm() {
  asm volatile("s_waitcnt vmcnt(%0)" ::"n"(N) : "memory");
}
DEV void wait_vm_n(int n) {
  switch (n) {
#define WC(X) case X: wait_vm<X>(); break;
    WC(0) WC(1) WC(2) WC(3) WC(4) WC(5) WC(6) WC(7) WC(8) WC(9) WC(10) WC(11) WC(12) WC(13) WC(14) WC(15)
    WC(16) WC(17) WC(18) WC(19) WC(20) WC(21) WC(22) WC(23) WC(24)
#undef WC
    default: wait_vm<0>(); break;
  }
}

DEV void glds(unsigned voff, const void* sbase, unsigned ldsaddr) {
  ldsaddr = __builtin_amdgcn_readfirstlane(ldsaddr);
  asm volatile("s_mov_b32 m0, %2\n\ts_nop 0\n\tglobal_load_lds_dwordx4 %0, %1" ::"v"(voff), "s"(sbase), "s"(ldsaddr)
               : "memory");
}
#define DSR128(dst, addr, off) asm volatile("ds_read_b128 %0, %1 offset:%2" : "=v"(dst) : "v"(addr), "i"(off))
#define DSRTR(dst, addr, off) asm volatile("ds_read_b64_tr_b16 %0, %1 offset:%2" : "=v"(dst) : "v"(addr), "i"(off))
#define PIN(x) asm volatile("" : "+v"(x))
#define GLD4(dst, voff, sbase, off) \
  asm volatile("global_load_dwordx4 %0, %1, %2 offset:%3" : "=v"(dst) : "v"(voff), "s"(sbase), "i"(off))
#define GLD2(dst, voff, sbase, off) \
  asm volatile("global_load_dwordx2 %0, %1, %2 offset:%3" : "=v"(dst) : "v"(voff), "s"(sbase), "i"(off))
#define GLD1(dst, voff, sbase, off) \
  asm volatile("global_load_dword %0, %1, %2 offset:%3" : "=v"(dst) : "v"(voff), "s"(sbase), "i"(off))
#define WT 31
template <bool W>
DEV void st16(void* p, u32x4 v) {
  if constexpr (W) asm volatile("global_store_dwordx4 %0, %1, off sc1\n\ts_nop 1" ::"v"(p), "v"(v) : "memory");
  else *(u32x4*)p = v;
}
template <bool W>
DEV void st8(void* p, u32x2 v) {
  if constexpr (W) asm volatile("global_store_dwordx2 %0, %1, off sc1\n\ts_nop 1" ::"v"(p), "v"(v) : "memory");
  else *(u32x2*)p = v;
}
template <bool W>
DEV void st4(void* p, unsigned v) {
  if constexpr (W) asm volatile("global_store_dword %0, %1, off sc1" ::"v"(p), "v"(v) : "memory");
  else *(unsigned*)p = v;
}
#define BAR() __builtin_amdgcn_s_barrier()
#define SCHED() __builtin_amdgcn_sched_barrier(0)

#define TDECL
#define TSTAMP(m, i) do { } while (0)
#define TFLUSH(m) do { } while (0)
#define TARG
#define TPASS

template <int V> struct IC { static constexpr int value = V; };
template <int I, int N, class F>
DEV void static_for(F&& f) {
  if constexpr (I < N) {
    f(IC<I>{});
    static_for<I + 1, N>(f);
  }
}
template <int P, int KS, int NST, int NK, int PRE, int R>
struct Sched {
  static constexpr int avail(int t) {
    int n = ((t + 1) / KS + NST - 1) * P + ((t + 1) % KS + 1) * (P / KS);
    return n > NK * P ? NK * P : n;
  }
  static constexpr int cum(int t) {
    int n = PRE;
    for (int i = -1; i < t; ++i) { int a = avail(i); n = (n + R < a) ? n + R : a; }
    return n;
  }
};

DEV void swap32(unsigned& a, unsigned& b) {
  const auto r = __builtin_amdgcn_permlane32_swap(a, b, false, false);
  a = r[0]; b = r[1];
}
DEV void swap16(unsigned& a, unsigned& b) {
  const auto r = __builtin_amdgcn_permlane16_swap(a, b, false, false);
  a = r[0]; b = r[1];
}
DEV void swap16f(float& a, float& b) {
  unsigned x = __builtin_bit_cast(unsigned, a), y = __builtin_bit_cast(unsigned, b);
  swap16(x, y);
  a = __builtin_bit_cast(float, x); b = __builtin_bit_cast(float, y);
}
DEV unsigned pk2(float a, float b) { return __builtin_bit_cast(unsigned, bf16x2{(bf16)a, (bf16)b}); }
DEV bf16x8 cat8(bf16x4 a, bf16x4 b) { return __builtin_shufflevector(a, b, 0, 1, 2, 3, 4, 5, 6, 7); }
DEV f32x4 mfma16(bf16x8 a, bf16x8 b, f32x4 c) { return __builtin_amdgcn_mfma_f32_16x16x32_bf16(a, b, c, 0, 0, 0); }
DEV f32x16 mfma32(bf16x8 a, bf16x8 b, f32x16 c) { return __builtin_amdgcn_mfma_f32_32x32x16_bf16(a, b, c, 0, 0, 0); }

DEV f32x2 ln_stats(float sum, float sq) {
  float m = sum * (1.0f / D_);
  float var = fmaxf(sq * (1.0f / D_) - m * m, 0.f);
  float vb = bfr(var);
  float sd = bfr(__builtin_sqrtf(bfr(vb + 1e-5f)));
  f32x2 r;
  r[0] = bfr(m);
  r[1] = 1.0f / sd;
  return r;
}

__global__ __launch_bounds__(512, 2) void k1_qkv(const bf16* __restrict__ x, const bf16* __restrict__ w,
                                                 bf16* __restrict__ QKV) {
  constexpr int NST = K1_NST, STAGE = 28672, NK = 8, NT = 16;
  extern __shared__ __attribute__((aligned(16))) char smem[];
  TDECL
  TSTAMP(0, 0);
  const int tid = threadIdx.x, lane = tid & 63;
  const int wid = __builtin_amdgcn_readfirstlane(tid >> 6);
  const int wm = wid & 3, wn = wid >> 2;
  const int l15 = lane & 15, gq = lane >> 4;
  const int xg = blockIdx.x & 7, idx = blockIdx.x >> 3;
  const int pm = (xg & 3) * 4 + (idx & 3), pn = (xg >> 2) * 8 + (idx >> 2);
  const unsigned lds0 = (unsigned)(uintptr_t)(LDSP(char))smem;

  const unsigned voffA = (unsigned)((wid * 8 + (lane >> 3)) * 1024 + (((lane & 7) ^ ((wid & 1) * 4 + gq)) * 16));
  const int r12 = (lane * 43) >> 9, c12 = lane - r12 * 12;
  const bool validB = lane < 48;
  const unsigned voffB = (unsigned)((wid * 8 + r12) * 3072 + (((c12 >> 1) ^ (wid & 1)) * 16 + (c12 & 1) * 8) * 2);
  const char* baseA = (const char*)x + (size_t)pm * (128 * 1024);
  const char* baseB = (const char*)w + pn * 192;
  const int rot = ROT1 * ((idx >> 2) + 2 * (idx & 3));
  auto part = [&](int kc, int p) {
    const unsigned sb = lds0 + (kc % NST) * STAGE;
    const int kk = (kc + rot) & 7;
    if (p == 0) {
      glds(voffA, baseA + kk * 128, sb + wid * 1024);
      glds(voffA, baseA + kk * 128 + 65536, sb + 8192 + wid * 1024);
    } else if (validB) {
      glds(voffB, baseB + kk * (64 * 3072), sb + 16384 + wid * 1536);
      glds(voffB, baseB + kk * (64 * 3072) + 4 * 3072, sb + 16384 + wid * 1536 + 768);
    }
  };
  auto issue = [&](int kc) { part(kc, 0); part(kc, 1); };
  issue(0);
  BAR();
  issue(1);
  BAR();
  issue(2);
  TSTAMP(0, 1);

  unsigned a_addr[2][2], b_addr[3];
#pragma unroll
  for (int mi = 0; mi < 2; ++mi)
#pragma unroll
    for (int ks = 0; ks < 2; ++ks)
      a_addr[mi][ks] = lds0 + (wm * 32 + ((mi ^ wn) * 16) + l15) * 128 + (((4 * ks + gq) ^ (l15 >> 1)) * 16);
  {
    const int kl = gq * 8 + (l15 >> 2);
#pragma unroll
    for (int j = 0; j < 3; ++j)
      b_addr[j] = lds0 + 16384 + kl * 192 + (((3 * wn + j) ^ (gq & 1)) * 32) + (l15 & 3) * 8;
  }

  f32x4 acc[2][3], s1, s2, cs[3];
  const f32x4 z4 = {0.f, 0.f, 0.f, 0.f};
#pragma unroll
  for (int j = 0; j < 3; ++j) { acc[0][j] = z4; acc[1][j] = z4; cs[j] = z4; }
  s1 = z4; s2 = z4;
  bf16x8 af[2][2];
  bf16x4 bl[2][3], bh[2][3];
  const bf16 one = (bf16)1.0f;
  const bf16x8 ones = {one, one, one, one, one, one, one, one};

  auto rd = [&](int t, int set) {
    const int kc = t >> 1, ks = t & 1, st = kc % NST;
    const unsigned hi = (unsigned)((st & ~1) * STAGE);
    const int so = (st & 1) * STAGE;
#pragma unroll
    for (int j = 0; j < 3; ++j) {
      DSRTR(bl[set][j], b_addr[j] + hi, so + ks * 6144);
      DSRTR(bh[set][j], b_addr[j] + hi, so + ks * 6144 + 768);
      if (j < 2) DSR128(af[set][j], a_addr[j][ks] + hi, so);
    }
  };
  auto wt = [&](int set) {
    asm volatile("s_waitcnt lgkmcnt(0)" ::: "memory");
    PIN(af[set][0]); PIN(af[set][1]);
#pragma unroll
    for (int j = 0; j < 3; ++j) { PIN(bl[set][j]); PIN(bh[set][j]); }
  };

  using SC = Sched<2, 2, NST, NK, 6, 2>;
  wait_vm<(SC::cum(-1) - 2) * 2>();
  BAR();
  TSTAMP(0, 2);
  rd(0, 0);
  static_for<SC::cum(-1), SC::cum(0)>([&](auto n) { part(n.value >> 1, n.value & 1); });
  wt(0);
  static_for<0, NT>([&](auto tc) {
    constexpr int t = tc.value, kc = t >> 1, ks = t & 1, cur = t & 1, nxt = cur ^ 1;
    if constexpr (ks == 1 && kc + 1 < NK) {
      static_assert(SC::cum(t) >= (kc + 2) * 2, "schedule");
      wait_vm<(SC::cum(t) - (kc + 2) * 2) * 2>();
      BAR();
      TSTAMP(0, 3 + kc);
    }
    if constexpr (t + 1 < NT) rd(t + 1, nxt);
    static_for<SC::cum(t), SC::cum(t + 1)>([&](auto n) { part(n.value >> 1, n.value & 1); });
    SCHED();
    {
      const bf16x8 b0 = cat8(bl[cur][0], bh[cur][0]), b1 = cat8(bl[cur][1], bh[cur][1]),
                   b2 = cat8(bl[cur][2], bh[cur][2]);
#pragma unroll
      for (int mi = 0; mi < 2; ++mi) {
        acc[mi][0] = mfma16(b0, af[cur][mi], acc[mi][0]);
        acc[mi][1] = mfma16(b1, af[cur][mi], acc[mi][1]);
        acc[mi][2] = mfma16(b2, af[cur][mi], acc[mi][2]);
      }
      s1 = mfma16(ones, af[cur][0], s1);
      s2 = mfma16(af[cur][0], af[cur][0], s2);
      if ((t & 3) == wm) {
        cs[0] = mfma16(b0, ones, cs[0]);
        cs[1] = mfma16(b1, ones, cs[1]);
        cs[2] = mfma16(b2, ones, cs[2]);
      }
    }
    SCHED();
    if constexpr (t + 1 < NT) wt(nxt);
  });
  TSTAMP(0, 20);

  float* st_ = (float*)(smem + NST * STAGE);
  float* csum = st_ + 256;
  {
    const float sq = (l15 & 2) ? ((l15 & 1) ? s2[3] : s2[2]) : ((l15 & 1) ? s2[1] : s2[0]);
    const f32x2 ms = ln_stats(s1[0], sq);
    if (gq == (l15 >> 2)) *(f32x2*)(st_ + (wm * 32 + wn * 16 + l15) * 2) = ms;
    if (l15 == 0) {
#pragma unroll
      for (int j = 0; j < 3; ++j) *(f32x4*)(csum + wm * 96 + (3 * wn + j) * 16 + gq * 4) = cs[j];
    }
  }
  __syncthreads();
  TSTAMP(0, 21);
  const int tile = pm * 4 + wm;
  const float qscale = 0.25f * 1.4426950408889634f;
  f32x2 ms[2];
#pragma unroll
  for (int mi = 0; mi < 2; ++mi) ms[mi] = *(const f32x2*)(st_ + (wm * 32 + (mi ^ wn) * 16 + l15) * 2);
  const int loq = (gq >> 1) * 256 + l15 * 8 + (gq & 1) * 4, lov = l15 * 16 + gq * 4;
#pragma unroll
  for (int j = 0; j < 3; ++j) {
    f32x4 c4 = z4;
#pragma unroll
    for (int p = 0; p < 4; ++p) c4 += *(const f32x4*)(csum + p * 96 + (3 * wn + j) * 16 + gq * 4);
    const int blk = pn * 6 + wn * 3 + j;
    const int typ = blk >> 5, h = blk & 31;
    const float sc = typ == 0 ? qscale : 1.0f;
    bf16* db = QKV + ((size_t)typ << 20) + (size_t)(h * 64 + tile) * 512 + (typ == 2 ? lov : loq);
    const int tfs = typ == 2 ? 256 : 128;
#pragma unroll
    for (int mi = 0; mi < 2; ++mi) {
      const float rs = ms[mi][1] * sc;
      bf16x4 o;
#pragma unroll
      for (int r = 0; r < 4; ++r) o[r] = (bf16)((acc[mi][j][r] - ms[mi][0] * c4[r]) * rs);
      st8<(WT & 1) != 0>(db + (mi ^ wn) * tfs, __builtin_bit_cast(u32x2, o));
    }
  }
  TSTAMP(0, 22);
  TFLUSH(0);
}

template <bool FULL, int NPF>
DEV void attn_body(unsigned kaddr, unsigned vaddr, bf16x8 qf, f32x16 cm0, f32x16 cm8, int wid, bf16* dst TARG) {
  const float NEG = -1e30f;
  f32x16 z16;
#pragma unroll
  for (int r = 0; r < 16; ++r) z16[r] = 0.f;
  bf16x8 kf[9];
#pragma unroll
  for (int t = 0; t < 9; ++t)
    if (FULL || wid + t >= 8) DSR128(kf[t], kaddr, t * 1024);
  asm volatile("s_waitcnt lgkmcnt(0)" ::: "memory");
#pragma unroll
  for (int t = 0; t < 9; ++t) PIN(kf[t]);
  TSTAMP(1, 3);
  float m0 = NEG, m1 = NEG;
#pragma unroll
  for (int t = 0; t < 9; ++t)
    if (FULL || wid + t >= 8) {
      const f32x16 s = mfma32(kf[t], qf, t == 0 ? cm0 : (t == 8 ? cm8 : z16));
#pragma unroll
      for (int r = 0; r < 16; r += 4) {
        m0 = fmaxf(fmaxf(m0, s[r]), s[r + 1]);
        m1 = fmaxf(fmaxf(m1, s[r + 2]), s[r + 3]);
      }
    }
  float m = fmaxf(m0, m1);
  m = fmaxf(m, __shfl_xor(m, 32));
  f32x16 nm;
#pragma unroll
  for (int r = 0; r < 16; ++r) { nm[r] = -m; cm0[r] -= m; cm8[r] -= m; }
  TSTAMP(1, 4);
  wait_vm<NPF>();
  BAR();
  TSTAMP(1, 5);
  f32x16 oa = z16, ob = z16;
  f32x16 sc_, sn_;
  bf16x4 v0l[2], v0h[2], v1l[2], v1h[2];
  auto vrd = [&](int t, int b) {
    DSRTR(v0l[b], vaddr, t * 1024);
    DSRTR(v0h[b], vaddr, t * 1024 + 256);
    DSRTR(v1l[b], vaddr, t * 1024 + 512);
    DSRTR(v1h[b], vaddr, t * 1024 + 768);
  };
  if (FULL) { sc_ = mfma32(kf[0], qf, cm0); vrd(0, 0); }
  else sc_ = z16;
  sn_ = z16;
#pragma unroll
  for (int t = 0; t < 9; ++t) {
    const int b = t & 1;
    const bool cur_ok = FULL || wid + t >= 8, nxt_ok = t < 8 && (FULL || wid + t + 1 >= 8);
    if (cur_ok) {
      asm volatile("s_waitcnt lgkmcnt(0)" ::: "memory");
      PIN(v0l[b]); PIN(v0h[b]); PIN(v1l[b]); PIN(v1h[b]);
    }
    if (nxt_ok) {
      vrd(t + 1, b ^ 1);
      sn_ = mfma32(kf[t + 1], qf, t + 1 == 8 ? cm8 : nm);
    }
    SCHED();
    if (cur_ok) {
      bf16x8 p0, p1;
#pragma unroll
      for (int j = 0; j < 8; ++j) p0[j] = (bf16)__builtin_amdgcn_exp2f(sc_[j]);
      SCHED();
      oa = mfma32(cat8(v0l[b], v0h[b]), p0, oa);
      SCHED();
#pragma unroll
      for (int j = 0; j < 8; ++j) p1[j] = (bf16)__builtin_amdgcn_exp2f(sc_[8 + j]);
      SCHED();
      ob = mfma32(cat8(v1l[b], v1h[b]), p1, ob);
      SCHED();
    }
    sc_ = sn_;
  }
  TSTAMP(1, 6);
  const float inv = 1.0f / (oa[8] + ob[8]);
  unsigned a0 = pk2((oa[0] + ob[0]) * inv, (oa[1] + ob[1]) * inv), a1 = pk2((oa[2] + ob[2]) * inv, (oa[3] + ob[3]) * inv);
  unsigned b0 = pk2((oa[4] + ob[4]) * inv, (oa[5] + ob[5]) * inv), b1 = pk2((oa[6] + ob[6]) * inv, (oa[7] + ob[7]) * inv);
  swap32(a0, b0);
  swap32(a1, b1);
  st16<(WT & 2) != 0>(dst, u32x4{a0, a1, b0, b1});
}

__global__ __launch_bounds__(512, 2) void k2_attn(const bf16* __restrict__ QKV, bf16* __restrict__ O,
                                                  const bf16* __restrict__ wo, const bf16* __restrict__ w1,
                                                  const bf16* __restrict__ w3, const bf16* __restrict__ w2,
                                                  bf16* __restrict__ Wt, float* __restrict__ CS) {
  constexpr int LV = 16384, LONE = 32768, LSJ = 43008, LPFX = 57344;
  extern __shared__ __attribute__((aligned(16))) char smem[];
  TDECL
  TSTAMP(1, 0);
  const int tid = threadIdx.x, lane = tid & 63;
  const int wid = __builtin_amdgcn_readfirstlane(tid >> 6);
  const int l15 = lane & 15, gq = lane >> 4;
  const int xg = blockIdx.x >> 5, h = (blockIdx.x & 7) * 4 + ((blockIdx.x >> 3) & 3);
  const unsigned lds0 = (unsigned)(uintptr_t)(LDSP(char))smem;
  const int t0 = xg * 8 - 8;
  const unsigned l16 = lane * 16;
  const bool ld = xg > 0 || wid >= 4;

  const char* kb = (const char*)QKV + (2u << 20) + (ptrdiff_t)(h * 64 + t0 + 2 * wid) * 1024;
  if (ld) {
    glds(l16, kb, lds0 + wid * 2048);
    glds(l16, kb + 1024, lds0 + wid * 2048 + 1024);
  }
  bf16x8 qf;
  GLD4(qf, l16, (const char*)QKV + (size_t)(h * 64 + xg * 8 + wid) * 1024, 0);
  if (ld) {
    glds(l16, kb + (2u << 20), lds0 + LV + wid * 2048);
    glds(l16, kb + (2u << 20) + 1024, lds0 + LV + wid * 2048 + 1024);
  }
  {
    const u32x4 o4 = {0x3f803f80u, 0x3f803f80u, 0x3f803f80u, 0x3f803f80u};
    *(u32x4*)(smem + LONE + tid * 16) = o4;
    if (tid < 128) *(u32x4*)(smem + LONE + 8192 + tid * 16) = o4;
  }
  f32x16 cm0, cm8;
  {
    const int c = lane & 31, hh = lane >> 5;
#pragma unroll
    for (int r = 0; r < 16; ++r) {
      const int kr = (r & 3) + 8 * (r >> 2) + 4 * hh;
      cm0[r] = (kr > c) ? 0.f : -1e30f;
      cm8[r] = (kr <= c) ? 0.f : -1e30f;
    }
    PIN(cm0); PIN(cm8);
  }
  TSTAMP(1, 1);
  if (ld) asm volatile("s_waitcnt vmcnt(2) lgkmcnt(0)" ::: "memory");
  else asm volatile("s_waitcnt vmcnt(0) lgkmcnt(0)" ::: "memory");
  PIN(qf);
  BAR();
  TSTAMP(1, 2);
  const int skc = blockIdx.x >> 5, smt = (blockIdx.x >> 4) & 1, spn = blockIdx.x & 15;
  {
    const unsigned sc = lds0 + LPFX + wid * 3072;
    if (wid < 7) {
      const char* sb = (const char*)(smt ? w3 : w1) + (size_t)(skc * 64) * (F_ * 2) + (spn * 112 + wid * 16) * 2;
      const unsigned vo = (unsigned)((lane >> 1) * (F_ * 2) + (lane & 1) * 16);
      glds(vo, sb, lds0 + LSJ + wid * 2048);
      glds(vo, sb + 32 * (F_ * 2), lds0 + LSJ + wid * 2048 + 1024);
      glds(l16, (const char*)w2 + (size_t)blockIdx.x * 7168 + wid * 1024, sc);
    } else {
      const size_t o = (size_t)blockIdx.x * 2048;
      glds(l16, (const char*)wo + o, sc);
      glds(l16, (const char*)wo + o + 1024, sc + 1024);
      glds(l16, (const char*)wo + o + 1024, sc + 2048);
    }
  }

  const unsigned kaddr = lds0 + wid * 1024 + l16;
  const unsigned vlane = (unsigned)(((lane >> 5) * 4 + (l15 >> 2)) * 32 + (l15 & 3) * 8);
  const unsigned vaddr = (lane & 16) ? (lds0 + LONE + (vlane ^ 128u)) : (lds0 + LV + wid * 1024 + vlane);
  bf16* dst = O + (size_t)(h * S_ + (xg * 8 + wid) * 32 + (lane & 31)) * 16 + (lane >> 5) * 8;
  if (xg > 0) attn_body<true, 3>(kaddr, vaddr, qf, cm0, cm8, wid, dst TPASS);
  else attn_body<false, 3>(kaddr, vaddr, qf, cm0, cm8, wid, dst TPASS);
  TSTAMP(1, 20);
  if (wid < 7) {
    wait_vm<2>();
    const unsigned ra = lds0 + LSJ + wid * 2048 + (gq * 8 + (l15 >> 2)) * 32 + (l15 & 3) * 8;
    bf16x4 f[4];
    DSRTR(f[0], ra, 0);
    DSRTR(f[1], ra, 128);
    DSRTR(f[2], ra, 1024);
    DSRTR(f[3], ra, 1024 + 128);
    asm volatile("s_waitcnt lgkmcnt(0)" ::: "memory");
    float c = 0.f;
#pragma unroll
    for (int i = 0; i < 4; ++i) {
      PIN(f[i]);
#pragma unroll
      for (int e = 0; e < 4; ++e) c += (float)f[i][e];
    }
    char* wd = (char*)Wt + (size_t)(((spn * 8 + skc) * 4 + smt) * 7 + wid) * 1024 + l16;
    st16<(WT & 2) != 0>(wd, __builtin_bit_cast(u32x4, cat8(f[0], f[1])));
    st16<(WT & 2) != 0>(wd + 14 * 1024, __builtin_bit_cast(u32x4, cat8(f[2], f[3])));
    c += __shfl_xor(c, 16);
    c += __shfl_xor(c, 32);
    if (lane < 16) st4<(WT & 2) != 0>(CS + skc * (2 * F_) + smt * F_ + spn * 112 + wid * 16 + lane, __builtin_bit_cast(unsigned, c));
  }
  TSTAMP(1, 22);
  wait_vm<0>();
  TFLUSH(1);
}

template <int MODE, int KTOT>
__global__ __launch_bounds__(512, 2) void k35_gemm(const bf16* __restrict__ A, const bf16* __restrict__ B,
                                                   const bf16* __restrict__ R, bf16* __restrict__ O,
                                                   float* __restrict__ RS) {
  constexpr int NST = G_NST, STAGE = 32768, NK = KTOT / 128, NT = NK * 4;
  extern __shared__ __attribute__((aligned(16))) char smem[];
  TDECL
  TSTAMP(MODE == 0 ? 2 : 4, 0);
  const int tid = threadIdx.x, lane = tid & 63;
  const int wid = __builtin_amdgcn_readfirstlane(tid >> 6);
  const int wm = wid & 3, wn = wid >> 2;
  const int l15 = lane & 15, gq = lane >> 4;
  const int xg = blockIdx.x & 7, idx = blockIdx.x >> 3;
  const int pm = xg * 4 + (idx >> 3), pn = idx & 7;
  const unsigned lds0 = (unsigned)(uintptr_t)(LDSP(char))smem;

  u32x4 res;
  const int tok = pm * 64 + wm * 16 + l15;
  const int ncol = pn * 64 + wn * 32 + (gq & 1) * 16 + (gq >> 1) * 8;
  GLD4(res, (unsigned)((tok * D_ + ncol) * 2), R, 0);

  const int ra = wid * 4 + gq;
  const int pa = l15 ^ (ra & 15);
  const unsigned voffA = (unsigned)((pa >> 1) * 65536 + ra * 32 + (pa & 1) * 16);
  const int pB = ((lane & 7) >> 1) ^ (((lane >> 4) & 1) | ((wid & 1) << 1));
  const unsigned voffB = (unsigned)((wid * 8 + (lane >> 3)) * 1024 + pB * 32 + (lane & 1) * 16);
  const char* baseA = (const char*)A + pm * 2048;
  const char* baseB = (const char*)B + pn * 128;
  const unsigned sbw = lds0 + wid * 1024;
  const int rot = MODE == 0 ? ROT3 * ((pn + (idx >> 3)) & 3) : ROT5 * ((pn * 2 + (idx >> 3) * 3) % NK);
  auto part = [&](int kc, int p) {
    const unsigned sb = sbw + (kc % NST) * STAGE;
    int kk = kc + rot;
    if (kk >= NK) kk -= NK;
    if (p == 0) glds(voffA, baseA + (size_t)kk * (8 * 65536), sb);
    if (p == 1) glds(voffA, baseA + (size_t)kk * (8 * 65536) + 1024, sb + 8192);
    if (p == 2) glds(voffB, baseB + (size_t)kk * (128 * 1024), sb + 16384);
    if (p == 3) glds(voffB, baseB + (size_t)kk * (128 * 1024) + 65536, sb + 16384 + 8192);
  };
  auto issue = [&](int kc) { part(kc, 0); part(kc, 1); part(kc, 2); part(kc, 3); };
  issue(0);
  BAR();
  issue(1);
  BAR();
  issue(2);
  TSTAMP(MODE == 0 ? 2 : 4, 1);

  unsigned a_addr[4], b_addr[2];
#pragma unroll
  for (int ks = 0; ks < 4; ++ks) a_addr[ks] = lds0 + (wm * 16 + l15) * 256 + (((4 * ks + gq) ^ l15) * 16);
  {
    const int kl = gq * 8 + (l15 >> 2);
    const int fr = ((l15 >> 3) & 1) | ((gq & 1) << 1);
#pragma unroll
    for (int j = 0; j < 2; ++j) b_addr[j] = lds0 + 16384 + kl * 128 + (((2 * wn + j) ^ fr) * 32) + (l15 & 3) * 8;
  }

  f32x4 acc[2];
  const f32x4 z4 = {0.f, 0.f, 0.f, 0.f};
  acc[0] = z4; acc[1] = z4;
  bf16x8 af[2];
  bf16x4 bl[2][2], bh[2][2];

  auto rd = [&](int t, int set) {
    const int kc = t >> 2, ks = t & 3, st = kc % NST;
    const unsigned hi = (unsigned)((st & ~1) * STAGE);
    const int so = (st & 1) * STAGE;
    DSRTR(bl[set][0], b_addr[0] + hi, so + ks * 4096);
    DSRTR(bh[set][0], b_addr[0] + hi, so + ks * 4096 + 512);
    DSR128(af[set], a_addr[ks] + hi, so);
    DSRTR(bl[set][1], b_addr[1] + hi, so + ks * 4096);
    DSRTR(bh[set][1], b_addr[1] + hi, so + ks * 4096 + 512);
  };
  auto wt = [&](int set) {
    asm volatile("s_waitcnt lgkmcnt(0)" ::: "memory");
    PIN(af[set]);
    PIN(bl[set][0]); PIN(bh[set][0]); PIN(bl[set][1]); PIN(bh[set][1]);
  };

  using SC = Sched<4, 4, NST, NK, 12, G_R>;
  wait_vm<SC::cum(-1) - 4>();
  BAR();
  TSTAMP(MODE == 0 ? 2 : 4, 2);
  rd(0, 0);
  static_for<SC::cum(-1), SC::cum(0)>([&](auto n) { part(n.value >> 2, n.value & 3); });
  wt(0);
  static_for<0, NT>([&](auto tc) {
    constexpr int t = tc.value, kc = t >> 2, ks = t & 3, cur = t & 1, nxt = cur ^ 1;
    if constexpr (ks == 3 && kc + 1 < NK) {
      static_assert(SC::cum(t) >= (kc + 2) * 4, "schedule");
      wait_vm<SC::cum(t) - (kc + 2) * 4>();
      BAR();
      TSTAMP(MODE == 0 ? 2 : 4, 3 + kc);
    }
    if constexpr (t + 1 < NT) rd(t + 1, nxt);
    static_for<SC::cum(t), SC::cum(t + 1)>([&](auto n) { part(n.value >> 2, n.value & 3); });
    SCHED();
    acc[0] = mfma16(cat8(bl[cur][0], bh[cur][0]), af[cur], acc[0]);
    acc[1] = mfma16(cat8(bl[cur][1], bh[cur][1]), af[cur], acc[1]);
    SCHED();
    if constexpr (t + 1 < NT) wt(nxt);
  });
  TSTAMP(MODE == 0 ? 2 : 4, 20);
  PIN(res);
  float s = 0.f, q = 0.f;
  {
#pragma unroll
    for (int r = 0; r < 4; ++r) { float a = acc[0][r], b = acc[1][r]; swap16f(a, b); acc[0][r] = a; acc[1][r] = b; }
    const bf16x8 rr = __builtin_bit_cast(bf16x8, res);
    bf16x8 o;
#pragma unroll
    for (int j = 0; j < 2; ++j)
#pragma unroll
      for (int r = 0; r < 4; ++r) {
        o[j * 4 + r] = (bf16)((float)rr[j * 4 + r] + bfr(acc[j][r]));
        const float v = (float)o[j * 4 + r];
        s += v;
        q += v * v;
      }
    st16<(WT & (MODE == 0 ? 4 : 16)) != 0>(O + (size_t)tok * D_ + ncol, __builtin_bit_cast(u32x4, o));
  }
  if (MODE == 0) {
    s += __shfl_xor(s, 16); s += __shfl_xor(s, 32);
    q += __shfl_xor(q, 16); q += __shfl_xor(q, 32);
    if (gq == 0) st8<(WT & 4) != 0>(RS + ((size_t)tok * 16 + pn * 2 + wn) * 2, __builtin_bit_cast(u32x2, f32x2{s, q}));
  }
  TSTAMP(MODE == 0 ? 2 : 4, 22);
  TFLUSH(MODE == 0 ? 2 : 4);
}

template <int NP, int P0>
DEV void gu_body(char* smem, unsigned lds0, int tid, int wid, int pm, int pn, const bf16* __restrict__ Hh,
                 const bf16* __restrict__ Wt, const float* __restrict__ RS, const float* __restrict__ CS,
                 bf16* __restrict__ Hm TARG) {
  constexpr int NST = 3, STAGE = 45056, NK = 8, LPC = 2 + NP, NT = 16;
  const int lane = tid & 63, wm = wid & 3;
  const int l15 = lane & 15, gq = lane >> 4;
  const unsigned l16 = lane * 16;
  const unsigned voffA = (unsigned)((wid * 8 + (lane >> 3)) * 1024 + (((lane & 7) ^ ((wid & 1) * 4 + gq)) * 16));
  const char* baseA = (const char*)Hh + (size_t)pm * (128 * 1024);
  const int pc0 = (P0 == 0 ? 0 : 16) + wm * NP;
  const char* baseW = (const char*)Wt + (size_t)pn * (8 * 28672) + pc0 * 1024;
  const int rot = ROT4 * (pm + 2 * pn);
  auto piece = [&](int kc, int i) {
    const unsigned sb = lds0 + (kc % NST) * STAGE;
    const int kk = (kc + rot) & 7;
    if (i == 0) glds(voffA, baseA + kk * 128, sb + wid * 1024);
    else if (i == 1) glds(voffA, baseA + kk * 128 + 65536, sb + 8192 + wid * 1024);
    else glds(l16, baseW + kk * 28672 + (i - 2) * 1024, sb + 16384 + (pc0 + i - 2) * 1024);
  };
  auto issue = [&](int kc) {
#pragma unroll
    for (int i = 0; i < LPC; ++i) piece(kc, i);
  };
  float* st_ = (float*)(smem + NST * STAGE);
  float* csum = st_ + 256;
  if (P0 == 0) {
    if (wid < 2) {
      f32x4 sv[8];
      const char* rb = (const char*)RS + (size_t)(pm * 128) * 128;
      const unsigned vo = (unsigned)(tid * 128);
#pragma unroll
      for (int i = 0; i < 8; ++i) GLD4(sv[i], vo, rb, i * 16);
      issue(0);
      BAR();
      issue(1);
      wait_vm<2 * LPC>();
#pragma unroll
      for (int i = 0; i < 8; ++i) PIN(sv[i]);
      f32x4 a = sv[0];
#pragma unroll
      for (int i = 1; i < 8; ++i) a += sv[i];
      *(f32x2*)(st_ + tid * 2) = ln_stats(a[0] + a[2], a[1] + a[3]);
    } else {
      issue(0);
      BAR();
      issue(1);
    }
  } else {
    float sc[8];
    int c = tid - 256;
    const bool ok = c < 224;
    if (!ok) c = 0;
    const unsigned vo = (unsigned)(((c < 112 ? 0 : F_ - 112) + pn * 112 + c) * 4);
#pragma unroll
    for (int i = 0; i < 8; ++i) GLD1(sc[i], vo, (const char*)CS + i * (2 * F_ * 4), 0);
    issue(0);
    BAR();
    issue(1);
    wait_vm<2 * LPC>();
#pragma unroll
    for (int i = 0; i < 8; ++i) PIN(sc[i]);
    float a = sc[0];
#pragma unroll
    for (int i = 1; i < 8; ++i) a += sc[i];
    if (ok) csum[c] = a;
  }
  TSTAMP(3, 1);

  unsigned a_addr[2];
#pragma unroll
  for (int ks = 0; ks < 2; ++ks) a_addr[ks] = lds0 + (wm * 32 + l15) * 128 + (((4 * ks + gq) ^ (l15 >> 1)) * 16);
  const unsigned b_addr = lds0 + 16384 + P0 * 1024 + l16;
  f32x4 ag[2][NP], au[2][NP];
  const f32x4 z4 = {0.f, 0.f, 0.f, 0.f};
#pragma unroll
  for (int j = 0; j < NP; ++j) { ag[0][j] = z4; ag[1][j] = z4; au[0][j] = z4; au[1][j] = z4; }
  bf16x8 af[2][2], bg[2][NP], bu[2][NP];
  auto rd = [&](int t, int set) {
    const int kc = t >> 1, ks = t & 1, st = kc % NST;
    const unsigned so = st * STAGE;
    DSR128(af[set][0], a_addr[ks] + so, 0);
    DSR128(af[set][1], a_addr[ks] + so, 2048);
#pragma unroll
    for (int j = 0; j < NP; ++j) {
      DSR128(bg[set][j], b_addr + so, (ks * 14 + j) * 1024);
      DSR128(bu[set][j], b_addr + so, (ks * 14 + 7 + j) * 1024);
    }
  };
  auto wt = [&](int set) {
    asm volatile("s_waitcnt lgkmcnt(0)" ::: "memory");
    PIN(af[set][0]); PIN(af[set][1]);
#pragma unroll
    for (int j = 0; j < NP; ++j) { PIN(bg[set][j]); PIN(bu[set][j]); }
  };
  asm volatile("s_waitcnt vmcnt(%0) lgkmcnt(0)" ::"n"(LPC) : "memory");
  BAR();
  TSTAMP(3, 2);
  rd(0, 0);
  piece(NST - 1, 0); piece(NST - 1, 1); piece(NST - 1, 2);
  wt(0);
#pragma unroll
  for (int t = 0; t < NT; ++t) {
    const int kc = t >> 1, ks = t & 1, cur = t & 1, nxt = cur ^ 1;
    if (ks == 1 && kc + 1 < NK) {
      const int last = (kc + NST - 1 < NK - 1) ? (kc + NST - 1) : (NK - 1);
      wait_vm_n((last - (kc + 1)) * LPC);
      BAR();
      TSTAMP(3, 3 + kc);
    }
    if (t + 1 < NT) rd(t + 1, nxt);
    const int ic = (t + 1) / 2 + NST - 1, ip = ((t + 1) & 1) * 3;
    SCHED();
#pragma unroll
    for (int j = 0; j < NP; ++j) {
      ag[0][j] = mfma16(bg[cur][j], af[cur][0], ag[0][j]);
      ag[1][j] = mfma16(bg[cur][j], af[cur][1], ag[1][j]);
      au[0][j] = mfma16(bu[cur][j], af[cur][0], au[0][j]);
      au[1][j] = mfma16(bu[cur][j], af[cur][1], au[1][j]);
      SCHED();
      if (j < 3 && ic < NK && ip + j < LPC) piece(ic, ip + j);
      SCHED();
    }
    if (t + 1 < NT) wt(nxt);
  }
  TSTAMP(3, 20);
  bf16* dst0 = Hm + ((size_t)(pn * 7 + P0) * S_ + pm * 128 + wm * 32 + l15) * 16;
#pragma unroll
  for (int mi = 0; mi < 2; ++mi) {
    const f32x2 ms = *(const f32x2*)(st_ + (wm * 32 + mi * 16 + l15) * 2);
    const f32x2 nm = {-ms[0], -ms[0]};
    const float le = ms[1] * -1.4426950408889634f, iv = 1.0f / (ms[1] * ms[1]);
    const f32x2 le2 = {le, le}, iv2 = {iv, iv};
    unsigned pk[NP][2];
#pragma unroll
    for (int j = 0; j < NP; ++j) {
      const f32x4 cg = *(const f32x4*)(csum + (P0 + j) * 16 + gq * 4);
      const f32x4 cu = *(const f32x4*)(csum + 112 + (P0 + j) * 16 + gq * 4);
#pragma unroll
      for (int hf = 0; hf < 2; ++hf) {
        const f32x2 g = __builtin_elementwise_fma(f32x2{cg[2 * hf], cg[2 * hf + 1]}, nm,
                                                  f32x2{ag[mi][j][2 * hf], ag[mi][j][2 * hf + 1]});
        const f32x2 u = __builtin_elementwise_fma(f32x2{cu[2 * hf], cu[2 * hf + 1]}, nm,
                                                  f32x2{au[mi][j][2 * hf], au[mi][j][2 * hf + 1]});
        const f32x2 ea = g * le2;
        const f32x2 e = {__builtin_amdgcn_exp2f(ea[0]), __builtin_amdgcn_exp2f(ea[1])};
        const f32x2 d = __builtin_elementwise_fma(e, iv2, iv2);
        const f32x2 rc = {__builtin_amdgcn_rcpf(d[0]), __builtin_amdgcn_rcpf(d[1])};
        const f32x2 o = g * u * rc;
        pk[j][hf] = pk2(o[0], o[1]);
      }
    }
#pragma unroll
    for (int j = 0; j + 1 < NP; j += 2) {
      swap16(pk[j][0], pk[j + 1][0]);
      swap16(pk[j][1], pk[j + 1][1]);
      st16<(WT & 8) != 0>(dst0 + ((size_t)(j + (gq & 1)) * S_ + mi * 16) * 16 + (gq >> 1) * 8,
                          u32x4{pk[j][0], pk[j][1], pk[j + 1][0], pk[j + 1][1]});
    }
    if (NP & 1) st8<(WT & 8) != 0>(dst0 + ((size_t)(NP - 1) * S_ + mi * 16) * 16 + gq * 4, u32x2{pk[NP - 1][0], pk[NP - 1][1]});
  }
}

__global__ __launch_bounds__(512, 2) void k4_gu(const bf16* __restrict__ Hh, const bf16* __restrict__ Wt,
                                                const float* __restrict__ RS, const float* __restrict__ CS,
                                                bf16* __restrict__ Hm) {
  extern __shared__ __attribute__((aligned(16))) char smem[];
  TDECL
  TSTAMP(3, 0);
  const int tid = threadIdx.x;
  const int wid = __builtin_amdgcn_readfirstlane(tid >> 6);
  const int xg = blockIdx.x & 7, idx = blockIdx.x >> 3;
  const int pm = (xg & 1) * 8 + (idx & 7), pn = (xg >> 1) * 4 + (idx >> 3);
  const unsigned lds0 = (unsigned)(uintptr_t)(LDSP(char))smem;
  if (wid < 4) gu_body<4, 0>(smem, lds0, tid, wid, pm, pn, Hh, Wt, RS, CS, Hm TPASS);
  else gu_body<3, 4>(smem, lds0, tid, wid, pm, pn, Hh, Wt, RS, CS, Hm TPASS);
  TSTAMP(3, 22);
  TFLUSH(3);
}

extern "C" void kernel_launch(void* const* d_in, const int* in_sizes, int n_in, void* d_out, int out_size, void* d_ws,
                              size_t ws_size, hipStream_t stream) {
  const bf16* x = (const bf16*)d_in[0];
  const bf16* wqkv = (const bf16*)d_in[1];
  const bf16* wo = (const bf16*)d_in[2];
  const bf16* w1 = (const bf16*)d_in[4];
  const bf16* w3 = (const bf16*)d_in[5];
  const bf16* w2 = (const bf16*)d_in[6];
  bf16* out = (bf16*)d_out;

  char* ws = (char*)d_ws;
  bf16* QKV = (bf16*)(ws);
  bf16* Oa = (bf16*)(ws + (7u << 20));
  bf16* Hh = (bf16*)(ws + (9u << 20));
  bf16* Hm = (bf16*)(ws + (11u << 20));
  float* RS = (float*)(ws + (18u << 20));
  float* CS = (float*)(ws + (19u << 20));
  bf16* Wt = (bf16*)(ws + (20u << 20));

  {
    constexpr int lds = K1_NST * 28672 + 1024 + 4 * 96 * 4;
    (void)hipFuncSetAttribute((const void*)k1_qkv, hipFuncAttributeMaxDynamicSharedMemorySize, lds);
    k1_qkv<<<256, 512, lds, stream>>>(x, wqkv, QKV);
  }
  {
    constexpr int lds = 57344 + 8 * 3072;
    (void)hipFuncSetAttribute((const void*)k2_attn, hipFuncAttributeMaxDynamicSharedMemorySize, lds);
    k2_attn<<<256, 512, lds, stream>>>(QKV, Oa, wo, w1, w3, w2, Wt, CS);
  }
  {
    auto k = k35_gemm<0, 512>;
    constexpr int lds = G_NST * 32768;
    (void)hipFuncSetAttribute((const void*)k, hipFuncAttributeMaxDynamicSharedMemorySize, lds);
    k<<<256, 512, lds, stream>>>(Oa, wo, x, Hh, RS);
  }
  {
    constexpr int lds = 3 * 45056 + 1024 + 224 * 4;
    (void)hipFuncSetAttribute((const void*)k4_gu, hipFuncAttributeMaxDynamicSharedMemorySize, lds);
    k4_gu<<<256, 512, lds, stream>>>(Hh, Wt, RS, CS, Hm);
  }
  {
    auto k = k35_gemm<1, 1792>;
    constexpr int lds = G_NST * 32768;
    (void)hipFuncSetAttribute((const void*)k, hipFuncAttributeMaxDynamicSharedMemorySize, lds);
    k<<<256, 512, lds, stream>>>(Hm, w2, Hh, out, nullptr);
  }
}
```

```cpp
#include <hip/hip_runtime.h>
#include <stdint.h>

typedef __attribute__((ext_vector_type(8))) __bf16 bf16x8;
typedef __attribute__((ext_vector_type(4))) __bf16 bf16x4;
typedef __attribute__((ext_vector_type(2))) __bf16 bf16x2;
typedef __attribute__((ext_vector_type(2))) float f32x2;
typedef __attribute__((ext_vector_type(4))) float f32x4;
typedef __attribute__((ext_vector_type(16))) float f32x16;
typedef __attribute__((ext_vector_type(4))) unsigned int u32x4;
typedef __attribute__((ext_vector_type(2))) unsigned int u32x2;
typedef __bf16 bf16;

#define LDSP(T) __attribute__((address_space(3))) T*
#define DEV __device__ __forceinline__

#define K1_NST 4
#define G_NST 4
#define G_R 1
#define ROT1 0
#define ROT3 0
#define ROT4 0
#define ROT5 0
constexpr int S_ = 2048, D_ = 512, H_ = 32, F_ = 1792;

DEV float bfr(float x) { return (float)(bf16)x; }

template <int N>
DEV void wait_vm() {
  asm volatile("s_waitcnt vmcnt(%0)" ::"n"(N) : "memory");
}
DEV void wait_vm_n(int n) {
  switch (n) {
#define WC(X) case X: wait_vm<X>(); break;
    WC(0) WC(1) WC(2) WC(3) WC(4) WC(5) WC(6) WC(7) WC(8) WC(9) WC(10) WC(11) WC(12) WC(13) WC(14) WC(15)
    WC(16) WC(17) WC(18) WC(19) WC(20) WC(21) WC(22) WC(23) WC(24)
#undef WC
    default: wait_vm<0>(); break;
  }
}

DEV void glds(unsigned voff, const void* sbase, unsigned ldsaddr) {
  ldsaddr = __builtin_amdgcn_readfirstlane(ldsaddr);
  asm volatile("s_mov_b32 m0, %2\n\ts_nop 0\n\tglobal_load_lds_dwordx4 %0, %1" ::"v"(voff), "s"(sbase), "s"(ldsaddr)
               : "memory");
}
#define DSR128(dst, addr, off) asm volatile("ds_read_b128 %0, %1 offset:%2" : "=v"(dst) : "v"(addr), "i"(off))
#define DSRTR(dst, addr, off) asm volatile("ds_read_b64_tr_b16 %0, %1 offset:%2" : "=v"(dst) : "v"(addr), "i"(off))
#define PIN(x) asm volatile("" : "+v"(x))
#define GLD4(dst, voff, sbase, off) \
  asm volatile("global_load_dwordx4 %0, %1, %2 offset:%3" : "=v"(dst) : "v"(voff), "s"(sbase), "i"(off))
#define GLD2(dst, voff, sbase, off) \
  asm volatile("global_load_dwordx2 %0, %1, %2 offset:%3" : "=v"(dst) : "v"(voff), "s"(sbase), "i"(off))
#define GLD1(dst, voff, sbase, off) \
  asm volatile("global_load_dword %0, %1, %2 offset:%3" : "=v"(dst) : "v"(voff), "s"(sbase), "i"(off))
#define WT 31
template <bool W>
DEV void st16(void* p, u32x4 v) {
  if constexpr (W) asm volatile("global_store_dwordx4 %0, %1, off sc1\n\ts_nop 1" ::"v"(p), "v"(v) : "memory");
  else *(u32x4*)p = v;
}
template <bool W>
DEV void st8(void* p, u32x2 v) {
  if constexpr (W) asm volatile("global_store_dwordx2 %0, %1, off sc1\n\ts_nop 1" ::"v"(p), "v"(v) : "memory");
  else *(u32x2*)p = v;
}
template <bool W>
DEV void st4(void* p, unsigned v) {
  if constexpr (W) asm volatile("global_store_dword %0, %1, off sc1" ::"v"(p), "v"(v) : "memory");
  else *(unsigned*)p = v;
}
#define BAR() __builtin_amdgcn_s_barrier()
#define SCHED() __builtin_amdgcn_sched_barrier(0)

#define TDECL
#define TSTAMP(m, i) do { } while (0)
#define TFLUSH(m) do { } while (0)
#define TARG
#define TPASS

template <int V> struct IC { static constexpr int value = V; };
template <int I, int N, class F>
DEV void static_for(F&& f) {
  if constexpr (I < N) {
    f(IC<I>{});
    static_for<I + 1, N>(f);
  }
}
template <int P, int KS, int NST, int NK, int PRE, int R>
struct Sched {
  static constexpr int avail(int t) {
    int n = ((t + 1) / KS + NST - 1) * P + ((t + 1) % KS + 1) * (P / KS);
    return n > NK * P ? NK * P : n;
  }
  static constexpr int cum(int t) {
    int n = PRE;
    for (int i = -1; i < t; ++i) { int a = avail(i); n = (n + R < a) ? n + R : a; }
    return n;
  }
};

DEV void swap32(unsigned& a, unsigned& b) {
  const auto r = __builtin_amdgcn_permlane32_swap(a, b, false, false);
  a = r[0]; b = r[1];
}
DEV void swap16(unsigned& a, unsigned& b) {
  const auto r = __builtin_amdgcn_permlane16_swap(a, b, false, false);
  a = r[0]; b = r[1];
}
DEV void swap16f(float& a, float& b) {
  unsigned x = __builtin_bit_cast(unsigned, a), y = __builtin_bit_cast(unsigned, b);
  swap16(x, y);
  a = __builtin_bit_cast(float, x); b = __builtin_bit_cast(float, y);
}
DEV unsigned pk2(float a, float b) { return __builtin_bit_cast(unsigned, bf16x2{(bf16)a, (bf16)b}); }
DEV bf16x8 cat8(bf16x4 a, bf16x4 b) { return __builtin_shufflevector(a, b, 0, 1, 2, 3, 4, 5, 6, 7); }
DEV f32x4 mfma16(bf16x8 a, bf16x8 b, f32x4 c) { return __builtin_amdgcn_mfma_f32_16x16x32_bf16(a, b, c, 0, 0, 0); }
DEV f32x16 mfma32(bf16x8 a, bf16x8 b, f32x16 c) { return __builtin_amdgcn_mfma_f32_32x32x16_bf16(a, b, c, 0, 0, 0); }

DEV f32x2 ln_stats(float sum, float sq) {
  float m = sum * (1.0f / D_);
  float var = fmaxf(sq * (1.0f / D_) - m * m, 0.f);
  float vb = bfr(var);
  float sd = bfr(__builtin_sqrtf(bfr(vb + 1e-5f)));
  f32x2 r;
  r[0] = bfr(m);
  r[1] = 1.0f / sd;
  return r;
}

__global__ __launch_bounds__(512, 2) void k1_qkv(const bf16* __restrict__ x, const bf16* __restrict__ w,
                                                 bf16* __restrict__ QKV) {
  constexpr int NST = K1_NST, STAGE = 28672, NK = 8, NT = 16;
  extern __shared__ __attribute__((aligned(16))) char smem[];
  TDECL
  TSTAMP(0, 0);
  const int tid = threadIdx.x, lane = tid & 63;
  const int wid = __builtin_amdgcn_readfirstlane(tid >> 6);
  const int wm = wid & 3, wn = wid >> 2;
  const int l15 = lane & 15, gq = lane >> 4;
  const int xg = blockIdx.x & 7, idx = blockIdx.x >> 3;
  const int pm = (xg & 3) * 4 + (idx & 3), pn = (xg >> 2) * 8 + (idx >> 2);
  const unsigned lds0 = (unsigned)(uintptr_t)(LDSP(char))smem;
  asm volatile("" : "+s"(QKV));

  const unsigned voffA = (unsigned)((wid * 8 + (lane >> 3)) * 1024 + (((lane & 7) ^ ((wid & 1) * 4 + gq)) * 16));
  const int r12 = (lane * 43) >> 9, c12 = lane - r12 * 12;
  const bool validB = lane < 48;
  const unsigned voffB = (unsigned)((wid * 8 + r12) * 3072 + (((c12 >> 1) ^ (wid & 1)) * 16 + (c12 & 1) * 8) * 2);
  const char* baseA = (const char*)x + (size_t)pm * (128 * 1024);
  const char* baseB = (const char*)w + pn * 192;
  const int rot = ROT1 * ((idx >> 2) + 2 * (idx & 3));
  auto part = [&](int kc, int p) {
    const unsigned sb = lds0 + (kc % NST) * STAGE;
    const int kk = (kc + rot) & 7;
    if (p == 0) {
      glds(voffA, baseA + kk * 128, sb + wid * 1024);
      glds(voffA, baseA + kk * 128 + 65536, sb + 8192 + wid * 1024);
    } else if (validB) {
      glds(voffB, baseB + kk * (64 * 3072), sb + 16384 + wid * 1536);
      glds(voffB, baseB + kk * (64 * 3072) + 4 * 3072, sb + 16384 + wid * 1536 + 768);
    }
  };
  auto issue = [&](int kc) { part(kc, 0); part(kc, 1); };
  issue(0);
  BAR();
  issue(1);
  BAR();
  issue(2);
  TSTAMP(0, 1);

  unsigned a_addr[2][2], b_addr[3];
#pragma unroll
  for (int mi = 0; mi < 2; ++mi)
#pragma unroll
    for (int ks = 0; ks < 2; ++ks)
      a_addr[mi][ks] = lds0 + (wm * 32 + ((mi ^ wn) * 16) + l15) * 128 + (((4 * ks + gq) ^ (l15 >> 1)) * 16);
  {
    const int kl = gq * 8 + (l15 >> 2);
#pragma unroll
    for (int j = 0; j < 3; ++j)
      b_addr[j] = lds0 + 16384 + kl * 192 + (((3 * wn + j) ^ (gq & 1)) * 32) + (l15 & 3) * 8;
  }

  f32x4 acc[2][3], s1, s2, cs[3];
  const f32x4 z4 = {0.f, 0.f, 0.f, 0.f};
#pragma unroll
  for (int j = 0; j < 3; ++j) { acc[0][j] = z4; acc[1][j] = z4; cs[j] = z4; }
  s1 = z4; s2 = z4;
  bf16x8 af[2][2];
  bf16x4 bl[2][3], bh[2][3];
  const bf16 one = (bf16)1.0f;
  const bf16x8 ones = {one, one, one, one, one, one, one, one};

  auto rd = [&](int t, int set) {
    const int kc = t >> 1, ks = t & 1, st = kc % NST;
    const unsigned hi = (unsigned)((st & ~1) * STAGE);
    const int so = (st & 1) * STAGE;
#pragma unroll
    for (int j = 0; j < 3; ++j) {
      DSRTR(bl[set][j], b_addr[j] + hi, so + ks * 6144);
      DSRTR(bh[set][j], b_addr[j] + hi, so + ks * 6144 + 768);
      if (j < 2) DSR128(af[set][j], a_addr[j][ks] + hi, so);
    }
  };
  auto wt = [&](int set) {
    asm volatile("s_waitcnt lgkmcnt(0)" ::: "memory");
    PIN(af[set][0]); PIN(af[set][1]);
#pragma unroll
    for (int j = 0; j < 3; ++j) { PIN(bl[set][j]); PIN(bh[set][j]); }
  };

  using SC = Sched<2, 2, NST, NK, 6, 2>;
  wait_vm<(SC::cum(-1) - 2) * 2>();
  BAR();
  TSTAMP(0, 2);
  rd(0, 0);
  static_for<SC::cum(-1), SC::cum(0)>([&](auto n) { part(n.value >> 1, n.value & 1); });
  wt(0);
  static_for<0, NT>([&](auto tc) {
    constexpr int t = tc.value, kc = t >> 1, ks = t & 1, cur = t & 1, nxt = cur ^ 1;
    if constexpr (ks == 1 && kc + 1 < NK) {
      static_assert(SC::cum(t) >= (kc + 2) * 2, "schedule");
      wait_vm<(SC::cum(t) - (kc + 2) * 2) * 2>();
      BAR();
      TSTAMP(0, 3 + kc);
    }
    if constexpr (t + 1 < NT) rd(t + 1, nxt);
    static_for<SC::cum(t), SC::cum(t + 1)>([&](auto n) { part(n.value >> 1, n.value & 1); });
    SCHED();
    {
      const bf16x8 b0 = cat8(bl[cur][0], bh[cur][0]), b1 = cat8(bl[cur][1], bh[cur][1]),
                   b2 = cat8(bl[cur][2], bh[cur][2]);
#pragma unroll
      for (int mi = 0; mi < 2; ++mi) {
        acc[mi][0] = mfma16(b0, af[cur][mi], acc[mi][0]);
        acc[mi][1] = mfma16(b1, af[cur][mi], acc[mi][1]);
        acc[mi][2] = mfma16(b2, af[cur][mi], acc[mi][2]);
      }
      s1 = mfma16(ones, af[cur][0], s1);
      s2 = mfma16(af[cur][0], af[cur][0], s2);
      if ((t & 3) == wm) {
        cs[0] = mfma16(b0, ones, cs[0]);
        cs[1] = mfma16(b1, ones, cs[1]);
        cs[2] = mfma16(b2, ones, cs[2]);
      }
    }
    SCHED();
    if constexpr (t + 1 < NT) wt(nxt);
  });
  TSTAMP(0, 20);

  float* st_ = (float*)(smem + NST * STAGE);
  float* csum = st_ + 256;
  {
    const float sq = (l15 & 2) ? ((l15 & 1) ? s2[3] : s2[2]) : ((l15 & 1) ? s2[1] : s2[0]);
    const f32x2 ms = ln_stats(s1[0], sq);
    if (gq == (l15 >> 2)) *(f32x2*)(st_ + (wm * 32 + wn * 16 + l15) * 2) = ms;
    if (l15 == 0) {
#pragma unroll
      for (int j = 0; j < 3; ++j) *(f32x4*)(csum + wm * 96 + (3 * wn + j) * 16 + gq * 4) = cs[j];
    }
  }
  __syncthreads();
  TSTAMP(0, 21);
  const int tile = pm * 4 + wm;
  const float qscale = 0.25f * 1.4426950408889634f;
  f32x2 ms[2];
#pragma unroll
  for (int mi = 0; mi < 2; ++mi) ms[mi] = *(const f32x2*)(st_ + (wm * 32 + (mi ^ wn) * 16 + l15) * 2);
  const int loq = (gq >> 1) * 256 + l15 * 8 + (gq & 1) * 4, lov = l15 * 16 + gq * 4;
#pragma unroll
  for (int j = 0; j < 3; ++j) {
    f32x4 c4 = z4;
#pragma unroll
    for (int p = 0; p < 4; ++p) c4 += *(const f32x4*)(csum + p * 96 + (3 * wn + j) * 16 + gq * 4);
    const int blk = pn * 6 + wn * 3 + j;
    const int typ = blk >> 5, h = blk & 31;
    const float sc = typ == 0 ? qscale : 1.0f;
    bf16* db = QKV + ((size_t)typ << 20) + (size_t)(h * 64 + tile) * 512 + (typ == 2 ? lov : loq);
    const int tfs = typ == 2 ? 256 : 128;
#pragma unroll
    for (int mi = 0; mi < 2; ++mi) {
      const float rs = ms[mi][1] * sc;
      bf16x4 o;
#pragma unroll
      for (int r = 0; r < 4; ++r) o[r] = (bf16)((acc[mi][j][r] - ms[mi][0] * c4[r]) * rs);
      st8<(WT & 1) != 0>(db + (mi ^ wn) * tfs, __builtin_bit_cast(u32x2, o));
    }
  }
  TSTAMP(0, 22);
  TFLUSH(0);
}

template <bool FULL, int NPF>
DEV void attn_body(unsigned kaddr, unsigned vaddr, bf16x8 qf, f32x16 cm0, f32x16 cm8, int wid, bf16* dst TARG) {
  const float NEG = -1e30f;
  f32x16 z16;
#pragma unroll
  for (int r = 0; r < 16; ++r) z16[r] = 0.f;
  bf16x8 kf[9];
#pragma unroll
  for (int t = 0; t < 9; ++t)
    if (FULL || wid + t >= 8) DSR128(kf[t], kaddr, t * 1024);
  asm volatile("s_waitcnt lgkmcnt(0)" ::: "memory");
#pragma unroll
  for (int t = 0; t < 9; ++t) PIN(kf[t]);
  TSTAMP(1, 3);
  float m0 = NEG, m1 = NEG;
#pragma unroll
  for (int t = 0; t < 9; ++t)
    if (FULL || wid + t >= 8) {
      const f32x16 s = mfma32(kf[t], qf, t == 0 ? cm0 : (t == 8 ? cm8 : z16));
#pragma unroll
      for (int r = 0; r < 16; r += 4) {
        m0 = fmaxf(fmaxf(m0, s[r]), s[r + 1]);
        m1 = fmaxf(fmaxf(m1, s[r + 2]), s[r + 3]);
      }
    }
  float m = fmaxf(m0, m1);
  m = fmaxf(m, __shfl_xor(m, 32));
  f32x16 nm;
#pragma unroll
  for (int r = 0; r < 16; ++r) { nm[r] = -m; cm0[r] -= m; cm8[r] -= m; }
  TSTAMP(1, 4);
  wait_vm<NPF>();
  BAR();
  TSTAMP(1, 5);
  f32x16 oa = z16, ob = z16;
  f32x16 sc_, sn_;
  bf16x4 v0l[2], v0h[2], v1l[2], v1h[2];
  auto vrd = [&](int t, int b) {
    DSRTR(v0l[b], vaddr, t * 1024);
    DSRTR(v0h[b], vaddr, t * 1024 + 256);
    DSRTR(v1l[b], vaddr, t * 1024 + 512);
    DSRTR(v1h[b], vaddr, t * 1024 + 768);
  };
  if (FULL) { sc_ = mfma32(kf[0], qf, cm0); vrd(0, 0); }
  else sc_ = z16;
  sn_ = z16;
#pragma unroll
  for (int t = 0; t < 9; ++t) {
    const int b = t & 1;
    const bool cur_ok = FULL || wid + t >= 8, nxt_ok = t < 8 && (FULL || wid + t + 1 >= 8);
    if (cur_ok) {
      asm volatile("s_waitcnt lgkmcnt(0)" ::: "memory");
      PIN(v0l[b]); PIN(v0h[b]); PIN(v1l[b]); PIN(v1h[b]);
    }
    if (nxt_ok) {
      vrd(t + 1, b ^ 1);
      sn_ = mfma32(kf[t + 1], qf, t + 1 == 8 ? cm8 : nm);
    }
    SCHED();
    if (cur_ok) {
      bf16x8 p0, p1;
#pragma unroll
      for (int j = 0; j < 8; ++j) p0[j] = (bf16)__builtin_amdgcn_exp2f(sc_[j]);
      SCHED();
      oa = mfma32(cat8(v0l[b], v0h[b]), p0, oa);
      SCHED();
#pragma unroll
      for (int j = 0; j < 8; ++j) p1[j] = (bf16)__builtin_amdgcn_exp2f(sc_[8 + j]);
      SCHED();
      ob = mfma32(cat8(v1l[b], v1h[b]), p1, ob);
      SCHED();
    }
    sc_ = sn_;
  }
  TSTAMP(1, 6);
  const float inv = 1.0f / (oa[8] + ob[8]);
  unsigned a0 = pk2((oa[0] + ob[0]) * inv, (oa[1] + ob[1]) * inv), a1 = pk2((oa[2] + ob[2]) * inv, (oa[3] + ob[3]) * inv);
  unsigned b0 = pk2((oa[4] + ob[4]) * inv, (oa[5] + ob[5]) * inv), b1 = pk2((oa[6] + ob[6]) * inv, (oa[7] + ob[7]) * inv);
  swap32(a0, b0);
  swap32(a1, b1);
  st16<(WT & 2) != 0>(dst, u32x4{a0, a1, b0, b1});
}

__global__ __launch_bounds__(512, 2) void k2_attn(const bf16* __restrict__ QKV, bf16* __restrict__ O,
                                                  const bf16* __restrict__ wo, const bf16* __restrict__ w1,
                                                  const bf16* __restrict__ w3, const bf16* __restrict__ w2,
                                                  bf16* __restrict__ Wt, float* __restrict__ CS,
                                                  bf16* __restrict__ W2t, bf16* __restrict__ Wot) {
  constexpr int LV = 16384, LONE = 32768, LSJ = 43008;
  extern __shared__ __attribute__((aligned(16))) char smem[];
  TDECL
  TSTAMP(1, 0);
  const int tid = threadIdx.x, lane = tid & 63;
  const int wid = __builtin_amdgcn_readfirstlane(tid >> 6);
  const int l15 = lane & 15, gq = lane >> 4;
  const int xg = blockIdx.x >> 5, h = (blockIdx.x & 7) * 4 + ((blockIdx.x >> 3) & 3);
  const unsigned lds0 = (unsigned)(uintptr_t)(LDSP(char))smem;
  const int t0 = xg * 8 - 8;
  const unsigned l16 = lane * 16;
  asm volatile("" : "+s"(O), "+s"(Wt), "+s"(CS), "+s"(W2t), "+s"(Wot));
  asm volatile("" : "+s"(wo), "+s"(w1), "+s"(w3), "+s"(w2));
  const bool ld = xg > 0 || wid >= 4;

  const char* kb = (const char*)QKV + (2u << 20) + (ptrdiff_t)(h * 64 + t0 + 2 * wid) * 1024;
  if (ld) {
    glds(l16, kb, lds0 + wid * 2048);
    glds(l16, kb + 1024, lds0 + wid * 2048 + 1024);
  }
  bf16x8 qf;
  GLD4(qf, l16, (const char*)QKV + (size_t)(h * 64 + xg * 8 + wid) * 1024, 0);
  if (ld) {
    glds(l16, kb + (2u << 20), lds0 + LV + wid * 2048);
    glds(l16, kb + (2u << 20) + 1024, lds0 + LV + wid * 2048 + 1024);
  }
  {
    const u32x4 o4 = {0x3f803f80u, 0x3f803f80u, 0x3f803f80u, 0x3f803f80u};
    *(u32x4*)(smem + LONE + tid * 16) = o4;
    if (tid < 128) *(u32x4*)(smem + LONE + 8192 + tid * 16) = o4;
  }
  f32x16 cm0, cm8;
  {
    const int c = lane & 31, hh = lane >> 5;
#pragma unroll
    for (int r = 0; r < 16; ++r) {
      const int kr = (r & 3) + 8 * (r >> 2) + 4 * hh;
      cm0[r] = (kr > c) ? 0.f : -1e30f;
      cm8[r] = (kr <= c) ? 0.f : -1e30f;
    }
    PIN(cm0); PIN(cm8);
  }
  TSTAMP(1, 1);
  if (ld) asm volatile("s_waitcnt vmcnt(2) lgkmcnt(0)" ::: "memory");
  else asm volatile("s_waitcnt vmcnt(0) lgkmcnt(0)" ::: "memory");
  PIN(qf);
  BAR();
  TSTAMP(1, 2);
  const int skc = blockIdx.x >> 5, smt = (blockIdx.x >> 4) & 1, spn = blockIdx.x & 15;
  u32x4 wr0, wr1;
  {
    const int wj = wid < 7 ? wid : 6;
    const char* sb = (const char*)(smt ? w3 : w1) + (size_t)(skc * 64) * (F_ * 2) + (spn * 112 + wj * 16) * 2;
    const unsigned vo = (unsigned)((lane >> 1) * (F_ * 2) + (lane & 1) * 16);
    glds(vo, sb, lds0 + LSJ + wid * 2048);
    glds(vo, sb + 32 * (F_ * 2), lds0 + LSJ + wid * 2048 + 1024);
    const char* o = (const char*)wo + (size_t)blockIdx.x * 2048;
    const char* r0 = wid < 7 ? (const char*)w2 + (size_t)blockIdx.x * 7168 + wid * 1024 : o;
    GLD4(wr0, l16, r0, 0);
    GLD4(wr1, l16, o, 1024);
  }

  const unsigned kaddr = lds0 + wid * 1024 + l16;
  const unsigned vlane = (unsigned)(((lane >> 5) * 4 + (l15 >> 2)) * 32 + (l15 & 3) * 8);
  const unsigned vaddr = (lane & 16) ? (lds0 + LONE + (vlane ^ 128u)) : (lds0 + LV + wid * 1024 + vlane);
  bf16* dst = O + (size_t)(h * S_ + (xg * 8 + wid) * 32 + (lane & 31)) * 16 + (lane >> 5) * 8;
  if (xg > 0) attn_body<true, 4>(kaddr, vaddr, qf, cm0, cm8, wid, dst TPASS);
  else attn_body<false, 4>(kaddr, vaddr, qf, cm0, cm8, wid, dst TPASS);
  TSTAMP(1, 20);
  wait_vm<1>();
  {
    const unsigned co = (unsigned)(lane & 7) * 16;
    PIN(wr0); PIN(wr1);
    if (wid < 7) {
      st16<(WT & 2) != 0>((char*)W2t + (size_t)(lane >> 3) * (F_ * 128) + (blockIdx.x * 7 + wid) * 128 + co, wr0);
    } else {
      char* o = (char*)Wot + (size_t)(lane >> 3) * (D_ * 128) + blockIdx.x * 256 + co;
      st16<(WT & 2) != 0>(o, wr0);
      st16<(WT & 2) != 0>(o + 128, wr1);
    }
  }
  if (wid < 7) {
    const unsigned ra = lds0 + LSJ + wid * 2048 + (gq * 8 + (l15 >> 2)) * 32 + (l15 & 3) * 8;
    bf16x4 f[4];
    DSRTR(f[0], ra, 0);
    DSRTR(f[1], ra, 128);
    DSRTR(f[2], ra, 1024);
    DSRTR(f[3], ra, 1024 + 128);
    asm volatile("s_waitcnt lgkmcnt(0)" ::: "memory");
    float c = 0.f;
#pragma unroll
    for (int i = 0; i < 4; ++i) {
      PIN(f[i]);
#pragma unroll
      for (int e = 0; e < 4; ++e) c += (float)f[i][e];
    }
    char* wd = (char*)Wt + (size_t)(((spn * 8 + skc) * 4 + smt) * 7 + wid) * 1024 + l16;
    st16<(WT & 2) != 0>(wd, __builtin_bit_cast(u32x4, cat8(f[0], f[1])));
    st16<(WT & 2) != 0>(wd + 14 * 1024, __builtin_bit_cast(u32x4, cat8(f[2], f[3])));
    c += __shfl_xor(c, 16);
    c += __shfl_xor(c, 32);
    if (lane < 16) st4<(WT & 2) != 0>(CS + skc * (2 * F_) + smt * F_ + spn * 112 + wid * 16 + lane, __builtin_bit_cast(unsigned, c));
  }
  TSTAMP(1, 22);
  wait_vm<0>();
  TFLUSH(1);
}

template <int MODE, int KTOT>
__global__ __launch_bounds__(512, 2) void k35_gemm(const bf16* __restrict__ A, const bf16* __restrict__ B,
                                                   const bf16* __restrict__ R, bf16* __restrict__ O,
                                                   float* __restrict__ RS) {
  constexpr int NST = G_NST, STAGE = 32768, NK = KTOT / 128, NT = NK * 4;
  extern __shared__ __attribute__((aligned(16))) char smem[];
  TDECL
  TSTAMP(MODE == 0 ? 2 : 4, 0);
  const int tid = threadIdx.x, lane = tid & 63;
  const int wid = __builtin_amdgcn_readfirstlane(tid >> 6);
  const int wm = wid & 3, wn = wid >> 2;
  const int l15 = lane & 15, gq = lane >> 4;
  const int xg = blockIdx.x & 7, idx = blockIdx.x >> 3;
#define PNX 0
  const int pm = xg * 4 + (idx >> 3), pn = (idx & 7) ^ PNX;
  if (MODE == 0) asm volatile("" : "+s"(RS));
  const unsigned lds0 = (unsigned)(uintptr_t)(LDSP(char))smem;

  u32x4 res;
  const int tok = pm * 64 + wm * 16 + l15;
  const int ncol = pn * 64 + wn * 32 + (gq & 1) * 16 + (gq >> 1) * 8;
  GLD4(res, (unsigned)((tok * D_ + ncol) * 2), R, 0);

  const int ra = wid * 4 + gq;
  const int pa = l15 ^ (ra & 15);
  const unsigned voffA = (unsigned)((pa >> 1) * 65536 + ra * 32 + (pa & 1) * 16);
  const int pB = ((lane & 7) >> 1) ^ (((lane >> 4) & 1) | ((wid & 1) << 1));
  const unsigned voffB = (unsigned)((wid * 8 + (lane >> 3)) * 128 + pB * 32 + (lane & 1) * 16);
  const char* baseA = (const char*)A + pm * 2048;
  const char* baseB = (const char*)B + pn * (KTOT * 128);
  const unsigned sbw = lds0 + wid * 1024;
  const int rot = MODE == 0 ? ROT3 * ((pn + (idx >> 3)) & 3) : ROT5 * ((pn * 2 + (idx >> 3) * 3) % NK);
  auto part = [&](int kc, int p) {
    const unsigned sb = sbw + (kc % NST) * STAGE;
    int kk = kc + rot;
    if (kk >= NK) kk -= NK;
    if (p == 0) glds(voffA, baseA + (size_t)kk * (8 * 65536), sb);
    if (p == 1) glds(voffA, baseA + (size_t)kk * (8 * 65536) + 1024, sb + 8192);
    if (p == 2) glds(voffB, baseB + (size_t)kk * 16384, sb + 16384);
    if (p == 3) glds(voffB, baseB + (size_t)kk * 16384 + 8192, sb + 16384 + 8192);
  };
  auto issue = [&](int kc) { part(kc, 0); part(kc, 1); part(kc, 2); part(kc, 3); };
  issue(0);
  BAR();
  issue(1);
  BAR();
  issue(2);
  TSTAMP(MODE == 0 ? 2 : 4, 1);

  unsigned a_addr[4], b_addr[2];
#pragma unroll
  for (int ks = 0; ks < 4; ++ks) a_addr[ks] = lds0 + (wm * 16 + l15) * 256 + (((4 * ks + gq) ^ l15) * 16);
  {
    const int kl = gq * 8 + (l15 >> 2);
    const int fr = ((l15 >> 3) & 1) | ((gq & 1) << 1);
#pragma unroll
    for (int j = 0; j < 2; ++j) b_addr[j] = lds0 + 16384 + kl * 128 + (((2 * wn + j) ^ fr) * 32) + (l15 & 3) * 8;
  }

  f32x4 acc[2];
  const f32x4 z4 = {0.f, 0.f, 0.f, 0.f};
  acc[0] = z4; acc[1] = z4;
  bf16x8 af[2];
  bf16x4 bl[2][2], bh[2][2];

  auto rd = [&](int t, int set) {
    const int kc = t >> 2, ks = t & 3, st = kc % NST;
    const unsigned hi = (unsigned)((st & ~1) * STAGE);
    const int so = (st & 1) * STAGE;
    DSRTR(bl[set][0], b_addr[0] + hi, so + ks * 4096);
    DSRTR(bh[set][0], b_addr[0] + hi, so + ks * 4096 + 512);
    DSR128(af[set], a_addr[ks] + hi, so);
    DSRTR(bl[set][1], b_addr[1] + hi, so + ks * 4096);
    DSRTR(bh[set][1], b_addr[1] + hi, so + ks * 4096 + 512);
  };
  auto wt = [&](int set) {
    asm volatile("s_waitcnt lgkmcnt(0)" ::: "memory");
    PIN(af[set]);
    PIN(bl[set][0]); PIN(bh[set][0]); PIN(bl[set][1]); PIN(bh[set][1]);
  };

  using SC = Sched<4, 4, NST, NK, 12, G_R>;
  wait_vm<SC::cum(-1) - 4>();
  BAR();
  TSTAMP(MODE == 0 ? 2 : 4, 2);
  rd(0, 0);
  static_for<SC::cum(-1), SC::cum(0)>([&](auto n) { part(n.value >> 2, n.value & 3); });
  wt(0);
  static_for<0, NT>([&](auto tc) {
    constexpr int t = tc.value, kc = t >> 2, ks = t & 3, cur = t & 1, nxt = cur ^ 1;
    if constexpr (ks == 3 && kc + 1 < NK) {
      static_assert(SC::cum(t) >= (kc + 2) * 4, "schedule");
      wait_vm<SC::cum(t) - (kc + 2) * 4>();
      BAR();
      TSTAMP(MODE == 0 ? 2 : 4, 3 + kc);
    }
    if constexpr (t + 1 < NT) rd(t + 1, nxt);
    static_for<SC::cum(t), SC::cum(t + 1)>([&](auto n) { part(n.value >> 2, n.value & 3); });
    SCHED();
    acc[0] = mfma16(cat8(bl[cur][0], bh[cur][0]), af[cur], acc[0]);
    acc[1] = mfma16(cat8(bl[cur][1], bh[cur][1]), af[cur], acc[1]);
    SCHED();
    if constexpr (t + 1 < NT) wt(nxt);
  });
  TSTAMP(MODE == 0 ? 2 : 4, 20);
  PIN(res);
  float s = 0.f, q = 0.f;
  {
#pragma unroll
    for (int r = 0; r < 4; ++r) { float a = acc[0][r], b = acc[1][r]; swap16f(a, b); acc[0][r] = a; acc[1][r] = b; }
    const bf16x8 rr = __builtin_bit_cast(bf16x8, res);
    bf16x8 o;
#pragma unroll
    for (int j = 0; j < 2; ++j)
#pragma unroll
      for (int r = 0; r < 4; ++r) {
        o[j * 4 + r] = (bf16)((float)rr[j * 4 + r] + bfr(acc[j][r]));
        const float v = (float)o[j * 4 + r];
        s += v;
        q += v * v;
      }
    st16<(WT & (MODE == 0 ? 4 : 16)) != 0>(O + (size_t)tok * D_ + ncol, __builtin_bit_cast(u32x4, o));
  }
  if (MODE == 0) {
    s += __shfl_xor(s, 16); s += __shfl_xor(s, 32);
    q += __shfl_xor(q, 16); q += __shfl_xor(q, 32);
    if (gq == 0) st8<(WT & 4) != 0>(RS + ((size_t)(pn * 2 + wn) * S_ + tok) * 2, __builtin_bit_cast(u32x2, f32x2{s, q}));
  }
  TSTAMP(MODE == 0 ? 2 : 4, 22);
  TFLUSH(MODE == 0 ? 2 : 4);
}

template <int NP, int P0>
DEV void gu_body(char* smem, unsigned lds0, int tid, int wid, int pm, int pn, const bf16* __restrict__ Hh,
                 const bf16* __restrict__ Wt, const float* __restrict__ RS, const float* __restrict__ CS,
                 bf16* __restrict__ Hm TARG) {
  constexpr int NST = 3, STAGE = 45056, NK = 8, LPC = 2 + NP, NT = 16;
  const int lane = tid & 63, wm = wid & 3;
  const int l15 = lane & 15, gq = lane >> 4;
  const unsigned l16 = lane * 16;
  const unsigned voffA = (unsigned)((wid * 8 + (lane >> 3)) * 1024 + (((lane & 7) ^ ((wid & 1) * 4 + gq)) * 16));
  const char* baseA = (const char*)Hh + (size_t)pm * (128 * 1024);
  const int pc0 = (P0 == 0 ? 0 : 16) + wm * NP;
  const char* baseW = (const char*)Wt + (size_t)pn * (8 * 28672) + pc0 * 1024;
  const int rot = ROT4 * (pm + 2 * pn);
  auto piece = [&](int kc, int i) {
    const unsigned sb = lds0 + (kc % NST) * STAGE;
    const int kk = (kc + rot) & 7;
    if (i == 0) glds(voffA, baseA + kk * 128, sb + wid * 1024);
    else if (i == 1) glds(voffA, baseA + kk * 128 + 65536, sb + 8192 + wid * 1024);
    else glds(l16, baseW + kk * 28672 + (i - 2) * 1024, sb + 16384 + (pc0 + i - 2) * 1024);
  };
  auto issue = [&](int kc) {
#pragma unroll
    for (int i = 0; i < LPC; ++i) piece(kc, i);
  };
  float* st_ = (float*)(smem + NST * STAGE);
  float* csum = st_ + 256;
  if (P0 == 0) {
    if (wid < 2) {
      f32x2 sv[16];
      const char* rb = (const char*)RS + pm * 1024;
      const unsigned vo = (unsigned)(tid * 8);
#pragma unroll
      for (int i = 0; i < 16; ++i) GLD2(sv[i], vo, rb + i * (S_ * 8), 0);
      issue(0);
      BAR();
      issue(1);
      wait_vm<2 * LPC>();
#pragma unroll
      for (int i = 0; i < 16; ++i) PIN(sv[i]);
      f32x2 a = sv[0];
#pragma unroll
      for (int i = 1; i < 16; ++i) a += sv[i];
      *(f32x2*)(st_ + tid * 2) = ln_stats(a[0], a[1]);
    } else {
      issue(0);
      BAR();
      issue(1);
    }
  } else {
    float sc[8];
    int c = tid - 256;
    const bool ok = c < 224;
    if (!ok) c = 0;
    const unsigned vo = (unsigned)(((c < 112 ? 0 : F_ - 112) + pn * 112 + c) * 4);
#pragma unroll
    for (int i = 0; i < 8; ++i) GLD1(sc[i], vo, (const char*)CS + i * (2 * F_ * 4), 0);
    issue(0);
    BAR();
    issue(1);
    wait_vm<2 * LPC>();
#pragma unroll
    for (int i = 0; i < 8; ++i) PIN(sc[i]);
    float a = sc[0];
#pragma unroll
    for (int i = 1; i < 8; ++i) a += sc[i];
    if (ok) csum[c] = a;
  }
  TSTAMP(3, 1);

  unsigned a_addr[2];
#pragma unroll
  for (int ks = 0; ks < 2; ++ks) a_addr[ks] = lds0 + (wm * 32 + l15) * 128 + (((4 * ks + gq) ^ (l15 >> 1)) * 16);
  const unsigned b_addr = lds0 + 16384 + P0 * 1024 + l16;
  f32x4 ag[2][NP], au[2][NP];
  const f32x4 z4 = {0.f, 0.f, 0.f, 0.f};
#pragma unroll
  for (int j = 0; j < NP; ++j) { ag[0][j] = z4; ag[1][j] = z4; au[0][j] = z4; au[1][j] = z4; }
  bf16x8 af[2][2], bg[2][NP], bu[2][NP];
  auto rd = [&](int t, int set) {
    const int kc = t >> 1, ks = t & 1, st = kc % NST;
    const unsigned so = st * STAGE;
    DSR128(af[set][0], a_addr[ks] + so, 0);
    DSR128(af[set][1], a_addr[ks] + so, 2048);
#pragma unroll
    for (int j = 0; j < NP; ++j) {
      DSR128(bg[set][j], b_addr + so, (ks * 14 + j) * 1024);
      DSR128(bu[set][j], b_addr + so, (ks * 14 + 7 + j) * 1024);
    }
  };
  auto wt = [&](int set) {
    asm volatile("s_waitcnt lgkmcnt(0)" ::: "memory");
    PIN(af[set][0]); PIN(af[set][1]);
#pragma unroll
    for (int j = 0; j < NP; ++j) { PIN(bg[set][j]); PIN(bu[set][j]); }
  };
  asm volatile("s_waitcnt vmcnt(%0) lgkmcnt(0)" ::"n"(LPC) : "memory");
  BAR();
  TSTAMP(3, 2);
  rd(0, 0);
  piece(NST - 1, 0); piece(NST - 1, 1); piece(NST - 1, 2);
  wt(0);
#pragma unroll
  for (int t = 0; t < NT; ++t) {
    const int kc = t >> 1, ks = t & 1, cur = t & 1, nxt = cur ^ 1;
    if (ks == 1 && kc + 1 < NK) {
      const int last = (kc + NST - 1 < NK - 1) ? (kc + NST - 1) : (NK - 1);
      wait_vm_n((last - (kc + 1)) * LPC);
      BAR();
      TSTAMP(3, 3 + kc);
    }
    if (t + 1 < NT) rd(t + 1, nxt);
    const int ic = (t + 1) / 2 + NST - 1, ip = ((t + 1) & 1) * 3;
    SCHED();
#pragma unroll
    for (int j = 0; j < NP; ++j) {
      ag[0][j] = mfma16(bg[cur][j], af[cur][0], ag[0][j]);
      ag[1][j] = mfma16(bg[cur][j], af[cur][1], ag[1][j]);
      au[0][j] = mfma16(bu[cur][j], af[cur][0], au[0][j]);
      au[1][j] = mfma16(bu[cur][j], af[cur][1], au[1][j]);
      SCHED();
      if (j < 3 && ic < NK && ip + j < LPC) piece(ic, ip + j);
      SCHED();
    }
    if (t + 1 < NT) wt(nxt);
  }
  TSTAMP(3, 20);
  bf16* dst0 = Hm + ((size_t)(pn * 7 + P0) * S_ + pm * 128 + wm * 32 + l15) * 16;
#pragma unroll
  for (int mi = 0; mi < 2; ++mi) {
    const f32x2 ms = *(const f32x2*)(st_ + (wm * 32 + mi * 16 + l15) * 2);
    const f32x2 nm = {-ms[0], -ms[0]};
    const float le = ms[1] * -1.4426950408889634f, iv = 1.0f / (ms[1] * ms[1]);
    const f32x2 le2 = {le, le}, iv2 = {iv, iv};
    unsigned pk[NP][2];
#pragma unroll
    for (int j = 0; j < NP; ++j) {
      const f32x4 cg = *(const f32x4*)(csum + (P0 + j) * 16 + gq * 4);
      const f32x4 cu = *(const f32x4*)(csum + 112 + (P0 + j) * 16 + gq * 4);
#pragma unroll
      for (int hf = 0; hf < 2; ++hf) {
        const f32x2 g = __builtin_elementwise_fma(f32x2{cg[2 * hf], cg[2 * hf + 1]}, nm,
                                                  f32x2{ag[mi][j][2 * hf], ag[mi][j][2 * hf + 1]});
        const f32x2 u = __builtin_elementwise_fma(f32x2{cu[2 * hf], cu[2 * hf + 1]}, nm,
                                                  f32x2{au[mi][j][2 * hf], au[mi][j][2 * hf + 1]});
        const f32x2 ea = g * le2;
        const f32x2 e = {__builtin_amdgcn_exp2f(ea[0]), __builtin_amdgcn_exp2f(ea[1])};
        const f32x2 d = __builtin_elementwise_fma(e, iv2, iv2);
        const f32x2 rc = {__builtin_amdgcn_rcpf(d[0]), __builtin_amdgcn_rcpf(d[1])};
        const f32x2 o = g * u * rc;
        pk[j][hf] = pk2(o[0], o[1]);
      }
    }
#pragma unroll
    for (int j = 0; j + 1 < NP; j += 2) {
      swap16(pk[j][0], pk[j + 1][0]);
      swap16(pk[j][1], pk[j + 1][1]);
      st16<(WT & 8) != 0>(dst0 + ((size_t)(j + (gq & 1)) * S_ + mi * 16) * 16 + (gq >> 1) * 8,
                          u32x4{pk[j][0], pk[j][1], pk[j + 1][0], pk[j + 1][1]});
    }
    if (NP & 1) st8<(WT & 8) != 0>(dst0 + ((size_t)(NP - 1) * S_ + mi * 16) * 16 + gq * 4, u32x2{pk[NP - 1][0], pk[NP - 1][1]});
  }
}

__global__ __launch_bounds__(512, 2) void k4_gu(const bf16* __restrict__ Hh, const bf16* __restrict__ Wt,
                                                const float* __restrict__ RS, const float* __restrict__ CS,
                                                bf16* __restrict__ Hm) {
  extern __shared__ __attribute__((aligned(16))) char smem[];
  TDECL
  TSTAMP(3, 0);
  const int tid = threadIdx.x;
  const int wid = __builtin_amdgcn_readfirstlane(tid >> 6);
  const int xg = blockIdx.x & 7, idx = blockIdx.x >> 3;
  const int pm = (xg & 1) * 8 + (idx & 7), pn = (xg >> 1) * 4 + (idx >> 3);
  asm volatile("" : "+s"(RS), "+s"(CS), "+s"(Hm));
  const unsigned lds0 = (unsigned)(uintptr_t)(LDSP(char))smem;
  if (wid < 4) gu_body<4, 0>(smem, lds0, tid, wid, pm, pn, Hh, Wt, RS, CS, Hm TPASS);
  else gu_body<3, 4>(smem, lds0, tid, wid, pm, pn, Hh, Wt, RS, CS, Hm TPASS);
  TSTAMP(3, 22);
  TFLUSH(3);
}

extern "C" void kernel_launch(void* const* d_in, const int* in_sizes, int n_in, void* d_out, int out_size, void* d_ws,
                              size_t ws_size, hipStream_t stream) {
  const bf16* x = (const bf16*)d_in[0];
  const bf16* wqkv = (const bf16*)d_in[1];
  const bf16* wo = (const bf16*)d_in[2];
  const bf16* w1 = (const bf16*)d_in[4];
  const bf16* w3 = (const bf16*)d_in[5];
  const bf16* w2 = (const bf16*)d_in[6];
  bf16* out = (bf16*)d_out;

  char* ws = (char*)d_ws;
  bf16* QKV = (bf16*)(ws);
  bf16* Oa = (bf16*)(ws + (7u << 20));
  bf16* Hh = (bf16*)(ws + (9u << 20));
  bf16* Hm = (bf16*)(ws + (11u << 20));
  float* RS = (float*)(ws + (18u << 20));
  float* CS = (float*)(ws + (19u << 20));
  bf16* Wt = (bf16*)(ws + (20u << 20));
  bf16* W2t = (bf16*)(ws + (24u << 20));
  bf16* Wot = (bf16*)(ws + (26u << 20));

  {
    constexpr int lds = K1_NST * 28672 + 1024 + 4 * 96 * 4;
    (void)hipFuncSetAttribute((const void*)k1_qkv, hipFuncAttributeMaxDynamicSharedMemorySize, lds);
    k1_qkv<<<256, 512, lds, stream>>>(x, wqkv, QKV);
  }
  {
    constexpr int lds = 59392;
    (void)hipFuncSetAttribute((const void*)k2_attn, hipFuncAttributeMaxDynamicSharedMemorySize, lds);
    k2_attn<<<256, 512, lds, stream>>>(QKV, Oa, wo, w1, w3, w2, Wt, CS, W2t, Wot);
  }
  {
    auto k = k35_gemm<0, 512>;
    constexpr int lds = G_NST * 32768;
    (void)hipFuncSetAttribute((const void*)k, hipFuncAttributeMaxDynamicSharedMemorySize, lds);
    k<<<256, 512, lds, stream>>>(Oa, Wot, x, Hh, RS);
  }
  {
    constexpr int lds = 3 * 45056 + 1024 + 224 * 4;
    (void)hipFuncSetAttribute((const void*)k4_gu, hipFuncAttributeMaxDynamicSharedMemorySize, lds);
    k4_gu<<<256, 512, lds, stream>>>(Hh, Wt, RS, CS, Hm);
  }
  {
    auto k = k35_gemm<1, 1792>;
    constexpr int lds = G_NST * 32768;
    (void)hipFuncSetAttribute((const void*)k, hipFuncAttributeMaxDynamicSharedMemorySize, lds);
    k<<<256, 512, lds, stream>>>(Hm, W2t, Hh, out, nullptr);
  }
}
```

```cpp
#include <hip/hip_runtime.h>
#include <stdint.h>

typedef __attribute__((ext_vector_type(8))) __bf16 bf16x8;
typedef __attribute__((ext_vector_type(4))) __bf16 bf16x4;
typedef __attribute__((ext_vector_type(2))) __bf16 bf16x2;
typedef __attribute__((ext_vector_type(2))) float f32x2;
typedef __attribute__((ext_vector_type(4))) float f32x4;
typedef __attribute__((ext_vector_type(16))) float f32x16;
typedef __attribute__((ext_vector_type(4))) unsigned int u32x4;
typedef __attribute__((ext_vector_type(2))) unsigned int u32x2;
typedef __bf16 bf16;

#define LDSP(T) __attribute__((address_space(3))) T*
#define DEV __device__ __forceinline__

#define K1_NST 4
#define G_NST 4
#define G_R 1
#define ROT1 0
#define ROT3 0
#define ROT4 0
#define ROT5 0
constexpr int S_ = 2048, D_ = 512, H_ = 32, F_ = 1792;

DEV float bfr(float x) { return (float)(bf16)x; }

template <int N>
DEV void wait_vm() {
  asm volatile("s_waitcnt vmcnt(%0)" ::"n"(N) : "memory");
}
DEV void wait_vm_n(int n) {
  switch (n) {
#define WC(X) case X: wait_vm<X>(); break;
    WC(0) WC(1) WC(2) WC(3) WC(4) WC(5) WC(6) WC(7) WC(8) WC(9) WC(10) WC(11) WC(12) WC(13) WC(14) WC(15)
    WC(16) WC(17) WC(18) WC(19) WC(20) WC(21) WC(22) WC(23) WC(24)
#undef WC
    default: wait_vm<0>(); break;
  }
}

DEV void glds(unsigned voff, const void* sbase, unsigned ldsaddr) {
  ldsaddr = __builtin_amdgcn_readfirstlane(ldsaddr);
  asm volatile("s_mov_b32 m0, %2\n\ts_nop 0\n\tglobal_load_lds_dwordx4 %0, %1" ::"v"(voff), "s"(sbase), "s"(ldsaddr)
               : "memory");
}
#define DSR128(dst, addr, off) asm volatile("ds_read_b128 %0, %1 offset:%2" : "=v"(dst) : "v"(addr), "i"(off))
#define DSRTR(dst, addr, off) asm volatile("ds_read_b64_tr_b16 %0, %1 offset:%2" : "=v"(dst) : "v"(addr), "i"(off))
#define PIN(x) asm volatile("" : "+v"(x))
#define GLD4(dst, voff, sbase, off) \
  asm volatile("global_load_dwordx4 %0, %1, %2 offset:%3" : "=v"(dst) : "v"(voff), "s"(sbase), "i"(off))
#define GLD2(dst, voff, sbase, off) \
  asm volatile("global_load_dwordx2 %0, %1, %2 offset:%3" : "=v"(dst) : "v"(voff), "s"(sbase), "i"(off))
#define GLD1(dst, voff, sbase, off) \
  asm volatile("global_load_dword %0, %1, %2 offset:%3" : "=v"(dst) : "v"(voff), "s"(sbase), "i"(off))
#define WT 31
template <bool W>
DEV void st16(void* p, u32x4 v) {
  if constexpr (W) asm volatile("global_store_dwordx4 %0, %1, off sc1\n\ts_nop 1" ::"v"(p), "v"(v) : "memory");
  else *(u32x4*)p = v;
}
template <bool W>
DEV void st8(void* p, u32x2 v) {
  if constexpr (W) asm volatile("global_store_dwordx2 %0, %1, off sc1\n\ts_nop 1" ::"v"(p), "v"(v) : "memory");
  else *(u32x2*)p = v;
}
template <bool W>
DEV void st4(void* p, unsigned v) {
  if constexpr (W) asm volatile("global_store_dword %0, %1, off sc1" ::"v"(p), "v"(v) : "memory");
  else *(unsigned*)p = v;
}
#define BAR() __builtin_amdgcn_s_barrier()
#define SCHED() __builtin_amdgcn_sched_barrier(0)

#define TDECL
#define TSTAMP(i) do { } while (0)
#define TRT(i) do { } while (0)
#define TFLUSH(m) do { } while (0)
#define TARG
#define TPASS

template <int V> struct IC { static constexpr int value = V; };
template <int I, int N, class F>
DEV void static_for(F&& f) {
  if constexpr (I < N) {
    f(IC<I>{});
    static_for<I + 1, N>(f);
  }
}
template <int P, int KS, int NST, int NK, int PRE, int R>
struct Sched {
  static constexpr int avail(int t) {
    int n = ((t + 1) / KS + NST - 1) * P + ((t + 1) % KS + 1) * (P / KS);
    return n > NK * P ? NK * P : n;
  }
  static constexpr int cum(int t) {
    int n = PRE;
    for (int i = -1; i < t; ++i) { int a = avail(i); n = (n + R < a) ? n + R : a; }
    return n;
  }
};

DEV void swap32(unsigned& a, unsigned& b) {
  const auto r = __builtin_amdgcn_permlane32_swap(a, b, false, false);
  a = r[0]; b = r[1];
}
DEV void swap16(unsigned& a, unsigned& b) {
  const auto r = __builtin_amdgcn_permlane16_swap(a, b, false, false);
  a = r[0]; b = r[1];
}
DEV void swap16f(float& a, float& b) {
  unsigned x = __builtin_bit_cast(unsigned, a), y = __builtin_bit_cast(unsigned, b);
  swap16(x, y);
  a = __builtin_bit_cast(float, x); b = __builtin_bit_cast(float, y);
}
DEV unsigned pk2(float a, float b) { return __builtin_bit_cast(unsigned, bf16x2{(bf16)a, (bf16)b}); }
DEV bf16x8 cat8(bf16x4 a, bf16x4 b) { return __builtin_shufflevector(a, b, 0, 1, 2, 3, 4, 5, 6, 7); }
DEV f32x4 mfma16(bf16x8 a, bf16x8 b, f32x4 c) { return __builtin_amdgcn_mfma_f32_16x16x32_bf16(a, b, c, 0, 0, 0); }
DEV f32x16 mfma32(bf16x8 a, bf16x8 b, f32x16 c) { return __builtin_amdgcn_mfma_f32_32x32x16_bf16(a, b, c, 0, 0, 0); }

DEV f32x2 ln_stats(float sum, float sq) {
  float m = sum * (1.0f / D_);
  float var = fmaxf(sq * (1.0f / D_) - m * m, 0.f);
  float vb = bfr(var);
  float sd = bfr(__builtin_sqrtf(bfr(vb + 1e-5f)));
  f32x2 r;
  r[0] = bfr(m);
  r[1] = 1.0f / sd;
  return r;
}

DEV void glds_c(unsigned voff, const void* sbase, unsigned ldsaddr) {
  ldsaddr = __builtin_amdgcn_readfirstlane(ldsaddr);
  asm volatile("s_mov_b32 m0, %2\n\ts_nop 0\n\tglobal_load_lds_dwordx4 %0, %1 sc1" ::"v"(voff), "s"(sbase), "s"(ldsaddr)
               : "memory");
}
#define GLD2C(dst, voff, sbase, off) \
  asm volatile("global_load_dwordx2 %0, %1, %2 offset:%3 sc1" : "=v"(dst) : "v"(voff), "s"(sbase), "i"(off))

constexpr int FLS = 64;
DEV void flag_set(unsigned* f, int tid) {
  if (tid == 0) asm volatile("global_store_dword %0, %1, off sc1" ::"v"(f), "v"(1u) : "memory");
}
DEV void flag_wait(const unsigned* line, int nmask, int wid, int lane) {
  if (wid == 0) {
    const unsigned vo = (unsigned)(lane & nmask) * 4;
    for (int it = 0; it < (1 << 16); ++it) {
      unsigned v;
      asm volatile("global_load_dword %0, %1, %2 sc1\n\ts_waitcnt vmcnt(0)" : "=v"(v) : "v"(vo), "s"(line) : "memory");
      if (__builtin_amdgcn_ballot_w64(v != 1u) == 0) break;
      __builtin_amdgcn_s_sleep(1);
    }
  }
  BAR();
}

__global__ __launch_bounds__(512, 2) void k1_qkv(const bf16* __restrict__ x, const bf16* __restrict__ w,
                                                 bf16* __restrict__ QKV, const bf16* __restrict__ w1,
                                                 const bf16* __restrict__ w3, const bf16* __restrict__ w2,
                                                 const bf16* __restrict__ wo, bf16* __restrict__ Wt,
                                                 float* __restrict__ CS, bf16* __restrict__ W2f,
                                                 bf16* __restrict__ Wof, unsigned* __restrict__ FL) {
  constexpr int NST = K1_NST, STAGE = 28672, NK = 8, NT = 16;
  constexpr int LSJ = 117760;
  extern __shared__ __attribute__((aligned(16))) char smem[];
  TDECL
  TSTAMP(0);
  TRT(1);
  const int tid = threadIdx.x, lane = tid & 63;
  const int wid = __builtin_amdgcn_readfirstlane(tid >> 6);
  const int wm = wid & 3, wn = wid >> 2;
  const int l15 = lane & 15, gq = lane >> 4;
  const int xg = blockIdx.x & 7, idx = blockIdx.x >> 3;
  const int pm = (xg & 3) * 4 + (idx & 3), pn = (xg >> 2) * 8 + (idx >> 2);
  const unsigned lds0 = (unsigned)(uintptr_t)(LDSP(char))smem;
  asm volatile("" : "+s"(QKV));

  const unsigned voffA = (unsigned)((wid * 8 + (lane >> 3)) * 1024 + (((lane & 7) ^ ((wid & 1) * 4 + gq)) * 16));
  const int r12 = (lane * 43) >> 9, c12 = lane - r12 * 12;
  const bool validB = lane < 48;
  const unsigned voffB = (unsigned)((wid * 8 + r12) * 3072 + (((c12 >> 1) ^ (wid & 1)) * 16 + (c12 & 1) * 8) * 2);
  const char* baseA = (const char*)x + (size_t)pm * (128 * 1024);
  const char* baseB = (const char*)w + pn * 192;
  auto part = [&](int kc, int p) {
    const unsigned sb = lds0 + (kc % NST) * STAGE;
    if (p == 0) {
      glds(voffA, baseA + kc * 128, sb + wid * 1024);
      glds(voffA, baseA + kc * 128 + 65536, sb + 8192 + wid * 1024);
    } else if (validB) {
      glds(voffB, baseB + kc * (64 * 3072), sb + 16384 + wid * 1536);
      glds(voffB, baseB + kc * (64 * 3072) + 4 * 3072, sb + 16384 + wid * 1536 + 768);
    }
  };
  auto issue = [&](int kc) { part(kc, 0); part(kc, 1); };
  issue(0);
  BAR();
  issue(1);
  BAR();
  issue(2);
  asm volatile("" : "+s"(w1), "+s"(w3), "+s"(w2), "+s"(wo));
  asm volatile("" : "+s"(Wt), "+s"(CS), "+s"(W2f), "+s"(Wof), "+s"(FL));
  const int skc = blockIdx.x >> 5, smt = (blockIdx.x >> 4) & 1, spn = blockIdx.x & 15;
  const int su0 = blockIdx.x * 8 + wid;
  const bool sval = su0 < 1152, sw2 = su0 < 896;
  const int su = sval ? (sw2 ? su0 : su0 - 896) : 0;
  const int sdk = su >> 5, snb = su & 31;
  const int wj = wid < 7 ? wid : 6;
  const uintptr_t pw13 = smt ? (uintptr_t)w3 : (uintptr_t)w1;
  const uintptr_t pw2o = (sw2 || !sval) ? (uintptr_t)w2 : (uintptr_t)wo;
  const char* sjb1 = (const char*)pw13 + (size_t)(skc * 64) * (F_ * 2) + (spn * 112 + wj * 16) * 2;
  const char* sjb2 = (const char*)pw2o + (size_t)(sdk * 64) * (D_ * 2) + snb * 32;
  auto side_issue = [=]() {
    const unsigned vo = (unsigned)((lane >> 1) * (F_ * 2) + (lane & 1) * 16);
    glds(vo, sjb1, lds0 + LSJ + wid * 2048);
    glds(vo, sjb1 + 32 * (F_ * 2), lds0 + LSJ + wid * 2048 + 1024);
    const unsigned v2 = (unsigned)((lane >> 1) * (D_ * 2) + (lane & 1) * 16);
    glds(v2, sjb2, lds0 + LSJ + 16384 + wid * 2048);
    glds(v2, sjb2 + 32 * (D_ * 2), lds0 + LSJ + 16384 + wid * 2048 + 1024);
  };

  unsigned a_addr[2][2], b_addr[3];
#pragma unroll
  for (int mi = 0; mi < 2; ++mi)
#pragma unroll
    for (int ks = 0; ks < 2; ++ks)
      a_addr[mi][ks] = lds0 + (wm * 32 + ((mi ^ wn) * 16) + l15) * 128 + (((4 * ks + gq) ^ (l15 >> 1)) * 16);
  {
    const int kl = gq * 8 + (l15 >> 2);
#pragma unroll
    for (int j = 0; j < 3; ++j)
      b_addr[j] = lds0 + 16384 + kl * 192 + (((3 * wn + j) ^ (gq & 1)) * 32) + (l15 & 3) * 8;
  }

  f32x4 acc[2][3], s1, s2, cs[3];
  const f32x4 z4 = {0.f, 0.f, 0.f, 0.f};
#pragma unroll
  for (int j = 0; j < 3; ++j) { acc[0][j] = z4; acc[1][j] = z4; cs[j] = z4; }
  s1 = z4; s2 = z4;
  bf16x8 af[2][2];
  bf16x4 bl[2][3], bh[2][3];
  const bf16 one = (bf16)1.0f;
  const bf16x8 ones = {one, one, one, one, one, one, one, one};

  auto rd = [&](int t, int set) {
    const int kc = t >> 1, ks = t & 1, st = kc % NST;
    const unsigned hi = (unsigned)((st & ~1) * STAGE);
    const int so = (st & 1) * STAGE;
#pragma unroll
    for (int j = 0; j < 3; ++j) {
      DSRTR(bl[set][j], b_addr[j] + hi, so + ks * 6144);
      DSRTR(bh[set][j], b_addr[j] + hi, so + ks * 6144 + 768);
      if (j < 2) DSR128(af[set][j], a_addr[j][ks] + hi, so);
    }
  };
  auto wt = [&](int set) {
    asm volatile("s_waitcnt lgkmcnt(0)" ::: "memory");
    PIN(af[set][0]); PIN(af[set][1]);
#pragma unroll
    for (int j = 0; j < 3; ++j) { PIN(bl[set][j]); PIN(bh[set][j]); }
  };

  using SC = Sched<2, 2, NST, NK, 6, 2>;
  constexpr int TS = []() { int t = 0; while (SC::cum(t) < NK * 2) ++t; return t; }();
  static_assert(TS < NT, "side job slot");
  wait_vm<(SC::cum(-1) - 2) * 2>();
  BAR();
  TSTAMP(2);
  rd(0, 0);
  static_for<SC::cum(-1), SC::cum(0)>([&](auto n) { part(n.value >> 1, n.value & 1); });
  wt(0);
  static_for<0, NT>([&](auto tc) {
    constexpr int t = tc.value, kc = t >> 1, ks = t & 1, cur = t & 1, nxt = cur ^ 1;
    if constexpr (ks == 1 && kc + 1 < NK) {
      static_assert(SC::cum(t) >= (kc + 2) * 2, "schedule");
      wait_vm<(SC::cum(t) - (kc + 2) * 2) * 2 + (t > TS ? 4 : 0)>();
      BAR();
      TSTAMP(3 + kc);
    }
    if constexpr (t + 1 < NT) rd(t + 1, nxt);
    static_for<SC::cum(t), SC::cum(t + 1)>([&](auto n) { part(n.value >> 1, n.value & 1); });
    if constexpr (t == TS) side_issue();
    SCHED();
    {
      const bf16x8 b0 = cat8(bl[cur][0], bh[cur][0]), b1 = cat8(bl[cur][1], bh[cur][1]),
                   b2 = cat8(bl[cur][2], bh[cur][2]);
#pragma unroll
      for (int mi = 0; mi < 2; ++mi) {
        acc[mi][0] = mfma16(b0, af[cur][mi], acc[mi][0]);
        acc[mi][1] = mfma16(b1, af[cur][mi], acc[mi][1]);
        acc[mi][2] = mfma16(b2, af[cur][mi], acc[mi][2]);
      }
      s1 = mfma16(ones, af[cur][0], s1);
      s2 = mfma16(af[cur][0], af[cur][0], s2);
      if ((t & 3) == wm) {
        cs[0] = mfma16(b0, ones, cs[0]);
        cs[1] = mfma16(b1, ones, cs[1]);
        cs[2] = mfma16(b2, ones, cs[2]);
      }
    }
    SCHED();
    if constexpr (t + 1 < NT) wt(nxt);
  });
  TSTAMP(20);

  float* st_ = (float*)(smem + NST * STAGE);
  float* csum = st_ + 256;
  {
    const float sq = (l15 & 2) ? ((l15 & 1) ? s2[3] : s2[2]) : ((l15 & 1) ? s2[1] : s2[0]);
    const f32x2 ms = ln_stats(s1[0], sq);
    if (gq == (l15 >> 2)) *(f32x2*)(st_ + (wm * 32 + wn * 16 + l15) * 2) = ms;
    if (l15 == 0) {
#pragma unroll
      for (int j = 0; j < 3; ++j) *(f32x4*)(csum + wm * 96 + (3 * wn + j) * 16 + gq * 4) = cs[j];
    }
  }
  asm volatile("s_waitcnt lgkmcnt(0)" ::: "memory");
  BAR();
  TSTAMP(21);
  const int tile = pm * 4 + wm;
  const float qscale = 0.25f * 1.4426950408889634f;
  f32x2 ms[2];
#pragma unroll
  for (int mi = 0; mi < 2; ++mi) ms[mi] = *(const f32x2*)(st_ + (wm * 32 + (mi ^ wn) * 16 + l15) * 2);
  const int loq = (gq >> 1) * 256 + l15 * 8 + (gq & 1) * 4, lov = l15 * 16 + gq * 4;
#pragma unroll
  for (int j = 0; j < 3; ++j) {
    f32x4 c4 = z4;
#pragma unroll
    for (int p = 0; p < 4; ++p) c4 += *(const f32x4*)(csum + p * 96 + (3 * wn + j) * 16 + gq * 4);
    const int blk = pn * 6 + wn * 3 + j;
    const int typ = blk >> 5, h = blk & 31;
    const float sc = typ == 0 ? qscale : 1.0f;
    bf16* db = QKV + ((size_t)typ << 20) + (size_t)(h * 64 + tile) * 512 + (typ == 2 ? lov : loq);
    const int tfs = typ == 2 ? 256 : 128;
#pragma unroll
    for (int mi = 0; mi < 2; ++mi) {
      const float rs = ms[mi][1] * sc;
      bf16x4 o;
#pragma unroll
      for (int r = 0; r < 4; ++r) o[r] = (bf16)((acc[mi][j][r] - ms[mi][0] * c4[r]) * rs);
      st8<true>(db + (mi ^ wn) * tfs, __builtin_bit_cast(u32x2, o));
    }
  }
  TSTAMP(22);
  wait_vm<6>();
  {
    const unsigned ra = lds0 + LSJ + wid * 2048 + (gq * 8 + (l15 >> 2)) * 32 + (l15 & 3) * 8;
    bf16x4 f[4], g[4];
    DSRTR(f[0], ra, 0);
    DSRTR(f[1], ra, 128);
    DSRTR(f[2], ra, 1024);
    DSRTR(f[3], ra, 1024 + 128);
    DSRTR(g[0], ra, 16384);
    DSRTR(g[1], ra, 16384 + 128);
    DSRTR(g[2], ra, 16384 + 1024);
    DSRTR(g[3], ra, 16384 + 1024 + 128);
    asm volatile("s_waitcnt lgkmcnt(0)" ::: "memory");
#pragma unroll
    for (int i = 0; i < 4; ++i) { PIN(f[i]); PIN(g[i]); }
    const unsigned l16 = lane * 16;
    if (sval) {
      const int fpn = snb >> 2, fwn = (snb >> 1) & 1, fcb = snb & 1;
      char* d2;
      if (sw2) {
        const int kq = sdk / 7, j = 2 * (sdk - kq * 7);
        d2 = (char*)W2f + (size_t)((fpn * 8 + kq * 2 + fwn) * 28 + j * 2 + fcb) * 1024;
      } else {
        const int kq = sdk >> 1, j = 2 * (sdk & 1);
        d2 = (char*)Wof + (size_t)((fpn * 8 + kq * 2 + fwn) * 8 + j * 2 + fcb) * 1024;
      }
      st16<true>(d2 + l16, __builtin_bit_cast(u32x4, cat8(g[0], g[1])));
      st16<true>(d2 + l16 + 2048, __builtin_bit_cast(u32x4, cat8(g[2], g[3])));
    }
    if (wid < 7) {
      float c = 0.f;
#pragma unroll
      for (int i = 0; i < 4; ++i)
#pragma unroll
        for (int e = 0; e < 4; ++e) c += (float)f[i][e];
      char* wd = (char*)Wt + (size_t)(((spn * 8 + skc) * 4 + smt) * 7 + wid) * 1024 + l16;
      st16<true>(wd, __builtin_bit_cast(u32x4, cat8(f[0], f[1])));
      st16<true>(wd + 14 * 1024, __builtin_bit_cast(u32x4, cat8(f[2], f[3])));
      c += __shfl_xor(c, 16);
      c += __shfl_xor(c, 32);
      if (lane < 16) st4<true>(CS + skc * (2 * F_) + smt * F_ + spn * 112 + wid * 16 + lane, __builtin_bit_cast(unsigned, c));
    } else if (blockIdx.x < 40) {
      st4<true>(FL + blockIdx.x * FLS + lane, 0u);
    }
  }
  TSTAMP(23);
  TFLUSH(0);
}

constexpr int L_W = 65536, L_ST = 65536 + 3 * 28672;

template <bool FULL, class PRE>
DEV void attn_body(unsigned kaddr, unsigned vaddr, bf16x8 qf, f32x16 cm0, f32x16 cm8, int wid, bf16* dst, PRE&& pre TARG) {
  const float NEG = -1e30f;
  f32x16 z16;
#pragma unroll
  for (int r = 0; r < 16; ++r) z16[r] = 0.f;
  bf16x8 kf[9];
#pragma unroll
  for (int t = 0; t < 9; ++t)
    if (FULL || wid + t >= 8) DSR128(kf[t], kaddr, t * 1024);
  asm volatile("s_waitcnt lgkmcnt(0)" ::: "memory");
#pragma unroll
  for (int t = 0; t < 9; ++t) PIN(kf[t]);
  float m0 = NEG, m1 = NEG;
#pragma unroll
  for (int t = 0; t < 9; ++t)
    if (FULL || wid + t >= 8) {
      const f32x16 s = mfma32(kf[t], qf, t == 0 ? cm0 : (t == 8 ? cm8 : z16));
#pragma unroll
      for (int r = 0; r < 16; r += 4) {
        m0 = fmaxf(fmaxf(m0, s[r]), s[r + 1]);
        m1 = fmaxf(fmaxf(m1, s[r + 2]), s[r + 3]);
      }
    }
  float m = fmaxf(m0, m1);
  m = fmaxf(m, __shfl_xor(m, 32));
  f32x16 nm;
#pragma unroll
  for (int r = 0; r < 16; ++r) { nm[r] = -m; cm0[r] -= m; cm8[r] -= m; }
  TSTAMP(3);
  wait_vm<0>();
  BAR();
  TSTAMP(4);
  pre();
  f32x16 oa = z16, ob = z16;
  f32x16 sc_, sn_;
  bf16x4 v0l[2], v0h[2], v1l[2], v1h[2];
  auto vrd = [&](int t, int b) {
    DSRTR(v0l[b], vaddr, t * 1024);
    DSRTR(v0h[b], vaddr, t * 1024 + 256);
    DSRTR(v1l[b], vaddr, t * 1024 + 512);
    DSRTR(v1h[b], vaddr, t * 1024 + 768);
  };
  if (FULL) { sc_ = mfma32(kf[0], qf, cm0); vrd(0, 0); }
  else sc_ = z16;
  sn_ = z16;
#pragma unroll
  for (int t = 0; t < 9; ++t) {
    const int b = t & 1;
    const bool cur_ok = FULL || wid + t >= 8, nxt_ok = t < 8 && (FULL || wid + t + 1 >= 8);
    if (cur_ok) {
      asm volatile("s_waitcnt lgkmcnt(0)" ::: "memory");
      PIN(v0l[b]); PIN(v0h[b]); PIN(v1l[b]); PIN(v1h[b]);
    }
    if (nxt_ok) {
      vrd(t + 1, b ^ 1);
      sn_ = mfma32(kf[t + 1], qf, t + 1 == 8 ? cm8 : nm);
    }
    SCHED();
    if (cur_ok) {
      bf16x8 p0, p1;
#pragma unroll
      for (int j = 0; j < 8; ++j) p0[j] = (bf16)__builtin_amdgcn_exp2f(sc_[j]);
      SCHED();
      oa = mfma32(cat8(v0l[b], v0h[b]), p0, oa);
      SCHED();
#pragma unroll
      for (int j = 0; j < 8; ++j) p1[j] = (bf16)__builtin_amdgcn_exp2f(sc_[8 + j]);
      SCHED();
      ob = mfma32(cat8(v1l[b], v1h[b]), p1, ob);
      SCHED();
    }
    sc_ = sn_;
  }
  TSTAMP(5);
  const float inv = 1.0f / (oa[8] + ob[8]);
  unsigned a0 = pk2((oa[0] + ob[0]) * inv, (oa[1] + ob[1]) * inv), a1 = pk2((oa[2] + ob[2]) * inv, (oa[3] + ob[3]) * inv);
  unsigned b0 = pk2((oa[4] + ob[4]) * inv, (oa[5] + ob[5]) * inv), b1 = pk2((oa[6] + ob[6]) * inv, (oa[7] + ob[7]) * inv);
  swap32(a0, b0);
  swap32(a1, b1);
  st16<true>(dst, u32x4{a0, a1, b0, b1});
}

template <int NKS, int NR, int TS0, class HOOK>
DEV void gemm64(char* smem, unsigned lds0, int tid, int wid, const char* baseA, u32x4 (&b)[NKS * 2], float (&r)[8],
                HOOK&& hook TARG) {
  constexpr int NS = NKS / 2, NI = NR < NS ? NR : NS;
  const int lane = tid & 63, l15 = lane & 15, gq = lane >> 4;
  const int kq = wid >> 1, wn = wid & 1;
  const int un = (lane & 7) ^ ((wid & 1) * 4 + gq);
  const unsigned voffA = (unsigned)((un >> 1) * 65536 + (wid * 8 + (lane >> 3)) * 32 + (un & 1) * 16);
  auto issue = [&](int s) {
#pragma unroll
    for (int q = 0; q < 4; ++q)
      glds_c(voffA, baseA + (size_t)(q * NKS * 2 + s * 4) * 65536, lds0 + (s % NR) * 32768 + q * 8192 + wid * 1024);
  };
#pragma unroll
  for (int s = 0; s < NI; ++s) issue(s);
  wait_vm<NI * 4>();
#pragma unroll
  for (int i = 0; i < NKS * 2; ++i) PIN(b[i]);
  hook();
  unsigned a_addr[2];
#pragma unroll
  for (int ks = 0; ks < 2; ++ks) a_addr[ks] = lds0 + kq * 8192 + l15 * 128 + (((4 * ks + gq) ^ (l15 >> 1)) * 16);
  f32x4 acc[4][2];
  const f32x4 z4 = {0.f, 0.f, 0.f, 0.f};
#pragma unroll
  for (int tb = 0; tb < 4; ++tb) { acc[tb][0] = z4; acc[tb][1] = z4; }
  bf16x8 af[2][4];
  auto rd = [&](int t, int set) {
    const int s = t >> 1, ks = t & 1, sl = s % NR;
    const unsigned hi = (unsigned)((sl >> 1) * 65536);
#pragma unroll
    for (int tb = 0; tb < 4; ++tb) DSR128(af[set][tb], a_addr[ks] + hi, (sl & 1) * 32768 + tb * 2048);
  };
  auto wt = [&](int set) {
    asm volatile("s_waitcnt lgkmcnt(0)" ::: "memory");
#pragma unroll
    for (int tb = 0; tb < 4; ++tb) PIN(af[set][tb]);
  };
  wait_vm<(NI - 1) * 4>();
  BAR();
  TSTAMP(TS0);
  rd(0, 0);
  wt(0);
  static_for<0, NKS>([&](auto tc) {
    constexpr int t = tc.value, s = t >> 1, ks = t & 1, cur = t & 1, nxt = cur ^ 1;
    if constexpr (ks == 1 && s + 1 < NS) {
      constexpr int iss = (NR + s < NS) ? NR + s : NS;
      wait_vm<(iss - (s + 2)) * 4>();
      BAR();
      TSTAMP(TS0 + 1 + s);
      if constexpr (s + NR < NS) issue(s + NR);
    }
    if constexpr (t + 1 < NKS) rd(t + 1, nxt);
    SCHED();
#pragma unroll
    for (int tb = 0; tb < 4; ++tb) {
      acc[tb][0] = mfma16(__builtin_bit_cast(bf16x8, b[t * 2]), af[cur][tb], acc[tb][0]);
      acc[tb][1] = mfma16(__builtin_bit_cast(bf16x8, b[t * 2 + 1]), af[cur][tb], acc[tb][1]);
    }
    SCHED();
    if constexpr (t + 1 < NKS) wt(nxt);
  });
  BAR();
  TSTAMP(TS0 + 8);
#pragma unroll
  for (int tb = 0; tb < 4; ++tb)
#pragma unroll
    for (int cb = 0; cb < 2; ++cb)
      *(f32x4*)(smem + kq * 16384 + (tb * 16 + l15) * 256 + (((wn * 8 + cb * 4 + gq) ^ l15) * 16)) = acc[tb][cb];
  asm volatile("s_waitcnt lgkmcnt(0)" ::: "memory");
  BAR();
  {
    const int tok = tid >> 3, c8 = tid & 7;
    f32x4 p[4][2];
#pragma unroll
    for (int q = 0; q < 4; ++q)
#pragma unroll
      for (int i = 0; i < 2; ++i)
        p[q][i] = *(const f32x4*)(smem + q * 16384 + tok * 256 + (((2 * c8 + i) ^ (tok & 15)) * 16));
#pragma unroll
    for (int i = 0; i < 2; ++i) {
      const f32x4 v = (p[0][i] + p[1][i]) + (p[2][i] + p[3][i]);
#pragma unroll
      for (int e = 0; e < 4; ++e) r[i * 4 + e] = v[e];
    }
  }
}

#define NR5 4
__global__ __launch_bounds__(512, 2) void kf(const bf16* __restrict__ QKV, bf16* __restrict__ O,
                                             const bf16* __restrict__ x, const bf16* __restrict__ Wof,
                                             bf16* __restrict__ Hh, float* __restrict__ RS,
                                             const bf16* __restrict__ Wt, const float* __restrict__ CS,
                                             bf16* __restrict__ Hm, const bf16* __restrict__ W2f,
                                             bf16* __restrict__ out, unsigned* __restrict__ FL) {
  constexpr int LV = 16384, LONE = 32768;
  extern __shared__ __attribute__((aligned(16))) char smem[];
  TDECL
  TSTAMP(0);
  TRT(1);
  const int tid = threadIdx.x, lane = tid & 63;
  const int wid = __builtin_amdgcn_readfirstlane(tid >> 6);
  const int l15 = lane & 15, gq = lane >> 4;
  const unsigned lds0 = (unsigned)(uintptr_t)(LDSP(char))smem;
  const unsigned l16 = lane * 16;
  const int bx = blockIdx.x & 7, bi = blockIdx.x >> 3;
  const int pm3 = bx * 4 + (bi >> 3), pn3 = bi & 7;
  const int pm4 = (bx & 1) * 8 + (bi & 7), pn4 = (bx >> 1) * 4 + (bi >> 3);
  const bool big = wid < 4;
  const int wm = wid & 3;
  const int pc0 = big ? wm * 4 : 16 + wm * 3;
  const char* baseW = (const char*)Wt + (size_t)pn4 * (8 * 28672) + pc0 * 1024;
  auto wpiece = [&](int kc, int i) {
    glds(l16, baseW + kc * 28672 + i * 1024, lds0 + L_W + (kc % 3) * 28672 + (pc0 + i) * 1024);
  };

  {
    const int xg = blockIdx.x >> 5, h = (blockIdx.x & 7) * 4 + ((blockIdx.x >> 3) & 3);
    const int t0 = xg * 8 - 8;
    const bool ld = xg > 0 || wid >= 4;
    const char* kb = (const char*)QKV + (2u << 20) + (ptrdiff_t)(h * 64 + t0 + 2 * wid) * 1024;
    if (ld) {
      glds(l16, kb, lds0 + wid * 2048);
      glds(l16, kb + 1024, lds0 + wid * 2048 + 1024);
    }
    bf16x8 qf;
    GLD4(qf, l16, (const char*)QKV + (size_t)(h * 64 + xg * 8 + wid) * 1024, 0);
    if (ld) {
      glds(l16, kb + (2u << 20), lds0 + LV + wid * 2048);
      glds(l16, kb + (2u << 20) + 1024, lds0 + LV + wid * 2048 + 1024);
    }
    asm volatile("" : "+s"(O), "+s"(x), "+s"(Wof), "+s"(Hh), "+s"(RS));
    asm volatile("" : "+s"(Wt), "+s"(CS), "+s"(Hm), "+s"(W2f), "+s"(out), "+s"(FL));
    {
      const u32x4 o4 = {0x3f803f80u, 0x3f803f80u, 0x3f803f80u, 0x3f803f80u};
      *(u32x4*)(smem + LONE + tid * 16) = o4;
      if (tid < 128) *(u32x4*)(smem + LONE + 8192 + tid * 16) = o4;
    }
    f32x16 cm0, cm8;
    {
      const int c = lane & 31, hh = lane >> 5;
#pragma unroll
      for (int r = 0; r < 16; ++r) {
        const int kr = (r & 3) + 8 * (r >> 2) + 4 * hh;
        cm0[r] = (kr > c) ? 0.f : -1e30f;
        cm8[r] = (kr <= c) ? 0.f : -1e30f;
      }
      PIN(cm0); PIN(cm8);
    }
    if (ld) asm volatile("s_waitcnt vmcnt(2) lgkmcnt(0)" ::: "memory");
    else asm volatile("s_waitcnt vmcnt(0) lgkmcnt(0)" ::: "memory");
    PIN(qf);
    BAR();
    TSTAMP(2);
    const unsigned kaddr = lds0 + wid * 1024 + l16;
    const unsigned vlane = (unsigned)(((lane >> 5) * 4 + (l15 >> 2)) * 32 + (l15 & 3) * 8);
    const unsigned vaddr = (lane & 16) ? (lds0 + LONE + (vlane ^ 128u)) : (lds0 + LV + wid * 1024 + vlane);
    bf16* dst = O + (size_t)(h * S_ + (xg * 8 + wid) * 32 + (lane & 31)) * 16 + (lane >> 5) * 8;
    auto pre = [&]() {
#pragma unroll
      for (int kc = 0; kc < 3; ++kc) {
        wpiece(kc, 0); wpiece(kc, 1); wpiece(kc, 2);
        if (big) wpiece(kc, 3);
      }
    };
    if (xg > 0) attn_body<true>(kaddr, vaddr, qf, cm0, cm8, wid, dst, pre TPASS);
    else attn_body<false>(kaddr, vaddr, qf, cm0, cm8, wid, dst, pre TPASS);
  }

  const int tok = pm3 * 64 + (tid >> 3), ncol = pn3 * 64 + (tid & 7) * 8;
  u32x4 hold;
  {
    u32x4 b3[8], res;
    float sc[8];
    const char* bw = (const char*)Wof + (size_t)(pn3 * 8 + wid) * 8192;
#pragma unroll
    for (int i = 0; i < 8; ++i) GLD4(b3[i], l16, bw + (i >> 2) * 4096, (i & 3) * 1024);
    GLD4(res, (unsigned)((tok * D_ + ncol) * 2), x, 0);
    const int c = tid < 224 ? tid : 0;
    {
      const unsigned vo = (unsigned)(((c < 112 ? 0 : F_ - 112) + pn4 * 112 + c) * 4);
#pragma unroll
      for (int i = 0; i < 8; ++i) GLD1(sc[i], vo, (const char*)CS + i * (2 * F_ * 4), 0);
    }
    wait_vm<17>();
    BAR();
    TSTAMP(6);
    flag_set(FL + (blockIdx.x >> 5) * FLS + (blockIdx.x & 31), tid);
    flag_wait(FL + bx * FLS, 31, wid, lane);
    TSTAMP(7);
    float r[8];
    gemm64<4, 2, 8>(smem, lds0, tid, wid, (const char*)O + pm3 * 2048, b3, r, [&]() {
      PIN(res);
#pragma unroll
      for (int i = 0; i < 8; ++i) PIN(sc[i]);
      float a = sc[0];
#pragma unroll
      for (int i = 1; i < 8; ++i) a += sc[i];
      if (tid < 224) *(float*)(smem + L_ST + 1024 + tid * 4) = a;
    } TPASS);
    const bf16x8 rr = __builtin_bit_cast(bf16x8, res);
    bf16x8 o;
    float s = 0.f, q = 0.f;
#pragma unroll
    for (int j = 0; j < 8; ++j) {
      o[j] = (bf16)((float)rr[j] + bfr(r[j]));
      const float v = (float)o[j];
      s += v;
      q += v * v;
    }
    hold = __builtin_bit_cast(u32x4, o);
    st16<true>(Hh + (size_t)tok * D_ + ncol, hold);
    s += __shfl_xor(s, 1); s += __shfl_xor(s, 2); s += __shfl_xor(s, 4);
    q += __shfl_xor(q, 1); q += __shfl_xor(q, 2); q += __shfl_xor(q, 4);
    if ((tid & 7) == 0) st8<true>(RS + ((size_t)pn3 * S_ + tok) * 2, __builtin_bit_cast(u32x2, f32x2{s, q}));
    TSTAMP(17);
    wait_vm<0>();
    BAR();
    TSTAMP(18);
    flag_set(FL + (8 + (pm3 >> 1)) * FLS + (pm3 & 1) * 8 + pn3, tid);
    flag_wait(FL + (8 + pm4) * FLS, 15, wid, lane);
    TSTAMP(19);
  }

  u32x4 b5[28];
  {
    constexpr int NK = 8, NT = 16;
    const unsigned voffA = (unsigned)((wid * 8 + (lane >> 3)) * 1024 + (((lane & 7) ^ ((wid & 1) * 4 + gq)) * 16));
    const char* baseA = (const char*)Hh + (size_t)pm4 * (128 * 1024);
    auto piece = [&](int kc, int i) {
      if (i < 2) glds_c(voffA, baseA + kc * 128 + i * 65536, lds0 + (kc % 3) * 16384 + i * 8192 + wid * 1024);
      else if (i < 5 || big) wpiece(kc, i - 2);
    };
    float* st_ = (float*)(smem + L_ST);
    float* csum = st_ + 256;
    if (wid < 2) {
      f32x2 sv[8];
      const char* rb = (const char*)RS + pm4 * 1024;
      const unsigned vo = (unsigned)(tid * 8);
#pragma unroll
      for (int i = 0; i < 8; ++i) GLD2C(sv[i], vo, rb + i * (S_ * 8), 0);
#pragma unroll
      for (int kc = 0; kc < 3; ++kc) { piece(kc, 0); piece(kc, 1); }
      wait_vm<6>();
#pragma unroll
      for (int i = 0; i < 8; ++i) PIN(sv[i]);
      f32x2 a = sv[0];
#pragma unroll
      for (int i = 1; i < 8; ++i) a += sv[i];
      *(f32x2*)(st_ + tid * 2) = ln_stats(a[0], a[1]);
    } else {
#pragma unroll
      for (int kc = 0; kc < 3; ++kc) { piece(kc, 0); piece(kc, 1); }
    }
    unsigned a_addr[2];
#pragma unroll
    for (int ks = 0; ks < 2; ++ks) a_addr[ks] = lds0 + (wm * 32 + l15) * 128 + (((4 * ks + gq) ^ (l15 >> 1)) * 16);
    const unsigned b_addr = lds0 + L_W + (big ? 0 : 4096) + l16;
    const unsigned b_addr2 = b_addr + 2 * 28672;
    f32x4 ag[2][4], au[2][4];
    const f32x4 z4 = {0.f, 0.f, 0.f, 0.f};
#pragma unroll
    for (int j = 0; j < 4; ++j) { ag[0][j] = z4; ag[1][j] = z4; au[0][j] = z4; au[1][j] = z4; }
    bf16x8 af[2][2], bg[2][4], bu[2][4];
    auto rd = [&](int t, int set) {
      const int kc = t >> 1, ks = t & 1, st = kc % 3;
      DSR128(af[set][0], a_addr[ks], st * 16384);
      DSR128(af[set][1], a_addr[ks], st * 16384 + 2048);
      const unsigned ba = st == 2 ? b_addr2 : b_addr;
      const int bo = st == 1 ? 28672 : 0;
#pragma unroll
      for (int j = 0; j < 3; ++j) {
        DSR128(bg[set][j], ba, bo + (ks * 14 + j) * 1024);
        DSR128(bu[set][j], ba, bo + (ks * 14 + 7 + j) * 1024);
      }
      if (big) {
        DSR128(bg[set][3], ba, bo + (ks * 14 + 3) * 1024);
        DSR128(bu[set][3], ba, bo + (ks * 14 + 7 + 3) * 1024);
      }
    };
    auto wt = [&](int set) {
      asm volatile("s_waitcnt lgkmcnt(0)" ::: "memory");
      PIN(af[set][0]); PIN(af[set][1]);
#pragma unroll
      for (int j = 0; j < 4; ++j) { PIN(bg[set][j]); PIN(bu[set][j]); }
    };
    asm volatile("s_waitcnt vmcnt(4) lgkmcnt(0)" ::: "memory");
    BAR();
    TSTAMP(20);
    rd(0, 0);
    wt(0);
#pragma unroll
    for (int t = 0; t < NT; ++t) {
      const int kc = t >> 1, ks = t & 1, cur = t & 1, nxt = cur ^ 1;
      if (ks == 1 && kc + 1 < NK) {
        if (kc == 0) wait_vm<2>();
        else if (kc + 2 < NK) { if (big) wait_vm<6>(); else wait_vm<5>(); }
        else wait_vm<0>();
        BAR();
        TSTAMP(21 + kc);
      }
      if (t + 1 < NT) rd(t + 1, nxt);
      const int ic = (t + 1) / 2 + 2, ip = ((t + 1) & 1) * 3;
      SCHED();
#pragma unroll
      for (int j = 0; j < 3; ++j) {
        ag[0][j] = mfma16(bg[cur][j], af[cur][0], ag[0][j]);
        ag[1][j] = mfma16(bg[cur][j], af[cur][1], ag[1][j]);
        au[0][j] = mfma16(bu[cur][j], af[cur][0], au[0][j]);
        au[1][j] = mfma16(bu[cur][j], af[cur][1], au[1][j]);
        SCHED();
        if (ic >= 3 && ic < NK) piece(ic, ip + j);
        SCHED();
      }
      if (big) {
        ag[0][3] = mfma16(bg[cur][3], af[cur][0], ag[0][3]);
        ag[1][3] = mfma16(bg[cur][3], af[cur][1], ag[1][3]);
        au[0][3] = mfma16(bu[cur][3], af[cur][0], au[0][3]);
        au[1][3] = mfma16(bu[cur][3], af[cur][1], au[1][3]);
      }
      SCHED();
      if (t + 1 < NT) wt(nxt);
    }
    TSTAMP(28);
    const int P0 = big ? 0 : 4;
    bf16* dst0 = Hm + ((size_t)(pn4 * 7 + P0) * S_ + pm4 * 128 + wm * 32 + l15) * 16;
#pragma unroll
    for (int mi = 0; mi < 2; ++mi) {
      const f32x2 ms = *(const f32x2*)(st_ + (wm * 32 + mi * 16 + l15) * 2);
      const f32x2 nm = {-ms[0], -ms[0]};
      const float le = ms[1] * -1.4426950408889634f, iv = 1.0f / (ms[1] * ms[1]);
      const f32x2 le2 = {le, le}, iv2 = {iv, iv};
      unsigned pk[4][2];
      auto blk = [&](int j) {
        const f32x4 cg = *(const f32x4*)(csum + (P0 + j) * 16 + gq * 4);
        const f32x4 cu = *(const f32x4*)(csum + 112 + (P0 + j) * 16 + gq * 4);
#pragma unroll
        for (int hf = 0; hf < 2; ++hf) {
          const f32x2 g = __builtin_elementwise_fma(f32x2{cg[2 * hf], cg[2 * hf + 1]}, nm,
                                                    f32x2{ag[mi][j][2 * hf], ag[mi][j][2 * hf + 1]});
          const f32x2 u = __builtin_elementwise_fma(f32x2{cu[2 * hf], cu[2 * hf + 1]}, nm,
                                                    f32x2{au[mi][j][2 * hf], au[mi][j][2 * hf + 1]});
          const f32x2 ea = g * le2;
          const f32x2 e = {__builtin_amdgcn_exp2f(ea[0]), __builtin_amdgcn_exp2f(ea[1])};
          const f32x2 d = __builtin_elementwise_fma(e, iv2, iv2);
          const f32x2 rc = {__builtin_amdgcn_rcpf(d[0]), __builtin_amdgcn_rcpf(d[1])};
          const f32x2 o = g * u * rc;
          pk[j][hf] = pk2(o[0], o[1]);
        }
      };
      blk(0); blk(1); blk(2);
      swap16(pk[0][0], pk[1][0]);
      swap16(pk[0][1], pk[1][1]);
      st16<true>(dst0 + ((size_t)(gq & 1) * S_ + mi * 16) * 16 + (gq >> 1) * 8, u32x4{pk[0][0], pk[0][1], pk[1][0], pk[1][1]});
      if (big) {
        blk(3);
        swap16(pk[2][0], pk[3][0]);
        swap16(pk[2][1], pk[3][1]);
        st16<true>(dst0 + ((size_t)(2 + (gq & 1)) * S_ + mi * 16) * 16 + (gq >> 1) * 8,
                   u32x4{pk[2][0], pk[2][1], pk[3][0], pk[3][1]});
      } else {
        st8<true>(dst0 + ((size_t)2 * S_ + mi * 16) * 16 + gq * 4, u32x2{pk[2][0], pk[2][1]});
      }
    }
    TSTAMP(29);
    SCHED();
    const char* bw = (const char*)W2f + (size_t)(pn3 * 8 + wid) * 28672;
#pragma unroll
    for (int i = 0; i < 28; ++i) GLD4(b5[i], l16, bw + (i >> 2) * 4096, (i & 3) * 1024);
    wait_vm<28>();
    BAR();
    TSTAMP(30);
    flag_set(FL + (24 + pm4) * FLS + pn4, tid);
    flag_wait(FL + (24 + (pm3 >> 1)) * FLS, 15, wid, lane);
    TSTAMP(31);
  }

  {
    float r[8];
    gemm64<14, NR5, 32>(smem, lds0, tid, wid, (const char*)Hm + pm3 * 2048, b5, r, [&]() {} TPASS);
    const bf16x8 rr = __builtin_bit_cast(bf16x8, hold);
    bf16x8 o;
#pragma unroll
    for (int j = 0; j < 8; ++j) o[j] = (bf16)((float)rr[j] + bfr(r[j]));
    st16<true>(out + (size_t)tok * D_ + ncol, __builtin_bit_cast(u32x4, o));
  }
  TSTAMP(41);
  TFLUSH(1);
}

extern "C" void kernel_launch(void* const* d_in, const int* in_sizes, int n_in, void* d_out, int out_size, void* d_ws,
                              size_t ws_size, hipStream_t stream) {
  const bf16* x = (const bf16*)d_in[0];
  const bf16* wqkv = (const bf16*)d_in[1];
  const bf16* wo = (const bf16*)d_in[2];
  const bf16* w1 = (const bf16*)d_in[4];
  const bf16* w3 = (const bf16*)d_in[5];
  const bf16* w2 = (const bf16*)d_in[6];
  bf16* out = (bf16*)d_out;

  char* ws = (char*)d_ws;
  bf16* QKV = (bf16*)(ws);
  bf16* Oa = (bf16*)(ws + (7u << 20));
  bf16* Hh = (bf16*)(ws + (9u << 20));
  bf16* Hm = (bf16*)(ws + (11u << 20));
  float* RS = (float*)(ws + (18u << 20));
  float* CS = (float*)(ws + (19u << 20));
  bf16* Wt = (bf16*)(ws + (20u << 20));
  bf16* W2f = (bf16*)(ws + (24u << 20));
  bf16* Wof = (bf16*)(ws + (26u << 20));
  unsigned* FL = (unsigned*)(ws + (27u << 20));

  {
    constexpr int lds = 117760 + 32768;
    (void)hipFuncSetAttribute((const void*)k1_qkv, hipFuncAttributeMaxDynamicSharedMemorySize, lds);
    k1_qkv<<<256, 512, lds, stream>>>(x, wqkv, QKV, w1, w3, w2, wo, Wt, CS, W2f, Wof, FL);
  }
  {
    constexpr int lds = 163840;
    (void)hipFuncSetAttribute((const void*)kf, hipFuncAttributeMaxDynamicSharedMemorySize, lds);
    kf<<<256, 512, lds, stream>>>(QKV, Oa, x, Wof, Hh, RS, Wt, CS, Hm, W2f, out, FL);
  }
}
```

```cpp
#include <hip/hip_runtime.h>
#include <stdint.h>

typedef __attribute__((ext_vector_type(8))) __bf16 bf16x8;
typedef __attribute__((ext_vector_type(4))) __bf16 bf16x4;
typedef __attribute__((ext_vector_type(2))) __bf16 bf16x2;
typedef __attribute__((ext_vector_type(2))) float f32x2;
typedef __attribute__((ext_vector_type(4))) float f32x4;
typedef __attribute__((ext_vector_type(16))) float f32x16;
typedef __attribute__((ext_vector_type(4))) unsigned int u32x4;
typedef __attribute__((ext_vector_type(2))) unsigned int u32x2;
typedef __bf16 bf16;

#define LDSP(T) __attribute__((address_space(3))) T*
#define DEV __device__ __forceinline__

#define K1_NST 4
#define G_NST 4
#define G_R 1
#define ROT1 0
#define ROT3 0
#define ROT4 0
#define ROT5 0
#define PF4 1
#define PF3 0
#define PF3T 4
constexpr int S_ = 2048, D_ = 512, H_ = 32, F_ = 1792;

DEV float bfr(float x) { return (float)(bf16)x; }

template <int N>
DEV void wait_vm() {
  asm volatile("s_waitcnt vmcnt(%0)" ::"n"(N) : "memory");
}
DEV void wait_vm_n(int n) {
  switch (n) {
#define WC(X) case X: wait_vm<X>(); break;
    WC(0) WC(1) WC(2) WC(3) WC(4) WC(5) WC(6) WC(7) WC(8) WC(9) WC(10) WC(11) WC(12) WC(13) WC(14) WC(15)
    WC(16) WC(17) WC(18) WC(19) WC(20) WC(21) WC(22) WC(23) WC(24)
#undef WC
    default: wait_vm<0>(); break;
  }
}

DEV void glds(unsigned voff, const void* sbase, unsigned ldsaddr) {
  ldsaddr = __builtin_amdgcn_readfirstlane(ldsaddr);
  asm volatile("s_mov_b32 m0, %2\n\ts_nop 0\n\tglobal_load_lds_dwordx4 %0, %1" ::"v"(voff), "s"(sbase), "s"(ldsaddr)
               : "memory");
}
#define DSR128(dst, addr, off) asm volatile("ds_read_b128 %0, %1 offset:%2" : "=v"(dst) : "v"(addr), "i"(off))
#define DSRTR(dst, addr, off) asm volatile("ds_read_b64_tr_b16 %0, %1 offset:%2" : "=v"(dst) : "v"(addr), "i"(off))
#define PIN(x) asm volatile("" : "+v"(x))
#define GLD4(dst, voff, sbase, off) \
  asm volatile("global_load_dwordx4 %0, %1, %2 offset:%3" : "=v"(dst) : "v"(voff), "s"(sbase), "i"(off))
#define GLD2(dst, voff, sbase, off) \
  asm volatile("global_load_dwordx2 %0, %1, %2 offset:%3" : "=v"(dst) : "v"(voff), "s"(sbase), "i"(off))
#define GLD1(dst, voff, sbase, off) \
  asm volatile("global_load_dword %0, %1, %2 offset:%3" : "=v"(dst) : "v"(voff), "s"(sbase), "i"(off))
#define WT 31
template <bool W>
DEV void st16(void* p, u32x4 v) {
  if constexpr (W) asm volatile("global_store_dwordx4 %0, %1, off sc1\n\ts_nop 1" ::"v"(p), "v"(v) : "memory");
  else *(u32x4*)p = v;
}
template <bool W>
DEV void st8(void* p, u32x2 v) {
  if constexpr (W) asm volatile("global_store_dwordx2 %0, %1, off sc1\n\ts_nop 1" ::"v"(p), "v"(v) : "memory");
  else *(u32x2*)p = v;
}
template <bool W>
DEV void st4(void* p, unsigned v) {
  if constexpr (W) asm volatile("global_store_dword %0, %1, off sc1" ::"v"(p), "v"(v) : "memory");
  else *(unsigned*)p = v;
}
#define BAR() __builtin_amdgcn_s_barrier()
#define SCHED() __builtin_amdgcn_sched_barrier(0)

#define TDECL
#define TSTAMP(m, i) do { } while (0)
#define TFLUSH(m) do { } while (0)
#define TARG
#define TPASS

template <int V> struct IC { static constexpr int value = V; };
template <int I, int N, class F>
DEV void static_for(F&& f) {
  if constexpr (I < N) {
    f(IC<I>{});
    static_for<I + 1, N>(f);
  }
}
template <int P, int KS, int NST, int NK, int PRE, int R>
struct Sched {
  static constexpr int avail(int t) {
    int n = ((t + 1) / KS + NST - 1) * P + ((t + 1) % KS + 1) * (P / KS);
    return n > NK * P ? NK * P : n;
  }
  static constexpr int cum(int t) {
    int n = PRE;
    for (int i = -1; i < t; ++i) { int a = avail(i); n = (n + R < a) ? n + R : a; }
    return n;
  }
};

DEV void swap32(unsigned& a, unsigned& b) {
  const auto r = __builtin_amdgcn_permlane32_swap(a, b, false, false);
  a = r[0]; b = r[1];
}
DEV void swap16(unsigned& a, unsigned& b) {
  const auto r = __builtin_amdgcn_permlane16_swap(a, b, false, false);
  a = r[0]; b = r[1];
}
DEV void swap16f(float& a, float& b) {
  unsigned x = __builtin_bit_cast(unsigned, a), y = __builtin_bit_cast(unsigned, b);
  swap16(x, y);
  a = __builtin_bit_cast(float, x); b = __builtin_bit_cast(float, y);
}
DEV unsigned pk2(float a, float b) { return __builtin_bit_cast(unsigned, bf16x2{(bf16)a, (bf16)b}); }
DEV bf16x8 cat8(bf16x4 a, bf16x4 b) { return __builtin_shufflevector(a, b, 0, 1, 2, 3, 4, 5, 6, 7); }
DEV f32x4 mfma16(bf16x8 a, bf16x8 b, f32x4 c) { return __builtin_amdgcn_mfma_f32_16x16x32_bf16(a, b, c, 0, 0, 0); }
DEV f32x16 mfma32(bf16x8 a, bf16x8 b, f32x16 c) { return __builtin_amdgcn_mfma_f32_32x32x16_bf16(a, b, c, 0, 0, 0); }

DEV f32x2 ln_stats(float sum, float sq) {
  float m = sum * (1.0f / D_);
  float var = fmaxf(sq * (1.0f / D_) - m * m, 0.f);
  float vb = bfr(var);
  float sd = bfr(__builtin_sqrtf(bfr(vb + 1e-5f)));
  f32x2 r;
  r[0] = bfr(m);
  r[1] = 1.0f / sd;
  return r;
}

__global__ __launch_bounds__(512, 2) void k1_qkv(const bf16* __restrict__ x, const bf16* __restrict__ w,
                                                 bf16* __restrict__ QKV) {
  constexpr int NST = K1_NST, STAGE = 28672, NK = 8, NT = 16;
  extern __shared__ __attribute__((aligned(16))) char smem[];
  TDECL
  TSTAMP(0, 0);
  const int tid = threadIdx.x, lane = tid & 63;
  const int wid = __builtin_amdgcn_readfirstlane(tid >> 6);
  const int wm = wid & 3, wn = wid >> 2;
  const int l15 = lane & 15, gq = lane >> 4;
  const int xg = blockIdx.x & 7, idx = blockIdx.x >> 3;
  const int pm = (xg & 3) * 4 + (idx & 3), pn = (xg >> 2) * 8 + (idx >> 2);
  const unsigned lds0 = (unsigned)(uintptr_t)(LDSP(char))smem;
  asm volatile("" : "+s"(QKV));

  const unsigned voffA = (unsigned)((wid * 8 + (lane >> 3)) * 1024 + (((lane & 7) ^ ((wid & 1) * 4 + gq)) * 16));
  const int r12 = (lane * 43) >> 9, c12 = lane - r12 * 12;
  const bool validB = lane < 48;
  const unsigned voffB = (unsigned)((wid * 8 + r12) * 3072 + (((c12 >> 1) ^ (wid & 1)) * 16 + (c12 & 1) * 8) * 2);
  const char* baseA = (const char*)x + (size_t)pm * (128 * 1024);
  const char* baseB = (const char*)w + pn * 192;
  const int rot = ROT1 * ((idx >> 2) + 2 * (idx & 3));
  auto part = [&](int kc, int p) {
    const unsigned sb = lds0 + (kc % NST) * STAGE;
    const int kk = (kc + rot) & 7;
    if (p == 0) {
      glds(voffA, baseA + kk * 128, sb + wid * 1024);
      glds(voffA, baseA + kk * 128 + 65536, sb + 8192 + wid * 1024);
    } else if (validB) {
      glds(voffB, baseB + kk * (64 * 3072), sb + 16384 + wid * 1536);
      glds(voffB, baseB + kk * (64 * 3072) + 4 * 3072, sb + 16384 + wid * 1536 + 768);
    }
  };
  auto issue = [&](int kc) { part(kc, 0); part(kc, 1); };
  issue(0);
  BAR();
  issue(1);
  BAR();
  issue(2);
  TSTAMP(0, 1);

  unsigned a_addr[2][2], b_addr[3];
#pragma unroll
  for (int mi = 0; mi < 2; ++mi)
#pragma unroll
    for (int ks = 0; ks < 2; ++ks)
      a_addr[mi][ks] = lds0 + (wm * 32 + ((mi ^ wn) * 16) + l15) * 128 + (((4 * ks + gq) ^ (l15 >> 1)) * 16);
  {
    const int kl = gq * 8 + (l15 >> 2);
#pragma unroll
    for (int j = 0; j < 3; ++j)
      b_addr[j] = lds0 + 16384 + kl * 192 + (((3 * wn + j) ^ (gq & 1)) * 32) + (l15 & 3) * 8;
  }

  f32x4 acc[2][3], s1, s2, cs[3];
  const f32x4 z4 = {0.f, 0.f, 0.f, 0.f};
#pragma unroll
  for (int j = 0; j < 3; ++j) { acc[0][j] = z4; acc[1][j] = z4; cs[j] = z4; }
  s1 = z4; s2 = z4;
  bf16x8 af[2][2];
  bf16x4 bl[2][3], bh[2][3];
  const bf16 one = (bf16)1.0f;
  const bf16x8 ones = {one, one, one, one, one, one, one, one};

  auto rd = [&](int t, int set) {
    const int kc = t >> 1, ks = t & 1, st = kc % NST;
    const unsigned hi = (unsigned)((st & ~1) * STAGE);
    const int so = (st & 1) * STAGE;
#pragma unroll
    for (int j = 0; j < 3; ++j) {
      DSRTR(bl[set][j], b_addr[j] + hi, so + ks * 6144);
      DSRTR(bh[set][j], b_addr[j] + hi, so + ks * 6144 + 768);
      if (j < 2) DSR128(af[set][j], a_addr[j][ks] + hi, so);
    }
  };
  auto wt = [&](int set) {
    asm volatile("s_waitcnt lgkmcnt(0)" ::: "memory");
    PIN(af[set][0]); PIN(af[set][1]);
#pragma unroll
    for (int j = 0; j < 3; ++j) { PIN(bl[set][j]); PIN(bh[set][j]); }
  };

  using SC = Sched<2, 2, NST, NK, 6, 2>;
  wait_vm<(SC::cum(-1) - 2) * 2>();
  BAR();
  TSTAMP(0, 2);
  rd(0, 0);
  static_for<SC::cum(-1), SC::cum(0)>([&](auto n) { part(n.value >> 1, n.value & 1); });
  wt(0);
  static_for<0, NT>([&](auto tc) {
    constexpr int t = tc.value, kc = t >> 1, ks = t & 1, cur = t & 1, nxt = cur ^ 1;
    if constexpr (ks == 1 && kc + 1 < NK) {
      static_assert(SC::cum(t) >= (kc + 2) * 2, "schedule");
      wait_vm<(SC::cum(t) - (kc + 2) * 2) * 2>();
      BAR();
      TSTAMP(0, 3 + kc);
    }
    if constexpr (t + 1 < NT) rd(t + 1, nxt);
    static_for<SC::cum(t), SC::cum(t + 1)>([&](auto n) { part(n.value >> 1, n.value & 1); });
    SCHED();
    {
      const bf16x8 b0 = cat8(bl[cur][0], bh[cur][0]), b1 = cat8(bl[cur][1], bh[cur][1]),
                   b2 = cat8(bl[cur][2], bh[cur][2]);
#pragma unroll
      for (int mi = 0; mi < 2; ++mi) {
        acc[mi][0] = mfma16(b0, af[cur][mi], acc[mi][0]);
        acc[mi][1] = mfma16(b1, af[cur][mi], acc[mi][1]);
        acc[mi][2] = mfma16(b2, af[cur][mi], acc[mi][2]);
      }
      s1 = mfma16(ones, af[cur][0], s1);
      s2 = mfma16(af[cur][0], af[cur][0], s2);
      if ((t & 3) == wm) {
        cs[0] = mfma16(b0, ones, cs[0]);
        cs[1] = mfma16(b1, ones, cs[1]);
        cs[2] = mfma16(b2, ones, cs[2]);
      }
    }
    SCHED();
    if constexpr (t + 1 < NT) wt(nxt);
  });
  TSTAMP(0, 20);

  float* st_ = (float*)(smem + NST * STAGE);
  float* csum = st_ + 256;
  {
    const float sq = (l15 & 2) ? ((l15 & 1) ? s2[3] : s2[2]) : ((l15 & 1) ? s2[1] : s2[0]);
    const f32x2 ms = ln_stats(s1[0], sq);
    if (gq == (l15 >> 2)) *(f32x2*)(st_ + (wm * 32 + wn * 16 + l15) * 2) = ms;
    if (l15 == 0) {
#pragma unroll
      for (int j = 0; j < 3; ++j) *(f32x4*)(csum + wm * 96 + (3 * wn + j) * 16 + gq * 4) = cs[j];
    }
  }
  __syncthreads();
  TSTAMP(0, 21);
  const int tile = pm * 4 + wm;
  const float qscale = 0.25f * 1.4426950408889634f;
  f32x2 ms[2];
#pragma unroll
  for (int mi = 0; mi < 2; ++mi) ms[mi] = *(const f32x2*)(st_ + (wm * 32 + (mi ^ wn) * 16 + l15) * 2);
  const int loq = (gq >> 1) * 256 + l15 * 8 + (gq & 1) * 4, lov = l15 * 16 + gq * 4;
#pragma unroll
  for (int j = 0; j < 3; ++j) {
    f32x4 c4 = z4;
#pragma unroll
    for (int p = 0; p < 4; ++p) c4 += *(const f32x4*)(csum + p * 96 + (3 * wn + j) * 16 + gq * 4);
    const int blk = pn * 6 + wn * 3 + j;
    const int typ = blk >> 5, h = blk & 31;
    const float sc = typ == 0 ? qscale : 1.0f;
    bf16* db = QKV + ((size_t)typ << 20) + (size_t)(h * 64 + tile) * 512 + (typ == 2 ? lov : loq);
    const int tfs = typ == 2 ? 256 : 128;
#pragma unroll
    for (int mi = 0; mi < 2; ++mi) {
      const float rs = ms[mi][1] * sc;
      bf16x4 o;
#pragma unroll
      for (int r = 0; r < 4; ++r) o[r] = (bf16)((acc[mi][j][r] - ms[mi][0] * c4[r]) * rs);
      st8<(WT & 1) != 0>(db + (mi ^ wn) * tfs, __builtin_bit_cast(u32x2, o));
    }
  }
  TSTAMP(0, 22);
  TFLUSH(0);
}

template <bool FULL, int NPF>
DEV void attn_body(unsigned kaddr, unsigned vaddr, bf16x8 qf, f32x16 cm0, f32x16 cm8, int wid, bf16* dst TARG) {
  const float NEG = -1e30f;
  f32x16 z16;
#pragma unroll
  for (int r = 0; r < 16; ++r) z16[r] = 0.f;
  bf16x8 kf[9];
#pragma unroll
  for (int t = 0; t < 9; ++t)
    if (FULL || wid + t >= 8) DSR128(kf[t], kaddr, t * 1024);
  asm volatile("s_waitcnt lgkmcnt(0)" ::: "memory");
#pragma unroll
  for (int t = 0; t < 9; ++t) PIN(kf[t]);
  TSTAMP(1, 3);
  float m0 = NEG, m1 = NEG;
#pragma unroll
  for (int t = 0; t < 9; ++t)
    if (FULL || wid + t >= 8) {
      const f32x16 s = mfma32(kf[t], qf, t == 0 ? cm0 : (t == 8 ? cm8 : z16));
#pragma unroll
      for (int r = 0; r < 16; r += 4) {
        m0 = fmaxf(fmaxf(m0, s[r]), s[r + 1]);
        m1 = fmaxf(fmaxf(m1, s[r + 2]), s[r + 3]);
      }
    }
  float m = fmaxf(m0, m1);
  m = fmaxf(m, __shfl_xor(m, 32));
  f32x16 nm;
#pragma unroll
  for (int r = 0; r < 16; ++r) { nm[r] = -m; cm0[r] -= m; cm8[r] -= m; }
  TSTAMP(1, 4);
  wait_vm<NPF>();
  BAR();
  TSTAMP(1, 5);
  f32x16 oa = z16, ob = z16;
  f32x16 sc_, sn_;
  bf16x4 v0l[2], v0h[2], v1l[2], v1h[2];
  auto vrd = [&](int t, int b) {
    DSRTR(v0l[b], vaddr, t * 1024);
    DSRTR(v0h[b], vaddr, t * 1024 + 256);
    DSRTR(v1l[b], vaddr, t * 1024 + 512);
    DSRTR(v1h[b], vaddr, t * 1024 + 768);
  };
  if (FULL) { sc_ = mfma32(kf[0], qf, cm0); vrd(0, 0); }
  else sc_ = z16;
  sn_ = z16;
#pragma unroll
  for (int t = 0; t < 9; ++t) {
    const int b = t & 1;
    const bool cur_ok = FULL || wid + t >= 8, nxt_ok = t < 8 && (FULL || wid + t + 1 >= 8);
    if (cur_ok) {
      asm volatile("s_waitcnt lgkmcnt(0)" ::: "memory");
      PIN(v0l[b]); PIN(v0h[b]); PIN(v1l[b]); PIN(v1h[b]);
    }
    if (nxt_ok) {
      vrd(t + 1, b ^ 1);
      sn_ = mfma32(kf[t + 1], qf, t + 1 == 8 ? cm8 : nm);
    }
    SCHED();
    if (cur_ok) {
      bf16x8 p0, p1;
#pragma unroll
      for (int j = 0; j < 8; ++j) p0[j] = (bf16)__builtin_amdgcn_exp2f(sc_[j]);
      SCHED();
      oa = mfma32(cat8(v0l[b], v0h[b]), p0, oa);
      SCHED();
#pragma unroll
      for (int j = 0; j < 8; ++j) p1[j] = (bf16)__builtin_amdgcn_exp2f(sc_[8 + j]);
      SCHED();
      ob = mfma32(cat8(v1l[b], v1h[b]), p1, ob);
      SCHED();
    }
    sc_ = sn_;
  }
  TSTAMP(1, 6);
  const float inv = 1.0f / (oa[8] + ob[8]);
  unsigned a0 = pk2((oa[0] + ob[0]) * inv, (oa[1] + ob[1]) * inv), a1 = pk2((oa[2] + ob[2]) * inv, (oa[3] + ob[3]) * inv);
  unsigned b0 = pk2((oa[4] + ob[4]) * inv, (oa[5] + ob[5]) * inv), b1 = pk2((oa[6] + ob[6]) * inv, (oa[7] + ob[7]) * inv);
  swap32(a0, b0);
  swap32(a1, b1);
  st16<(WT & 2) != 0>(dst, u32x4{a0, a1, b0, b1});
}

__global__ __launch_bounds__(512, 2) void k2_attn(const bf16* __restrict__ QKV, bf16* __restrict__ O,
                                                  const bf16* __restrict__ wo, const bf16* __restrict__ w1,
                                                  const bf16* __restrict__ w3, const bf16* __restrict__ w2,
                                                  bf16* __restrict__ Wt, float* __restrict__ CS,
                                                  bf16* __restrict__ W2t, bf16* __restrict__ Wot) {
  constexpr int LV = 16384, LONE = 32768, LSJ = 43008;
  extern __shared__ __attribute__((aligned(16))) char smem[];
  TDECL
  TSTAMP(1, 0);
  const int tid = threadIdx.x, lane = tid & 63;
  const int wid = __builtin_amdgcn_readfirstlane(tid >> 6);
  const int l15 = lane & 15, gq = lane >> 4;
  const int xg = blockIdx.x >> 5, h = (blockIdx.x & 7) * 4 + ((blockIdx.x >> 3) & 3);
  const unsigned lds0 = (unsigned)(uintptr_t)(LDSP(char))smem;
  const int t0 = xg * 8 - 8;
  const unsigned l16 = lane * 16;
  asm volatile("" : "+s"(O), "+s"(Wt), "+s"(CS), "+s"(W2t), "+s"(Wot));
  asm volatile("" : "+s"(wo), "+s"(w1), "+s"(w3), "+s"(w2));
  const bool ld = xg > 0 || wid >= 4;

  const char* kb = (const char*)QKV + (2u << 20) + (ptrdiff_t)(h * 64 + t0 + 2 * wid) * 1024;
  if (ld) {
    glds(l16, kb, lds0 + wid * 2048);
    glds(l16, kb + 1024, lds0 + wid * 2048 + 1024);
  }
  bf16x8 qf;
  GLD4(qf, l16, (const char*)QKV + (size_t)(h * 64 + xg * 8 + wid) * 1024, 0);
  if (ld) {
    glds(l16, kb + (2u << 20), lds0 + LV + wid * 2048);
    glds(l16, kb + (2u << 20) + 1024, lds0 + LV + wid * 2048 + 1024);
  }
  {
    const u32x4 o4 = {0x3f803f80u, 0x3f803f80u, 0x3f803f80u, 0x3f803f80u};
    *(u32x4*)(smem + LONE + tid * 16) = o4;
    if (tid < 128) *(u32x4*)(smem + LONE + 8192 + tid * 16) = o4;
  }
  f32x16 cm0, cm8;
  {
    const int c = lane & 31, hh = lane >> 5;
#pragma unroll
    for (int r = 0; r < 16; ++r) {
      const int kr = (r & 3) + 8 * (r >> 2) + 4 * hh;
      cm0[r] = (kr > c) ? 0.f : -1e30f;
      cm8[r] = (kr <= c) ? 0.f : -1e30f;
    }
    PIN(cm0); PIN(cm8);
  }
  TSTAMP(1, 1);
  if (ld) asm volatile("s_waitcnt vmcnt(2) lgkmcnt(0)" ::: "memory");
  else asm volatile("s_waitcnt vmcnt(0) lgkmcnt(0)" ::: "memory");
  PIN(qf);
  BAR();
  TSTAMP(1, 2);
  const int skc = blockIdx.x >> 5, smt = (blockIdx.x >> 4) & 1, spn = blockIdx.x & 15;
  u32x4 wr0, wr1;
  {
    const int wj = wid < 7 ? wid : 6;
    const char* sb = (const char*)(smt ? w3 : w1) + (size_t)(skc * 64) * (F_ * 2) + (spn * 112 + wj * 16) * 2;
    const unsigned vo = (unsigned)((lane >> 1) * (F_ * 2) + (lane & 1) * 16);
    glds(vo, sb, lds0 + LSJ + wid * 2048);
    glds(vo, sb + 32 * (F_ * 2), lds0 + LSJ + wid * 2048 + 1024);
    const char* o = (const char*)wo + (size_t)blockIdx.x * 2048;
    const char* r0 = wid < 7 ? (const char*)w2 + (size_t)blockIdx.x * 7168 + wid * 1024 : o;
    GLD4(wr0, l16, r0, 0);
    GLD4(wr1, l16, o, 1024);
  }

  const unsigned kaddr = lds0 + wid * 1024 + l16;
  const unsigned vlane = (unsigned)(((lane >> 5) * 4 + (l15 >> 2)) * 32 + (l15 & 3) * 8);
  const unsigned vaddr = (lane & 16) ? (lds0 + LONE + (vlane ^ 128u)) : (lds0 + LV + wid * 1024 + vlane);
  bf16* dst = O + (size_t)(((xg * 4 + (wid >> 1)) * H_ + h) * 64 + (wid & 1) * 32 + (lane & 31)) * 16 + (lane >> 5) * 8;
  if (xg > 0) attn_body<true, 4>(kaddr, vaddr, qf, cm0, cm8, wid, dst TPASS);
  else attn_body<false, 4>(kaddr, vaddr, qf, cm0, cm8, wid, dst TPASS);
  TSTAMP(1, 20);
  wait_vm<1>();
  {
    const unsigned co = (unsigned)(lane & 7) * 16;
    PIN(wr0); PIN(wr1);
    if (wid < 7) {
      st16<(WT & 2) != 0>((char*)W2t + (size_t)(lane >> 3) * (F_ * 128) + (blockIdx.x * 7 + wid) * 128 + co, wr0);
    } else {
      char* o = (char*)Wot + (size_t)(lane >> 3) * (D_ * 128) + blockIdx.x * 256 + co;
      st16<(WT & 2) != 0>(o, wr0);
      st16<(WT & 2) != 0>(o + 128, wr1);
    }
  }
  if (wid < 7) {
    const unsigned ra = lds0 + LSJ + wid * 2048 + (gq * 8 + (l15 >> 2)) * 32 + (l15 & 3) * 8;
    bf16x4 f[4];
    DSRTR(f[0], ra, 0);
    DSRTR(f[1], ra, 128);
    DSRTR(f[2], ra, 1024);
    DSRTR(f[3], ra, 1024 + 128);
    asm volatile("s_waitcnt lgkmcnt(0)" ::: "memory");
    float c = 0.f;
#pragma unroll
    for (int i = 0; i < 4; ++i) {
      PIN(f[i]);
#pragma unroll
      for (int e = 0; e < 4; ++e) c += (float)f[i][e];
    }
    char* wd = (char*)Wt + (size_t)(((spn * 8 + skc) * 4 + smt) * 7 + wid) * 1024 + l16;
    st16<(WT & 2) != 0>(wd, __builtin_bit_cast(u32x4, cat8(f[0], f[1])));
    st16<(WT & 2) != 0>(wd + 14 * 1024, __builtin_bit_cast(u32x4, cat8(f[2], f[3])));
    c += __shfl_xor(c, 16);
    c += __shfl_xor(c, 32);
    if (lane < 16) st4<(WT & 2) != 0>(CS + skc * (2 * F_) + smt * F_ + spn * 112 + wid * 16 + lane, __builtin_bit_cast(unsigned, c));
  }
  TSTAMP(1, 22);
  wait_vm<0>();
  TFLUSH(1);
}

template <int MODE, int KTOT>
__global__ __launch_bounds__(512, 2) void k35_gemm(const bf16* __restrict__ A, const bf16* __restrict__ B,
                                                   const bf16* __restrict__ R, bf16* __restrict__ O,
                                                   float* __restrict__ RS, const bf16* __restrict__ PFW) {
  constexpr int NST = G_NST, STAGE = 32768, NK = KTOT / 128, NT = NK * 4;
  extern __shared__ __attribute__((aligned(16))) char smem[];
  TDECL
  TSTAMP(MODE == 0 ? 2 : 4, 0);
  const int tid = threadIdx.x, lane = tid & 63;
  const int wid = __builtin_amdgcn_readfirstlane(tid >> 6);
  const int wm = wid & 3, wn = wid >> 2;
  const int l15 = lane & 15, gq = lane >> 4;
  const int xg = blockIdx.x & 7, idx = blockIdx.x >> 3;
#define PNX 0
  const int pm = xg * 4 + (idx >> 3), pn = (idx & 7) ^ PNX;
  if (MODE == 0) asm volatile("" : "+s"(RS), "+s"(PFW));
  const unsigned lds0 = (unsigned)(uintptr_t)(LDSP(char))smem;

  u32x4 res;
  const int tok = pm * 64 + wm * 16 + l15;
  const int ncol = pn * 64 + wn * 32 + (gq & 1) * 16 + (gq >> 1) * 8;
  const unsigned hoff = (unsigned)(((((tok >> 7) * 8 + pn) * 128 + (tok & 127)) * 64 + (ncol & 63)) * 2);
  GLD4(res, MODE == 0 ? (unsigned)((tok * D_ + ncol) * 2) : hoff, R, 0);

  const unsigned voffA = (unsigned)(wid * 1024 + lane * 16);
  const int pB = ((lane & 7) >> 1) ^ (((lane >> 4) & 1) | ((wid & 1) << 1));
  const unsigned voffB = (unsigned)((wid * 8 + (lane >> 3)) * 128 + pB * 32 + (lane & 1) * 16);
  const char* baseA = (const char*)A + (size_t)pm * (KTOT * 128);
  const char* baseB = (const char*)B + pn * (KTOT * 128);
  const unsigned sbw = lds0 + wid * 1024;
  const int rot = MODE == 0 ? ROT3 * ((pn + (idx >> 3)) & 3) : ROT5 * ((pn * 2 + (idx >> 3) * 3) % NK);
  auto part = [&](int kc, int p) {
    const unsigned sb = sbw + (kc % NST) * STAGE;
    int kk = kc + rot;
    if (kk >= NK) kk -= NK;
    if (p == 0) glds(voffA, baseA + (size_t)kk * 16384, sb);
    if (p == 1) glds(voffA, baseA + (size_t)kk * 16384 + 8192, sb + 8192);
    if (p == 2) glds(voffB, baseB + (size_t)kk * 16384, sb + 16384);
    if (p == 3) glds(voffB, baseB + (size_t)kk * 16384 + 8192, sb + 16384 + 8192);
  };
  auto issue = [&](int kc) { part(kc, 0); part(kc, 1); part(kc, 2); part(kc, 3); };
  issue(0);
  BAR();
  issue(1);
  BAR();
  issue(2);
  TSTAMP(MODE == 0 ? 2 : 4, 1);

  unsigned a_addr[4], b_addr[2];
#pragma unroll
  for (int ks = 0; ks < 4; ++ks) a_addr[ks] = lds0 + (2 * ks + (gq >> 1)) * 2048 + (wm * 16 + l15) * 32 + (gq & 1) * 16;
  {
    const int kl = gq * 8 + (l15 >> 2);
    const int fr = ((l15 >> 3) & 1) | ((gq & 1) << 1);
#pragma unroll
    for (int j = 0; j < 2; ++j) b_addr[j] = lds0 + 16384 + kl * 128 + (((2 * wn + j) ^ fr) * 32) + (l15 & 3) * 8;
  }

  f32x4 acc[2];
  const f32x4 z4 = {0.f, 0.f, 0.f, 0.f};
  acc[0] = z4; acc[1] = z4;
  bf16x8 af[2];
  bf16x4 bl[2][2], bh[2][2];

  auto rd = [&](int t, int set) {
    const int kc = t >> 2, ks = t & 3, st = kc % NST;
    const unsigned hi = (unsigned)((st & ~1) * STAGE);
    const int so = (st & 1) * STAGE;
    DSRTR(bl[set][0], b_addr[0] + hi, so + ks * 4096);
    DSRTR(bh[set][0], b_addr[0] + hi, so + ks * 4096 + 512);
    DSR128(af[set], a_addr[ks] + hi, so);
    DSRTR(bl[set][1], b_addr[1] + hi, so + ks * 4096);
    DSRTR(bh[set][1], b_addr[1] + hi, so + ks * 4096 + 512);
  };
  auto wt = [&](int set) {
    asm volatile("s_waitcnt lgkmcnt(0)" ::: "memory");
    PIN(af[set]);
    PIN(bl[set][0]); PIN(bh[set][0]); PIN(bl[set][1]); PIN(bh[set][1]);
  };

  using SC = Sched<4, 4, NST, NK, 12, G_R>;
  wait_vm<SC::cum(-1) - 4>();
  BAR();
  TSTAMP(MODE == 0 ? 2 : 4, 2);
  rd(0, 0);
  static_for<SC::cum(-1), SC::cum(0)>([&](auto n) { part(n.value >> 2, n.value & 3); });
  wt(0);
  static_for<0, NT>([&](auto tc) {
    constexpr int t = tc.value, kc = t >> 2, ks = t & 3, cur = t & 1, nxt = cur ^ 1;
    if constexpr (ks == 3 && kc + 1 < NK) {
      static_assert(SC::cum(t) >= (kc + 2) * 4, "schedule");
      constexpr int npf = (MODE == 0 && PF3 && t > PF3T) ? (t - PF3T > 4 ? 4 : t - PF3T) : 0;
      static_assert(npf == 0 || SC::cum(PF3T) == NK * 4, "warm-up loads come after all operand loads");
      wait_vm<SC::cum(t) - (kc + 2) * 4 + npf>();
      BAR();
      TSTAMP(MODE == 0 ? 2 : 4, 3 + kc);
    }
    if constexpr (t + 1 < NT) rd(t + 1, nxt);
    static_for<SC::cum(t), SC::cum(t + 1)>([&](auto n) { part(n.value >> 2, n.value & 3); });
    if constexpr (MODE == 0 && PF3 && t >= PF3T && t < PF3T + 4) {
      glds((unsigned)(lane * 16),
           (const char*)PFW + (size_t)(xg >> 1) * (4 * 229376) + idx * 28672 + (t - PF3T) * 8192 + (t - PF3T < 3 ? wid : (wid & 3)) * 1024,
           lds0 + NST * STAGE + wid * 1024);
    }
    SCHED();
    acc[0] = mfma16(cat8(bl[cur][0], bh[cur][0]), af[cur], acc[0]);
    acc[1] = mfma16(cat8(bl[cur][1], bh[cur][1]), af[cur], acc[1]);
    SCHED();
    if constexpr (t + 1 < NT) wt(nxt);
  });
  TSTAMP(MODE == 0 ? 2 : 4, 20);
  PIN(res);
  float s = 0.f, q = 0.f;
  {
#pragma unroll
    for (int r = 0; r < 4; ++r) { float a = acc[0][r], b = acc[1][r]; swap16f(a, b); acc[0][r] = a; acc[1][r] = b; }
    const bf16x8 rr = __builtin_bit_cast(bf16x8, res);
    bf16x8 o;
#pragma unroll
    for (int j = 0; j < 2; ++j)
#pragma unroll
      for (int r = 0; r < 4; ++r) {
        o[j * 4 + r] = (bf16)((float)rr[j * 4 + r] + bfr(acc[j][r]));
        const float v = (float)o[j * 4 + r];
        s += v;
        q += v * v;
      }
    st16<(WT & (MODE == 0 ? 4 : 16)) != 0>((char*)O + (MODE == 0 ? hoff : (unsigned)((tok * D_ + ncol) * 2)), __builtin_bit_cast(u32x4, o));
  }
  if (MODE == 0) {
    s += __shfl_xor(s, 16); s += __shfl_xor(s, 32);
    q += __shfl_xor(q, 16); q += __shfl_xor(q, 32);
    if (gq == 0) st8<(WT & 4) != 0>(RS + ((size_t)(pn * 2 + wn) * S_ + tok) * 2, __builtin_bit_cast(u32x2, f32x2{s, q}));
  }
  if (MODE == 0) wait_vm<0>();
  TSTAMP(MODE == 0 ? 2 : 4, 22);
  TFLUSH(MODE == 0 ? 2 : 4);
}

template <int NP, int P0>
DEV void gu_body(char* smem, unsigned lds0, int tid, int wid, int pm, int pn, const bf16* __restrict__ Hh,
                 const bf16* __restrict__ Wt, const float* __restrict__ RS, const float* __restrict__ CS,
                 bf16* __restrict__ Hm, const char* pf TARG) {
  constexpr int NST = 3, STAGE = 45056, NK = 8, LPC = 2 + NP, NT = 16;
  const int lane = tid & 63, wm = wid & 3;
  const int l15 = lane & 15, gq = lane >> 4;
  const unsigned l16 = lane * 16;
  const unsigned voffA = (unsigned)((wid * 8 + (lane >> 3)) * 128 + (((lane & 7) ^ ((wid & 1) * 4 + gq)) * 16));
  const char* baseA = (const char*)Hh + (size_t)pm * (128 * 1024);
  const int pc0 = (P0 == 0 ? 0 : 16) + wm * NP;
  const char* baseW = (const char*)Wt + (size_t)pn * (8 * 28672) + pc0 * 1024;
  const int rot = ROT4 * (pm + 2 * pn);
  auto piece = [&](int kc, int i) {
    const unsigned sb = lds0 + (kc % NST) * STAGE;
    const int kk = (kc + rot) & 7;
    if (i == 0) glds(voffA, baseA + kk * 16384, sb + wid * 1024);
    else if (i == 1) glds(voffA, baseA + kk * 16384 + 8192, sb + 8192 + wid * 1024);
    else glds(l16, baseW + kk * 28672 + (i - 2) * 1024, sb + 16384 + (pc0 + i - 2) * 1024);
  };
  auto issue = [&](int kc) {
#pragma unroll
    for (int i = 0; i < LPC; ++i) piece(kc, i);
  };
  float* st_ = (float*)(smem + NST * STAGE);
  float* csum = st_ + 256;
  if (P0 == 0) {
    if (wid < 2) {
      f32x2 sv[16];
      const char* rb = (const char*)RS + pm * 1024;
      const unsigned vo = (unsigned)(tid * 8);
#pragma unroll
      for (int i = 0; i < 16; ++i) GLD2(sv[i], vo, rb + i * (S_ * 8), 0);
      issue(0);
      BAR();
      issue(1);
      wait_vm<2 * LPC>();
#pragma unroll
      for (int i = 0; i < 16; ++i) PIN(sv[i]);
      f32x2 a = sv[0];
#pragma unroll
      for (int i = 1; i < 16; ++i) a += sv[i];
      *(f32x2*)(st_ + tid * 2) = ln_stats(a[0], a[1]);
    } else {
      issue(0);
      BAR();
      issue(1);
    }
  } else {
    float sc[8];
    int c = tid - 256;
    const bool ok = c < 224;
    if (!ok) c = 0;
    const unsigned vo = (unsigned)(((c < 112 ? 0 : F_ - 112) + pn * 112 + c) * 4);
#pragma unroll
    for (int i = 0; i < 8; ++i) GLD1(sc[i], vo, (const char*)CS + i * (2 * F_ * 4), 0);
    issue(0);
    BAR();
    issue(1);
    wait_vm<2 * LPC>();
#pragma unroll
    for (int i = 0; i < 8; ++i) PIN(sc[i]);
    float a = sc[0];
#pragma unroll
    for (int i = 1; i < 8; ++i) a += sc[i];
    if (ok) csum[c] = a;
  }
  TSTAMP(3, 1);

  unsigned a_addr[2];
#pragma unroll
  for (int ks = 0; ks < 2; ++ks) a_addr[ks] = lds0 + (wm * 32 + l15) * 128 + (((4 * ks + gq) ^ (l15 >> 1)) * 16);
  const unsigned b_addr = lds0 + 16384 + P0 * 1024 + l16;
  f32x4 ag[2][NP], au[2][NP];
  const f32x4 z4 = {0.f, 0.f, 0.f, 0.f};
#pragma unroll
  for (int j = 0; j < NP; ++j) { ag[0][j] = z4; ag[1][j] = z4; au[0][j] = z4; au[1][j] = z4; }
  bf16x8 af[2][2], bg[2][NP], bu[2][NP];
  auto rd = [&](int t, int set) {
    const int kc = t >> 1, ks = t & 1, st = kc % NST;
    const unsigned so = st * STAGE;
    DSR128(af[set][0], a_addr[ks] + so, 0);
    DSR128(af[set][1], a_addr[ks] + so, 2048);
#pragma unroll
    for (int j = 0; j < NP; ++j) {
      DSR128(bg[set][j], b_addr + so, (ks * 14 + j) * 1024);
      DSR128(bu[set][j], b_addr + so, (ks * 14 + 7 + j) * 1024);
    }
  };
  auto wt = [&](int set) {
    asm volatile("s_waitcnt lgkmcnt(0)" ::: "memory");
    PIN(af[set][0]); PIN(af[set][1]);
#pragma unroll
    for (int j = 0; j < NP; ++j) { PIN(bg[set][j]); PIN(bu[set][j]); }
  };
  asm volatile("s_waitcnt vmcnt(%0) lgkmcnt(0)" ::"n"(LPC) : "memory");
  BAR();
  TSTAMP(3, 2);
  rd(0, 0);
  piece(NST - 1, 0); piece(NST - 1, 1); piece(NST - 1, 2);
  wt(0);
#pragma unroll
  for (int t = 0; t < NT; ++t) {
    const int kc = t >> 1, ks = t & 1, cur = t & 1, nxt = cur ^ 1;
    if (ks == 1 && kc + 1 < NK) {
      const int last = (kc + NST - 1 < NK - 1) ? (kc + NST - 1) : (NK - 1);
      wait_vm_n((last - (kc + 1)) * LPC + ((PF4 && t > 11) ? (t - 11) * 2 : 0));
      BAR();
      TSTAMP(3, 3 + kc);
    }
    if (t + 1 < NT) rd(t + 1, nxt);
    const int ic = (t + 1) / 2 + NST - 1, ip = ((t + 1) & 1) * 3;
    SCHED();
#pragma unroll
    for (int j = 0; j < NP; ++j) {
      ag[0][j] = mfma16(bg[cur][j], af[cur][0], ag[0][j]);
      ag[1][j] = mfma16(bg[cur][j], af[cur][1], ag[1][j]);
      au[0][j] = mfma16(bu[cur][j], af[cur][0], au[0][j]);
      au[1][j] = mfma16(bu[cur][j], af[cur][1], au[1][j]);
      SCHED();
      if (j < 3 && ic < NK && ip + j < LPC) piece(ic, ip + j);
      if (PF4 && j < 2 && t >= 11 && (t - 11) * 2 + j < 7)
        glds(l16, pf + ((t - 11) * 2 + j) * 8192, lds0 + NST * STAGE + 2048 + wid * 1024);
      SCHED();
    }
    if (t + 1 < NT) wt(nxt);
  }
  TSTAMP(3, 20);
  bf16* dst0 = Hm + ((size_t)((pm * 2 + (wm >> 1)) * 112 + pn * 7 + P0) * 64 + (wm & 1) * 32 + l15) * 16;
#pragma unroll
  for (int mi = 0; mi < 2; ++mi) {
    const f32x2 ms = *(const f32x2*)(st_ + (wm * 32 + mi * 16 + l15) * 2);
    const f32x2 nm = {-ms[0], -ms[0]};
    const float le = ms[1] * -1.4426950408889634f, iv = 1.0f / (ms[1] * ms[1]);
    const f32x2 le2 = {le, le}, iv2 = {iv, iv};
    unsigned pk[NP][2];
#pragma unroll
    for (int j = 0; j < NP; ++j) {
      const f32x4 cg = *(const f32x4*)(csum + (P0 + j) * 16 + gq * 4);
      const f32x4 cu = *(const f32x4*)(csum + 112 + (P0 + j) * 16 + gq * 4);
#pragma unroll
      for (int hf = 0; hf < 2; ++hf) {
        const f32x2 g = __builtin_elementwise_fma(f32x2{cg[2 * hf], cg[2 * hf + 1]}, nm,
                                                  f32x2{ag[mi][j][2 * hf], ag[mi][j][2 * hf + 1]});
        const f32x2 u = __builtin_elementwise_fma(f32x2{cu[2 * hf], cu[2 * hf + 1]}, nm,
                                                  f32x2{au[mi][j][2 * hf], au[mi][j][2 * hf + 1]});
        const f32x2 ea = g * le2;
        const f32x2 e = {__builtin_amdgcn_exp2f(ea[0]), __builtin_amdgcn_exp2f(ea[1])};
        const f32x2 d = __builtin_elementwise_fma(e, iv2, iv2);
        const f32x2 rc = {__builtin_amdgcn_rcpf(d[0]), __builtin_amdgcn_rcpf(d[1])};
        const f32x2 o = g * u * rc;
        pk[j][hf] = pk2(o[0], o[1]);
      }
    }
#pragma unroll
    for (int j = 0; j + 1 < NP; j += 2) {
      swap16(pk[j][0], pk[j + 1][0]);
      swap16(pk[j][1], pk[j + 1][1]);
      st16<(WT & 8) != 0>(dst0 + ((size_t)(j + (gq & 1)) * 64 + mi * 16) * 16 + (gq >> 1) * 8,
                          u32x4{pk[j][0], pk[j][1], pk[j + 1][0], pk[j + 1][1]});
    }
    if (NP & 1) st8<(WT & 8) != 0>(dst0 + ((size_t)(NP - 1) * 64 + mi * 16) * 16 + gq * 4, u32x2{pk[NP - 1][0], pk[NP - 1][1]});
  }
}

__global__ __launch_bounds__(512, 2) void k4_gu(const bf16* __restrict__ Hh, const bf16* __restrict__ Wt,
                                                const float* __restrict__ RS, const float* __restrict__ CS,
                                                bf16* __restrict__ Hm, const bf16* __restrict__ W2t) {
  extern __shared__ __attribute__((aligned(16))) char smem[];
  TDECL
  TSTAMP(3, 0);
  const int tid = threadIdx.x;
  const int wid = __builtin_amdgcn_readfirstlane(tid >> 6);
  const int xg = blockIdx.x & 7, idx = blockIdx.x >> 3;
  const int pm = (xg & 1) * 8 + (idx & 7), pn = (xg >> 1) * 4 + (idx >> 3);
  asm volatile("" : "+s"(RS), "+s"(CS), "+s"(Hm), "+s"(W2t));
  const unsigned lds0 = (unsigned)(uintptr_t)(LDSP(char))smem;
  const char* pf = (const char*)W2t + idx * 57344 + wid * 1024;
  if (wid < 4) gu_body<4, 0>(smem, lds0, tid, wid, pm, pn, Hh, Wt, RS, CS, Hm, pf TPASS);
  else gu_body<3, 4>(smem, lds0, tid, wid, pm, pn, Hh, Wt, RS, CS, Hm, pf TPASS);
  wait_vm<0>();
  TSTAMP(3, 22);
  TFLUSH(3);
}

extern "C" void kernel_launch(void* const* d_in, const int* in_sizes, int n_in, void* d_out, int out_size, void* d_ws,
                              size_t ws_size, hipStream_t stream) {
  const bf16* x = (const bf16*)d_in[0];
  const bf16* wqkv = (const bf16*)d_in[1];
  const bf16* wo = (const bf16*)d_in[2];
  const bf16* w1 = (const bf16*)d_in[4];
  const bf16* w3 = (const bf16*)d_in[5];
  const bf16* w2 = (const bf16*)d_in[6];
  bf16* out = (bf16*)d_out;

  char* ws = (char*)d_ws;
  bf16* QKV = (bf16*)(ws);
  bf16* Oa = (bf16*)(ws + (7u << 20));
  bf16* Hh = (bf16*)(ws + (9u << 20));
  bf16* Hm = (bf16*)(ws + (11u << 20));
  float* RS = (float*)(ws + (18u << 20));
  float* CS = (float*)(ws + (19u << 20));
  bf16* Wt = (bf16*)(ws + (20u << 20));
  bf16* W2t = (bf16*)(ws + (24u << 20));
  bf16* Wot = (bf16*)(ws + (26u << 20));

  {
    constexpr int lds = K1_NST * 28672 + 1024 + 4 * 96 * 4;
    (void)hipFuncSetAttribute((const void*)k1_qkv, hipFuncAttributeMaxDynamicSharedMemorySize, lds);
    k1_qkv<<<256, 512, lds, stream>>>(x, wqkv, QKV);
  }
  {
    constexpr int lds = 59392;
    (void)hipFuncSetAttribute((const void*)k2_attn, hipFuncAttributeMaxDynamicSharedMemorySize, lds);
    k2_attn<<<256, 512, lds, stream>>>(QKV, Oa, wo, w1, w3, w2, Wt, CS, W2t, Wot);
  }
  {
    auto k = k35_gemm<0, 512>;
    constexpr int lds = G_NST * 32768 + 8192;
    (void)hipFuncSetAttribute((const void*)k, hipFuncAttributeMaxDynamicSharedMemorySize, lds);
    k<<<256, 512, lds, stream>>>(Oa, Wot, x, Hh, RS, Wt);
  }
  {
    constexpr int lds = 3 * 45056 + 2048 + 8192;
    (void)hipFuncSetAttribute((const void*)k4_gu, hipFuncAttributeMaxDynamicSharedMemorySize, lds);
    k4_gu<<<256, 512, lds, stream>>>(Hh, Wt, RS, CS, Hm, W2t);
  }
  {
    auto k = k35_gemm<1, 1792>;
    constexpr int lds = G_NST * 32768;
    (void)hipFuncSetAttribute((const void*)k, hipFuncAttributeMaxDynamicSharedMemorySize, lds);
    k<<<256, 512, lds, stream>>>(Hm, W2t, Hh, out, nullptr, nullptr);
  }
}
```

```cpp
#include <hip/hip_runtime.h>
#include <stdint.h>

typedef __attribute__((ext_vector_type(8))) __bf16 bf16x8;
typedef __attribute__((ext_vector_type(4))) __bf16 bf16x4;
typedef __attribute__((ext_vector_type(2))) __bf16 bf16x2;
typedef __attribute__((ext_vector_type(2))) float f32x2;
typedef __attribute__((ext_vector_type(4))) float f32x4;
typedef __attribute__((ext_vector_type(16))) float f32x16;
typedef __attribute__((ext_vector_type(4))) unsigned int u32x4;
typedef __attribute__((ext_vector_type(2))) unsigned int u32x2;
typedef __bf16 bf16;

#define LDSP(T) __attribute__((address_space(3))) T*
#define DEV __device__ __forceinline__

#define K1_NST 4
#define G_NST 4
#define G_R 1
#define ROT1 0
#define ROT3 0
#define ROT4 0
#define ROT5 0
#define PF4 1
#define PF3 0
#define PF3T 4
constexpr int S_ = 2048, D_ = 512, H_ = 32, F_ = 1792;

DEV float bfr(float x) { return (float)(bf16)x; }

template <int N>
DEV void wait_vm() {
  asm volatile("s_waitcnt vmcnt(%0)" ::"n"(N) : "memory");
}
DEV void wait_vm_n(int n) {
  switch (n) {
#define WC(X) case X: wait_vm<X>(); break;
    WC(0) WC(1) WC(2) WC(3) WC(4) WC(5) WC(6) WC(7) WC(8) WC(9) WC(10) WC(11) WC(12) WC(13) WC(14) WC(15)
    WC(16) WC(17) WC(18) WC(19) WC(20) WC(21) WC(22) WC(23) WC(24)
#undef WC
    default: wait_vm<0>(); break;
  }
}

DEV void glds(unsigned voff, const void* sbase, unsigned ldsaddr) {
  ldsaddr = __builtin_amdgcn_readfirstlane(ldsaddr);
  asm volatile("s_mov_b32 m0, %2\n\ts_nop 0\n\tglobal_load_lds_dwordx4 %0, %1" ::"v"(voff), "s"(sbase), "s"(ldsaddr)
               : "memory");
}
#define DSR128(dst, addr, off) asm volatile("ds_read_b128 %0, %1 offset:%2" : "=v"(dst) : "v"(addr), "i"(off))
#define DSRTR(dst, addr, off) asm volatile("ds_read_b64_tr_b16 %0, %1 offset:%2" : "=v"(dst) : "v"(addr), "i"(off))
#define PIN(x) asm volatile("" : "+v"(x))
#define GLD4(dst, voff, sbase, off) \
  asm volatile("global_load_dwordx4 %0, %1, %2 offset:%3" : "=v"(dst) : "v"(voff), "s"(sbase), "i"(off))
#define GLD2(dst, voff, sbase, off) \
  asm volatile("global_load_dwordx2 %0, %1, %2 offset:%3" : "=v"(dst) : "v"(voff), "s"(sbase), "i"(off))
#define GLD1(dst, voff, sbase, off) \
  asm volatile("global_load_dword %0, %1, %2 offset:%3" : "=v"(dst) : "v"(voff), "s"(sbase), "i"(off))
#define WT 31
template <bool W>
DEV void st16(void* p, u32x4 v) {
  if constexpr (W) asm volatile("global_store_dwordx4 %0, %1, off sc1\n\ts_nop 1" ::"v"(p), "v"(v) : "memory");
  else *(u32x4*)p = v;
}
template <bool W>
DEV void st8(void* p, u32x2 v) {
  if constexpr (W) asm volatile("global_store_dwordx2 %0, %1, off sc1\n\ts_nop 1" ::"v"(p), "v"(v) : "memory");
  else *(u32x2*)p = v;
}
template <bool W>
DEV void st4(void* p, unsigned v) {
  if constexpr (W) asm volatile("global_store_dword %0, %1, off sc1" ::"v"(p), "v"(v) : "memory");
  else *(unsigned*)p = v;
}
#define BAR() __builtin_amdgcn_s_barrier()
#define SCHED() __builtin_amdgcn_sched_barrier(0)

#define TDECL
#define TSTAMP(m, i) do { } while (0)
#define TFLUSH(m) do { } while (0)
#define TARG
#define TPASS

#define CPF 1
#define CODE_ADDR(dst, sym)                                                                                  \
  asm volatile("s_getpc_b64 s[30:31]\n\ts_add_u32 s30, s30, " #sym "@rel32@lo+4\n\ts_addc_u32 s31, s31, " #sym \
               "@rel32@hi+12\n\ts_mov_b64 %0, s[30:31]"                                                      \
               : "=s"(dst)::"s30", "s31", "scc")
#define CPW 5
constexpr int CW1 = 8, CW3 = 3, CW4 = 11;
DEV void code_warm(int wid, int lane, unsigned ldsaddr) {
  if constexpr (CPF) {
    const char* a;
    int d;
    CODE_ADDR(a, k1_qkv);
    asm volatile("s_mov_b32 %0, k4_gu_tail-k1_qkv" : "=s"(d));
    const int last = d < 0 ? 0 : d >> 7;
    const bool sel = wid == CPW && blockIdx.x < 64;
    const int ln = sel ? (int)(blockIdx.x >> 3) * 64 + lane : 0;
    const unsigned vo = (unsigned)((ln < last ? ln : last) * 128);
    ldsaddr = __builtin_amdgcn_readfirstlane(ldsaddr);
    asm volatile("s_mov_b32 m0, %2\n\ts_nop 0\n\tglobal_load_lds_dword %0, %1" ::"v"(vo), "s"(a), "s"(ldsaddr) : "memory");
  }
}

template <int V> struct IC { static constexpr int value = V; };
template <int I, int N, class F>
DEV void static_for(F&& f) {
  if constexpr (I < N) {
    f(IC<I>{});
    static_for<I + 1, N>(f);
  }
}
template <int P, int KS, int NST, int NK, int PRE, int R>
struct Sched {
  static constexpr int avail(int t) {
    int n = ((t + 1) / KS + NST - 1) * P + ((t + 1) % KS + 1) * (P / KS);
    return n > NK * P ? NK * P : n;
  }
  static constexpr int cum(int t) {
    int n = PRE;
    for (int i = -1; i < t; ++i) { int a = avail(i); n = (n + R < a) ? n + R : a; }
    return n;
  }
};

DEV void swap32(unsigned& a, unsigned& b) {
  const auto r = __builtin_amdgcn_permlane32_swap(a, b, false, false);
  a = r[0]; b = r[1];
}
DEV void swap16(unsigned& a, unsigned& b) {
  const auto r = __builtin_amdgcn_permlane16_swap(a, b, false, false);
  a = r[0]; b = r[1];
}
DEV void swap16f(float& a, float& b) {
  unsigned x = __builtin_bit_cast(unsigned, a), y = __builtin_bit_cast(unsigned, b);
  swap16(x, y);
  a = __builtin_bit_cast(float, x); b = __builtin_bit_cast(float, y);
}
DEV unsigned pk2(float a, float b) { return __builtin_bit_cast(unsigned, bf16x2{(bf16)a, (bf16)b}); }
DEV bf16x8 cat8(bf16x4 a, bf16x4 b) { return __builtin_shufflevector(a, b, 0, 1, 2, 3, 4, 5, 6, 7); }
DEV f32x4 mfma16(bf16x8 a, bf16x8 b, f32x4 c) { return __builtin_amdgcn_mfma_f32_16x16x32_bf16(a, b, c, 0, 0, 0); }
DEV f32x16 mfma32(bf16x8 a, bf16x8 b, f32x16 c) { return __builtin_amdgcn_mfma_f32_32x32x16_bf16(a, b, c, 0, 0, 0); }

DEV f32x2 ln_stats(float sum, float sq) {
  float m = sum * (1.0f / D_);
  float var = fmaxf(sq * (1.0f / D_) - m * m, 0.f);
  float vb = bfr(var);
  float sd = bfr(__builtin_sqrtf(bfr(vb + 1e-5f)));
  f32x2 r;
  r[0] = bfr(m);
  r[1] = 1.0f / sd;
  return r;
}

extern "C" __global__ __launch_bounds__(512, 2) void k1_qkv(const bf16* __restrict__ x, const bf16* __restrict__ w,
                                                            bf16* __restrict__ QKV) {
  constexpr int NST = K1_NST, STAGE = 28672, NK = 8, NT = 16;
  extern __shared__ __attribute__((aligned(16))) char smem[];
  TDECL
  TSTAMP(0, 0);
  const int tid = threadIdx.x, lane = tid & 63;
  const int wid = __builtin_amdgcn_readfirstlane(tid >> 6);
  const int wm = wid & 3, wn = wid >> 2;
  const int l15 = lane & 15, gq = lane >> 4;
  const int xg = blockIdx.x & 7, idx = blockIdx.x >> 3;
  const int pm = (xg & 3) * 4 + (idx & 3), pn = (xg >> 2) * 8 + (idx >> 2);
  const unsigned lds0 = (unsigned)(uintptr_t)(LDSP(char))smem;
  asm volatile("" : "+s"(QKV));

  const unsigned voffA = (unsigned)((wid * 8 + (lane >> 3)) * 1024 + (((lane & 7) ^ ((wid & 1) * 4 + gq)) * 16));
  const int r12 = (lane * 43) >> 9, c12 = lane - r12 * 12;
  const bool validB = lane < 48;
  const unsigned voffB = (unsigned)((wid * 8 + r12) * 3072 + (((c12 >> 1) ^ (wid & 1)) * 16 + (c12 & 1) * 8) * 2);
  const char* baseA = (const char*)x + (size_t)pm * (128 * 1024);
  const char* baseB = (const char*)w + pn * 192;
  const int rot = ROT1 * ((idx >> 2) + 2 * (idx & 3));
  auto part = [&](int kc, int p) {
    const unsigned sb = lds0 + (kc % NST) * STAGE;
    const int kk = (kc + rot) & 7;
    if (p == 0) {
      glds(voffA, baseA + kk * 128, sb + wid * 1024);
      glds(voffA, baseA + kk * 128 + 65536, sb + 8192 + wid * 1024);
    } else if (validB) {
      glds(voffB, baseB + kk * (64 * 3072), sb + 16384 + wid * 1536);
      glds(voffB, baseB + kk * (64 * 3072) + 4 * 3072, sb + 16384 + wid * 1536 + 768);
    }
  };
  auto issue = [&](int kc) { part(kc, 0); part(kc, 1); };
  issue(0);
  BAR();
  issue(1);
  BAR();
  issue(2);
  TSTAMP(0, 1);

  unsigned a_addr[2][2], b_addr[3];
#pragma unroll
  for (int mi = 0; mi < 2; ++mi)
#pragma unroll
    for (int ks = 0; ks < 2; ++ks)
      a_addr[mi][ks] = lds0 + (wm * 32 + ((mi ^ wn) * 16) + l15) * 128 + (((4 * ks + gq) ^ (l15 >> 1)) * 16);
  {
    const int kl = gq * 8 + (l15 >> 2);
#pragma unroll
    for (int j = 0; j < 3; ++j)
      b_addr[j] = lds0 + 16384 + kl * 192 + (((3 * wn + j) ^ (gq & 1)) * 32) + (l15 & 3) * 8;
  }

  f32x4 acc[2][3], s1, s2, cs[3];
  const f32x4 z4 = {0.f, 0.f, 0.f, 0.f};
#pragma unroll
  for (int j = 0; j < 3; ++j) { acc[0][j] = z4; acc[1][j] = z4; cs[j] = z4; }
  s1 = z4; s2 = z4;
  bf16x8 af[2][2];
  bf16x4 bl[2][3], bh[2][3];
  const bf16 one = (bf16)1.0f;
  const bf16x8 ones = {one, one, one, one, one, one, one, one};

  auto rd = [&](int t, int set) {
    const int kc = t >> 1, ks = t & 1, st = kc % NST;
    const unsigned hi = (unsigned)((st & ~1) * STAGE);
    const int so = (st & 1) * STAGE;
#pragma unroll
    for (int j = 0; j < 3; ++j) {
      DSRTR(bl[set][j], b_addr[j] + hi, so + ks * 6144);
      DSRTR(bh[set][j], b_addr[j] + hi, so + ks * 6144 + 768);
      if (j < 2) DSR128(af[set][j], a_addr[j][ks] + hi, so);
    }
  };
  auto wt = [&](int set) {
    asm volatile("s_waitcnt lgkmcnt(0)" ::: "memory");
    PIN(af[set][0]); PIN(af[set][1]);
#pragma unroll
    for (int j = 0; j < 3; ++j) { PIN(bl[set][j]); PIN(bh[set][j]); }
  };

  using SC = Sched<2, 2, NST, NK, 6, 2>;
  wait_vm<(SC::cum(-1) - 2) * 2>();
  BAR();
  TSTAMP(0, 2);
  rd(0, 0);
  static_for<SC::cum(-1), SC::cum(0)>([&](auto n) { part(n.value >> 1, n.value & 1); });
  wt(0);
  static_for<0, NT>([&](auto tc) {
    constexpr int t = tc.value, kc = t >> 1, ks = t & 1, cur = t & 1, nxt = cur ^ 1;
    if constexpr (ks == 1 && kc + 1 < NK) {
      static_assert(SC::cum(t) >= (kc + 2) * 2, "schedule");
      wait_vm<(SC::cum(t) - (kc + 2) * 2) * 2 + ((CPF && t > CW1) ? 1 : 0)>();
      BAR();
      TSTAMP(0, 3 + kc);
    }
    if constexpr (t + 1 < NT) rd(t + 1, nxt);
    static_for<SC::cum(t), SC::cum(t + 1)>([&](auto n) { part(n.value >> 1, n.value & 1); });
    if constexpr (t == CW1) {
      static_assert(SC::cum(t + 1) == NK * 2, "issued after all operand loads");
      code_warm(wid, lane, lds0 + NST * STAGE + 2560 + wid * 256);
    }
    SCHED();
    {
      const bf16x8 b0 = cat8(bl[cur][0], bh[cur][0]), b1 = cat8(bl[cur][1], bh[cur][1]),
                   b2 = cat8(bl[cur][2], bh[cur][2]);
#pragma unroll
      for (int mi = 0; mi < 2; ++mi) {
        acc[mi][0] = mfma16(b0, af[cur][mi], acc[mi][0]);
        acc[mi][1] = mfma16(b1, af[cur][mi], acc[mi][1]);
        acc[mi][2] = mfma16(b2, af[cur][mi], acc[mi][2]);
      }
      s1 = mfma16(ones, af[cur][0], s1);
      s2 = mfma16(af[cur][0], af[cur][0], s2);
      if ((t & 3) == wm) {
        cs[0] = mfma16(b0, ones, cs[0]);
        cs[1] = mfma16(b1, ones, cs[1]);
        cs[2] = mfma16(b2, ones, cs[2]);
      }
    }
    SCHED();
    if constexpr (t + 1 < NT) wt(nxt);
  });
  TSTAMP(0, 20);

  float* st_ = (float*)(smem + NST * STAGE);
  float* csum = st_ + 256;
  {
    const float sq = (l15 & 2) ? ((l15 & 1) ? s2[3] : s2[2]) : ((l15 & 1) ? s2[1] : s2[0]);
    const f32x2 ms = ln_stats(s1[0], sq);
    if (gq == (l15 >> 2)) *(f32x2*)(st_ + (wm * 32 + wn * 16 + l15) * 2) = ms;
    if (l15 == 0) {
#pragma unroll
      for (int j = 0; j < 3; ++j) *(f32x4*)(csum + wm * 96 + (3 * wn + j) * 16 + gq * 4) = cs[j];
    }
  }
  __syncthreads();
  TSTAMP(0, 21);
  const int tile = pm * 4 + wm;
  const float qscale = 0.25f * 1.4426950408889634f;
  f32x2 ms[2];
#pragma unroll
  for (int mi = 0; mi < 2; ++mi) ms[mi] = *(const f32x2*)(st_ + (wm * 32 + (mi ^ wn) * 16 + l15) * 2);
  const int loq = (gq >> 1) * 256 + l15 * 8 + (gq & 1) * 4, lov = l15 * 16 + gq * 4;
#pragma unroll
  for (int j = 0; j < 3; ++j) {
    f32x4 c4 = z4;
#pragma unroll
    for (int p = 0; p < 4; ++p) c4 += *(const f32x4*)(csum + p * 96 + (3 * wn + j) * 16 + gq * 4);
    const int blk = pn * 6 + wn * 3 + j;
    const int typ = blk >> 5, h = blk & 31;
    const float sc = typ == 0 ? qscale : 1.0f;
    bf16* db = QKV + ((size_t)typ << 20) + (size_t)(h * 64 + tile) * 512 + (typ == 2 ? lov : loq);
    const int tfs = typ == 2 ? 256 : 128;
#pragma unroll
    for (int mi = 0; mi < 2; ++mi) {
      const float rs = ms[mi][1] * sc;
      bf16x4 o;
#pragma unroll
      for (int r = 0; r < 4; ++r) o[r] = (bf16)((acc[mi][j][r] - ms[mi][0] * c4[r]) * rs);
      st8<(WT & 1) != 0>(db + (mi ^ wn) * tfs, __builtin_bit_cast(u32x2, o));
    }
  }
  TSTAMP(0, 22);
  TFLUSH(0);
}

template <bool FULL, int NPF>
DEV void attn_body(unsigned kaddr, unsigned vaddr, bf16x8 qf, f32x16 cm0, f32x16 cm8, int wid, bf16* dst TARG) {
  const float NEG = -1e30f;
  f32x16 z16;
#pragma unroll
  for (int r = 0; r < 16; ++r) z16[r] = 0.f;
  bf16x8 kf[9];
#pragma unroll
  for (int t = 0; t < 9; ++t)
    if (FULL || wid + t >= 8) DSR128(kf[t], kaddr, t * 1024);
  asm volatile("s_waitcnt lgkmcnt(0)" ::: "memory");
#pragma unroll
  for (int t = 0; t < 9; ++t) PIN(kf[t]);
  TSTAMP(1, 3);
  float m0 = NEG, m1 = NEG;
#pragma unroll
  for (int t = 0; t < 9; ++t)
    if (FULL || wid + t >= 8) {
      const f32x16 s = mfma32(kf[t], qf, t == 0 ? cm0 : (t == 8 ? cm8 : z16));
#pragma unroll
      for (int r = 0; r < 16; r += 4) {
        m0 = fmaxf(fmaxf(m0, s[r]), s[r + 1]);
        m1 = fmaxf(fmaxf(m1, s[r + 2]), s[r + 3]);
      }
    }
  float m = fmaxf(m0, m1);
  m = fmaxf(m, __shfl_xor(m, 32));
  f32x16 nm;
#pragma unroll
  for (int r = 0; r < 16; ++r) { nm[r] = -m; cm0[r] -= m; cm8[r] -= m; }
  TSTAMP(1, 4);
  wait_vm<NPF>();
  BAR();
  TSTAMP(1, 5);
  f32x16 oa = z16, ob = z16;
  f32x16 sc_, sn_;
  bf16x4 v0l[2], v0h[2], v1l[2], v1h[2];
  auto vrd = [&](int t, int b) {
    DSRTR(v0l[b], vaddr, t * 1024);
    DSRTR(v0h[b], vaddr, t * 1024 + 256);
    DSRTR(v1l[b], vaddr, t * 1024 + 512);
    DSRTR(v1h[b], vaddr, t * 1024 + 768);
  };
  if (FULL) { sc_ = mfma32(kf[0], qf, cm0); vrd(0, 0); }
  else sc_ = z16;
  sn_ = z16;
#pragma unroll
  for (int t = 0; t < 9; ++t) {
    const int b = t & 1;
    const bool cur_ok = FULL || wid + t >= 8, nxt_ok = t < 8 && (FULL || wid + t + 1 >= 8);
    if (cur_ok) {
      asm volatile("s_waitcnt lgkmcnt(0)" ::: "memory");
      PIN(v0l[b]); PIN(v0h[b]); PIN(v1l[b]); PIN(v1h[b]);
    }
    if (nxt_ok) {
      vrd(t + 1, b ^ 1);
      sn_ = mfma32(kf[t + 1], qf, t + 1 == 8 ? cm8 : nm);
    }
    SCHED();
    if (cur_ok) {
      bf16x8 p0, p1;
#pragma unroll
      for (int j = 0; j < 8; ++j) p0[j] = (bf16)__builtin_amdgcn_exp2f(sc_[j]);
      SCHED();
      oa = mfma32(cat8(v0l[b], v0h[b]), p0, oa);
      SCHED();
#pragma unroll
      for (int j = 0; j < 8; ++j) p1[j] = (bf16)__builtin_amdgcn_exp2f(sc_[8 + j]);
      SCHED();
      ob = mfma32(cat8(v1l[b], v1h[b]), p1, ob);
      SCHED();
    }
    sc_ = sn_;
  }
  TSTAMP(1, 6);
  const float inv = 1.0f / (oa[8] + ob[8]);
  unsigned a0 = pk2((oa[0] + ob[0]) * inv, (oa[1] + ob[1]) * inv), a1 = pk2((oa[2] + ob[2]) * inv, (oa[3] + ob[3]) * inv);
  unsigned b0 = pk2((oa[4] + ob[4]) * inv, (oa[5] + ob[5]) * inv), b1 = pk2((oa[6] + ob[6]) * inv, (oa[7] + ob[7]) * inv);
  swap32(a0, b0);
  swap32(a1, b1);
  st16<(WT & 2) != 0>(dst, u32x4{a0, a1, b0, b1});
}

extern "C" __global__ __launch_bounds__(512, 2) void k2_attn(const bf16* __restrict__ QKV, bf16* __restrict__ O,
                                                  const bf16* __restrict__ wo, const bf16* __restrict__ w1,
                                                  const bf16* __restrict__ w3, const bf16* __restrict__ w2,
                                                  bf16* __restrict__ Wt, float* __restrict__ CS,
                                                  bf16* __restrict__ W2t, bf16* __restrict__ Wot) {
  constexpr int LV = 16384, LONE = 32768, LSJ = 43008;
  extern __shared__ __attribute__((aligned(16))) char smem[];
  TDECL
  TSTAMP(1, 0);
  const int tid = threadIdx.x, lane = tid & 63;
  const int wid = __builtin_amdgcn_readfirstlane(tid >> 6);
  const int l15 = lane & 15, gq = lane >> 4;
  const int xg = blockIdx.x >> 5, h = (blockIdx.x & 7) * 4 + ((blockIdx.x >> 3) & 3);
  const unsigned lds0 = (unsigned)(uintptr_t)(LDSP(char))smem;
  const int t0 = xg * 8 - 8;
  const unsigned l16 = lane * 16;
  asm volatile("" : "+s"(O), "+s"(Wt), "+s"(CS), "+s"(W2t), "+s"(Wot));
  asm volatile("" : "+s"(wo), "+s"(w1), "+s"(w3), "+s"(w2));
  const bool ld = xg > 0 || wid >= 4;

  const char* kb = (const char*)QKV + (2u << 20) + (ptrdiff_t)(h * 64 + t0 + 2 * wid) * 1024;
  if (ld) {
    glds(l16, kb, lds0 + wid * 2048);
    glds(l16, kb + 1024, lds0 + wid * 2048 + 1024);
  }
  bf16x8 qf;
  GLD4(qf, l16, (const char*)QKV + (size_t)(h * 64 + xg * 8 + wid) * 1024, 0);
  if (ld) {
    glds(l16, kb + (2u << 20), lds0 + LV + wid * 2048);
    glds(l16, kb + (2u << 20) + 1024, lds0 + LV + wid * 2048 + 1024);
  }
  {
    const u32x4 o4 = {0x3f803f80u, 0x3f803f80u, 0x3f803f80u, 0x3f803f80u};
    *(u32x4*)(smem + LONE + tid * 16) = o4;
    if (tid < 128) *(u32x4*)(smem + LONE + 8192 + tid * 16) = o4;
  }
  f32x16 cm0, cm8;
  {
    const int c = lane & 31, hh = lane >> 5;
#pragma unroll
    for (int r = 0; r < 16; ++r) {
      const int kr = (r & 3) + 8 * (r >> 2) + 4 * hh;
      cm0[r] = (kr > c) ? 0.f : -1e30f;
      cm8[r] = (kr <= c) ? 0.f : -1e30f;
    }
    PIN(cm0); PIN(cm8);
  }
  TSTAMP(1, 1);
  if (ld) asm volatile("s_waitcnt vmcnt(2) lgkmcnt(0)" ::: "memory");
  else asm volatile("s_waitcnt vmcnt(0) lgkmcnt(0)" ::: "memory");
  PIN(qf);
  BAR();
  TSTAMP(1, 2);
  const int skc = blockIdx.x >> 5, smt = (blockIdx.x >> 4) & 1, spn = blockIdx.x & 15;
  u32x4 wr0, wr1;
  {
    const int wj = wid < 7 ? wid : 6;
    const char* sb = (const char*)(smt ? w3 : w1) + (size_t)(skc * 64) * (F_ * 2) + (spn * 112 + wj * 16) * 2;
    const unsigned vo = (unsigned)((lane >> 1) * (F_ * 2) + (lane & 1) * 16);
    glds(vo, sb, lds0 + LSJ + wid * 2048);
    glds(vo, sb + 32 * (F_ * 2), lds0 + LSJ + wid * 2048 + 1024);
    const char* o = (const char*)wo + (size_t)blockIdx.x * 2048;
    const char* r0 = wid < 7 ? (const char*)w2 + (size_t)blockIdx.x * 7168 + wid * 1024 : o;
    GLD4(wr0, l16, r0, 0);
    GLD4(wr1, l16, o, 1024);
  }
  code_warm(wid, lane, lds0 + 59392 + wid * 256);

  const unsigned kaddr = lds0 + wid * 1024 + l16;
  const unsigned vlane = (unsigned)(((lane >> 5) * 4 + (l15 >> 2)) * 32 + (l15 & 3) * 8);
  const unsigned vaddr = (lane & 16) ? (lds0 + LONE + (vlane ^ 128u)) : (lds0 + LV + wid * 1024 + vlane);
  bf16* dst = O + (size_t)(((xg * 4 + (wid >> 1)) * H_ + h) * 64 + (wid & 1) * 32 + (lane & 31)) * 16 + (lane >> 5) * 8;
  if (xg > 0) attn_body<true, 4 + CPF>(kaddr, vaddr, qf, cm0, cm8, wid, dst TPASS);
  else attn_body<false, 4 + CPF>(kaddr, vaddr, qf, cm0, cm8, wid, dst TPASS);
  TSTAMP(1, 20);
  wait_vm<1>();
  {
    const unsigned co = (unsigned)(lane & 7) * 16;
    PIN(wr0); PIN(wr1);
    if (wid < 7) {
      st16<(WT & 2) != 0>((char*)W2t + (size_t)(lane >> 3) * (F_ * 128) + (blockIdx.x * 7 + wid) * 128 + co, wr0);
    } else {
      char* o = (char*)Wot + (size_t)(lane >> 3) * (D_ * 128) + blockIdx.x * 256 + co;
      st16<(WT & 2) != 0>(o, wr0);
      st16<(WT & 2) != 0>(o + 128, wr1);
    }
  }
  if (wid < 7) {
    const unsigned ra = lds0 + LSJ + wid * 2048 + (gq * 8 + (l15 >> 2)) * 32 + (l15 & 3) * 8;
    bf16x4 f[4];
    DSRTR(f[0], ra, 0);
    DSRTR(f[1], ra, 128);
    DSRTR(f[2], ra, 1024);
    DSRTR(f[3], ra, 1024 + 128);
    asm volatile("s_waitcnt lgkmcnt(0)" ::: "memory");
    float c = 0.f;
#pragma unroll
    for (int i = 0; i < 4; ++i) {
      PIN(f[i]);
#pragma unroll
      for (int e = 0; e < 4; ++e) c += (float)f[i][e];
    }
    char* wd = (char*)Wt + (size_t)(((spn * 8 + skc) * 4 + smt) * 7 + wid) * 1024 + l16;
    st16<(WT & 2) != 0>(wd, __builtin_bit_cast(u32x4, cat8(f[0], f[1])));
    st16<(WT & 2) != 0>(wd + 14 * 1024, __builtin_bit_cast(u32x4, cat8(f[2], f[3])));
    c += __shfl_xor(c, 16);
    c += __shfl_xor(c, 32);
    if (lane < 16) st4<(WT & 2) != 0>(CS + skc * (2 * F_) + smt * F_ + spn * 112 + wid * 16 + lane, __builtin_bit_cast(unsigned, c));
  }
  TSTAMP(1, 22);
  wait_vm<0>();
  TFLUSH(1);
}

template <int MODE, int KTOT>
DEV void gemm_body(const bf16* __restrict__ A, const bf16* __restrict__ B, const bf16* __restrict__ R,
                   bf16* __restrict__ O, float* __restrict__ RS, const bf16* __restrict__ PFW) {
  constexpr int NST = G_NST, STAGE = 32768, NK = KTOT / 128, NT = NK * 4;
  extern __shared__ __attribute__((aligned(16))) char smem[];
  TDECL
  TSTAMP(MODE == 0 ? 2 : 4, 0);
  const int tid = threadIdx.x, lane = tid & 63;
  const int wid = __builtin_amdgcn_readfirstlane(tid >> 6);
  const int wm = wid & 3, wn = wid >> 2;
  const int l15 = lane & 15, gq = lane >> 4;
  const int xg = blockIdx.x & 7, idx = blockIdx.x >> 3;
#define PNX 0
  const int pm = xg * 4 + (idx >> 3), pn = (idx & 7) ^ PNX;
  if (MODE == 0) asm volatile("" : "+s"(RS), "+s"(PFW));
  const unsigned lds0 = (unsigned)(uintptr_t)(LDSP(char))smem;

  u32x4 res;
  const int tok = pm * 64 + wm * 16 + l15;
  const int ncol = pn * 64 + wn * 32 + (gq & 1) * 16 + (gq >> 1) * 8;
  const unsigned hoff = (unsigned)(((((tok >> 7) * 8 + pn) * 128 + (tok & 127)) * 64 + (ncol & 63)) * 2);
  GLD4(res, MODE == 0 ? (unsigned)((tok * D_ + ncol) * 2) : hoff, R, 0);

  const unsigned voffA = (unsigned)(wid * 1024 + lane * 16);
  const int pB = ((lane & 7) >> 1) ^ (((lane >> 4) & 1) | ((wid & 1) << 1));
  const unsigned voffB = (unsigned)((wid * 8 + (lane >> 3)) * 128 + pB * 32 + (lane & 1) * 16);
  const char* baseA = (const char*)A + (size_t)pm * (KTOT * 128);
  const char* baseB = (const char*)B + pn * (KTOT * 128);
  const unsigned sbw = lds0 + wid * 1024;
  const int rot = MODE == 0 ? ROT3 * ((pn + (idx >> 3)) & 3) : ROT5 * ((pn * 2 + (idx >> 3) * 3) % NK);
  auto part = [&](int kc, int p) {
    const unsigned sb = sbw + (kc % NST) * STAGE;
    int kk = kc + rot;
    if (kk >= NK) kk -= NK;
    if (p == 0) glds(voffA, baseA + (size_t)kk * 16384, sb);
    if (p == 1) glds(voffA, baseA + (size_t)kk * 16384 + 8192, sb + 8192);
    if (p == 2) glds(voffB, baseB + (size_t)kk * 16384, sb + 16384);
    if (p == 3) glds(voffB, baseB + (size_t)kk * 16384 + 8192, sb + 16384 + 8192);
  };
  auto issue = [&](int kc) { part(kc, 0); part(kc, 1); part(kc, 2); part(kc, 3); };
  issue(0);
  BAR();
  issue(1);
  BAR();
  issue(2);
  TSTAMP(MODE == 0 ? 2 : 4, 1);

  unsigned a_addr[4], b_addr[2];
#pragma unroll
  for (int ks = 0; ks < 4; ++ks) a_addr[ks] = lds0 + (2 * ks + (gq >> 1)) * 2048 + (wm * 16 + l15) * 32 + (gq & 1) * 16;
  {
    const int kl = gq * 8 + (l15 >> 2);
    const int fr = ((l15 >> 3) & 1) | ((gq & 1) << 1);
#pragma unroll
    for (int j = 0; j < 2; ++j) b_addr[j] = lds0 + 16384 + kl * 128 + (((2 * wn + j) ^ fr) * 32) + (l15 & 3) * 8;
  }

  f32x4 acc[2];
  const f32x4 z4 = {0.f, 0.f, 0.f, 0.f};
  acc[0] = z4; acc[1] = z4;
  bf16x8 af[2];
  bf16x4 bl[2][2], bh[2][2];

  auto rd = [&](int t, int set) {
    const int kc = t >> 2, ks = t & 3, st = kc % NST;
    const unsigned hi = (unsigned)((st & ~1) * STAGE);
    const int so = (st & 1) * STAGE;
    DSRTR(bl[set][0], b_addr[0] + hi, so + ks * 4096);
    DSRTR(bh[set][0], b_addr[0] + hi, so + ks * 4096 + 512);
    DSR128(af[set], a_addr[ks] + hi, so);
    DSRTR(bl[set][1], b_addr[1] + hi, so + ks * 4096);
    DSRTR(bh[set][1], b_addr[1] + hi, so + ks * 4096 + 512);
  };
  auto wt = [&](int set) {
    asm volatile("s_waitcnt lgkmcnt(0)" ::: "memory");
    PIN(af[set]);
    PIN(bl[set][0]); PIN(bh[set][0]); PIN(bl[set][1]); PIN(bh[set][1]);
  };

  using SC = Sched<4, 4, NST, NK, 12, G_R>;
  wait_vm<SC::cum(-1) - 4>();
  BAR();
  TSTAMP(MODE == 0 ? 2 : 4, 2);
  rd(0, 0);
  static_for<SC::cum(-1), SC::cum(0)>([&](auto n) { part(n.value >> 2, n.value & 3); });
  wt(0);
  static_for<0, NT>([&](auto tc) {
    constexpr int t = tc.value, kc = t >> 2, ks = t & 3, cur = t & 1, nxt = cur ^ 1;
    if constexpr (ks == 3 && kc + 1 < NK) {
      static_assert(SC::cum(t) >= (kc + 2) * 4, "schedule");
      constexpr int npf = (MODE == 0 && PF3 && t > PF3T) ? (t - PF3T > 4 ? 4 : t - PF3T) : 0;
      static_assert(npf == 0 || SC::cum(PF3T) == NK * 4, "warm-up loads come after all operand loads");
      wait_vm<SC::cum(t) - (kc + 2) * 4 + npf + ((MODE == 0 && CPF && t > CW3) ? 1 : 0)>();
      BAR();
      TSTAMP(MODE == 0 ? 2 : 4, 3 + kc);
    }
    if constexpr (t + 1 < NT) rd(t + 1, nxt);
    static_for<SC::cum(t), SC::cum(t + 1)>([&](auto n) { part(n.value >> 2, n.value & 3); });
    if constexpr (MODE == 0 && t == CW3) {
      static_assert(SC::cum(t + 1) == NK * 4, "issued after all operand loads");
      code_warm(wid, lane, lds0 + NST * STAGE + wid * 1024);
    }
    if constexpr (MODE == 0 && PF3 && t >= PF3T && t < PF3T + 4) {
      glds((unsigned)(lane * 16),
           (const char*)PFW + (size_t)(xg >> 1) * (4 * 229376) + idx * 28672 + (t - PF3T) * 8192 + (t - PF3T < 3 ? wid : (wid & 3)) * 1024,
           lds0 + NST * STAGE + wid * 1024);
    }
    SCHED();
    acc[0] = mfma16(cat8(bl[cur][0], bh[cur][0]), af[cur], acc[0]);
    acc[1] = mfma16(cat8(bl[cur][1], bh[cur][1]), af[cur], acc[1]);
    SCHED();
    if constexpr (t + 1 < NT) wt(nxt);
  });
  TSTAMP(MODE == 0 ? 2 : 4, 20);
  PIN(res);
  float s = 0.f, q = 0.f;
  {
#pragma unroll
    for (int r = 0; r < 4; ++r) { float a = acc[0][r], b = acc[1][r]; swap16f(a, b); acc[0][r] = a; acc[1][r] = b; }
    const bf16x8 rr = __builtin_bit_cast(bf16x8, res);
    bf16x8 o;
#pragma unroll
    for (int j = 0; j < 2; ++j)
#pragma unroll
      for (int r = 0; r < 4; ++r) {
        o[j * 4 + r] = (bf16)((float)rr[j * 4 + r] + bfr(acc[j][r]));
        const float v = (float)o[j * 4 + r];
        s += v;
        q += v * v;
      }
    st16<(WT & (MODE == 0 ? 4 : 16)) != 0>((char*)O + (MODE == 0 ? hoff : (unsigned)((tok * D_ + ncol) * 2)), __builtin_bit_cast(u32x4, o));
  }
  if (MODE == 0) {
    s += __shfl_xor(s, 16); s += __shfl_xor(s, 32);
    q += __shfl_xor(q, 16); q += __shfl_xor(q, 32);
    if (gq == 0) st8<(WT & 4) != 0>(RS + ((size_t)(pn * 2 + wn) * S_ + tok) * 2, __builtin_bit_cast(u32x2, f32x2{s, q}));
  }
  if (MODE == 0) wait_vm<0>();
  TSTAMP(MODE == 0 ? 2 : 4, 22);
  TFLUSH(MODE == 0 ? 2 : 4);
}
extern "C" __global__ __launch_bounds__(512, 2) void k3_wo(const bf16* __restrict__ A, const bf16* __restrict__ B,
                                                           const bf16* __restrict__ R, bf16* __restrict__ O,
                                                           float* __restrict__ RS, const bf16* __restrict__ PFW) {
  gemm_body<0, 512>(A, B, R, O, RS, PFW);
}
extern "C" __global__ __launch_bounds__(512, 2) void k5_down(const bf16* __restrict__ A, const bf16* __restrict__ B,
                                                             const bf16* __restrict__ R, bf16* __restrict__ O) {
  gemm_body<1, 1792>(A, B, R, O, nullptr, nullptr);
}

template <int NP, int P0>
DEV void gu_body(char* smem, unsigned lds0, int tid, int wid, int pm, int pn, const bf16* __restrict__ Hh,
                 const bf16* __restrict__ Wt, const float* __restrict__ RS, const float* __restrict__ CS,
                 bf16* __restrict__ Hm, const char* pf TARG) {
  constexpr int NST = 3, STAGE = 45056, NK = 8, LPC = 2 + NP, NT = 16;
  const int lane = tid & 63, wm = wid & 3;
  const int l15 = lane & 15, gq = lane >> 4;
  const unsigned l16 = lane * 16;
  const unsigned voffA = (unsigned)((wid * 8 + (lane >> 3)) * 128 + (((lane & 7) ^ ((wid & 1) * 4 + gq)) * 16));
  const char* baseA = (const char*)Hh + (size_t)pm * (128 * 1024);
  const int pc0 = (P0 == 0 ? 0 : 16) + wm * NP;
  const char* baseW = (const char*)Wt + (size_t)pn * (8 * 28672) + pc0 * 1024;
  const int rot = ROT4 * (pm + 2 * pn);
  auto piece = [&](int kc, int i) {
    const unsigned sb = lds0 + (kc % NST) * STAGE;
    const int kk = (kc + rot) & 7;
    if (i == 0) glds(voffA, baseA + kk * 16384, sb + wid * 1024);
    else if (i == 1) glds(voffA, baseA + kk * 16384 + 8192, sb + 8192 + wid * 1024);
    else glds(l16, baseW + kk * 28672 + (i - 2) * 1024, sb + 16384 + (pc0 + i - 2) * 1024);
  };
  auto issue = [&](int kc) {
#pragma unroll
    for (int i = 0; i < LPC; ++i) piece(kc, i);
  };
  float* st_ = (float*)(smem + NST * STAGE);
  float* csum = st_ + 256;
  if (P0 == 0) {
    if (wid < 2) {
      f32x2 sv[16];
      const char* rb = (const char*)RS + pm * 1024;
      const unsigned vo = (unsigned)(tid * 8);
#pragma unroll
      for (int i = 0; i < 16; ++i) GLD2(sv[i], vo, rb + i * (S_ * 8), 0);
      issue(0);
      BAR();
      issue(1);
      wait_vm<2 * LPC>();
#pragma unroll
      for (int i = 0; i < 16; ++i) PIN(sv[i]);
      f32x2 a = sv[0];
#pragma unroll
      for (int i = 1; i < 16; ++i) a += sv[i];
      *(f32x2*)(st_ + tid * 2) = ln_stats(a[0], a[1]);
    } else {
      issue(0);
      BAR();
      issue(1);
    }
  } else {
    float sc[8];
    int c = tid - 256;
    const bool ok = c < 224;
    if (!ok) c = 0;
    const unsigned vo = (unsigned)(((c < 112 ? 0 : F_ - 112) + pn * 112 + c) * 4);
#pragma unroll
    for (int i = 0; i < 8; ++i) GLD1(sc[i], vo, (const char*)CS + i * (2 * F_ * 4), 0);
    issue(0);
    BAR();
    issue(1);
    wait_vm<2 * LPC>();
#pragma unroll
    for (int i = 0; i < 8; ++i) PIN(sc[i]);
    float a = sc[0];
#pragma unroll
    for (int i = 1; i < 8; ++i) a += sc[i];
    if (ok) csum[c] = a;
  }
  TSTAMP(3, 1);

  unsigned a_addr[2];
#pragma unroll
  for (int ks = 0; ks < 2; ++ks) a_addr[ks] = lds0 + (wm * 32 + l15) * 128 + (((4 * ks + gq) ^ (l15 >> 1)) * 16);
  const unsigned b_addr = lds0 + 16384 + P0 * 1024 + l16;
  f32x4 ag[2][NP], au[2][NP];
  const f32x4 z4 = {0.f, 0.f, 0.f, 0.f};
#pragma unroll
  for (int j = 0; j < NP; ++j) { ag[0][j] = z4; ag[1][j] = z4; au[0][j] = z4; au[1][j] = z4; }
  bf16x8 af[2][2], bg[2][NP], bu[2][NP];
  auto rd = [&](int t, int set) {
    const int kc = t >> 1, ks = t & 1, st = kc % NST;
    const unsigned so = st * STAGE;
    DSR128(af[set][0], a_addr[ks] + so, 0);
    DSR128(af[set][1], a_addr[ks] + so, 2048);
#pragma unroll
    for (int j = 0; j < NP; ++j) {
      DSR128(bg[set][j], b_addr + so, (ks * 14 + j) * 1024);
      DSR128(bu[set][j], b_addr + so, (ks * 14 + 7 + j) * 1024);
    }
  };
  auto wt = [&](int set) {
    asm volatile("s_waitcnt lgkmcnt(0)" ::: "memory");
    PIN(af[set][0]); PIN(af[set][1]);
#pragma unroll
    for (int j = 0; j < NP; ++j) { PIN(bg[set][j]); PIN(bu[set][j]); }
  };
  asm volatile("s_waitcnt vmcnt(%0) lgkmcnt(0)" ::"n"(LPC) : "memory");
  BAR();
  TSTAMP(3, 2);
  rd(0, 0);
  piece(NST - 1, 0); piece(NST - 1, 1); piece(NST - 1, 2);
  wt(0);
#pragma unroll
  for (int t = 0; t < NT; ++t) {
    const int kc = t >> 1, ks = t & 1, cur = t & 1, nxt = cur ^ 1;
    if (ks == 1 && kc + 1 < NK) {
      const int last = (kc + NST - 1 < NK - 1) ? (kc + NST - 1) : (NK - 1);
      wait_vm_n((last - (kc + 1)) * LPC + ((PF4 && t > 11) ? (t - 11) * 2 : 0) + ((CPF && t > CW4) ? 1 : 0));
      BAR();
      TSTAMP(3, 3 + kc);
    }
    if (t + 1 < NT) rd(t + 1, nxt);
    const int ic = (t + 1) / 2 + NST - 1, ip = ((t + 1) & 1) * 3;
    SCHED();
#pragma unroll
    for (int j = 0; j < NP; ++j) {
      ag[0][j] = mfma16(bg[cur][j], af[cur][0], ag[0][j]);
      ag[1][j] = mfma16(bg[cur][j], af[cur][1], ag[1][j]);
      au[0][j] = mfma16(bu[cur][j], af[cur][0], au[0][j]);
      au[1][j] = mfma16(bu[cur][j], af[cur][1], au[1][j]);
      SCHED();
      if (j < 3 && ic < NK && ip + j < LPC) piece(ic, ip + j);
      if (PF4 && j < 2 && t >= 11 && (t - 11) * 2 + j < 7)
        glds(l16, pf + ((t - 11) * 2 + j) * 8192, lds0 + NST * STAGE + 2048 + wid * 1024);
      if (t == CW4 && j == 2) code_warm(wid, lane, lds0 + NST * STAGE + 2048 + wid * 1024);
      SCHED();
    }
    if (t + 1 < NT) wt(nxt);
  }
  TSTAMP(3, 20);
  bf16* dst0 = Hm + ((size_t)((pm * 2 + (wm >> 1)) * 112 + pn * 7 + P0) * 64 + (wm & 1) * 32 + l15) * 16;
#pragma unroll
  for (int mi = 0; mi < 2; ++mi) {
    const f32x2 ms = *(const f32x2*)(st_ + (wm * 32 + mi * 16 + l15) * 2);
    const f32x2 nm = {-ms[0], -ms[0]};
    const float le = ms[1] * -1.4426950408889634f, iv = 1.0f / (ms[1] * ms[1]);
    const f32x2 le2 = {le, le}, iv2 = {iv, iv};
    unsigned pk[NP][2];
#pragma unroll
    for (int j = 0; j < NP; ++j) {
      const f32x4 cg = *(const f32x4*)(csum + (P0 + j) * 16 + gq * 4);
      const f32x4 cu = *(const f32x4*)(csum + 112 + (P0 + j) * 16 + gq * 4);
#pragma unroll
      for (int hf = 0; hf < 2; ++hf) {
        const f32x2 g = __builtin_elementwise_fma(f32x2{cg[2 * hf], cg[2 * hf + 1]}, nm,
                                                  f32x2{ag[mi][j][2 * hf], ag[mi][j][2 * hf + 1]});
        const f32x2 u = __builtin_elementwise_fma(f32x2{cu[2 * hf], cu[2 * hf + 1]}, nm,
                                                  f32x2{au[mi][j][2 * hf], au[mi][j][2 * hf + 1]});
        const f32x2 ea = g * le2;
        const f32x2 e = {__builtin_amdgcn_exp2f(ea[0]), __builtin_amdgcn_exp2f(ea[1])};
        const f32x2 d = __builtin_elementwise_fma(e, iv2, iv2);
        const f32x2 rc = {__builtin_amdgcn_rcpf(d[0]), __builtin_amdgcn_rcpf(d[1])};
        const f32x2 o = g * u * rc;
        pk[j][hf] = pk2(o[0], o[1]);
      }
    }
#pragma unroll
    for (int j = 0; j + 1 < NP; j += 2) {
      swap16(pk[j][0], pk[j + 1][0]);
      swap16(pk[j][1], pk[j + 1][1]);
      st16<(WT & 8) != 0>(dst0 + ((size_t)(j + (gq & 1)) * 64 + mi * 16) * 16 + (gq >> 1) * 8,
                          u32x4{pk[j][0], pk[j][1], pk[j + 1][0], pk[j + 1][1]});
    }
    if (NP & 1) st8<(WT & 8) != 0>(dst0 + ((size_t)(NP - 1) * 64 + mi * 16) * 16 + gq * 4, u32x2{pk[NP - 1][0], pk[NP - 1][1]});
  }
}

extern "C" __global__ __launch_bounds__(512, 2) void k4_gu(const bf16* __restrict__ Hh, const bf16* __restrict__ Wt,
                                                const float* __restrict__ RS, const float* __restrict__ CS,
                                                bf16* __restrict__ Hm, const bf16* __restrict__ W2t) {
  extern __shared__ __attribute__((aligned(16))) char smem[];
  TDECL
  TSTAMP(3, 0);
  const int tid = threadIdx.x;
  const int wid = __builtin_amdgcn_readfirstlane(tid >> 6);
  const int xg = blockIdx.x & 7, idx = blockIdx.x >> 3;
  const int pm = (xg & 1) * 8 + (idx & 7), pn = (xg >> 1) * 4 + (idx >> 3);
  asm volatile("" : "+s"(RS), "+s"(CS), "+s"(Hm), "+s"(W2t));
  const unsigned lds0 = (unsigned)(uintptr_t)(LDSP(char))smem;
  const char* pf = (const char*)W2t + idx * 57344 + wid * 1024;
  if (wid < 4) gu_body<4, 0>(smem, lds0, tid, wid, pm, pn, Hh, Wt, RS, CS, Hm, pf TPASS);
  else gu_body<3, 4>(smem, lds0, tid, wid, pm, pn, Hh, Wt, RS, CS, Hm, pf TPASS);
  wait_vm<0>();
  TSTAMP(3, 22);
  TFLUSH(3);
  asm volatile("k4_gu_tail:");
}

extern "C" void kernel_launch(void* const* d_in, const int* in_sizes, int n_in, void* d_out, int out_size, void* d_ws,
                              size_t ws_size, hipStream_t stream) {
  const bf16* x = (const bf16*)d_in[0];
  const bf16* wqkv = (const bf16*)d_in[1];
  const bf16* wo = (const bf16*)d_in[2];
  const bf16* w1 = (const bf16*)d_in[4];
  const bf16* w3 = (const bf16*)d_in[5];
  const bf16* w2 = (const bf16*)d_in[6];
  bf16* out = (bf16*)d_out;

  char* ws = (char*)d_ws;
  bf16* QKV = (bf16*)(ws);
  bf16* Oa = (bf16*)(ws + (7u << 20));
  bf16* Hh = (bf16*)(ws + (9u << 20));
  bf16* Hm = (bf16*)(ws + (11u << 20));
  float* RS = (float*)(ws + (18u << 20));
  float* CS = (float*)(ws + (19u << 20));
  bf16* Wt = (bf16*)(ws + (20u << 20));
  bf16* W2t = (bf16*)(ws + (24u << 20));
  bf16* Wot = (bf16*)(ws + (26u << 20));

  {
    constexpr int lds = K1_NST * 28672 + 2560 + 2048;
    (void)hipFuncSetAttribute((const void*)k1_qkv, hipFuncAttributeMaxDynamicSharedMemorySize, lds);
    k1_qkv<<<256, 512, lds, stream>>>(x, wqkv, QKV);
  }
  {
    constexpr int lds = 59392 + 2048;
    (void)hipFuncSetAttribute((const void*)k2_attn, hipFuncAttributeMaxDynamicSharedMemorySize, lds);
    k2_attn<<<256, 512, lds, stream>>>(QKV, Oa, wo, w1, w3, w2, Wt, CS, W2t, Wot);
  }
  {
    auto k = k3_wo;
    constexpr int lds = G_NST * 32768 + 8192;
    (void)hipFuncSetAttribute((const void*)k, hipFuncAttributeMaxDynamicSharedMemorySize, lds);
    k<<<256, 512, lds, stream>>>(Oa, Wot, x, Hh, RS, Wt);
  }
  {
    constexpr int lds = 3 * 45056 + 2048 + 8192;
    (void)hipFuncSetAttribute((const void*)k4_gu, hipFuncAttributeMaxDynamicSharedMemorySize, lds);
    k4_gu<<<256, 512, lds, stream>>>(Hh, Wt, RS, CS, Hm, W2t);
  }
  {
    auto k = k5_down;
    constexpr int lds = G_NST * 32768;
    (void)hipFuncSetAttribute((const void*)k, hipFuncAttributeMaxDynamicSharedMemorySize, lds);
    k<<<256, 512, lds, stream>>>(Hm, W2t, Hh, out);
  }
}
```

```cpp
#include <hip/hip_runtime.h>
#include <stdint.h>

typedef __attribute__((ext_vector_type(8))) __bf16 bf16x8;
typedef __attribute__((ext_vector_type(4))) __bf16 bf16x4;
typedef __attribute__((ext_vector_type(2))) __bf16 bf16x2;
typedef __attribute__((ext_vector_type(2))) float f32x2;
typedef __attribute__((ext_vector_type(4))) float f32x4;
typedef __attribute__((ext_vector_type(16))) float f32x16;
typedef __attribute__((ext_vector_type(4))) unsigned int u32x4;
typedef __attribute__((ext_vector_type(2))) unsigned int u32x2;
typedef __bf16 bf16;

#define LDSP(T) __attribute__((address_space(3))) T*
#define DEV __device__ __forceinline__

#define K1_NST 4
#define G_NST 4
#define G_R 1
#define ROT1 0
#define ROT3 0
#define ROT4 0
#define ROT5 0
#define PF4 1
#define PF3 0
#define PF3T 4
constexpr int S_ = 2048, D_ = 512, H_ = 32, F_ = 1792;

DEV float bfr(float x) { return (float)(bf16)x; }

template <int N>
DEV void wait_vm() {
  asm volatile("s_waitcnt vmcnt(%0)" ::"n"(N) : "memory");
}
DEV void wait_vm_n(int n) {
  switch (n) {
#define WC(X) case X: wait_vm<X>(); break;
    WC(0) WC(1) WC(2) WC(3) WC(4) WC(5) WC(6) WC(7) WC(8) WC(9) WC(10) WC(11) WC(12) WC(13) WC(14) WC(15)
    WC(16) WC(17) WC(18) WC(19) WC(20) WC(21) WC(22) WC(23) WC(24)
#undef WC
    default: wait_vm<0>(); break;
  }
}

DEV void glds(unsigned voff, const void* sbase, unsigned ldsaddr) {
  ldsaddr = __builtin_amdgcn_readfirstlane(ldsaddr);
  asm volatile("s_mov_b32 m0, %2\n\ts_nop 0\n\tglobal_load_lds_dwordx4 %0, %1" ::"v"(voff), "s"(sbase), "s"(ldsaddr)
               : "memory");
}
#define DSR128(dst, addr, off) asm volatile("ds_read_b128 %0, %1 offset:%2" : "=v"(dst) : "v"(addr), "i"(off))
#define DSRTR(dst, addr, off) asm volatile("ds_read_b64_tr_b16 %0, %1 offset:%2" : "=v"(dst) : "v"(addr), "i"(off))
#define PIN(x) asm volatile("" : "+v"(x))
#define GLD4(dst, voff, sbase, off) \
  asm volatile("global_load_dwordx4 %0, %1, %2 offset:%3" : "=v"(dst) : "v"(voff), "s"(sbase), "i"(off))
#define GLD2(dst, voff, sbase, off) \
  asm volatile("global_load_dwordx2 %0, %1, %2 offset:%3" : "=v"(dst) : "v"(voff), "s"(sbase), "i"(off))
#define GLD1(dst, voff, sbase, off) \
  asm volatile("global_load_dword %0, %1, %2 offset:%3" : "=v"(dst) : "v"(voff), "s"(sbase), "i"(off))
#define WT 31
template <bool W>
DEV void st16(void* p, u32x4 v) {
  if constexpr (W) asm volatile("global_store_dwordx4 %0, %1, off sc1\n\ts_nop 1" ::"v"(p), "v"(v) : "memory");
  else *(u32x4*)p = v;
}
template <bool W>
DEV void st8(void* p, u32x2 v) {
  if constexpr (W) asm volatile("global_store_dwordx2 %0, %1, off sc1\n\ts_nop 1" ::"v"(p), "v"(v) : "memory");
  else *(u32x2*)p = v;
}
template <bool W>
DEV void st4(void* p, unsigned v) {
  if constexpr (W) asm volatile("global_store_dword %0, %1, off sc1" ::"v"(p), "v"(v) : "memory");
  else *(unsigned*)p = v;
}
#define BAR() __builtin_amdgcn_s_barrier()
#define SCHED() __builtin_amdgcn_sched_barrier(0)

#define TDECL
#define TSTAMP(m, i) do { } while (0)
#define TFLUSH(m) do { } while (0)
#define TARG
#define TPASS

#define CPF 15
#define CODE_ADDR(dst, sym)                                                                                  \
  asm volatile("s_getpc_b64 s[30:31]\n\ts_add_u32 s30, s30, " #sym "@rel32@lo+4\n\ts_addc_u32 s31, s31, " #sym \
               "@rel32@hi+12\n\ts_mov_b64 %0, s[30:31]"                                                      \
               : "=s"(dst)::"s30", "s31", "scc")
#define CPW 5
constexpr int CW1 = 8, CW3 = 3, CW4 = 14;
template <bool ALL>
DEV void code_warm(int wid, int lane, unsigned ldsaddr, const char* other) {
  const bool sel = wid == CPW && blockIdx.x < 64;
  if (!ALL && !sel) return;
  const char* a;
  int d;
  CODE_ADDR(a, k1_qkv);
  asm volatile("s_mov_b32 %0, k4_gu_tail-k1_qkv" : "=s"(d));
  const int last = d < 0 ? 0 : d >> 7;
  const int ln = sel ? (int)(blockIdx.x >> 3) * 64 + lane : 0;
  const unsigned vo = (unsigned)((ln < last ? ln : last) * 128);
  if (ALL && !sel) a = other;
  ldsaddr = __builtin_amdgcn_readfirstlane(ldsaddr);
  asm volatile("s_mov_b32 m0, %2\n\ts_nop 0\n\tglobal_load_lds_dword %0, %1" ::"v"(vo), "s"(a), "s"(ldsaddr) : "memory");
}

template <int V> struct IC { static constexpr int value = V; };
template <int I, int N, class F>
DEV void static_for(F&& f) {
  if constexpr (I < N) {
    f(IC<I>{});
    static_for<I + 1, N>(f);
  }
}
template <int P, int KS, int NST, int NK, int PRE, int R>
struct Sched {
  static constexpr int avail(int t) {
    int n = ((t + 1) / KS + NST - 1) * P + ((t + 1) % KS + 1) * (P / KS);
    return n > NK * P ? NK * P : n;
  }
  static constexpr int cum(int t) {
    int n = PRE;
    for (int i = -1; i < t; ++i) { int a = avail(i); n = (n + R < a) ? n + R : a; }
    return n;
  }
};

DEV void swap32(unsigned& a, unsigned& b) {
  const auto r = __builtin_amdgcn_permlane32_swap(a, b, false, false);
  a = r[0]; b = r[1];
}
DEV void swap16(unsigned& a, unsigned& b) {
  const auto r = __builtin_amdgcn_permlane16_swap(a, b, false, false);
  a = r[0]; b = r[1];
}
DEV void swap16f(float& a, float& b) {
  unsigned x = __builtin_bit_cast(unsigned, a), y = __builtin_bit_cast(unsigned, b);
  swap16(x, y);
  a = __builtin_bit_cast(float, x); b = __builtin_bit_cast(float, y);
}
DEV unsigned pk2(float a, float b) { return __builtin_bit_cast(unsigned, bf16x2{(bf16)a, (bf16)b}); }
DEV bf16x8 cat8(bf16x4 a, bf16x4 b) { return __builtin_shufflevector(a, b, 0, 1, 2, 3, 4, 5, 6, 7); }
DEV f32x4 mfma16(bf16x8 a, bf16x8 b, f32x4 c) { return __builtin_amdgcn_mfma_f32_16x16x32_bf16(a, b, c, 0, 0, 0); }
DEV f32x16 mfma32(bf16x8 a, bf16x8 b, f32x16 c) { return __builtin_amdgcn_mfma_f32_32x32x16_bf16(a, b, c, 0, 0, 0); }

DEV f32x2 ln_stats(float sum, float sq) {
  float m = sum * (1.0f / D_);
  float var = fmaxf(sq * (1.0f / D_) - m * m, 0.f);
  float vb = bfr(var);
  float sd = bfr(__builtin_sqrtf(bfr(vb + 1e-5f)));
  f32x2 r;
  r[0] = bfr(m);
  r[1] = 1.0f / sd;
  return r;
}

extern "C" __global__ __launch_bounds__(512, 2) void k1_qkv(const bf16* __restrict__ x, const bf16* __restrict__ w,
                                                            bf16* __restrict__ QKV) {
  constexpr int NST = K1_NST, STAGE = 28672, NK = 8, NT = 16;
  extern __shared__ __attribute__((aligned(16))) char smem[];
  TDECL
  TSTAMP(0, 0);
  const int tid = threadIdx.x, lane = tid & 63;
  const int wid = __builtin_amdgcn_readfirstlane(tid >> 6);
  const int wm = wid & 3, wn = wid >> 2;
  const int l15 = lane & 15, gq = lane >> 4;
  const int xg = blockIdx.x & 7, idx = blockIdx.x >> 3;
  const int pm = (xg & 3) * 4 + (idx & 3), pn = (xg >> 2) * 8 + (idx >> 2);
  const unsigned lds0 = (unsigned)(uintptr_t)(LDSP(char))smem;
  asm volatile("" : "+s"(QKV));

  const unsigned voffA = (unsigned)((wid * 8 + (lane >> 3)) * 1024 + (((lane & 7) ^ ((wid & 1) * 4 + gq)) * 16));
  const int r12 = (lane * 43) >> 9, c12 = lane - r12 * 12;
  const bool validB = lane < 48;
  const unsigned voffB = (unsigned)((wid * 8 + r12) * 3072 + (((c12 >> 1) ^ (wid & 1)) * 16 + (c12 & 1) * 8) * 2);
  const char* baseA = (const char*)x + (size_t)pm * (128 * 1024);
  const char* baseB = (const char*)w + pn * 192;
  const int rot = ROT1 * ((idx >> 2) + 2 * (idx & 3));
  auto part = [&](int kc, int p) {
    const unsigned sb = lds0 + (kc % NST) * STAGE;
    const int kk = (kc + rot) & 7;
    if (p == 0) {
      glds(voffA, baseA + kk * 128, sb + wid * 1024);
      glds(voffA, baseA + kk * 128 + 65536, sb + 8192 + wid * 1024);
    } else if (validB) {
      glds(voffB, baseB + kk * (64 * 3072), sb + 16384 + wid * 1536);
      glds(voffB, baseB + kk * (64 * 3072) + 4 * 3072, sb + 16384 + wid * 1536 + 768);
    }
  };
  auto issue = [&](int kc) { part(kc, 0); part(kc, 1); };
  issue(0);
  BAR();
  issue(1);
  BAR();
  issue(2);
  TSTAMP(0, 1);

  unsigned a_addr[2][2], b_addr[3];
#pragma unroll
  for (int mi = 0; mi < 2; ++mi)
#pragma unroll
    for (int ks = 0; ks < 2; ++ks)
      a_addr[mi][ks] = lds0 + (wm * 32 + ((mi ^ wn) * 16) + l15) * 128 + (((4 * ks + gq) ^ (l15 >> 1)) * 16);
  {
    const int kl = gq * 8 + (l15 >> 2);
#pragma unroll
    for (int j = 0; j < 3; ++j)
      b_addr[j] = lds0 + 16384 + kl * 192 + (((3 * wn + j) ^ (gq & 1)) * 32) + (l15 & 3) * 8;
  }

  f32x4 acc[2][3], s1, s2, cs[3];
  const f32x4 z4 = {0.f, 0.f, 0.f, 0.f};
#pragma unroll
  for (int j = 0; j < 3; ++j) { acc[0][j] = z4; acc[1][j] = z4; cs[j] = z4; }
  s1 = z4; s2 = z4;
  bf16x8 af[2][2];
  bf16x4 bl[2][3], bh[2][3];
  const bf16 one = (bf16)1.0f;
  const bf16x8 ones = {one, one, one, one, one, one, one, one};

  auto rd = [&](int t, int set) {
    const int kc = t >> 1, ks = t & 1, st = kc % NST;
    const unsigned hi = (unsigned)((st & ~1) * STAGE);
    const int so = (st & 1) * STAGE;
#pragma unroll
    for (int j = 0; j < 3; ++j) {
      DSRTR(bl[set][j], b_addr[j] + hi, so + ks * 6144);
      DSRTR(bh[set][j], b_addr[j] + hi, so + ks * 6144 + 768);
      if (j < 2) DSR128(af[set][j], a_addr[j][ks] + hi, so);
    }
  };
  auto wt = [&](int set) {
    asm volatile("s_waitcnt lgkmcnt(0)" ::: "memory");
    PIN(af[set][0]); PIN(af[set][1]);
#pragma unroll
    for (int j = 0; j < 3; ++j) { PIN(bl[set][j]); PIN(bh[set][j]); }
  };

  using SC = Sched<2, 2, NST, NK, 6, 2>;
  wait_vm<(SC::cum(-1) - 2) * 2>();
  BAR();
  TSTAMP(0, 2);
  rd(0, 0);
  static_for<SC::cum(-1), SC::cum(0)>([&](auto n) { part(n.value >> 1, n.value & 1); });
  wt(0);
  static_for<0, NT>([&](auto tc) {
    constexpr int t = tc.value, kc = t >> 1, ks = t & 1, cur = t & 1, nxt = cur ^ 1;
    if constexpr (ks == 1 && kc + 1 < NK) {
      static_assert(SC::cum(t) >= (kc + 2) * 2, "schedule");
      wait_vm<(SC::cum(t) - (kc + 2) * 2) * 2 + (((CPF & 1) && t > CW1) ? 1 : 0)>();
      BAR();
      TSTAMP(0, 3 + kc);
    }
    if constexpr (t + 1 < NT) rd(t + 1, nxt);
    static_for<SC::cum(t), SC::cum(t + 1)>([&](auto n) { part(n.value >> 1, n.value & 1); });
    if constexpr ((CPF & 1) && t == CW1) {
      static_assert(SC::cum(t + 1) == NK * 2, "issued after all operand loads");
      code_warm<true>(wid, lane, lds0 + NST * STAGE + 2560 + wid * 256, baseA);
    }
    SCHED();
    {
      const bf16x8 b0 = cat8(bl[cur][0], bh[cur][0]), b1 = cat8(bl[cur][1], bh[cur][1]),
                   b2 = cat8(bl[cur][2], bh[cur][2]);
#pragma unroll
      for (int mi = 0; mi < 2; ++mi) {
        acc[mi][0] = mfma16(b0, af[cur][mi], acc[mi][0]);
        acc[mi][1] = mfma16(b1, af[cur][mi], acc[mi][1]);
        acc[mi][2] = mfma16(b2, af[cur][mi], acc[mi][2]);
      }
      s1 = mfma16(ones, af[cur][0], s1);
      s2 = mfma16(af[cur][0], af[cur][0], s2);
      if ((t & 3) == wm) {
        cs[0] = mfma16(b0, ones, cs[0]);
        cs[1] = mfma16(b1, ones, cs[1]);
        cs[2] = mfma16(b2, ones, cs[2]);
      }
    }
    SCHED();
    if constexpr (t + 1 < NT) wt(nxt);
  });
  TSTAMP(0, 20);

  float* st_ = (float*)(smem + NST * STAGE);
  float* csum = st_ + 256;
  {
    const float sq = (l15 & 2) ? ((l15 & 1) ? s2[3] : s2[2]) : ((l15 & 1) ? s2[1] : s2[0]);
    const f32x2 ms = ln_stats(s1[0], sq);
    if (gq == (l15 >> 2)) *(f32x2*)(st_ + (wm * 32 + wn * 16 + l15) * 2) = ms;
    if (l15 == 0) {
#pragma unroll
      for (int j = 0; j < 3; ++j) *(f32x4*)(csum + wm * 96 + (3 * wn + j) * 16 + gq * 4) = cs[j];
    }
  }
  __syncthreads();
  TSTAMP(0, 21);
  const int tile = pm * 4 + wm;
  const float qscale = 0.25f * 1.4426950408889634f;
  f32x2 ms[2];
#pragma unroll
  for (int mi = 0; mi < 2; ++mi) ms[mi] = *(const f32x2*)(st_ + (wm * 32 + (mi ^ wn) * 16 + l15) * 2);
  const int loq = (gq >> 1) * 256 + l15 * 8 + (gq & 1) * 4, lov = l15 * 16 + gq * 4;
#pragma unroll
  for (int j = 0; j < 3; ++j) {
    f32x4 c4 = z4;
#pragma unroll
    for (int p = 0; p < 4; ++p) c4 += *(const f32x4*)(csum + p * 96 + (3 * wn + j) * 16 + gq * 4);
    const int blk = pn * 6 + wn * 3 + j;
    const int typ = blk >> 5, h = blk & 31;
    const float sc = typ == 0 ? qscale : 1.0f;
    bf16* db = QKV + ((size_t)typ << 20) + (size_t)(h * 64 + tile) * 512 + (typ == 2 ? lov : loq);
    const int tfs = typ == 2 ? 256 : 128;
#pragma unroll
    for (int mi = 0; mi < 2; ++mi) {
      const float rs = ms[mi][1] * sc;
      bf16x4 o;
#pragma unroll
      for (int r = 0; r < 4; ++r) o[r] = (bf16)((acc[mi][j][r] - ms[mi][0] * c4[r]) * rs);
      st8<(WT & 1) != 0>(db + (mi ^ wn) * tfs, __builtin_bit_cast(u32x2, o));
    }
  }
  TSTAMP(0, 22);
  TFLUSH(0);
}

template <bool FULL, int NPF>
DEV void attn_body(unsigned kaddr, unsigned vaddr, bf16x8 qf, f32x16 cm0, f32x16 cm8, int wid, bf16* dst, unsigned lds0 TARG) {
  const float NEG = -1e30f;
  f32x16 z16;
#pragma unroll
  for (int r = 0; r < 16; ++r) z16[r] = 0.f;
  bf16x8 kf[9];
#pragma unroll
  for (int t = 0; t < 9; ++t)
    if (FULL || wid + t >= 8) DSR128(kf[t], kaddr, t * 1024);
  asm volatile("s_waitcnt lgkmcnt(0)" ::: "memory");
#pragma unroll
  for (int t = 0; t < 9; ++t) PIN(kf[t]);
  TSTAMP(1, 3);
  float m0 = NEG, m1 = NEG;
#pragma unroll
  for (int t = 0; t < 9; ++t)
    if (FULL || wid + t >= 8) {
      const f32x16 s = mfma32(kf[t], qf, t == 0 ? cm0 : (t == 8 ? cm8 : z16));
#pragma unroll
      for (int r = 0; r < 16; r += 4) {
        m0 = fmaxf(fmaxf(m0, s[r]), s[r + 1]);
        m1 = fmaxf(fmaxf(m1, s[r + 2]), s[r + 3]);
      }
    }
  float m = fmaxf(m0, m1);
  m = fmaxf(m, __shfl_xor(m, 32));
  f32x16 nm;
#pragma unroll
  for (int r = 0; r < 16; ++r) { nm[r] = -m; cm0[r] -= m; cm8[r] -= m; }
  TSTAMP(1, 4);
  wait_vm<NPF>();
  BAR();
  TSTAMP(1, 5);
  if (CPF & 2) code_warm<false>(wid, (int)(threadIdx.x & 63), lds0 + 59392 + wid * 256, nullptr);
  f32x16 oa = z16, ob = z16;
  f32x16 sc_, sn_;
  bf16x4 v0l[2], v0h[2], v1l[2], v1h[2];
  auto vrd = [&](int t, int b) {
    DSRTR(v0l[b], vaddr, t * 1024);
    DSRTR(v0h[b], vaddr, t * 1024 + 256);
    DSRTR(v1l[b], vaddr, t * 1024 + 512);
    DSRTR(v1h[b], vaddr, t * 1024 + 768);
  };
  if (FULL) { sc_ = mfma32(kf[0], qf, cm0); vrd(0, 0); }
  else sc_ = z16;
  sn_ = z16;
#pragma unroll
  for (int t = 0; t < 9; ++t) {
    const int b = t & 1;
    const bool cur_ok = FULL || wid + t >= 8, nxt_ok = t < 8 && (FULL || wid + t + 1 >= 8);
    if (cur_ok) {
      asm volatile("s_waitcnt lgkmcnt(0)" ::: "memory");
      PIN(v0l[b]); PIN(v0h[b]); PIN(v1l[b]); PIN(v1h[b]);
    }
    if (nxt_ok) {
      vrd(t + 1, b ^ 1);
      sn_ = mfma32(kf[t + 1], qf, t + 1 == 8 ? cm8 : nm);
    }
    SCHED();
    if (cur_ok) {
      bf16x8 p0, p1;
#pragma unroll
      for (int j = 0; j < 8; ++j) p0[j] = (bf16)__builtin_amdgcn_exp2f(sc_[j]);
      SCHED();
      oa = mfma32(cat8(v0l[b], v0h[b]), p0, oa);
      SCHED();
#pragma unroll
      for (int j = 0; j < 8; ++j) p1[j] = (bf16)__builtin_amdgcn_exp2f(sc_[8 + j]);
      SCHED();
      ob = mfma32(cat8(v1l[b], v1h[b]), p1, ob);
      SCHED();
    }
    sc_ = sn_;
  }
  TSTAMP(1, 6);
  const float inv = 1.0f / (oa[8] + ob[8]);
  unsigned a0 = pk2((oa[0] + ob[0]) * inv, (oa[1] + ob[1]) * inv), a1 = pk2((oa[2] + ob[2]) * inv, (oa[3] + ob[3]) * inv);
  unsigned b0 = pk2((oa[4] + ob[4]) * inv, (oa[5] + ob[5]) * inv), b1 = pk2((oa[6] + ob[6]) * inv, (oa[7] + ob[7]) * inv);
  swap32(a0, b0);
  swap32(a1, b1);
  st16<(WT & 2) != 0>(dst, u32x4{a0, a1, b0, b1});
}

extern "C" __global__ __launch_bounds__(512, 2) void k2_attn(const bf16* __restrict__ QKV, bf16* __restrict__ O,
                                                  const bf16* __restrict__ wo, const bf16* __restrict__ w1,
                                                  const bf16* __restrict__ w3, const bf16* __restrict__ w2,
                                                  bf16* __restrict__ Wt, float* __restrict__ CS,
                                                  bf16* __restrict__ W2t, bf16* __restrict__ Wot) {
  constexpr int LV = 16384, LONE = 32768, LSJ = 43008;
  extern __shared__ __attribute__((aligned(16))) char smem[];
  TDECL
  TSTAMP(1, 0);
  const int tid = threadIdx.x, lane = tid & 63;
  const int wid = __builtin_amdgcn_readfirstlane(tid >> 6);
  const int l15 = lane & 15, gq = lane >> 4;
  const int xg = blockIdx.x >> 5, h = (blockIdx.x & 7) * 4 + ((blockIdx.x >> 3) & 3);
  const unsigned lds0 = (unsigned)(uintptr_t)(LDSP(char))smem;
  const int t0 = xg * 8 - 8;
  const unsigned l16 = lane * 16;
  asm volatile("" : "+s"(O), "+s"(Wt), "+s"(CS), "+s"(W2t), "+s"(Wot));
  asm volatile("" : "+s"(wo), "+s"(w1), "+s"(w3), "+s"(w2));
  const bool ld = xg > 0 || wid >= 4;

  const char* kb = (const char*)QKV + (2u << 20) + (ptrdiff_t)(h * 64 + t0 + 2 * wid) * 1024;
  if (ld) {
    glds(l16, kb, lds0 + wid * 2048);
    glds(l16, kb + 1024, lds0 + wid * 2048 + 1024);
  }
  bf16x8 qf;
  GLD4(qf, l16, (const char*)QKV + (size_t)(h * 64 + xg * 8 + wid) * 1024, 0);
  if (ld) {
    glds(l16, kb + (2u << 20), lds0 + LV + wid * 2048);
    glds(l16, kb + (2u << 20) + 1024, lds0 + LV + wid * 2048 + 1024);
  }
  {
    const u32x4 o4 = {0x3f803f80u, 0x3f803f80u, 0x3f803f80u, 0x3f803f80u};
    *(u32x4*)(smem + LONE + tid * 16) = o4;
    if (tid < 128) *(u32x4*)(smem + LONE + 8192 + tid * 16) = o4;
  }
  f32x16 cm0, cm8;
  {
    const int c = lane & 31, hh = lane >> 5;
#pragma unroll
    for (int r = 0; r < 16; ++r) {
      const int kr = (r & 3) + 8 * (r >> 2) + 4 * hh;
      cm0[r] = (kr > c) ? 0.f : -1e30f;
      cm8[r] = (kr <= c) ? 0.f : -1e30f;
    }
    PIN(cm0); PIN(cm8);
  }
  TSTAMP(1, 1);
  if (ld) asm volatile("s_waitcnt vmcnt(2) lgkmcnt(0)" ::: "memory");
  else asm volatile("s_waitcnt vmcnt(0) lgkmcnt(0)" ::: "memory");
  PIN(qf);
  BAR();
  TSTAMP(1, 2);
  const int skc = blockIdx.x >> 5, smt = (blockIdx.x >> 4) & 1, spn = blockIdx.x & 15;
  u32x4 wr0, wr1;
  {
    const int wj = wid < 7 ? wid : 6;
    const char* sb = (const char*)(smt ? w3 : w1) + (size_t)(skc * 64) * (F_ * 2) + (spn * 112 + wj * 16) * 2;
    const unsigned vo = (unsigned)((lane >> 1) * (F_ * 2) + (lane & 1) * 16);
    glds(vo, sb, lds0 + LSJ + wid * 2048);
    glds(vo, sb + 32 * (F_ * 2), lds0 + LSJ + wid * 2048 + 1024);
    const char* o = (const char*)wo + (size_t)blockIdx.x * 2048;
    const char* r0 = wid < 7 ? (const char*)w2 + (size_t)blockIdx.x * 7168 + wid * 1024 : o;
    GLD4(wr0, l16, r0, 0);
    GLD4(wr1, l16, o, 1024);
  }

  const unsigned kaddr = lds0 + wid * 1024 + l16;
  const unsigned vlane = (unsigned)(((lane >> 5) * 4 + (l15 >> 2)) * 32 + (l15 & 3) * 8);
  const unsigned vaddr = (lane & 16) ? (lds0 + LONE + (vlane ^ 128u)) : (lds0 + LV + wid * 1024 + vlane);
  bf16* dst = O + (size_t)(((xg * 4 + (wid >> 1)) * H_ + h) * 64 + (wid & 1) * 32 + (lane & 31)) * 16 + (lane >> 5) * 8;
  if (xg > 0) attn_body<true, 4>(kaddr, vaddr, qf, cm0, cm8, wid, dst, lds0 TPASS);
  else attn_body<false, 4>(kaddr, vaddr, qf, cm0, cm8, wid, dst, lds0 TPASS);
  TSTAMP(1, 20);
  wait_vm<1>();
  {
    const unsigned co = (unsigned)(lane & 7) * 16;
    PIN(wr0); PIN(wr1);
    if (wid < 7) {
      st16<(WT & 2) != 0>((char*)W2t + (size_t)(lane >> 3) * (F_ * 128) + (blockIdx.x * 7 + wid) * 128 + co, wr0);
    } else {
      char* o = (char*)Wot + (size_t)(lane >> 3) * (D_ * 128) + blockIdx.x * 256 + co;
      st16<(WT & 2) != 0>(o, wr0);
      st16<(WT & 2) != 0>(o + 128, wr1);
    }
  }
  if (wid < 7) {
    const unsigned ra = lds0 + LSJ + wid * 2048 + (gq * 8 + (l15 >> 2)) * 32 + (l15 & 3) * 8;
    bf16x4 f[4];
    DSRTR(f[0], ra, 0);
    DSRTR(f[1], ra, 128);
    DSRTR(f[2], ra, 1024);
    DSRTR(f[3], ra, 1024 + 128);
    asm volatile("s_waitcnt lgkmcnt(0)" ::: "memory");
    float c = 0.f;
#pragma unroll
    for (int i = 0; i < 4; ++i) {
      PIN(f[i]);
#pragma unroll
      for (int e = 0; e < 4; ++e) c += (float)f[i][e];
    }
    char* wd = (char*)Wt + (size_t)(((spn * 8 + skc) * 4 + smt) * 7 + wid) * 1024 + l16;
    st16<(WT & 2) != 0>(wd, __builtin_bit_cast(u32x4, cat8(f[0], f[1])));
    st16<(WT & 2) != 0>(wd + 14 * 1024, __builtin_bit_cast(u32x4, cat8(f[2], f[3])));
    c += __shfl_xor(c, 16);
    c += __shfl_xor(c, 32);
    if (lane < 16) st4<(WT & 2) != 0>(CS + skc * (2 * F_) + smt * F_ + spn * 112 + wid * 16 + lane, __builtin_bit_cast(unsigned, c));
  }
  TSTAMP(1, 22);
  wait_vm<0>();
  TFLUSH(1);
}

template <int MODE, int KTOT>
DEV void gemm_body(const bf16* __restrict__ A, const bf16* __restrict__ B, const bf16* __restrict__ R,
                   bf16* __restrict__ O, float* __restrict__ RS, const bf16* __restrict__ PFW) {
  constexpr int NST = G_NST, STAGE = 32768, NK = KTOT / 128, NT = NK * 4;
  extern __shared__ __attribute__((aligned(16))) char smem[];
  TDECL
  TSTAMP(MODE == 0 ? 2 : 4, 0);
  const int tid = threadIdx.x, lane = tid & 63;
  const int wid = __builtin_amdgcn_readfirstlane(tid >> 6);
  const int wm = wid & 3, wn = wid >> 2;
  const int l15 = lane & 15, gq = lane >> 4;
  const int xg = blockIdx.x & 7, idx = blockIdx.x >> 3;
#define PNX 0
  const int pm = xg * 4 + (idx >> 3), pn = (idx & 7) ^ PNX;
  if (MODE == 0) asm volatile("" : "+s"(RS), "+s"(PFW));
  const unsigned lds0 = (unsigned)(uintptr_t)(LDSP(char))smem;

  u32x4 res;
  const int tok = pm * 64 + wm * 16 + l15;
  const int ncol = pn * 64 + wn * 32 + (gq & 1) * 16 + (gq >> 1) * 8;
  const unsigned hoff = (unsigned)(((((tok >> 7) * 8 + pn) * 128 + (tok & 127)) * 64 + (ncol & 63)) * 2);
  GLD4(res, MODE == 0 ? (unsigned)((tok * D_ + ncol) * 2) : hoff, R, 0);

  const unsigned voffA = (unsigned)(wid * 1024 + lane * 16);
  const int pB = ((lane & 7) >> 1) ^ (((lane >> 4) & 1) | ((wid & 1) << 1));
  const unsigned voffB = (unsigned)((wid * 8 + (lane >> 3)) * 128 + pB * 32 + (lane & 1) * 16);
  const char* baseA = (const char*)A + (size_t)pm * (KTOT * 128);
  const char* baseB = (const char*)B + pn * (KTOT * 128);
  const unsigned sbw = lds0 + wid * 1024;
  const int rot = MODE == 0 ? ROT3 * ((pn + (idx >> 3)) & 3) : ROT5 * ((pn * 2 + (idx >> 3) * 3) % NK);
  auto part = [&](int kc, int p) {
    const unsigned sb = sbw + (kc % NST) * STAGE;
    int kk = kc + rot;
    if (kk >= NK) kk -= NK;
    if (p == 0) glds(voffA, baseA + (size_t)kk * 16384, sb);
    if (p == 1) glds(voffA, baseA + (size_t)kk * 16384 + 8192, sb + 8192);
    if (p == 2) glds(voffB, baseB + (size_t)kk * 16384, sb + 16384);
    if (p == 3) glds(voffB, baseB + (size_t)kk * 16384 + 8192, sb + 16384 + 8192);
  };
  auto issue = [&](int kc) { part(kc, 0); part(kc, 1); part(kc, 2); part(kc, 3); };
  issue(0);
  BAR();
  issue(1);
  BAR();
  issue(2);
  TSTAMP(MODE == 0 ? 2 : 4, 1);

  unsigned a_addr[4], b_addr[2];
#pragma unroll
  for (int ks = 0; ks < 4; ++ks) a_addr[ks] = lds0 + (2 * ks + (gq >> 1)) * 2048 + (wm * 16 + l15) * 32 + (gq & 1) * 16;
  {
    const int kl = gq * 8 + (l15 >> 2);
    const int fr = ((l15 >> 3) & 1) | ((gq & 1) << 1);
#pragma unroll
    for (int j = 0; j < 2; ++j) b_addr[j] = lds0 + 16384 + kl * 128 + (((2 * wn + j) ^ fr) * 32) + (l15 & 3) * 8;
  }

  f32x4 acc[2];
  const f32x4 z4 = {0.f, 0.f, 0.f, 0.f};
  acc[0] = z4; acc[1] = z4;
  bf16x8 af[2];
  bf16x4 bl[2][2], bh[2][2];

  auto rd = [&](int t, int set) {
    const int kc = t >> 2, ks = t & 3, st = kc % NST;
    const unsigned hi = (unsigned)((st & ~1) * STAGE);
    const int so = (st & 1) * STAGE;
    DSRTR(bl[set][0], b_addr[0] + hi, so + ks * 4096);
    DSRTR(bh[set][0], b_addr[0] + hi, so + ks * 4096 + 512);
    DSR128(af[set], a_addr[ks] + hi, so);
    DSRTR(bl[set][1], b_addr[1] + hi, so + ks * 4096);
    DSRTR(bh[set][1], b_addr[1] + hi, so + ks * 4096 + 512);
  };
  auto wt = [&](int set) {
    asm volatile("s_waitcnt lgkmcnt(0)" ::: "memory");
    PIN(af[set]);
    PIN(bl[set][0]); PIN(bh[set][0]); PIN(bl[set][1]); PIN(bh[set][1]);
  };

  using SC = Sched<4, 4, NST, NK, 12, G_R>;
  wait_vm<SC::cum(-1) - 4>();
  BAR();
  TSTAMP(MODE == 0 ? 2 : 4, 2);
  rd(0, 0);
  static_for<SC::cum(-1), SC::cum(0)>([&](auto n) { part(n.value >> 2, n.value & 3); });
  wt(0);
  static_for<0, NT>([&](auto tc) {
    constexpr int t = tc.value, kc = t >> 2, ks = t & 3, cur = t & 1, nxt = cur ^ 1;
    if constexpr (ks == 3 && kc + 1 < NK) {
      static_assert(SC::cum(t) >= (kc + 2) * 4, "schedule");
      constexpr int npf = (MODE == 0 && PF3 && t > PF3T) ? (t - PF3T > 4 ? 4 : t - PF3T) : 0;
      static_assert(npf == 0 || SC::cum(PF3T) == NK * 4, "warm-up loads come after all operand loads");
      wait_vm<SC::cum(t) - (kc + 2) * 4 + npf + ((MODE == 0 && (CPF & 4) && t > CW3) ? 1 : 0)>();
      BAR();
      TSTAMP(MODE == 0 ? 2 : 4, 3 + kc);
    }
    if constexpr (t + 1 < NT) rd(t + 1, nxt);
    static_for<SC::cum(t), SC::cum(t + 1)>([&](auto n) { part(n.value >> 2, n.value & 3); });
    if constexpr (MODE == 0 && (CPF & 4) && t == CW3) {
      static_assert(SC::cum(t + 1) == NK * 4, "issued after all operand loads");
      code_warm<true>(wid, lane, lds0 + NST * STAGE + wid * 1024, baseA);
    }
    if constexpr (MODE == 0 && PF3 && t >= PF3T && t < PF3T + 4) {
      glds((unsigned)(lane * 16),
           (const char*)PFW + (size_t)(xg >> 1) * (4 * 229376) + idx * 28672 + (t - PF3T) * 8192 + (t - PF3T < 3 ? wid : (wid & 3)) * 1024,
           lds0 + NST * STAGE + wid * 1024);
    }
    SCHED();
    acc[0] = mfma16(cat8(bl[cur][0], bh[cur][0]), af[cur], acc[0]);
    acc[1] = mfma16(cat8(bl[cur][1], bh[cur][1]), af[cur], acc[1]);
    SCHED();
    if constexpr (t + 1 < NT) wt(nxt);
  });
  TSTAMP(MODE == 0 ? 2 : 4, 20);
  PIN(res);
  float s = 0.f, q = 0.f;
  {
#pragma unroll
    for (int r = 0; r < 4; ++r) { float a = acc[0][r], b = acc[1][r]; swap16f(a, b); acc[0][r] = a; acc[1][r] = b; }
    const bf16x8 rr = __builtin_bit_cast(bf16x8, res);
    bf16x8 o;
#pragma unroll
    for (int j = 0; j < 2; ++j)
#pragma unroll
      for (int r = 0; r < 4; ++r) {
        o[j * 4 + r] = (bf16)((float)rr[j * 4 + r] + bfr(acc[j][r]));
        const float v = (float)o[j * 4 + r];
        s += v;
        q += v * v;
      }
    st16<(WT & (MODE == 0 ? 4 : 16)) != 0>((char*)O + (MODE == 0 ? hoff : (unsigned)((tok * D_ + ncol) * 2)), __builtin_bit_cast(u32x4, o));
  }
  if (MODE == 0) {
    s += __shfl_xor(s, 16); s += __shfl_xor(s, 32);
    q += __shfl_xor(q, 16); q += __shfl_xor(q, 32);
    if (gq == 0) st8<(WT & 4) != 0>(RS + ((size_t)(pn * 2 + wn) * S_ + tok) * 2, __builtin_bit_cast(u32x2, f32x2{s, q}));
  }
  if (MODE == 0) wait_vm<0>();
  TSTAMP(MODE == 0 ? 2 : 4, 22);
  TFLUSH(MODE == 0 ? 2 : 4);
}
extern "C" __global__ __launch_bounds__(512, 2) void k3_wo(const bf16* __restrict__ A, const bf16* __restrict__ B,
                                                           const bf16* __restrict__ R, bf16* __restrict__ O,
                                                           float* __restrict__ RS, const bf16* __restrict__ PFW) {
  gemm_body<0, 512>(A, B, R, O, RS, PFW);
}
extern "C" __global__ __launch_bounds__(512, 2) void k5_down(const bf16* __restrict__ A, const bf16* __restrict__ B,
                                                             const bf16* __restrict__ R, bf16* __restrict__ O) {
  gemm_body<1, 1792>(A, B, R, O, nullptr, nullptr);
}

template <int NP, int P0>
DEV void gu_body(char* smem, unsigned lds0, int tid, int wid, int pm, int pn, const bf16* __restrict__ Hh,
                 const bf16* __restrict__ Wt, const float* __restrict__ RS, const float* __restrict__ CS,
                 bf16* __restrict__ Hm, const char* pf TARG) {
  constexpr int NST = 3, STAGE = 45056, NK = 8, LPC = 2 + NP, NT = 16;
  const int lane = tid & 63, wm = wid & 3;
  const int l15 = lane & 15, gq = lane >> 4;
  const unsigned l16 = lane * 16;
  const unsigned voffA = (unsigned)((wid * 8 + (lane >> 3)) * 128 + (((lane & 7) ^ ((wid & 1) * 4 + gq)) * 16));
  const char* baseA = (const char*)Hh + (size_t)pm * (128 * 1024);
  const int pc0 = (P0 == 0 ? 0 : 16) + wm * NP;
  const char* baseW = (const char*)Wt + (size_t)pn * (8 * 28672) + pc0 * 1024;
  const int rot = ROT4 * (pm + 2 * pn);
  auto piece = [&](int kc, int i) {
    const unsigned sb = lds0 + (kc % NST) * STAGE;
    const int kk = (kc + rot) & 7;
    if (i == 0) glds(voffA, baseA + kk * 16384, sb + wid * 1024);
    else if (i == 1) glds(voffA, baseA + kk * 16384 + 8192, sb + 8192 + wid * 1024);
    else glds(l16, baseW + kk * 28672 + (i - 2) * 1024, sb + 16384 + (pc0 + i - 2) * 1024);
  };
  auto issue = [&](int kc) {
#pragma unroll
    for (int i = 0; i < LPC; ++i) piece(kc, i);
  };
  float* st_ = (float*)(smem + NST * STAGE);
  float* csum = st_ + 256;
  if (P0 == 0) {
    if (wid < 2) {
      f32x2 sv[16];
      const char* rb = (const char*)RS + pm * 1024;
      const unsigned vo = (unsigned)(tid * 8);
#pragma unroll
      for (int i = 0; i < 16; ++i) GLD2(sv[i], vo, rb + i * (S_ * 8), 0);
      issue(0);
      BAR();
      issue(1);
      wait_vm<2 * LPC>();
#pragma unroll
      for (int i = 0; i < 16; ++i) PIN(sv[i]);
      f32x2 a = sv[0];
#pragma unroll
      for (int i = 1; i < 16; ++i) a += sv[i];
      *(f32x2*)(st_ + tid * 2) = ln_stats(a[0], a[1]);
    } else {
      issue(0);
      BAR();
      issue(1);
    }
  } else {
    float sc[8];
    int c = tid - 256;
    const bool ok = c < 224;
    if (!ok) c = 0;
    const unsigned vo = (unsigned)(((c < 112 ? 0 : F_ - 112) + pn * 112 + c) * 4);
#pragma unroll
    for (int i = 0; i < 8; ++i) GLD1(sc[i], vo, (const char*)CS + i * (2 * F_ * 4), 0);
    issue(0);
    BAR();
    issue(1);
    wait_vm<2 * LPC>();
#pragma unroll
    for (int i = 0; i < 8; ++i) PIN(sc[i]);
    float a = sc[0];
#pragma unroll
    for (int i = 1; i < 8; ++i) a += sc[i];
    if (ok) csum[c] = a;
  }
  TSTAMP(3, 1);

  unsigned a_addr[2];
#pragma unroll
  for (int ks = 0; ks < 2; ++ks) a_addr[ks] = lds0 + (wm * 32 + l15) * 128 + (((4 * ks + gq) ^ (l15 >> 1)) * 16);
  const unsigned b_addr = lds0 + 16384 + P0 * 1024 + l16;
  f32x4 ag[2][NP], au[2][NP];
  const f32x4 z4 = {0.f, 0.f, 0.f, 0.f};
#pragma unroll
  for (int j = 0; j < NP; ++j) { ag[0][j] = z4; ag[1][j] = z4; au[0][j] = z4; au[1][j] = z4; }
  bf16x8 af[2][2], bg[2][NP], bu[2][NP];
  auto rd = [&](int t, int set) {
    const int kc = t >> 1, ks = t & 1, st = kc % NST;
    const unsigned so = st * STAGE;
    DSR128(af[set][0], a_addr[ks] + so, 0);
    DSR128(af[set][1], a_addr[ks] + so, 2048);
#pragma unroll
    for (int j = 0; j < NP; ++j) {
      DSR128(bg[set][j], b_addr + so, (ks * 14 + j) * 1024);
      DSR128(bu[set][j], b_addr + so, (ks * 14 + 7 + j) * 1024);
    }
  };
  auto wt = [&](int set) {
    asm volatile("s_waitcnt lgkmcnt(0)" ::: "memory");
    PIN(af[set][0]); PIN(af[set][1]);
#pragma unroll
    for (int j = 0; j < NP; ++j) { PIN(bg[set][j]); PIN(bu[set][j]); }
  };
  asm volatile("s_waitcnt vmcnt(%0) lgkmcnt(0)" ::"n"(LPC) : "memory");
  BAR();
  TSTAMP(3, 2);
  rd(0, 0);
  piece(NST - 1, 0); piece(NST - 1, 1); piece(NST - 1, 2);
  wt(0);
#pragma unroll
  for (int t = 0; t < NT; ++t) {
    const int kc = t >> 1, ks = t & 1, cur = t & 1, nxt = cur ^ 1;
    if (ks == 1 && kc + 1 < NK) {
      const int last = (kc + NST - 1 < NK - 1) ? (kc + NST - 1) : (NK - 1);
      wait_vm_n((last - (kc + 1)) * LPC + ((PF4 && t > 11) ? (t - 11) * 2 : 0));
      BAR();
      TSTAMP(3, 3 + kc);
    }
    if (t + 1 < NT) rd(t + 1, nxt);
    const int ic = (t + 1) / 2 + NST - 1, ip = ((t + 1) & 1) * 3;
    SCHED();
#pragma unroll
    for (int j = 0; j < NP; ++j) {
      ag[0][j] = mfma16(bg[cur][j], af[cur][0], ag[0][j]);
      ag[1][j] = mfma16(bg[cur][j], af[cur][1], ag[1][j]);
      au[0][j] = mfma16(bu[cur][j], af[cur][0], au[0][j]);
      au[1][j] = mfma16(bu[cur][j], af[cur][1], au[1][j]);
      SCHED();
      if (j < 3 && ic < NK && ip + j < LPC) piece(ic, ip + j);
      if (PF4 && j < 2 && t >= 11 && (t - 11) * 2 + j < 7)
        glds(l16, pf + ((t - 11) * 2 + j) * 8192, lds0 + NST * STAGE + 2048 + wid * 1024);
      if ((CPF & 8) && t == CW4 && j == 1) code_warm<false>(wid, lane, lds0 + NST * STAGE + 2048 + wid * 1024, nullptr);
      SCHED();
    }
    if (t + 1 < NT) wt(nxt);
  }
  TSTAMP(3, 20);
  bf16* dst0 = Hm + ((size_t)((pm * 2 + (wm >> 1)) * 112 + pn * 7 + P0) * 64 + (wm & 1) * 32 + l15) * 16;
#pragma unroll
  for (int mi = 0; mi < 2; ++mi) {
    const f32x2 ms = *(const f32x2*)(st_ + (wm * 32 + mi * 16 + l15) * 2);
    const f32x2 nm = {-ms[0], -ms[0]};
    const float le = ms[1] * -1.4426950408889634f, iv = 1.0f / (ms[1] * ms[1]);
    const f32x2 le2 = {le, le}, iv2 = {iv, iv};
    unsigned pk[NP][2];
#pragma unroll
    for (int j = 0; j < NP; ++j) {
      const f32x4 cg = *(const f32x4*)(csum + (P0 + j) * 16 + gq * 4);
      const f32x4 cu = *(const f32x4*)(csum + 112 + (P0 + j) * 16 + gq * 4);
#pragma unroll
      for (int hf = 0; hf < 2; ++hf) {
        const f32x2 g = __builtin_elementwise_fma(f32x2{cg[2 * hf], cg[2 * hf + 1]}, nm,
                                                  f32x2{ag[mi][j][2 * hf], ag[mi][j][2 * hf + 1]});
        const f32x2 u = __builtin_elementwise_fma(f32x2{cu[2 * hf], cu[2 * hf + 1]}, nm,
                                                  f32x2{au[mi][j][2 * hf], au[mi][j][2 * hf + 1]});
        const f32x2 ea = g * le2;
        const f32x2 e = {__builtin_amdgcn_exp2f(ea[0]), __builtin_amdgcn_exp2f(ea[1])};
        const f32x2 d = __builtin_elementwise_fma(e, iv2, iv2);
        const f32x2 rc = {__builtin_amdgcn_rcpf(d[0]), __builtin_amdgcn_rcpf(d[1])};
        const f32x2 o = g * u * rc;
        pk[j][hf] = pk2(o[0], o[1]);
      }
    }
#pragma unroll
    for (int j = 0; j + 1 < NP; j += 2) {
      swap16(pk[j][0], pk[j + 1][0]);
      swap16(pk[j][1], pk[j + 1][1]);
      st16<(WT & 8) != 0>(dst0 + ((size_t)(j + (gq & 1)) * 64 + mi * 16) * 16 + (gq >> 1) * 8,
                          u32x4{pk[j][0], pk[j][1], pk[j + 1][0], pk[j + 1][1]});
    }
    if (NP & 1) st8<(WT & 8) != 0>(dst0 + ((size_t)(NP - 1) * 64 + mi * 16) * 16 + gq * 4, u32x2{pk[NP - 1][0], pk[NP - 1][1]});
  }
}

extern "C" __global__ __launch_bounds__(512, 2) void k4_gu(const bf16* __restrict__ Hh, const bf16* __restrict__ Wt,
                                                const float* __restrict__ RS, const float* __restrict__ CS,
                                                bf16* __restrict__ Hm, const bf16* __restrict__ W2t) {
  extern __shared__ __attribute__((aligned(16))) char smem[];
  TDECL
  TSTAMP(3, 0);
  const int tid = threadIdx.x;
  const int wid = __builtin_amdgcn_readfirstlane(tid >> 6);
  const int xg = blockIdx.x & 7, idx = blockIdx.x >> 3;
  const int pm = (xg & 1) * 8 + (idx & 7), pn = (xg >> 1) * 4 + (idx >> 3);
  asm volatile("" : "+s"(RS), "+s"(CS), "+s"(Hm), "+s"(W2t));
  const unsigned lds0 = (unsigned)(uintptr_t)(LDSP(char))smem;
  const char* pf = (const char*)W2t + idx * 57344 + wid * 1024;
  if (wid < 4) gu_body<4, 0>(smem, lds0, tid, wid, pm, pn, Hh, Wt, RS, CS, Hm, pf TPASS);
  else gu_body<3, 4>(smem, lds0, tid, wid, pm, pn, Hh, Wt, RS, CS, Hm, pf TPASS);
  wait_vm<0>();
  TSTAMP(3, 22);
  TFLUSH(3);
  asm volatile("k4_gu_tail:");
}

extern "C" void kernel_launch(void* const* d_in, const int* in_sizes, int n_in, void* d_out, int out_size, void* d_ws,
                              size_t ws_size, hipStream_t stream) {
  const bf16* x = (const bf16*)d_in[0];
  const bf16* wqkv = (const bf16*)d_in[1];
  const bf16* wo = (const bf16*)d_in[2];
  const bf16* w1 = (const bf16*)d_in[4];
  const bf16* w3 = (const bf16*)d_in[5];
  const bf16* w2 = (const bf16*)d_in[6];
  bf16* out = (bf16*)d_out;

  char* ws = (char*)d_ws;
  bf16* QKV = (bf16*)(ws);
  bf16* Oa = (bf16*)(ws + (7u << 20));
  bf16* Hh = (bf16*)(ws + (9u << 20));
  bf16* Hm = (bf16*)(ws + (11u << 20));
  float* RS = (float*)(ws + (18u << 20));
  float* CS = (float*)(ws + (19u << 20));
  bf16* Wt = (bf16*)(ws + (20u << 20));
  bf16* W2t = (bf16*)(ws + (24u << 20));
  bf16* Wot = (bf16*)(ws + (26u << 20));

  {
    constexpr int lds = K1_NST * 28672 + 2560 + 2048;
    (void)hipFuncSetAttribute((const void*)k1_qkv, hipFuncAttributeMaxDynamicSharedMemorySize, lds);
    k1_qkv<<<256, 512, lds, stream>>>(x, wqkv, QKV);
  }
  {
    constexpr int lds = 59392 + 2048;
    (void)hipFuncSetAttribute((const void*)k2_attn, hipFuncAttributeMaxDynamicSharedMemorySize, lds);
    k2_attn<<<256, 512, lds, stream>>>(QKV, Oa, wo, w1, w3, w2, Wt, CS, W2t, Wot);
  }
  {
    auto k = k3_wo;
    constexpr int lds = G_NST * 32768 + 8192;
    (void)hipFuncSetAttribute((const void*)k, hipFuncAttributeMaxDynamicSharedMemorySize, lds);
    k<<<256, 512, lds, stream>>>(Oa, Wot, x, Hh, RS, Wt);
  }
  {
    constexpr int lds = 3 * 45056 + 2048 + 8192;
    (void)hipFuncSetAttribute((const void*)k4_gu, hipFuncAttributeMaxDynamicSharedMemorySize, lds);
    k4_gu<<<256, 512, lds, stream>>>(Hh, Wt, RS, CS, Hm, W2t);
  }
  {
    auto k = k5_down;
    constexpr int lds = G_NST * 32768;
    (void)hipFuncSetAttribute((const void*)k, hipFuncAttributeMaxDynamicSharedMemorySize, lds);
    k<<<256, 512, lds, stream>>>(Hm, W2t, Hh, out);
  }
}
```

```cpp
#include <hip/hip_runtime.h>
#include <stdint.h>

typedef __attribute__((ext_vector_type(8))) __bf16 bf16x8;
typedef __attribute__((ext_vector_type(4))) __bf16 bf16x4;
typedef __attribute__((ext_vector_type(2))) __bf16 bf16x2;
typedef __attribute__((ext_vector_type(2))) float f32x2;
typedef __attribute__((ext_vector_type(4))) float f32x4;
typedef __attribute__((ext_vector_type(16))) float f32x16;
typedef __attribute__((ext_vector_type(4))) unsigned int u32x4;
typedef __attribute__((ext_vector_type(2))) unsigned int u32x2;
typedef __bf16 bf16;

#define LDSP(T) __attribute__((address_space(3))) T*
#define DEV __device__ __forceinline__

#define K1_NST 4
#define G_NST 4
#define G_R 1
#define ROT1 0
#define ROT3 0
#define ROT4 0
#define ROT5 0
#define PF4 1
#define PF3 0
#define PF3T 4
constexpr int S_ = 2048, D_ = 512, H_ = 32, F_ = 1792;

DEV float bfr(float x) { return (float)(bf16)x; }

template <int N>
DEV void wait_vm() {
  asm volatile("s_waitcnt vmcnt(%0)" ::"n"(N) : "memory");
}
DEV void wait_vm_n(int n) {
  switch (n) {
#define WC(X) case X: wait_vm<X>(); break;
    WC(0) WC(1) WC(2) WC(3) WC(4) WC(5) WC(6) WC(7) WC(8) WC(9) WC(10) WC(11) WC(12) WC(13) WC(14) WC(15)
    WC(16) WC(17) WC(18) WC(19) WC(20) WC(21) WC(22) WC(23) WC(24)
#undef WC
    default: wait_vm<0>(); break;
  }
}

DEV void glds(unsigned voff, const void* sbase, unsigned ldsaddr) {
  ldsaddr = __builtin_amdgcn_readfirstlane(ldsaddr);
  asm volatile("s_mov_b32 m0, %2\n\ts_nop 0\n\tglobal_load_lds_dwordx4 %0, %1" ::"v"(voff), "s"(sbase), "s"(ldsaddr)
               : "memory");
}
#define DSR128(dst, addr, off) asm volatile("ds_read_b128 %0, %1 offset:%2" : "=v"(dst) : "v"(addr), "i"(off))
#define DSRTR(dst, addr, off) asm volatile("ds_read_b64_tr_b16 %0, %1 offset:%2" : "=v"(dst) : "v"(addr), "i"(off))
#define PIN(x) asm volatile("" : "+v"(x))
#define GLD4(dst, voff, sbase, off) \
  asm volatile("global_load_dwordx4 %0, %1, %2 offset:%3" : "=v"(dst) : "v"(voff), "s"(sbase), "i"(off))
#define GLD2(dst, voff, sbase, off) \
  asm volatile("global_load_dwordx2 %0, %1, %2 offset:%3" : "=v"(dst) : "v"(voff), "s"(sbase), "i"(off))
#define GLD1(dst, voff, sbase, off) \
  asm volatile("global_load_dword %0, %1, %2 offset:%3" : "=v"(dst) : "v"(voff), "s"(sbase), "i"(off))
#define WT 31
template <bool W>
DEV void st16(void* p, u32x4 v) {
  if constexpr (W) asm volatile("global_store_dwordx4 %0, %1, off sc1\n\ts_nop 1" ::"v"(p), "v"(v) : "memory");
  else *(u32x4*)p = v;
}
template <bool W>
DEV void st8(void* p, u32x2 v) {
  if constexpr (W) asm volatile("global_store_dwordx2 %0, %1, off sc1\n\ts_nop 1" ::"v"(p), "v"(v) : "memory");
  else *(u32x2*)p = v;
}
template <bool W>
DEV void st4(void* p, unsigned v) {
  if constexpr (W) asm volatile("global_store_dword %0, %1, off sc1" ::"v"(p), "v"(v) : "memory");
  else *(unsigned*)p = v;
}
#define BAR() __builtin_amdgcn_s_barrier()
#define SCHED() __builtin_amdgcn_sched_barrier(0)

#define TDECL
#define TSTAMP(m, i) do { } while (0)
#define TFLUSH(m) do { } while (0)
#define TARG
#define TPASS

#define CPF 15
#define CODE_ADDR(dst, sym)                                                                                  \
  asm volatile("s_getpc_b64 s[30:31]\n\ts_add_u32 s30, s30, " #sym "@rel32@lo+4\n\ts_addc_u32 s31, s31, " #sym \
               "@rel32@hi+12\n\ts_mov_b64 %0, s[30:31]"                                                      \
               : "=s"(dst)::"s30", "s31", "scc")
#define CPW 5
#define XPF 3
constexpr int CW1 = 8, CW3 = 3, CW4 = 14;
template <bool ALL>
DEV void code_warm(int wid, int lane, unsigned ldsaddr, const char* other) {
  const bool sel = wid == CPW && blockIdx.x < 64;
  if (!ALL && !sel) return;
  const char* a;
  int d;
  CODE_ADDR(a, k1_qkv);
  asm volatile("s_mov_b32 %0, k4_gu_tail-k1_qkv" : "=s"(d));
  const int last = d < 0 ? 0 : d >> 7;
  const int ln = sel ? (int)(blockIdx.x >> 3) * 64 + lane : 0;
  unsigned vo = (unsigned)((ln < last ? ln : last) * 128);
  if ((XPF & 1) && (blockIdx.x >> 3) == 6) {
    a = (const char*)((uintptr_t)__builtin_amdgcn_kernarg_segment_ptr() & ~(uintptr_t)4095);
    vo = (unsigned)((lane & 31) * 128);
  }
  if ((XPF & 2) && (blockIdx.x >> 3) == 7) {
    int d2;
    CODE_ADDR(a, k1_qkv.kd);
    asm volatile("s_mov_b32 %0, k4_gu.kd-k1_qkv.kd" : "=s"(d2));
    const int l2 = d2 < 0 ? 0 : d2 >> 6;
    vo = (unsigned)((lane < l2 ? lane : l2) * 64);
  }
  if (ALL && !sel) { a = other; vo = 0; }
  ldsaddr = __builtin_amdgcn_readfirstlane(ldsaddr);
  asm volatile("s_mov_b32 m0, %2\n\ts_nop 0\n\tglobal_load_lds_dword %0, %1" ::"v"(vo), "s"(a), "s"(ldsaddr) : "memory");
}

template <int V> struct IC { static constexpr int value = V; };
template <int I, int N, class F>
DEV void static_for(F&& f) {
  if constexpr (I < N) {
    f(IC<I>{});
    static_for<I + 1, N>(f);
  }
}
template <int P, int KS, int NST, int NK, int PRE, int R>
struct Sched {
  static constexpr int avail(int t) {
    int n = ((t + 1) / KS + NST - 1) * P + ((t + 1) % KS + 1) * (P / KS);
    return n > NK * P ? NK * P : n;
  }
  static constexpr int cum(int t) {
    int n = PRE;
    for (int i = -1; i < t; ++i) { int a = avail(i); n = (n + R < a) ? n + R : a; }
    return n;
  }
};

DEV void swap32(unsigned& a, unsigned& b) {
  const auto r = __builtin_amdgcn_permlane32_swap(a, b, false, false);
  a = r[0]; b = r[1];
}
DEV void swap16(unsigned& a, unsigned& b) {
  const auto r = __builtin_amdgcn_permlane16_swap(a, b, false, false);
  a = r[0]; b = r[1];
}
DEV void swap16f(float& a, float& b) {
  unsigned x = __builtin_bit_cast(unsigned, a), y = __builtin_bit_cast(unsigned, b);
  swap16(x, y);
  a = __builtin_bit_cast(float, x); b = __builtin_bit_cast(float, y);
}
DEV unsigned pk2(float a, float b) { return __builtin_bit_cast(unsigned, bf16x2{(bf16)a, (bf16)b}); }
DEV bf16x8 cat8(bf16x4 a, bf16x4 b) { return __builtin_shufflevector(a, b, 0, 1, 2, 3, 4, 5, 6, 7); }
DEV f32x4 mfma16(bf16x8 a, bf16x8 b, f32x4 c) { return __builtin_amdgcn_mfma_f32_16x16x32_bf16(a, b, c, 0, 0, 0); }
DEV f32x16 mfma32(bf16x8 a, bf16x8 b, f32x16 c) { return __builtin_amdgcn_mfma_f32_32x32x16_bf16(a, b, c, 0, 0, 0); }

DEV f32x2 ln_stats(float sum, float sq) {
  float m = sum * (1.0f / D_);
  float var = fmaxf(sq * (1.0f / D_) - m * m, 0.f);
  float vb = bfr(var);
  float sd = bfr(__builtin_sqrtf(bfr(vb + 1e-5f)));
  f32x2 r;
  r[0] = bfr(m);
  r[1] = 1.0f / sd;
  return r;
}

extern "C" __global__ __launch_bounds__(512, 2) void k1_qkv(const bf16* __restrict__ x, const bf16* __restrict__ w,
                                                            bf16* __restrict__ QKV) {
  constexpr int NST = K1_NST, STAGE = 28672, NK = 8, NT = 16;
  extern __shared__ __attribute__((aligned(16))) char smem[];
  TDECL
  TSTAMP(0, 0);
  const int tid = threadIdx.x, lane = tid & 63;
  const int wid = __builtin_amdgcn_readfirstlane(tid >> 6);
  const int wm = wid & 3, wn = wid >> 2;
  const int l15 = lane & 15, gq = lane >> 4;
  const int xg = blockIdx.x & 7, idx = blockIdx.x >> 3;
  const int pm = (xg & 3) * 4 + (idx & 3), pn = (xg >> 2) * 8 + (idx >> 2);
  const unsigned lds0 = (unsigned)(uintptr_t)(LDSP(char))smem;
  asm volatile("" : "+s"(QKV));

  const unsigned voffA = (unsigned)((wid * 8 + (lane >> 3)) * 1024 + (((lane & 7) ^ ((wid & 1) * 4 + gq)) * 16));
  const int r12 = (lane * 43) >> 9, c12 = lane - r12 * 12;
  const bool validB = lane < 48;
  const unsigned voffB = (unsigned)((wid * 8 + r12) * 3072 + (((c12 >> 1) ^ (wid & 1)) * 16 + (c12 & 1) * 8) * 2);
  const char* baseA = (const char*)x + (size_t)pm * (128 * 1024);
  const char* baseB = (const char*)w + pn * 192;
  const int rot = ROT1 * ((idx >> 2) + 2 * (idx & 3));
  auto part = [&](int kc, int p) {
    const unsigned sb = lds0 + (kc % NST) * STAGE;
    const int kk = (kc + rot) & 7;
    if (p == 0) {
      glds(voffA, baseA + kk * 128, sb + wid * 1024);
      glds(voffA, baseA + kk * 128 + 65536, sb + 8192 + wid * 1024);
    } else if (validB) {
      glds(voffB, baseB + kk * (64 * 3072), sb + 16384 + wid * 1536);
      glds(voffB, baseB + kk * (64 * 3072) + 4 * 3072, sb + 16384 + wid * 1536 + 768);
    }
  };
  auto issue = [&](int kc) { part(kc, 0); part(kc, 1); };
  issue(0);
  BAR();
  issue(1);
  BAR();
  issue(2);
  TSTAMP(0, 1);

  unsigned a_addr[2][2], b_addr[3];
#pragma unroll
  for (int mi = 0; mi < 2; ++mi)
#pragma unroll
    for (int ks = 0; ks < 2; ++ks)
      a_addr[mi][ks] = lds0 + (wm * 32 + ((mi ^ wn) * 16) + l15) * 128 + (((4 * ks + gq) ^ (l15 >> 1)) * 16);
  {
    const int kl = gq * 8 + (l15 >> 2);
#pragma unroll
    for (int j = 0; j < 3; ++j)
      b_addr[j] = lds0 + 16384 + kl * 192 + (((3 * wn + j) ^ (gq & 1)) * 32) + (l15 & 3) * 8;
  }

  f32x4 acc[2][3], s1, s2, cs[3];
  const f32x4 z4 = {0.f, 0.f, 0.f, 0.f};
#pragma unroll
  for (int j = 0; j < 3; ++j) { acc[0][j] = z4; acc[1][j] = z4; cs[j] = z4; }
  s1 = z4; s2 = z4;
  bf16x8 af[2][2];
  bf16x4 bl[2][3], bh[2][3];
  const bf16 one = (bf16)1.0f;
  const bf16x8 ones = {one, one, one, one, one, one, one, one};

  auto rd = [&](int t, int set) {
    const int kc = t >> 1, ks = t & 1, st = kc % NST;
    const unsigned hi = (unsigned)((st & ~1) * STAGE);
    const int so = (st & 1) * STAGE;
#pragma unroll
    for (int j = 0; j < 3; ++j) {
      DSRTR(bl[set][j], b_addr[j] + hi, so + ks * 6144);
      DSRTR(bh[set][j], b_addr[j] + hi, so + ks * 6144 + 768);
      if (j < 2) DSR128(af[set][j], a_addr[j][ks] + hi, so);
    }
  };
  auto wt = [&](int set) {
    asm volatile("s_waitcnt lgkmcnt(0)" ::: "memory");
    PIN(af[set][0]); PIN(af[set][1]);
#pragma unroll
    for (int j = 0; j < 3; ++j) { PIN(bl[set][j]); PIN(bh[set][j]); }
  };

  using SC = Sched<2, 2, NST, NK, 6, 2>;
  wait_vm<(SC::cum(-1) - 2) * 2>();
  BAR();
  TSTAMP(0, 2);
  rd(0, 0);
  static_for<SC::cum(-1), SC::cum(0)>([&](auto n) { part(n.value >> 1, n.value & 1); });
  wt(0);
  static_for<0, NT>([&](auto tc) {
    constexpr int t = tc.value, kc = t >> 1, ks = t & 1, cur = t & 1, nxt = cur ^ 1;
    if constexpr (ks == 1 && kc + 1 < NK) {
      static_assert(SC::cum(t) >= (kc + 2) * 2, "schedule");
      wait_vm<(SC::cum(t) - (kc + 2) * 2) * 2 + (((CPF & 1) && t > CW1) ? 1 : 0)>();
      BAR();
      TSTAMP(0, 3 + kc);
    }
    if constexpr (t + 1 < NT) rd(t + 1, nxt);
    static_for<SC::cum(t), SC::cum(t + 1)>([&](auto n) { part(n.value >> 1, n.value & 1); });
    if constexpr ((CPF & 1) && t == CW1) {
      static_assert(SC::cum(t + 1) == NK * 2, "issued after all operand loads");
      code_warm<true>(wid, lane, lds0 + NST * STAGE + 2560 + wid * 256, baseA);
    }
    SCHED();
    {
      const bf16x8 b0 = cat8(bl[cur][0], bh[cur][0]), b1 = cat8(bl[cur][1], bh[cur][1]),
                   b2 = cat8(bl[cur][2], bh[cur][2]);
#pragma unroll
      for (int mi = 0; mi < 2; ++mi) {
        acc[mi][0] = mfma16(b0, af[cur][mi], acc[mi][0]);
        acc[mi][1] = mfma16(b1, af[cur][mi], acc[mi][1]);
        acc[mi][2] = mfma16(b2, af[cur][mi], acc[mi][2]);
      }
      s1 = mfma16(ones, af[cur][0], s1);
      s2 = mfma16(af[cur][0], af[cur][0], s2);
      if ((t & 3) == wm) {
        cs[0] = mfma16(b0, ones, cs[0]);
        cs[1] = mfma16(b1, ones, cs[1]);
        cs[2] = mfma16(b2, ones, cs[2]);
      }
    }
    SCHED();
    if constexpr (t + 1 < NT) wt(nxt);
  });
  TSTAMP(0, 20);

  float* st_ = (float*)(smem + NST * STAGE);
  float* csum = st_ + 256;
  {
    const float sq = (l15 & 2) ? ((l15 & 1) ? s2[3] : s2[2]) : ((l15 & 1) ? s2[1] : s2[0]);
    const f32x2 ms = ln_stats(s1[0], sq);
    if (gq == (l15 >> 2)) *(f32x2*)(st_ + (wm * 32 + wn * 16 + l15) * 2) = ms;
    if (l15 == 0) {
#pragma unroll
      for (int j = 0; j < 3; ++j) *(f32x4*)(csum + wm * 96 + (3 * wn + j) * 16 + gq * 4) = cs[j];
    }
  }
  __syncthreads();
  TSTAMP(0, 21);
  const int tile = pm * 4 + wm;
  const float qscale = 0.25f * 1.4426950408889634f;
  f32x2 ms[2];
#pragma unroll
  for (int mi = 0; mi < 2; ++mi) ms[mi] = *(const f32x2*)(st_ + (wm * 32 + (mi ^ wn) * 16 + l15) * 2);
  const int loq = (gq >> 1) * 256 + l15 * 8 + (gq & 1) * 4, lov = l15 * 16 + gq * 4;
#pragma unroll
  for (int j = 0; j < 3; ++j) {
    f32x4 c4 = z4;
#pragma unroll
    for (int p = 0; p < 4; ++p) c4 += *(const f32x4*)(csum + p * 96 + (3 * wn + j) * 16 + gq * 4);
    const int blk = pn * 6 + wn * 3 + j;
    const int typ = blk >> 5, h = blk & 31;
    const float sc = typ == 0 ? qscale : 1.0f;
    bf16* db = QKV + ((size_t)typ << 20) + (size_t)(h * 64 + tile) * 512 + (typ == 2 ? lov : loq);
    const int tfs = typ == 2 ? 256 : 128;
#pragma unroll
    for (int mi = 0; mi < 2; ++mi) {
      const float rs = ms[mi][1] * sc;
      bf16x4 o;
#pragma unroll
      for (int r = 0; r < 4; ++r) o[r] = (bf16)((acc[mi][j][r] - ms[mi][0] * c4[r]) * rs);
      st8<(WT & 1) != 0>(db + (mi ^ wn) * tfs, __builtin_bit_cast(u32x2, o));
    }
  }
  TSTAMP(0, 22);
  TFLUSH(0);
}

template <bool FULL, int NPF>
DEV void attn_body(unsigned kaddr, unsigned vaddr, bf16x8 qf, f32x16 cm0, f32x16 cm8, int wid, bf16* dst, unsigned lds0 TARG) {
  const float NEG = -1e30f;
  f32x16 z16;
#pragma unroll
  for (int r = 0; r < 16; ++r) z16[r] = 0.f;
  bf16x8 kf[9];
#pragma unroll
  for (int t = 0; t < 9; ++t)
    if (FULL || wid + t >= 8) DSR128(kf[t], kaddr, t * 1024);
  asm volatile("s_waitcnt lgkmcnt(0)" ::: "memory");
#pragma unroll
  for (int t = 0; t < 9; ++t) PIN(kf[t]);
  TSTAMP(1, 3);
  float m0 = NEG, m1 = NEG;
#pragma unroll
  for (int t = 0; t < 9; ++t)
    if (FULL || wid + t >= 8) {
      const f32x16 s = mfma32(kf[t], qf, t == 0 ? cm0 : (t == 8 ? cm8 : z16));
#pragma unroll
      for (int r = 0; r < 16; r += 4) {
        m0 = fmaxf(fmaxf(m0, s[r]), s[r + 1]);
        m1 = fmaxf(fmaxf(m1, s[r + 2]), s[r + 3]);
      }
    }
  float m = fmaxf(m0, m1);
  m = fmaxf(m, __shfl_xor(m, 32));
  f32x16 nm;
#pragma unroll
  for (int r = 0; r < 16; ++r) { nm[r] = -m; cm0[r] -= m; cm8[r] -= m; }
  TSTAMP(1, 4);
  wait_vm<NPF>();
  BAR();
  TSTAMP(1, 5);
  if (CPF & 2) code_warm<false>(wid, (int)(threadIdx.x & 63), lds0 + 59392 + wid * 256, nullptr);
  f32x16 oa = z16, ob = z16;
  f32x16 sc_, sn_;
  bf16x4 v0l[2], v0h[2], v1l[2], v1h[2];
  auto vrd = [&](int t, int b) {
    DSRTR(v0l[b], vaddr, t * 1024);
    DSRTR(v0h[b], vaddr, t * 1024 + 256);
    DSRTR(v1l[b], vaddr, t * 1024 + 512);
    DSRTR(v1h[b], vaddr, t * 1024 + 768);
  };
  if (FULL) { sc_ = mfma32(kf[0], qf, cm0); vrd(0, 0); }
  else sc_ = z16;
  sn_ = z16;
#pragma unroll
  for (int t = 0; t < 9; ++t) {
    const int b = t & 1;
    const bool cur_ok = FULL || wid + t >= 8, nxt_ok = t < 8 && (FULL || wid + t + 1 >= 8);
    if (cur_ok) {
      asm volatile("s_waitcnt lgkmcnt(0)" ::: "memory");
      PIN(v0l[b]); PIN(v0h[b]); PIN(v1l[b]); PIN(v1h[b]);
    }
    if (nxt_ok) {
      vrd(t + 1, b ^ 1);
      sn_ = mfma32(kf[t + 1], qf, t + 1 == 8 ? cm8 : nm);
    }
    SCHED();
    if (cur_ok) {
      bf16x8 p0, p1;
#pragma unroll
      for (int j = 0; j < 8; ++j) p0[j] = (bf16)__builtin_amdgcn_exp2f(sc_[j]);
      SCHED();
      oa = mfma32(cat8(v0l[b], v0h[b]), p0, oa);
      SCHED();
#pragma unroll
      for (int j = 0; j < 8; ++j) p1[j] = (bf16)__builtin_amdgcn_exp2f(sc_[8 + j]);
      SCHED();
      ob = mfma32(cat8(v1l[b], v1h[b]), p1, ob);
      SCHED();
    }
    sc_ = sn_;
  }
  TSTAMP(1, 6);
  const float inv = 1.0f / (oa[8] + ob[8]);
  unsigned a0 = pk2((oa[0] + ob[0]) * inv, (oa[1] + ob[1]) * inv), a1 = pk2((oa[2] + ob[2]) * inv, (oa[3] + ob[3]) * inv);
  unsigned b0 = pk2((oa[4] + ob[4]) * inv, (oa[5] + ob[5]) * inv), b1 = pk2((oa[6] + ob[6]) * inv, (oa[7] + ob[7]) * inv);
  swap32(a0, b0);
  swap32(a1, b1);
  st16<(WT & 2) != 0>(dst, u32x4{a0, a1, b0, b1});
}

extern "C" __global__ __launch_bounds__(512, 2) void k2_attn(const bf16* __restrict__ QKV, bf16* __restrict__ O,
                                                  const bf16* __restrict__ wo, const bf16* __restrict__ w1,
                                                  const bf16* __restrict__ w3, const bf16* __restrict__ w2,
                                                  bf16* __restrict__ Wt, float* __restrict__ CS,
                                                  bf16* __restrict__ W2t, bf16* __restrict__ Wot) {
  constexpr int LV = 16384, LONE = 32768, LSJ = 43008;
  extern __shared__ __attribute__((aligned(16))) char smem[];
  TDECL
  TSTAMP(1, 0);
  const int tid = threadIdx.x, lane = tid & 63;
  const int wid = __builtin_amdgcn_readfirstlane(tid >> 6);
  const int l15 = lane & 15, gq = lane >> 4;
  const int xg = blockIdx.x >> 5, h = (blockIdx.x & 7) * 4 + ((blockIdx.x >> 3) & 3);
  const unsigned lds0 = (unsigned)(uintptr_t)(LDSP(char))smem;
  const int t0 = xg * 8 - 8;
  const unsigned l16 = lane * 16;
  asm volatile("" : "+s"(O), "+s"(Wt), "+s"(CS), "+s"(W2t), "+s"(Wot));
  asm volatile("" : "+s"(wo), "+s"(w1), "+s"(w3), "+s"(w2));
  const bool ld = xg > 0 || wid >= 4;

  const char* kb = (const char*)QKV + (2u << 20) + (ptrdiff_t)(h * 64 + t0 + 2 * wid) * 1024;
  if (ld) {
    glds(l16, kb, lds0 + wid * 2048);
    glds(l16, kb + 1024, lds0 + wid * 2048 + 1024);
  }
  bf16x8 qf;
  GLD4(qf, l16, (const char*)QKV + (size_t)(h * 64 + xg * 8 + wid) * 1024, 0);
  if (ld) {
    glds(l16, kb + (2u << 20), lds0 + LV + wid * 2048);
    glds(l16, kb + (2u << 20) + 1024, lds0 + LV + wid * 2048 + 1024);
  }
  {
    const u32x4 o4 = {0x3f803f80u, 0x3f803f80u, 0x3f803f80u, 0x3f803f80u};
    *(u32x4*)(smem + LONE + tid * 16) = o4;
    if (tid < 128) *(u32x4*)(smem + LONE + 8192 + tid * 16) = o4;
  }
  f32x16 cm0, cm8;
  {
    const int c = lane & 31, hh = lane >> 5;
#pragma unroll
    for (int r = 0; r < 16; ++r) {
      const int kr = (r & 3) + 8 * (r >> 2) + 4 * hh;
      cm0[r] = (kr > c) ? 0.f : -1e30f;
      cm8[r] = (kr <= c) ? 0.f : -1e30f;
    }
    PIN(cm0); PIN(cm8);
  }
  TSTAMP(1, 1);
  if (ld) asm volatile("s_waitcnt vmcnt(2) lgkmcnt(0)" ::: "memory");
  else asm volatile("s_waitcnt vmcnt(0) lgkmcnt(0)" ::: "memory");
  PIN(qf);
  BAR();
  TSTAMP(1, 2);
  const int skc = blockIdx.x >> 5, smt = (blockIdx.x >> 4) & 1, spn = blockIdx.x & 15;
  u32x4 wr0, wr1;
  {
    const int wj = wid < 7 ? wid : 6;
    const char* sb = (const char*)(smt ? w3 : w1) + (size_t)(skc * 64) * (F_ * 2) + (spn * 112 + wj * 16) * 2;
    const unsigned vo = (unsigned)((lane >> 1) * (F_ * 2) + (lane & 1) * 16);
    glds(vo, sb, lds0 + LSJ + wid * 2048);
    glds(vo, sb + 32 * (F_ * 2), lds0 + LSJ + wid * 2048 + 1024);
    const char* o = (const char*)wo + (size_t)blockIdx.x * 2048;
    const char* r0 = wid < 7 ? (const char*)w2 + (size_t)blockIdx.x * 7168 + wid * 1024 : o;
    GLD4(wr0, l16, r0, 0);
    GLD4(wr1, l16, o, 1024);
  }

  const unsigned kaddr = lds0 + wid * 1024 + l16;
  const unsigned vlane = (unsigned)(((lane >> 5) * 4 + (l15 >> 2)) * 32 + (l15 & 3) * 8);
  const unsigned vaddr = (lane & 16) ? (lds0 + LONE + (vlane ^ 128u)) : (lds0 + LV + wid * 1024 + vlane);
  bf16* dst = O + (size_t)(((xg * 4 + (wid >> 1)) * H_ + h) * 64 + (wid & 1) * 32 + (lane & 31)) * 16 + (lane >> 5) * 8;
  if (xg > 0) attn_body<true, 4>(kaddr, vaddr, qf, cm0, cm8, wid, dst, lds0 TPASS);
  else attn_body<false, 4>(kaddr, vaddr, qf, cm0, cm8, wid, dst, lds0 TPASS);
  TSTAMP(1, 20);
  wait_vm<1>();
  {
    const unsigned co = (unsigned)(lane & 7) * 16;
    PIN(wr0); PIN(wr1);
    if (wid < 7) {
      st16<(WT & 2) != 0>((char*)W2t + (size_t)(lane >> 3) * (F_ * 128) + (blockIdx.x * 7 + wid) * 128 + co, wr0);
    } else {
      char* o = (char*)Wot + (size_t)(lane >> 3) * (D_ * 128) + blockIdx.x * 256 + co;
      st16<(WT & 2) != 0>(o, wr0);
      st16<(WT & 2) != 0>(o + 128, wr1);
    }
  }
  if (wid < 7) {
    const unsigned ra = lds0 + LSJ + wid * 2048 + (gq * 8 + (l15 >> 2)) * 32 + (l15 & 3) * 8;
    bf16x4 f[4];
    DSRTR(f[0], ra, 0);
    DSRTR(f[1], ra, 128);
    DSRTR(f[2], ra, 1024);
    DSRTR(f[3], ra, 1024 + 128);
    asm volatile("s_waitcnt lgkmcnt(0)" ::: "memory");
    float c = 0.f;
#pragma unroll
    for (int i = 0; i < 4; ++i) {
      PIN(f[i]);
#pragma unroll
      for (int e = 0; e < 4; ++e) c += (float)f[i][e];
    }
    char* wd = (char*)Wt + (size_t)(((spn * 8 + skc) * 4 + smt) * 7 + wid) * 1024 + l16;
    st16<(WT & 2) != 0>(wd, __builtin_bit_cast(u32x4, cat8(f[0], f[1])));
    st16<(WT & 2) != 0>(wd + 14 * 1024, __builtin_bit_cast(u32x4, cat8(f[2], f[3])));
    c += __shfl_xor(c, 16);
    c += __shfl_xor(c, 32);
    if (lane < 16) st4<(WT & 2) != 0>(CS + skc * (2 * F_) + smt * F_ + spn * 112 + wid * 16 + lane, __builtin_bit_cast(unsigned, c));
  }
  TSTAMP(1, 22);
  wait_vm<0>();
  TFLUSH(1);
}

template <int MODE, int KTOT>
DEV void gemm_body(const bf16* __restrict__ A, const bf16* __restrict__ B, const bf16* __restrict__ R,
                   bf16* __restrict__ O, float* __restrict__ RS, const bf16* __restrict__ PFW) {
  constexpr int NST = G_NST, STAGE = 32768, NK = KTOT / 128, NT = NK * 4;
  extern __shared__ __attribute__((aligned(16))) char smem[];
  TDECL
  TSTAMP(MODE == 0 ? 2 : 4, 0);
  const int tid = threadIdx.x, lane = tid & 63;
  const int wid = __builtin_amdgcn_readfirstlane(tid >> 6);
  const int wm = wid & 3, wn = wid >> 2;
  const int l15 = lane & 15, gq = lane >> 4;
  const int xg = blockIdx.x & 7, idx = blockIdx.x >> 3;
#define PNX 0
  const int pm = xg * 4 + (idx >> 3), pn = (idx & 7) ^ PNX;
  if (MODE == 0) asm volatile("" : "+s"(RS), "+s"(PFW));
  const unsigned lds0 = (unsigned)(uintptr_t)(LDSP(char))smem;

  u32x4 res;
  const int tok = pm * 64 + wm * 16 + l15;
  const int ncol = pn * 64 + wn * 32 + (gq & 1) * 16 + (gq >> 1) * 8;
  const unsigned hoff = (unsigned)(((((tok >> 7) * 8 + pn) * 128 + (tok & 127)) * 64 + (ncol & 63)) * 2);
  GLD4(res, MODE == 0 ? (unsigned)((tok * D_ + ncol) * 2) : hoff, R, 0);

  const unsigned voffA = (unsigned)(wid * 1024 + lane * 16);
  const int pB = ((lane & 7) >> 1) ^ (((lane >> 4) & 1) | ((wid & 1) << 1));
  const unsigned voffB = (unsigned)((wid * 8 + (lane >> 3)) * 128 + pB * 32 + (lane & 1) * 16);
  const char* baseA = (const char*)A + (size_t)pm * (KTOT * 128);
  const char* baseB = (const char*)B + pn * (KTOT * 128);
  const unsigned sbw = lds0 + wid * 1024;
  const int rot = MODE == 0 ? ROT3 * ((pn + (idx >> 3)) & 3) : ROT5 * ((pn * 2 + (idx >> 3) * 3) % NK);
  auto part = [&](int kc, int p) {
    const unsigned sb = sbw + (kc % NST) * STAGE;
    int kk = kc + rot;
    if (kk >= NK) kk -= NK;
    if (p == 0) glds(voffA, baseA + (size_t)kk * 16384, sb);
    if (p == 1) glds(voffA, baseA + (size_t)kk * 16384 + 8192, sb + 8192);
    if (p == 2) glds(voffB, baseB + (size_t)kk * 16384, sb + 16384);
    if (p == 3) glds(voffB, baseB + (size_t)kk * 16384 + 8192, sb + 16384 + 8192);
  };
  auto issue = [&](int kc) { part(kc, 0); part(kc, 1); part(kc, 2); part(kc, 3); };
  issue(0);
  BAR();
  issue(1);
  BAR();
  issue(2);
  TSTAMP(MODE == 0 ? 2 : 4, 1);

  unsigned a_addr[4], b_addr[2];
#pragma unroll
  for (int ks = 0; ks < 4; ++ks) a_addr[ks] = lds0 + (2 * ks + (gq >> 1)) * 2048 + (wm * 16 + l15) * 32 + (gq & 1) * 16;
  {
    const int kl = gq * 8 + (l15 >> 2);
    const int fr = ((l15 >> 3) & 1) | ((gq & 1) << 1);
#pragma unroll
    for (int j = 0; j < 2; ++j) b_addr[j] = lds0 + 16384 + kl * 128 + (((2 * wn + j) ^ fr) * 32) + (l15 & 3) * 8;
  }

  f32x4 acc[2];
  const f32x4 z4 = {0.f, 0.f, 0.f, 0.f};
  acc[0] = z4; acc[1] = z4;
  bf16x8 af[2];
  bf16x4 bl[2][2], bh[2][2];

  auto rd = [&](int t, int set) {
    const int kc = t >> 2, ks = t & 3, st = kc % NST;
    const unsigned hi = (unsigned)((st & ~1) * STAGE);
    const int so = (st & 1) * STAGE;
    DSRTR(bl[set][0], b_addr[0] + hi, so + ks * 4096);
    DSRTR(bh[set][0], b_addr[0] + hi, so + ks * 4096 + 512);
    DSR128(af[set], a_addr[ks] + hi, so);
    DSRTR(bl[set][1], b_addr[1] + hi, so + ks * 4096);
    DSRTR(bh[set][1], b_addr[1] + hi, so + ks * 4096 + 512);
  };
  auto wt = [&](int set) {
    asm volatile("s_waitcnt lgkmcnt(0)" ::: "memory");
    PIN(af[set]);
    PIN(bl[set][0]); PIN(bh[set][0]); PIN(bl[set][1]); PIN(bh[set][1]);
  };

  using SC = Sched<4, 4, NST, NK, 12, G_R>;
  wait_vm<SC::cum(-1) - 4>();
  BAR();
  TSTAMP(MODE == 0 ? 2 : 4, 2);
  rd(0, 0);
  static_for<SC::cum(-1), SC::cum(0)>([&](auto n) { part(n.value >> 2, n.value & 3); });
  wt(0);
  static_for<0, NT>([&](auto tc) {
    constexpr int t = tc.value, kc = t >> 2, ks = t & 3, cur = t & 1, nxt = cur ^ 1;
    if constexpr (ks == 3 && kc + 1 < NK) {
      static_assert(SC::cum(t) >= (kc + 2) * 4, "schedule");
      constexpr int npf = (MODE == 0 && PF3 && t > PF3T) ? (t - PF3T > 4 ? 4 : t - PF3T) : 0;
      static_assert(npf == 0 || SC::cum(PF3T) == NK * 4, "warm-up loads come after all operand loads");
      wait_vm<SC::cum(t) - (kc + 2) * 4 + npf + ((MODE == 0 && (CPF & 4) && t > CW3) ? 1 : 0)>();
      BAR();
      TSTAMP(MODE == 0 ? 2 : 4, 3 + kc);
    }
    if constexpr (t + 1 < NT) rd(t + 1, nxt);
    static_for<SC::cum(t), SC::cum(t + 1)>([&](auto n) { part(n.value >> 2, n.value & 3); });
    if constexpr (MODE == 0 && (CPF & 4) && t == CW3) {
      static_assert(SC::cum(t + 1) == NK * 4, "issued after all operand loads");
      code_warm<true>(wid, lane, lds0 + NST * STAGE + wid * 1024, baseA);
    }
    if constexpr (MODE == 0 && PF3 && t >= PF3T && t < PF3T + 4) {
      glds((unsigned)(lane * 16),
           (const char*)PFW + (size_t)(xg >> 1) * (4 * 229376) + idx * 28672 + (t - PF3T) * 8192 + (t - PF3T < 3 ? wid : (wid & 3)) * 1024,
           lds0 + NST * STAGE + wid * 1024);
    }
    SCHED();
    acc[0] = mfma16(cat8(bl[cur][0], bh[cur][0]), af[cur], acc[0]);
    acc[1] = mfma16(cat8(bl[cur][1], bh[cur][1]), af[cur], acc[1]);
    SCHED();
    if constexpr (t + 1 < NT) wt(nxt);
  });
  TSTAMP(MODE == 0 ? 2 : 4, 20);
  PIN(res);
  float s = 0.f, q = 0.f;
  {
#pragma unroll
    for (int r = 0; r < 4; ++r) { float a = acc[0][r], b = acc[1][r]; swap16f(a, b); acc[0][r] = a; acc[1][r] = b; }
    const bf16x8 rr = __builtin_bit_cast(bf16x8, res);
    bf16x8 o;
#pragma unroll
    for (int j = 0; j < 2; ++j)
#pragma unroll
      for (int r = 0; r < 4; ++r) {
        o[j * 4 + r] = (bf16)((float)rr[j * 4 + r] + bfr(acc[j][r]));
        const float v = (float)o[j * 4 + r];
        s += v;
        q += v * v;
      }
    st16<(WT & (MODE == 0 ? 4 : 16)) != 0>((char*)O + (MODE == 0 ? hoff : (unsigned)((tok * D_ + ncol) * 2)), __builtin_bit_cast(u32x4, o));
  }
  if (MODE == 0) {
    s += __shfl_xor(s, 16); s += __shfl_xor(s, 32);
    q += __shfl_xor(q, 16); q += __shfl_xor(q, 32);
    if (gq == 0) st8<(WT & 4) != 0>(RS + ((size_t)(pn * 2 + wn) * S_ + tok) * 2, __builtin_bit_cast(u32x2, f32x2{s, q}));
  }
  if (MODE == 0) wait_vm<0>();
  TSTAMP(MODE == 0 ? 2 : 4, 22);
  TFLUSH(MODE == 0 ? 2 : 4);
}
extern "C" __global__ __launch_bounds__(512, 2) void k3_wo(const bf16* __restrict__ A, const bf16* __restrict__ B,
                                                           const bf16* __restrict__ R, bf16* __restrict__ O,
                                                           float* __restrict__ RS, const bf16* __restrict__ PFW) {
  gemm_body<0, 512>(A, B, R, O, RS, PFW);
}
extern "C" __global__ __launch_bounds__(512, 2) void k5_down(const bf16* __restrict__ A, const bf16* __restrict__ B,
                                                             const bf16* __restrict__ R, bf16* __restrict__ O) {
  gemm_body<1, 1792>(A, B, R, O, nullptr, nullptr);
}

template <int NP, int P0>
DEV void gu_body(char* smem, unsigned lds0, int tid, int wid, int pm, int pn, const bf16* __restrict__ Hh,
                 const bf16* __restrict__ Wt, const float* __restrict__ RS, const float* __restrict__ CS,
                 bf16* __restrict__ Hm, const char* pf TARG) {
  constexpr int NST = 3, STAGE = 45056, NK = 8, LPC = 2 + NP, NT = 16;
  const int lane = tid & 63, wm = wid & 3;
  const int l15 = lane & 15, gq = lane >> 4;
  const unsigned l16 = lane * 16;
  const unsigned voffA = (unsigned)((wid * 8 + (lane >> 3)) * 128 + (((lane & 7) ^ ((wid & 1) * 4 + gq)) * 16));
  const char* baseA = (const char*)Hh + (size_t)pm * (128 * 1024);
  const int pc0 = (P0 == 0 ? 0 : 16) + wm * NP;
  const char* baseW = (const char*)Wt + (size_t)pn * (8 * 28672) + pc0 * 1024;
  const int rot = ROT4 * (pm + 2 * pn);
  auto piece = [&](int kc, int i) {
    const unsigned sb = lds0 + (kc % NST) * STAGE;
    const int kk = (kc + rot) & 7;
    if (i == 0) glds(voffA, baseA + kk * 16384, sb + wid * 1024);
    else if (i == 1) glds(voffA, baseA + kk * 16384 + 8192, sb + 8192 + wid * 1024);
    else glds(l16, baseW + kk * 28672 + (i - 2) * 1024, sb + 16384 + (pc0 + i - 2) * 1024);
  };
  auto issue = [&](int kc) {
#pragma unroll
    for (int i = 0; i < LPC; ++i) piece(kc, i);
  };
  float* st_ = (float*)(smem + NST * STAGE);
  float* csum = st_ + 256;
  if (P0 == 0) {
    if (wid < 2) {
      f32x2 sv[16];
      const char* rb = (const char*)RS + pm * 1024;
      const unsigned vo = (unsigned)(tid * 8);
#pragma unroll
      for (int i = 0; i < 16; ++i) GLD2(sv[i], vo, rb + i * (S_ * 8), 0);
      issue(0);
      BAR();
      issue(1);
      wait_vm<2 * LPC>();
#pragma unroll
      for (int i = 0; i < 16; ++i) PIN(sv[i]);
      f32x2 a = sv[0];
#pragma unroll
      for (int i = 1; i < 16; ++i) a += sv[i];
      *(f32x2*)(st_ + tid * 2) = ln_stats(a[0], a[1]);
    } else {
      issue(0);
      BAR();
      issue(1);
    }
  } else {
    float sc[8];
    int c = tid - 256;
    const bool ok = c < 224;
    if (!ok) c = 0;
    const unsigned vo = (unsigned)(((c < 112 ? 0 : F_ - 112) + pn * 112 + c) * 4);
#pragma unroll
    for (int i = 0; i < 8; ++i) GLD1(sc[i], vo, (const char*)CS + i * (2 * F_ * 4), 0);
    issue(0);
    BAR();
    issue(1);
    wait_vm<2 * LPC>();
#pragma unroll
    for (int i = 0; i < 8; ++i) PIN(sc[i]);
    float a = sc[0];
#pragma unroll
    for (int i = 1; i < 8; ++i) a += sc[i];
    if (ok) csum[c] = a;
  }
  TSTAMP(3, 1);

  unsigned a_addr[2];
#pragma unroll
  for (int ks = 0; ks < 2; ++ks) a_addr[ks] = lds0 + (wm * 32 + l15) * 128 + (((4 * ks + gq) ^ (l15 >> 1)) * 16);
  const unsigned b_addr = lds0 + 16384 + P0 * 1024 + l16;
  f32x4 ag[2][NP], au[2][NP];
  const f32x4 z4 = {0.f, 0.f, 0.f, 0.f};
#pragma unroll
  for (int j = 0; j < NP; ++j) { ag[0][j] = z4; ag[1][j] = z4; au[0][j] = z4; au[1][j] = z4; }
  bf16x8 af[2][2], bg[2][NP], bu[2][NP];
  auto rd = [&](int t, int set) {
    const int kc = t >> 1, ks = t & 1, st = kc % NST;
    const unsigned so = st * STAGE;
    DSR128(af[set][0], a_addr[ks] + so, 0);
    DSR128(af[set][1], a_addr[ks] + so, 2048);
#pragma unroll
    for (int j = 0; j < NP; ++j) {
      DSR128(bg[set][j], b_addr + so, (ks * 14 + j) * 1024);
      DSR128(bu[set][j], b_addr + so, (ks * 14 + 7 + j) * 1024);
    }
  };
  auto wt = [&](int set) {
    asm volatile("s_waitcnt lgkmcnt(0)" ::: "memory");
    PIN(af[set][0]); PIN(af[set][1]);
#pragma unroll
    for (int j = 0; j < NP; ++j) { PIN(bg[set][j]); PIN(bu[set][j]); }
  };
  asm volatile("s_waitcnt vmcnt(%0) lgkmcnt(0)" ::"n"(LPC) : "memory");
  BAR();
  TSTAMP(3, 2);
  rd(0, 0);
  piece(NST - 1, 0); piece(NST - 1, 1); piece(NST - 1, 2);
  wt(0);
#pragma unroll
  for (int t = 0; t < NT; ++t) {
    const int kc = t >> 1, ks = t & 1, cur = t & 1, nxt = cur ^ 1;
    if (ks == 1 && kc + 1 < NK) {
      const int last = (kc + NST - 1 < NK - 1) ? (kc + NST - 1) : (NK - 1);
      wait_vm_n((last - (kc + 1)) * LPC + ((PF4 && t > 11) ? (t - 11) * 2 : 0));
      BAR();
      TSTAMP(3, 3 + kc);
    }
    if (t + 1 < NT) rd(t + 1, nxt);
    const int ic = (t + 1) / 2 + NST - 1, ip = ((t + 1) & 1) * 3;
    SCHED();
#pragma unroll
    for (int j = 0; j < NP; ++j) {
      ag[0][j] = mfma16(bg[cur][j], af[cur][0], ag[0][j]);
      ag[1][j] = mfma16(bg[cur][j], af[cur][1], ag[1][j]);
      au[0][j] = mfma16(bu[cur][j], af[cur][0], au[0][j]);
      au[1][j] = mfma16(bu[cur][j], af[cur][1], au[1][j]);
      SCHED();
      if (j < 3 && ic < NK && ip + j < LPC) piece(ic, ip + j);
      if (PF4 && j < 2 && t >= 11 && (t - 11) * 2 + j < 7)
        glds(l16, pf + ((t - 11) * 2 + j) * 8192, lds0 + NST * STAGE + 2048 + wid * 1024);
      if ((CPF & 8) && t == CW4 && j == 1) code_warm<false>(wid, lane, lds0 + NST * STAGE + 2048 + wid * 1024, nullptr);
      SCHED();
    }
    if (t + 1 < NT) wt(nxt);
  }
  TSTAMP(3, 20);
  bf16* dst0 = Hm + ((size_t)((pm * 2 + (wm >> 1)) * 112 + pn * 7 + P0) * 64 + (wm & 1) * 32 + l15) * 16;
#pragma unroll
  for (int mi = 0; mi < 2; ++mi) {
    const f32x2 ms = *(const f32x2*)(st_ + (wm * 32 + mi * 16 + l15) * 2);
    const f32x2 nm = {-ms[0], -ms[0]};
    const float le = ms[1] * -1.4426950408889634f, iv = 1.0f / (ms[1] * ms[1]);
    const f32x2 le2 = {le, le}, iv2 = {iv, iv};
    unsigned pk[NP][2];
#pragma unroll
    for (int j = 0; j < NP; ++j) {
      const f32x4 cg = *(const f32x4*)(csum + (P0 + j) * 16 + gq * 4);
      const f32x4 cu = *(const f32x4*)(csum + 112 + (P0 + j) * 16 + gq * 4);
#pragma unroll
      for (int hf = 0; hf < 2; ++hf) {
        const f32x2 g = __builtin_elementwise_fma(f32x2{cg[2 * hf], cg[2 * hf + 1]}, nm,
                                                  f32x2{ag[mi][j][2 * hf], ag[mi][j][2 * hf + 1]});
        const f32x2 u = __builtin_elementwise_fma(f32x2{cu[2 * hf], cu[2 * hf + 1]}, nm,
                                                  f32x2{au[mi][j][2 * hf], au[mi][j][2 * hf + 1]});
        const f32x2 ea = g * le2;
        const f32x2 e = {__builtin_amdgcn_exp2f(ea[0]), __builtin_amdgcn_exp2f(ea[1])};
        const f32x2 d = __builtin_elementwise_fma(e, iv2, iv2);
        const f32x2 rc = {__builtin_amdgcn_rcpf(d[0]), __builtin_amdgcn_rcpf(d[1])};
        const f32x2 o = g * u * rc;
        pk[j][hf] = pk2(o[0], o[1]);
      }
    }
#pragma unroll
    for (int j = 0; j + 1 < NP; j += 2) {
      swap16(pk[j][0], pk[j + 1][0]);
      swap16(pk[j][1], pk[j + 1][1]);
      st16<(WT & 8) != 0>(dst0 + ((size_t)(j + (gq & 1)) * 64 + mi * 16) * 16 + (gq >> 1) * 8,
                          u32x4{pk[j][0], pk[j][1], pk[j + 1][0], pk[j + 1][1]});
    }
    if (NP & 1) st8<(WT & 8) != 0>(dst0 + ((size_t)(NP - 1) * 64 + mi * 16) * 16 + gq * 4, u32x2{pk[NP - 1][0], pk[NP - 1][1]});
  }
}

extern "C" __global__ __launch_bounds__(512, 2) void k4_gu(const bf16* __restrict__ Hh, const bf16* __restrict__ Wt,
                                                const float* __restrict__ RS, const float* __restrict__ CS,
                                                bf16* __restrict__ Hm, const bf16* __restrict__ W2t) {
  extern __shared__ __attribute__((aligned(16))) char smem[];
  TDECL
  TSTAMP(3, 0);
  const int tid = threadIdx.x;
  const int wid = __builtin_amdgcn_readfirstlane(tid >> 6);
  const int xg = blockIdx.x & 7, idx = blockIdx.x >> 3;
  const int pm = (xg & 1) * 8 + (idx & 7), pn = (xg >> 1) * 4 + (idx >> 3);
  asm volatile("" : "+s"(RS), "+s"(CS), "+s"(Hm), "+s"(W2t));
  const unsigned lds0 = (unsigned)(uintptr_t)(LDSP(char))smem;
  const char* pf = (const char*)W2t + idx * 57344 + wid * 1024;
  if (wid < 4) gu_body<4, 0>(smem, lds0, tid, wid, pm, pn, Hh, Wt, RS, CS, Hm, pf TPASS);
  else gu_body<3, 4>(smem, lds0, tid, wid, pm, pn, Hh, Wt, RS, CS, Hm, pf TPASS);
  wait_vm<0>();
  TSTAMP(3, 22);
  TFLUSH(3);
  asm volatile("k4_gu_tail:");
}

extern "C" void kernel_launch(void* const* d_in, const int* in_sizes, int n_in, void* d_out, int out_size, void* d_ws,
                              size_t ws_size, hipStream_t stream) {
  const bf16* x = (const bf16*)d_in[0];
  const bf16* wqkv = (const bf16*)d_in[1];
  const bf16* wo = (const bf16*)d_in[2];
  const bf16* w1 = (const bf16*)d_in[4];
  const bf16* w3 = (const bf16*)d_in[5];
  const bf16* w2 = (const bf16*)d_in[6];
  bf16* out = (bf16*)d_out;

  char* ws = (char*)d_ws;
  bf16* QKV = (bf16*)(ws);
  bf16* Oa = (bf16*)(ws + (7u << 20));
  bf16* Hh = (bf16*)(ws + (9u << 20));
  bf16* Hm = (bf16*)(ws + (11u << 20));
  float* RS = (float*)(ws + (18u << 20));
  float* CS = (float*)(ws + (19u << 20));
  bf16* Wt = (bf16*)(ws + (20u << 20));
  bf16* W2t = (bf16*)(ws + (24u << 20));
  bf16* Wot = (bf16*)(ws + (26u << 20));

  {
    constexpr int lds = K1_NST * 28672 + 2560 + 2048;
    (void)hipFuncSetAttribute((const void*)k1_qkv, hipFuncAttributeMaxDynamicSharedMemorySize, lds);
    k1_qkv<<<256, 512, lds, stream>>>(x, wqkv, QKV);
  }
  {
    constexpr int lds = 59392 + 2048;
    (void)hipFuncSetAttribute((const void*)k2_attn, hipFuncAttributeMaxDynamicSharedMemorySize, lds);
    k2_attn<<<256, 512, lds, stream>>>(QKV, Oa, wo, w1, w3, w2, Wt, CS, W2t, Wot);
  }
  {
    auto k = k3_wo;
    constexpr int lds = G_NST * 32768 + 8192;
    (void)hipFuncSetAttribute((const void*)k, hipFuncAttributeMaxDynamicSharedMemorySize, lds);
    k<<<256, 512, lds, stream>>>(Oa, Wot, x, Hh, RS, Wt);
  }
  {
    constexpr int lds = 3 * 45056 + 2048 + 8192;
    (void)hipFuncSetAttribute((const void*)k4_gu, hipFuncAttributeMaxDynamicSharedMemorySize, lds);
    k4_gu<<<256, 512, lds, stream>>>(Hh, Wt, RS, CS, Hm, W2t);
  }
  {
    auto k = k5_down;
    constexpr int lds = G_NST * 32768;
    (void)hipFuncSetAttribute((const void*)k, hipFuncAttributeMaxDynamicSharedMemorySize, lds);
    k<<<256, 512, lds, stream>>>(Hm, W2t, Hh, out);
  }
}
```

```cpp
#include <hip/hip_runtime.h>
#include <stdint.h>

typedef __attribute__((ext_vector_type(8))) __bf16 bf16x8;
typedef __attribute__((ext_vector_type(4))) __bf16 bf16x4;
typedef __attribute__((ext_vector_type(2))) __bf16 bf16x2;
typedef __attribute__((ext_vector_type(2))) float f32x2;
typedef __attribute__((ext_vector_type(4))) float f32x4;
typedef __attribute__((ext_vector_type(16))) float f32x16;
typedef __attribute__((ext_vector_type(4))) unsigned int u32x4;
typedef __attribute__((ext_vector_type(2))) unsigned int u32x2;
typedef __bf16 bf16;

#define LDSP(T) __attribute__((address_space(3))) T*
#define DEV __device__ __forceinline__

#define K1_NST 4
#define G_NST 4
#define G_R 1
#define ROT1 0
#define ROT3 0
#define ROT4 0
#define ROT5 0
#define PF4 1
#define PF3 0
#define PF3T 4
constexpr int S_ = 2048, D_ = 512, H_ = 32, F_ = 1792;

DEV float bfr(float x) { return (float)(bf16)x; }

template <int N>
DEV void wait_vm() {
  asm volatile("s_waitcnt vmcnt(%0)" ::"n"(N) : "memory");
}
DEV void wait_vm_n(int n) {
  switch (n) {
#define WC(X) case X: wait_vm<X>(); break;
    WC(0) WC(1) WC(2) WC(3) WC(4) WC(5) WC(6) WC(7) WC(8) WC(9) WC(10) WC(11) WC(12) WC(13) WC(14) WC(15)
    WC(16) WC(17) WC(18) WC(19) WC(20) WC(21) WC(22) WC(23) WC(24)
#undef WC
    default: wait_vm<0>(); break;
  }
}

DEV void glds(unsigned voff, const void* sbase, unsigned ldsaddr) {
  ldsaddr = __builtin_amdgcn_readfirstlane(ldsaddr);
  asm volatile("s_mov_b32 m0, %2\n\ts_nop 0\n\tglobal_load_lds_dwordx4 %0, %1" ::"v"(voff), "s"(sbase), "s"(ldsaddr)
               : "memory");
}
#define DSR128(dst, addr, off) asm volatile("ds_read_b128 %0, %1 offset:%2" : "=v"(dst) : "v"(addr), "i"(off))
#define DSRTR(dst, addr, off) asm volatile("ds_read_b64_tr_b16 %0, %1 offset:%2" : "=v"(dst) : "v"(addr), "i"(off))
#define PIN(x) asm volatile("" : "+v"(x))
#define GLD4(dst, voff, sbase, off) \
  asm volatile("global_load_dwordx4 %0, %1, %2 offset:%3" : "=v"(dst) : "v"(voff), "s"(sbase), "i"(off))
#define GLD2(dst, voff, sbase, off) \
  asm volatile("global_load_dwordx2 %0, %1, %2 offset:%3" : "=v"(dst) : "v"(voff), "s"(sbase), "i"(off))
#define GLD1(dst, voff, sbase, off) \
  asm volatile("global_load_dword %0, %1, %2 offset:%3" : "=v"(dst) : "v"(voff), "s"(sbase), "i"(off))
#define WT 31
template <bool W>
DEV void st16(void* p, u32x4 v) {
  if constexpr (W) asm volatile("global_store_dwordx4 %0, %1, off sc1\n\ts_nop 1" ::"v"(p), "v"(v) : "memory");
  else *(u32x4*)p = v;
}
template <bool W>
DEV void st8(void* p, u32x2 v) {
  if constexpr (W) asm volatile("global_store_dwordx2 %0, %1, off sc1\n\ts_nop 1" ::"v"(p), "v"(v) : "memory");
  else *(u32x2*)p = v;
}
template <bool W>
DEV void st4(void* p, unsigned v) {
  if constexpr (W) asm volatile("global_store_dword %0, %1, off sc1" ::"v"(p), "v"(v) : "memory");
  else *(unsigned*)p = v;
}
#define BAR() __builtin_amdgcn_s_barrier()
#define SCHED() __builtin_amdgcn_sched_barrier(0)

#define TDECL
#define TSTAMP(m, i) do { } while (0)
#define TFLUSH(m) do { } while (0)
#define TARG
#define TPASS

#define CPF 15
#define CODE_ADDR(dst, sym)                                                                                  \
  asm volatile("s_getpc_b64 s[30:31]\n\ts_add_u32 s30, s30, " #sym "@rel32@lo+4\n\ts_addc_u32 s31, s31, " #sym \
               "@rel32@hi+12\n\ts_mov_b64 %0, s[30:31]"                                                      \
               : "=s"(dst)::"s30", "s31", "scc")
#define CPW 5
#define XPF 3
constexpr int CW1 = 8, CW3 = 3, CW4 = 14;
template <bool ALL>
DEV void code_warm(int wid, int lane, unsigned ldsaddr, const char* other) {
  const bool sel = wid == CPW && blockIdx.x < 64;
  if (!ALL && !sel) return;
  const char* a;
  int d;
  CODE_ADDR(a, k1_qkv);
  asm volatile("s_mov_b32 %0, k4_gu_tail-k1_qkv" : "=s"(d));
  const int last = d < 0 ? 0 : d >> 7;
  const int ln = sel ? (int)(blockIdx.x >> 3) * 64 + lane : 0;
  unsigned vo = (unsigned)((ln < last ? ln : last) * 128);
  if ((XPF & 1) && (blockIdx.x >> 3) == 6) {
    a = (const char*)((uintptr_t)__builtin_amdgcn_kernarg_segment_ptr() & ~(uintptr_t)4095);
    vo = (unsigned)((lane & 31) * 128);
  }
  if ((XPF & 2) && (blockIdx.x >> 3) == 7) {
    int d2;
    CODE_ADDR(a, k1_qkv.kd);
    asm volatile("s_mov_b32 %0, k4_gu.kd-k1_qkv.kd" : "=s"(d2));
    const int l2 = d2 < 0 ? 0 : d2 >> 6;
    vo = (unsigned)((lane < l2 ? lane : l2) * 64);
  }
  if (ALL && !sel) { a = other; vo = 0; }
  ldsaddr = __builtin_amdgcn_readfirstlane(ldsaddr);
  asm volatile("s_mov_b32 m0, %2\n\ts_nop 0\n\tglobal_load_lds_dword %0, %1" ::"v"(vo), "s"(a), "s"(ldsaddr) : "memory");
}

template <int V> struct IC { static constexpr int value = V; };
template <int I, int N, class F>
DEV void static_for(F&& f) {
  if constexpr (I < N) {
    f(IC<I>{});
    static_for<I + 1, N>(f);
  }
}
template <int P, int KS, int NST, int NK, int PRE, int R>
struct Sched {
  static constexpr int avail(int t) {
    int n = ((t + 1) / KS + NST - 1) * P + ((t + 1) % KS + 1) * (P / KS);
    return n > NK * P ? NK * P : n;
  }
  static constexpr int cum(int t) {
    int n = PRE;
    for (int i = -1; i < t; ++i) { int a = avail(i); n = (n + R < a) ? n + R : a; }
    return n;
  }
};

DEV void swap32(unsigned& a, unsigned& b) {
  const auto r = __builtin_amdgcn_permlane32_swap(a, b, false, false);
  a = r[0]; b = r[1];
}
DEV void swap16(unsigned& a, unsigned& b) {
  const auto r = __builtin_amdgcn_permlane16_swap(a, b, false, false);
  a = r[0]; b = r[1];
}
DEV void swap16f(float& a, float& b) {
  unsigned x = __builtin_bit_cast(unsigned, a), y = __builtin_bit_cast(unsigned, b);
  swap16(x, y);
  a = __builtin_bit_cast(float, x); b = __builtin_bit_cast(float, y);
}
DEV unsigned pk2(float a, float b) { return __builtin_bit_cast(unsigned, bf16x2{(bf16)a, (bf16)b}); }
DEV bf16x8 cat8(bf16x4 a, bf16x4 b) { return __builtin_shufflevector(a, b, 0, 1, 2, 3, 4, 5, 6, 7); }
DEV f32x4 mfma16(bf16x8 a, bf16x8 b, f32x4 c) { return __builtin_amdgcn_mfma_f32_16x16x32_bf16(a, b, c, 0, 0, 0); }
DEV f32x16 mfma32(bf16x8 a, bf16x8 b, f32x16 c) { return __builtin_amdgcn_mfma_f32_32x32x16_bf16(a, b, c, 0, 0, 0); }

DEV f32x2 ln_stats(float sum, float sq) {
  float m = sum * (1.0f / D_);
  float var = fmaxf(sq * (1.0f / D_) - m * m, 0.f);
  float vb = bfr(var);
  float sd = bfr(__builtin_sqrtf(bfr(vb + 1e-5f)));
  f32x2 r;
  r[0] = bfr(m);
  r[1] = 1.0f / sd;
  return r;
}

extern "C" __global__ __launch_bounds__(512, 2) void k1_qkv(const bf16* __restrict__ x, const bf16* __restrict__ w,
                                                            bf16* __restrict__ QKV) {
  constexpr int NST = K1_NST, STAGE = 28672, NK = 8, NT = 16;
  extern __shared__ __attribute__((aligned(16))) char smem[];
  TDECL
  TSTAMP(0, 0);
  const int tid = threadIdx.x, lane = tid & 63;
  const int wid = __builtin_amdgcn_readfirstlane(tid >> 6);
  const int wm = wid & 3, wn = wid >> 2;
  const int l15 = lane & 15, gq = lane >> 4;
  const int xg = blockIdx.x & 7, idx = blockIdx.x >> 3;
  const int pm = (xg & 3) * 4 + (idx & 3), pn = (xg >> 2) * 8 + (idx >> 2);
  const unsigned lds0 = (unsigned)(uintptr_t)(LDSP(char))smem;
  asm volatile("" : "+s"(QKV));

  const unsigned voffA = (unsigned)((wid * 8 + (lane >> 3)) * 1024 + (((lane & 7) ^ ((wid & 1) * 4 + gq)) * 16));
  const int r12 = (lane * 43) >> 9, c12 = lane - r12 * 12;
  const bool validB = lane < 48;
  const unsigned voffB = (unsigned)((wid * 8 + r12) * 3072 + (((c12 >> 1) ^ (wid & 1)) * 16 + (c12 & 1) * 8) * 2);
  const char* baseA = (const char*)x + (size_t)pm * (128 * 1024);
  const char* baseB = (const char*)w + pn * 192;
  const int rot = ROT1 * ((idx >> 2) + 2 * (idx & 3));
  auto part = [&](int kc, int p) {
    const unsigned sb = lds0 + (kc % NST) * STAGE;
    const int kk = (kc + rot) & 7;
    if (p == 0) {
      glds(voffA, baseA + kk * 128, sb + wid * 1024);
      glds(voffA, baseA + kk * 128 + 65536, sb + 8192 + wid * 1024);
    } else if (validB) {
      glds(voffB, baseB + kk * (64 * 3072), sb + 16384 + wid * 1536);
      glds(voffB, baseB + kk * (64 * 3072) + 4 * 3072, sb + 16384 + wid * 1536 + 768);
    }
  };
  auto issue = [&](int kc) { part(kc, 0); part(kc, 1); };
  issue(0);
  BAR();
  issue(1);
  BAR();
  issue(2);
  TSTAMP(0, 1);

  unsigned a_addr[2][2], b_addr[3];
#pragma unroll
  for (int mi = 0; mi < 2; ++mi)
#pragma unroll
    for (int ks = 0; ks < 2; ++ks)
      a_addr[mi][ks] = lds0 + (wm * 32 + ((mi ^ wn) * 16) + l15) * 128 + (((4 * ks + gq) ^ (l15 >> 1)) * 16);
  {
    const int kl = gq * 8 + (l15 >> 2);
#pragma unroll
    for (int j = 0; j < 3; ++j)
      b_addr[j] = lds0 + 16384 + kl * 192 + (((3 * wn + j) ^ (gq & 1)) * 32) + (l15 & 3) * 8;
  }

  f32x4 acc[2][3], s1, s2, cs[3];
  const f32x4 z4 = {0.f, 0.f, 0.f, 0.f};
#pragma unroll
  for (int j = 0; j < 3; ++j) { acc[0][j] = z4; acc[1][j] = z4; cs[j] = z4; }
  s1 = z4; s2 = z4;
  bf16x8 af[2][2];
  bf16x4 bl[2][3], bh[2][3];
  const bf16 one = (bf16)1.0f;
  const bf16x8 ones = {one, one, one, one, one, one, one, one};

  auto rd = [&](int t, int set) {
    const int kc = t >> 1, ks = t & 1, st = kc % NST;
    const unsigned hi = (unsigned)((st & ~1) * STAGE);
    const int so = (st & 1) * STAGE;
#pragma unroll
    for (int j = 0; j < 3; ++j) {
      DSRTR(bl[set][j], b_addr[j] + hi, so + ks * 6144);
      DSRTR(bh[set][j], b_addr[j] + hi, so + ks * 6144 + 768);
      if (j < 2) DSR128(af[set][j], a_addr[j][ks] + hi, so);
    }
  };
  auto wt = [&](int set) {
    asm volatile("s_waitcnt lgkmcnt(0)" ::: "memory");
    PIN(af[set][0]); PIN(af[set][1]);
#pragma unroll
    for (int j = 0; j < 3; ++j) { PIN(bl[set][j]); PIN(bh[set][j]); }
  };

  using SC = Sched<2, 2, NST, NK, 6, 2>;
  wait_vm<(SC::cum(-1) - 2) * 2>();
  BAR();
  TSTAMP(0, 2);
  rd(0, 0);
  static_for<SC::cum(-1), SC::cum(0)>([&](auto n) { part(n.value >> 1, n.value & 1); });
  wt(0);
  static_for<0, NT>([&](auto tc) {
    constexpr int t = tc.value, kc = t >> 1, ks = t & 1, cur = t & 1, nxt = cur ^ 1;
    if constexpr (ks == 1 && kc + 1 < NK) {
      static_assert(SC::cum(t) >= (kc + 2) * 2, "schedule");
      wait_vm<(SC::cum(t) - (kc + 2) * 2) * 2 + (((CPF & 1) && t > CW1) ? 1 : 0)>();
      BAR();
      TSTAMP(0, 3 + kc);
    }
    if constexpr (t + 1 < NT) rd(t + 1, nxt);
    static_for<SC::cum(t), SC::cum(t + 1)>([&](auto n) { part(n.value >> 1, n.value & 1); });
    if constexpr ((CPF & 1) && t == CW1) {
      static_assert(SC::cum(t + 1) == NK * 2, "issued after all operand loads");
      code_warm<true>(wid, lane, lds0 + NST * STAGE + 2560 + wid * 256, baseA);
    }
    SCHED();
    {
      const bf16x8 b0 = cat8(bl[cur][0], bh[cur][0]), b1 = cat8(bl[cur][1], bh[cur][1]),
                   b2 = cat8(bl[cur][2], bh[cur][2]);
#pragma unroll
      for (int mi = 0; mi < 2; ++mi) {
        acc[mi][0] = mfma16(b0, af[cur][mi], acc[mi][0]);
        acc[mi][1] = mfma16(b1, af[cur][mi], acc[mi][1]);
        acc[mi][2] = mfma16(b2, af[cur][mi], acc[mi][2]);
      }
      s1 = mfma16(ones, af[cur][0], s1);
      s2 = mfma16(af[cur][0], af[cur][0], s2);
      if ((t & 3) == wm) {
        cs[0] = mfma16(b0, ones, cs[0]);
        cs[1] = mfma16(b1, ones, cs[1]);
        cs[2] = mfma16(b2, ones, cs[2]);
      }
    }
    SCHED();
    if constexpr (t + 1 < NT) wt(nxt);
  });
  TSTAMP(0, 20);

  float* st_ = (float*)(smem + NST * STAGE);
  float* csum = st_ + 256;
  {
    const float sq = (l15 & 2) ? ((l15 & 1) ? s2[3] : s2[2]) : ((l15 & 1) ? s2[1] : s2[0]);
    const f32x2 ms = ln_stats(s1[0], sq);
    if (gq == (l15 >> 2)) *(f32x2*)(st_ + (wm * 32 + wn * 16 + l15) * 2) = ms;
    if (l15 == 0) {
#pragma unroll
      for (int j = 0; j < 3; ++j) *(f32x4*)(csum + wm * 96 + (3 * wn + j) * 16 + gq * 4) = cs[j];
    }
  }
  __syncthreads();
  TSTAMP(0, 21);
  const int tile = pm * 4 + wm;
  const float qscale = 0.25f * 1.4426950408889634f;
  f32x2 ms[2];
#pragma unroll
  for (int mi = 0; mi < 2; ++mi) ms[mi] = *(const f32x2*)(st_ + (wm * 32 + (mi ^ wn) * 16 + l15) * 2);
  const int loq = (gq >> 1) * 256 + l15 * 8 + (gq & 1) * 4, lov = l15 * 16 + gq * 4;
#pragma unroll
  for (int j = 0; j < 3; ++j) {
    f32x4 c4 = z4;
#pragma unroll
    for (int p = 0; p < 4; ++p) c4 += *(const f32x4*)(csum + p * 96 + (3 * wn + j) * 16 + gq * 4);
    const int blk = pn * 6 + wn * 3 + j;
    const int typ = blk >> 5, h = blk & 31;
    const float sc = typ == 0 ? qscale : 1.0f;
    bf16* db = QKV + ((size_t)typ << 20) + (size_t)(h * 64 + tile) * 512 + (typ == 2 ? lov : loq);
    const int tfs = typ == 2 ? 256 : 128;
#pragma unroll
    for (int mi = 0; mi < 2; ++mi) {
      const float rs = ms[mi][1] * sc;
      bf16x4 o;
#pragma unroll
      for (int r = 0; r < 4; ++r) o[r] = (bf16)((acc[mi][j][r] - ms[mi][0] * c4[r]) * rs);
      st8<(WT & 1) != 0>(db + (mi ^ wn) * tfs, __builtin_bit_cast(u32x2, o));
    }
  }
  TSTAMP(0, 22);
  TFLUSH(0);
}

template <bool FULL, int NPF>
DEV void attn_body(unsigned kaddr, unsigned vaddr, bf16x8 qf, f32x16 cm0, f32x16 cm8, int wid, bf16* dst, unsigned lds0 TARG) {
  const float NEG = -1e30f;
  f32x16 z16;
#pragma unroll
  for (int r = 0; r < 16; ++r) z16[r] = 0.f;
  bf16x8 kf[9];
#pragma unroll
  for (int t = 0; t < 9; ++t)
    if (FULL || wid + t >= 8) DSR128(kf[t], kaddr, t * 1024);
  asm volatile("s_waitcnt lgkmcnt(0)" ::: "memory");
#pragma unroll
  for (int t = 0; t < 9; ++t) PIN(kf[t]);
  TSTAMP(1, 3);
  float m0 = NEG, m1 = NEG;
#pragma unroll
  for (int t = 0; t < 9; ++t)
    if (FULL || wid + t >= 8) {
      const f32x16 s = mfma32(kf[t], qf, t == 0 ? cm0 : (t == 8 ? cm8 : z16));
#pragma unroll
      for (int r = 0; r < 16; r += 4) {
        m0 = fmaxf(fmaxf(m0, s[r]), s[r + 1]);
        m1 = fmaxf(fmaxf(m1, s[r + 2]), s[r + 3]);
      }
    }
  float m = fmaxf(m0, m1);
  m = fmaxf(m, __shfl_xor(m, 32));
  f32x16 nm;
#pragma unroll
  for (int r = 0; r < 16; ++r) { nm[r] = -m; cm0[r] -= m; cm8[r] -= m; }
  TSTAMP(1, 4);
  wait_vm<NPF>();
  BAR();
  TSTAMP(1, 5);
  if (CPF & 2) code_warm<false>(wid, (int)(threadIdx.x & 63), lds0 + 59392 + wid * 256, nullptr);
  f32x16 oa = z16, ob = z16;
  f32x16 sc_, sn_;
  bf16x4 v0l[2], v0h[2], v1l[2], v1h[2];
  auto vrd = [&](int t, int b) {
    DSRTR(v0l[b], vaddr, t * 1024);
    DSRTR(v0h[b], vaddr, t * 1024 + 256);
    DSRTR(v1l[b], vaddr, t * 1024 + 512);
    DSRTR(v1h[b], vaddr, t * 1024 + 768);
  };
  if (FULL) { sc_ = mfma32(kf[0], qf, cm0); vrd(0, 0); }
  else sc_ = z16;
  sn_ = z16;
#pragma unroll
  for (int t = 0; t < 9; ++t) {
    const int b = t & 1;
    const bool cur_ok = FULL || wid + t >= 8, nxt_ok = t < 8 && (FULL || wid + t + 1 >= 8);
    if (cur_ok) {
      asm volatile("s_waitcnt lgkmcnt(0)" ::: "memory");
      PIN(v0l[b]); PIN(v0h[b]); PIN(v1l[b]); PIN(v1h[b]);
    }
    if (nxt_ok) {
      vrd(t + 1, b ^ 1);
      sn_ = mfma32(kf[t + 1], qf, t + 1 == 8 ? cm8 : nm);
    }
    SCHED();
    if (cur_ok) {
      bf16x8 p0, p1;
#pragma unroll
      for (int j = 0; j < 8; ++j) p0[j] = (bf16)__builtin_amdgcn_exp2f(sc_[j]);
      SCHED();
      oa = mfma32(cat8(v0l[b], v0h[b]), p0, oa);
      SCHED();
#pragma unroll
      for (int j = 0; j < 8; ++j) p1[j] = (bf16)__builtin_amdgcn_exp2f(sc_[8 + j]);
      SCHED();
      ob = mfma32(cat8(v1l[b], v1h[b]), p1, ob);
      SCHED();
    }
    sc_ = sn_;
  }
  TSTAMP(1, 6);
  const float inv = 1.0f / (oa[8] + ob[8]);
  unsigned a0 = pk2((oa[0] + ob[0]) * inv, (oa[1] + ob[1]) * inv), a1 = pk2((oa[2] + ob[2]) * inv, (oa[3] + ob[3]) * inv);
  unsigned b0 = pk2((oa[4] + ob[4]) * inv, (oa[5] + ob[5]) * inv), b1 = pk2((oa[6] + ob[6]) * inv, (oa[7] + ob[7]) * inv);
  swap32(a0, b0);
  swap32(a1, b1);
  st16<(WT & 2) != 0>(dst, u32x4{a0, a1, b0, b1});
}

extern "C" __global__ __launch_bounds__(512, 2) void k2_attn(const bf16* __restrict__ QKV, bf16* __restrict__ O,
                                                  const bf16* __restrict__ wo, const bf16* __restrict__ w1,
                                                  const bf16* __restrict__ w3, const bf16* __restrict__ w2,
                                                  bf16* __restrict__ Wt, float* __restrict__ CS,
                                                  bf16* __restrict__ W2t, bf16* __restrict__ Wot) {
  constexpr int LV = 16384, LONE = 32768, LSJ = 43008;
  extern __shared__ __attribute__((aligned(16))) char smem[];
  TDECL
  TSTAMP(1, 0);
  const int tid = threadIdx.x, lane = tid & 63;
  const int wid = __builtin_amdgcn_readfirstlane(tid >> 6);
  const int l15 = lane & 15, gq = lane >> 4;
  const int xg = blockIdx.x >> 5, h = (blockIdx.x & 7) * 4 + ((blockIdx.x >> 3) & 3);
  const unsigned lds0 = (unsigned)(uintptr_t)(LDSP(char))smem;
  const int t0 = xg * 8 - 8;
  const unsigned l16 = lane * 16;
  asm volatile("" : "+s"(O), "+s"(Wt), "+s"(CS), "+s"(W2t), "+s"(Wot));
  asm volatile("" : "+s"(wo), "+s"(w1), "+s"(w3), "+s"(w2));
  const bool ld = xg > 0 || wid >= 4;

  const char* kb = (const char*)QKV + (2u << 20) + (ptrdiff_t)(h * 64 + t0 + 2 * wid) * 1024;
  if (ld) {
    glds(l16, kb, lds0 + wid * 2048);
    glds(l16, kb + 1024, lds0 + wid * 2048 + 1024);
  }
  bf16x8 qf;
  GLD4(qf, l16, (const char*)QKV + (size_t)(h * 64 + xg * 8 + wid) * 1024, 0);
  if (ld) {
    glds(l16, kb + (2u << 20), lds0 + LV + wid * 2048);
    glds(l16, kb + (2u << 20) + 1024, lds0 + LV + wid * 2048 + 1024);
  }
  {
    const u32x4 o4 = {0x3f803f80u, 0x3f803f80u, 0x3f803f80u, 0x3f803f80u};
    *(u32x4*)(smem + LONE + tid * 16) = o4;
    if (tid < 128) *(u32x4*)(smem + LONE + 8192 + tid * 16) = o4;
  }
  f32x16 cm0, cm8;
  {
    const int c = lane & 31, hh = lane >> 5;
#pragma unroll
    for (int r = 0; r < 16; ++r) {
      const int kr = (r & 3) + 8 * (r >> 2) + 4 * hh;
      cm0[r] = (kr > c) ? 0.f : -1e30f;
      cm8[r] = (kr <= c) ? 0.f : -1e30f;
    }
    PIN(cm0); PIN(cm8);
  }
  TSTAMP(1, 1);
  if (ld) asm volatile("s_waitcnt vmcnt(2) lgkmcnt(0)" ::: "memory");
  else asm volatile("s_waitcnt vmcnt(0) lgkmcnt(0)" ::: "memory");
  PIN(qf);
  BAR();
  TSTAMP(1, 2);
  const int skc = blockIdx.x >> 5, smt = (blockIdx.x >> 4) & 1, spn = blockIdx.x & 15;
  u32x4 wr0, wr1;
  {
    const int wj = wid < 7 ? wid : 6;
    const char* sb = (const char*)(smt ? w3 : w1) + (size_t)(skc * 64) * (F_ * 2) + (spn * 112 + wj * 16) * 2;
    const unsigned vo = (unsigned)((lane >> 1) * (F_ * 2) + (lane & 1) * 16);
    glds(vo, sb, lds0 + LSJ + wid * 2048);
    glds(vo, sb + 32 * (F_ * 2), lds0 + LSJ + wid * 2048 + 1024);
    const char* o = (const char*)wo + (size_t)blockIdx.x * 2048;
    const char* r0 = wid < 7 ? (const char*)w2 + (size_t)blockIdx.x * 7168 + wid * 1024 : o;
    GLD4(wr0, l16, r0, 0);
    GLD4(wr1, l16, o, 1024);
  }

  const unsigned kaddr = lds0 + wid * 1024 + l16;
  const unsigned vlane = (unsigned)(((lane >> 5) * 4 + (l15 >> 2)) * 32 + (l15 & 3) * 8);
  const unsigned vaddr = (lane & 16) ? (lds0 + LONE + (vlane ^ 128u)) : (lds0 + LV + wid * 1024 + vlane);
  bf16* dst = O + (size_t)(((xg * 4 + (wid >> 1)) * H_ + h) * 64 + (wid & 1) * 32 + (lane & 31)) * 16 + (lane >> 5) * 8;
  if (xg > 0) attn_body<true, 4>(kaddr, vaddr, qf, cm0, cm8, wid, dst, lds0 TPASS);
  else attn_body<false, 4>(kaddr, vaddr, qf, cm0, cm8, wid, dst, lds0 TPASS);
  TSTAMP(1, 20);
  wait_vm<1>();
  {
    const unsigned co = (unsigned)(lane & 7) * 16;
    PIN(wr0); PIN(wr1);
    if (wid < 7) {
      st16<(WT & 2) != 0>((char*)W2t + (size_t)(lane >> 3) * (F_ * 128) + (blockIdx.x * 7 + wid) * 128 + co, wr0);
    } else {
      char* o = (char*)Wot + (size_t)(lane >> 3) * (D_ * 128) + blockIdx.x * 256 + co;
      st16<(WT & 2) != 0>(o, wr0);
      st16<(WT & 2) != 0>(o + 128, wr1);
    }
  }
  if (wid < 7) {
    const unsigned ra = lds0 + LSJ + wid * 2048 + (gq * 8 + (l15 >> 2)) * 32 + (l15 & 3) * 8;
    bf16x4 f[4];
    DSRTR(f[0], ra, 0);
    DSRTR(f[1], ra, 128);
    DSRTR(f[2], ra, 1024);
    DSRTR(f[3], ra, 1024 + 128);
    asm volatile("s_waitcnt lgkmcnt(0)" ::: "memory");
    float c = 0.f;
#pragma unroll
    for (int i = 0; i < 4; ++i) {
      PIN(f[i]);
#pragma unroll
      for (int e = 0; e < 4; ++e) c += (float)f[i][e];
    }
    char* wd = (char*)Wt + (size_t)(((spn * 8 + skc) * 4 + smt) * 7 + wid) * 1024 + l16;
    st16<(WT & 2) != 0>(wd, __builtin_bit_cast(u32x4, cat8(f[0], f[1])));
    st16<(WT & 2) != 0>(wd + 14 * 1024, __builtin_bit_cast(u32x4, cat8(f[2], f[3])));
    c += __shfl_xor(c, 16);
    c += __shfl_xor(c, 32);
    if (lane < 16) st4<(WT & 2) != 0>(CS + skc * (2 * F_) + smt * F_ + spn * 112 + wid * 16 + lane, __builtin_bit_cast(unsigned, c));
  }
  TSTAMP(1, 22);
  wait_vm<0>();
  TFLUSH(1);
}

template <int MODE, int KTOT>
DEV void gemm_body(const bf16* __restrict__ A, const bf16* __restrict__ B, const bf16* __restrict__ R,
                   bf16* __restrict__ O, float* __restrict__ RS, const bf16* __restrict__ PFW) {
  constexpr int NST = G_NST, STAGE = 32768, NK = KTOT / 128, NT = NK * 4;
  extern __shared__ __attribute__((aligned(16))) char smem[];
  TDECL
  TSTAMP(MODE == 0 ? 2 : 4, 0);
  const int tid = threadIdx.x, lane = tid & 63;
  const int wid = __builtin_amdgcn_readfirstlane(tid >> 6);
  const int wm = wid & 3, wn = wid >> 2;
  const int l15 = lane & 15, gq = lane >> 4;
  const int xg = blockIdx.x & 7, idx = blockIdx.x >> 3;
#define PNX 0
  const int pm = xg * 4 + (idx >> 3), pn = (idx & 7) ^ PNX;
  if (MODE == 0) asm volatile("" : "+s"(RS), "+s"(PFW));
  const unsigned lds0 = (unsigned)(uintptr_t)(LDSP(char))smem;

  u32x4 res;
  const int tok = pm * 64 + wm * 16 + l15;
  const int ncol = pn * 64 + wn * 32 + (gq & 1) * 16 + (gq >> 1) * 8;
  const unsigned hoff = (unsigned)(((((tok >> 7) * 8 + pn) * 128 + (tok & 127)) * 64 + (ncol & 63)) * 2);
  GLD4(res, MODE == 0 ? (unsigned)((tok * D_ + ncol) * 2) : hoff, R, 0);

  const unsigned voffA = (unsigned)(wid * 1024 + lane * 16);
  const int pB = ((lane & 7) >> 1) ^ (((lane >> 4) & 1) | ((wid & 1) << 1));
  const unsigned voffB = (unsigned)((wid * 8 + (lane >> 3)) * 128 + pB * 32 + (lane & 1) * 16);
  const char* baseA = (const char*)A + (size_t)pm * (KTOT * 128);
  const char* baseB = (const char*)B + pn * (KTOT * 128);
  const unsigned sbw = lds0 + wid * 1024;
  const int rot = MODE == 0 ? ROT3 * ((pn + (idx >> 3)) & 3) : ROT5 * ((pn * 2 + (idx >> 3) * 3) % NK);
  auto part = [&](int kc, int p) {
    const unsigned sb = sbw + (kc % NST) * STAGE;
    int kk = kc + rot;
    if (kk >= NK) kk -= NK;
    if (p == 0) glds(voffA, baseA + (size_t)kk * 16384, sb);
    if (p == 1) glds(voffA, baseA + (size_t)kk * 16384 + 8192, sb + 8192);
    if (p == 2) glds(voffB, baseB + (size_t)kk * 16384, sb + 16384);
    if (p == 3) glds(voffB, baseB + (size_t)kk * 16384 + 8192, sb + 16384 + 8192);
  };
  auto issue = [&](int kc) { part(kc, 0); part(kc, 1); part(kc, 2); part(kc, 3); };
  issue(0);
  BAR();
  issue(1);
  BAR();
  issue(2);
  TSTAMP(MODE == 0 ? 2 : 4, 1);

  unsigned a_addr[4], b_addr[2];
#pragma unroll
  for (int ks = 0; ks < 4; ++ks) a_addr[ks] = lds0 + (2 * ks + (gq >> 1)) * 2048 + (wm * 16 + l15) * 32 + (gq & 1) * 16;
  {
    const int kl = gq * 8 + (l15 >> 2);
    const int fr = ((l15 >> 3) & 1) | ((gq & 1) << 1);
#pragma unroll
    for (int j = 0; j < 2; ++j) b_addr[j] = lds0 + 16384 + kl * 128 + (((2 * wn + j) ^ fr) * 32) + (l15 & 3) * 8;
  }

  f32x4 acc[2];
  const f32x4 z4 = {0.f, 0.f, 0.f, 0.f};
  acc[0] = z4; acc[1] = z4;
  bf16x8 af[2];
  bf16x4 bl[2][2], bh[2][2];

  auto rd = [&](int t, int set) {
    const int kc = t >> 2, ks = t & 3, st = kc % NST;
    const unsigned hi = (unsigned)((st & ~1) * STAGE);
    const int so = (st & 1) * STAGE;
    DSRTR(bl[set][0], b_addr[0] + hi, so + ks * 4096);
    DSRTR(bh[set][0], b_addr[0] + hi, so + ks * 4096 + 512);
    DSR128(af[set], a_addr[ks] + hi, so);
    DSRTR(bl[set][1], b_addr[1] + hi, so + ks * 4096);
    DSRTR(bh[set][1], b_addr[1] + hi, so + ks * 4096 + 512);
  };
  auto wt = [&](int set) {
    asm volatile("s_waitcnt lgkmcnt(0)" ::: "memory");
    PIN(af[set]);
    PIN(bl[set][0]); PIN(bh[set][0]); PIN(bl[set][1]); PIN(bh[set][1]);
  };

  using SC = Sched<4, 4, NST, NK, 12, G_R>;
  wait_vm<SC::cum(-1) - 4>();
  BAR();
  TSTAMP(MODE == 0 ? 2 : 4, 2);
  rd(0, 0);
  static_for<SC::cum(-1), SC::cum(0)>([&](auto n) { part(n.value >> 2, n.value & 3); });
  wt(0);
  static_for<0, NT>([&](auto tc) {
    constexpr int t = tc.value, kc = t >> 2, ks = t & 3, cur = t & 1, nxt = cur ^ 1;
    if constexpr (ks == 3 && kc + 1 < NK) {
      static_assert(SC::cum(t) >= (kc + 2) * 4, "schedule");
      constexpr int npf = (MODE == 0 && PF3 && t > PF3T) ? (t - PF3T > 4 ? 4 : t - PF3T) : 0;
      static_assert(npf == 0 || SC::cum(PF3T) == NK * 4, "warm-up loads come after all operand loads");
      wait_vm<SC::cum(t) - (kc + 2) * 4 + npf + ((MODE == 0 && (CPF & 4) && t > CW3) ? 1 : 0)>();
      BAR();
      TSTAMP(MODE == 0 ? 2 : 4, 3 + kc);
    }
    if constexpr (t + 1 < NT) rd(t + 1, nxt);
    static_for<SC::cum(t), SC::cum(t + 1)>([&](auto n) { part(n.value >> 2, n.value & 3); });
    if constexpr (MODE == 0 && (CPF & 4) && t == CW3) {
      static_assert(SC::cum(t + 1) == NK * 4, "issued after all operand loads");
      code_warm<true>(wid, lane, lds0 + NST * STAGE + wid * 1024, baseA);
    }
    if constexpr (MODE == 0 && PF3 && t >= PF3T && t < PF3T + 4) {
      glds((unsigned)(lane * 16),
           (const char*)PFW + (size_t)(xg >> 1) * (4 * 229376) + idx * 28672 + (t - PF3T) * 8192 + (t - PF3T < 3 ? wid : (wid & 3)) * 1024,
           lds0 + NST * STAGE + wid * 1024);
    }
    SCHED();
    acc[0] = mfma16(cat8(bl[cur][0], bh[cur][0]), af[cur], acc[0]);
    acc[1] = mfma16(cat8(bl[cur][1], bh[cur][1]), af[cur], acc[1]);
    SCHED();
    if constexpr (t + 1 < NT) wt(nxt);
  });
  TSTAMP(MODE == 0 ? 2 : 4, 20);
  PIN(res);
  float s = 0.f, q = 0.f;
  {
#pragma unroll
    for (int r = 0; r < 4; ++r) { float a = acc[0][r], b = acc[1][r]; swap16f(a, b); acc[0][r] = a; acc[1][r] = b; }
    const bf16x8 rr = __builtin_bit_cast(bf16x8, res);
    bf16x8 o;
#pragma unroll
    for (int j = 0; j < 2; ++j)
#pragma unroll
      for (int r = 0; r < 4; ++r) {
        o[j * 4 + r] = (bf16)((float)rr[j * 4 + r] + bfr(acc[j][r]));
        const float v = (float)o[j * 4 + r];
        s += v;
        q += v * v;
      }
    st16<(WT & (MODE == 0 ? 4 : 16)) != 0>((char*)O + (MODE == 0 ? hoff : (unsigned)((tok * D_ + ncol) * 2)), __builtin_bit_cast(u32x4, o));
  }
  if (MODE == 0) {
    s += __shfl_xor(s, 16); s += __shfl_xor(s, 32);
    q += __shfl_xor(q, 16); q += __shfl_xor(q, 32);
    if (gq == 0) st8<(WT & 4) != 0>(RS + ((size_t)(pn * 2 + wn) * S_ + tok) * 2, __builtin_bit_cast(u32x2, f32x2{s, q}));
  }
  if (MODE == 0) wait_vm<0>();
  TSTAMP(MODE == 0 ? 2 : 4, 22);
  TFLUSH(MODE == 0 ? 2 : 4);
}
extern "C" __global__ __launch_bounds__(512, 2) void k3_wo(const bf16* __restrict__ A, const bf16* __restrict__ B,
                                                           const bf16* __restrict__ R, bf16* __restrict__ O,
                                                           float* __restrict__ RS, const bf16* __restrict__ PFW) {
  gemm_body<0, 512>(A, B, R, O, RS, PFW);
}
extern "C" __global__ __launch_bounds__(512, 2) void k5_down(const bf16* __restrict__ A, const bf16* __restrict__ B,
                                                             const bf16* __restrict__ R, bf16* __restrict__ O) {
  gemm_body<1, 1792>(A, B, R, O, nullptr, nullptr);
}

template <int NP, int P0>
DEV void gu_body(char* smem, unsigned lds0, int tid, int wid, int pm, int pn, const bf16* __restrict__ Hh,
                 const bf16* __restrict__ Wt, const float* __restrict__ RS, const float* __restrict__ CS,
                 bf16* __restrict__ Hm, const char* pf TARG) {
  constexpr int NST = 3, STAGE = 45056, NK = 8, LPC = 2 + NP, NT = 16;
  const int lane = tid & 63, wm = wid & 3;
  const int l15 = lane & 15, gq = lane >> 4;
  const unsigned l16 = lane * 16;
  const unsigned voffA = (unsigned)((wid * 8 + (lane >> 3)) * 128 + (((lane & 7) ^ ((wid & 1) * 4 + gq)) * 16));
  const char* baseA = (const char*)Hh + (size_t)pm * (128 * 1024);
  const int pc0 = (P0 == 0 ? 0 : 16) + wm * NP;
  const char* baseW = (const char*)Wt + (size_t)pn * (8 * 28672) + pc0 * 1024;
  const int rot = ROT4 * (pm + 2 * pn);
  auto piece = [&](int kc, int i) {
    const unsigned sb = lds0 + (kc % NST) * STAGE;
    const int kk = (kc + rot) & 7;
    if (i == 0) glds(voffA, baseA + kk * 16384, sb + wid * 1024);
    else if (i == 1) glds(voffA, baseA + kk * 16384 + 8192, sb + 8192 + wid * 1024);
    else glds(l16, baseW + kk * 28672 + (i - 2) * 1024, sb + 16384 + (pc0 + i - 2) * 1024);
  };
  auto issue = [&](int kc) {
#pragma unroll
    for (int i = 0; i < LPC; ++i) piece(kc, i);
  };
  float* st_ = (float*)(smem + NST * STAGE);
  float* csum = st_ + 256;
  if (P0 == 0) {
    if (wid < 2) {
      f32x2 sv[16];
      const char* rb = (const char*)RS + pm * 1024;
      const unsigned vo = (unsigned)(tid * 8);
#pragma unroll
      for (int i = 0; i < 16; ++i) GLD2(sv[i], vo, rb + i * (S_ * 8), 0);
      issue(0);
      BAR();
      issue(1);
      wait_vm<2 * LPC>();
#pragma unroll
      for (int i = 0; i < 16; ++i) PIN(sv[i]);
      f32x2 a = sv[0];
#pragma unroll
      for (int i = 1; i < 16; ++i) a += sv[i];
      *(f32x2*)(st_ + tid * 2) = ln_stats(a[0], a[1]);
    } else {
      issue(0);
      BAR();
      issue(1);
    }
  } else {
    float sc[8];
    int c = tid - 256;
    const bool ok = c < 224;
    if (!ok) c = 0;
    const unsigned vo = (unsigned)(((c < 112 ? 0 : F_ - 112) + pn * 112 + c) * 4);
#pragma unroll
    for (int i = 0; i < 8; ++i) GLD1(sc[i], vo, (const char*)CS + i * (2 * F_ * 4), 0);
    issue(0);
    BAR();
    issue(1);
    wait_vm<2 * LPC>();
#pragma unroll
    for (int i = 0; i < 8; ++i) PIN(sc[i]);
    float a = sc[0];
#pragma unroll
    for (int i = 1; i < 8; ++i) a += sc[i];
    if (ok) csum[c] = a;
  }
  TSTAMP(3, 1);

  unsigned a_addr[2];
#pragma unroll
  for (int ks = 0; ks < 2; ++ks) a_addr[ks] = lds0 + (wm * 32 + l15) * 128 + (((4 * ks + gq) ^ (l15 >> 1)) * 16);
  const unsigned b_addr = lds0 + 16384 + P0 * 1024 + l16;
  f32x4 ag[2][NP], au[2][NP];
  const f32x4 z4 = {0.f, 0.f, 0.f, 0.f};
#pragma unroll
  for (int j = 0; j < NP; ++j) { ag[0][j] = z4; ag[1][j] = z4; au[0][j] = z4; au[1][j] = z4; }
  bf16x8 af[2][2], bg[2][NP], bu[2][NP];
  auto rd = [&](int t, int set) {
    const int kc = t >> 1, ks = t & 1, st = kc % NST;
    const unsigned so = st * STAGE;
    DSR128(af[set][0], a_addr[ks] + so, 0);
    DSR128(af[set][1], a_addr[ks] + so, 2048);
#pragma unroll
    for (int j = 0; j < NP; ++j) {
      DSR128(bg[set][j], b_addr + so, (ks * 14 + j) * 1024);
      DSR128(bu[set][j], b_addr + so, (ks * 14 + 7 + j) * 1024);
    }
  };
  auto wt = [&](int set) {
    asm volatile("s_waitcnt lgkmcnt(0)" ::: "memory");
    PIN(af[set][0]); PIN(af[set][1]);
#pragma unroll
    for (int j = 0; j < NP; ++j) { PIN(bg[set][j]); PIN(bu[set][j]); }
  };
  asm volatile("s_waitcnt vmcnt(%0) lgkmcnt(0)" ::"n"(LPC) : "memory");
  BAR();
  TSTAMP(3, 2);
  rd(0, 0);
  piece(NST - 1, 0); piece(NST - 1, 1); piece(NST - 1, 2);
  wt(0);
  constexpr int NTM = NT - 4, NU = 2 * NP;
  bf16x8 aT[4][2], bT[2][2][2];
  auto uj = [&](int u) { return u == 0 ? 0 : (u == NU - 1 ? NP - 1 : ((u & 1) ? (u + 1) / 2 : u / 2 - 1)); };
  auto uc = [&](int u) { return (u == 0 || ((u & 1) && u != NU - 1)) ? 0 : 1; };
  auto rdA = [&](int c) {
#pragma unroll
    for (int ks = 0; ks < 2; ++ks) {
      DSR128(aT[c * 2 + ks][0], a_addr[ks] + ((NK - 2 + c) % NST) * STAGE, 0);
      DSR128(aT[c * 2 + ks][1], a_addr[ks] + ((NK - 2 + c) % NST) * STAGE, 2048);
    }
  };
  auto rdB = [&](int u) {
#pragma unroll
    for (int ks = 0; ks < 2; ++ks) {
      DSR128(bT[u & 1][ks][0], b_addr + ((NK - 2 + uc(u)) % NST) * STAGE, (ks * 14 + uj(u)) * 1024);
      DSR128(bT[u & 1][ks][1], b_addr + ((NK - 2 + uc(u)) % NST) * STAGE, (ks * 14 + 7 + uj(u)) * 1024);
    }
  };
#pragma unroll
  for (int t = 0; t < NTM; ++t) {
    const int kc = t >> 1, ks = t & 1, cur = t & 1, nxt = cur ^ 1;
    if (ks == 1) {
      const int last = (kc + NST - 1 < NK - 1) ? (kc + NST - 1) : (NK - 1);
      wait_vm_n((last - (kc + 1)) * LPC);
      BAR();
      TSTAMP(3, 3 + kc);
    }
    if (t + 1 < NTM) rd(t + 1, nxt);
    else { rdA(0); rdB(0); }
    const int ic = (t + 1) / 2 + NST - 1, ip = ((t + 1) & 1) * 3;
    SCHED();
#pragma unroll
    for (int j = 0; j < NP; ++j) {
      ag[0][j] = mfma16(bg[cur][j], af[cur][0], ag[0][j]);
      ag[1][j] = mfma16(bg[cur][j], af[cur][1], ag[1][j]);
      au[0][j] = mfma16(bu[cur][j], af[cur][0], au[0][j]);
      au[1][j] = mfma16(bu[cur][j], af[cur][1], au[1][j]);
      SCHED();
      if (j < 3 && ic < NK && ip + j < LPC) piece(ic, ip + j);
      if (PF4 && j < 2 && t == NTM - 1) glds(l16, pf + j * 8192, lds0 + NST * STAGE + 2048 + wid * 1024);
      SCHED();
    }
    if (t + 1 < NTM) wt(nxt);
  }
  auto wtT = [&](bool a0, bool a1, int u) {
    asm volatile("s_waitcnt lgkmcnt(0)" ::: "memory");
    if (a0) { PIN(aT[0][0]); PIN(aT[0][1]); PIN(aT[1][0]); PIN(aT[1][1]); }
    if (a1) { PIN(aT[2][0]); PIN(aT[2][1]); PIN(aT[3][0]); PIN(aT[3][1]); }
    PIN(bT[u & 1][0][0]); PIN(bT[u & 1][0][1]); PIN(bT[u & 1][1][0]); PIN(bT[u & 1][1][1]);
  };
  wtT(true, false, 0);
  TSTAMP(3, 20);
  bf16* dst0 = Hm + ((size_t)((pm * 2 + (wm >> 1)) * 112 + pn * 7 + P0) * 64 + (wm & 1) * 32 + l15) * 16;
  const unsigned caddr = lds0 + NST * STAGE + 1024 + (P0 * 16 + gq * 4) * 4;
  f32x2 nm[2], le2[2], iv2[2];
  f32x4 cg[2], cu[2];
  unsigned pk[NP][2][2];
  f32x2 tg, tu, te, td;
  auto epi = [&](int j, int mi, int q) {
    const int hf = q >> 2;
    if ((q & 3) == 0) {
      tg = __builtin_elementwise_fma(f32x2{cg[j & 1][2 * hf], cg[j & 1][2 * hf + 1]}, nm[mi],
                                     f32x2{ag[mi][j][2 * hf], ag[mi][j][2 * hf + 1]});
      tu = __builtin_elementwise_fma(f32x2{cu[j & 1][2 * hf], cu[j & 1][2 * hf + 1]}, nm[mi],
                                     f32x2{au[mi][j][2 * hf], au[mi][j][2 * hf + 1]});
      te = tg * le2[mi];
    } else if ((q & 3) == 1) {
      te = f32x2{__builtin_amdgcn_exp2f(te[0]), __builtin_amdgcn_exp2f(te[1])};
    } else if ((q & 3) == 2) {
      td = __builtin_elementwise_fma(te, iv2[mi], iv2[mi]);
      tu = tg * tu;
    } else {
      const f32x2 rc = {__builtin_amdgcn_rcpf(td[0]), __builtin_amdgcn_rcpf(td[1])};
      const f32x2 o = tu * rc;
      pk[j][mi][hf] = pk2(o[0], o[1]);
    }
    if (q == 7) {
      if (j & 1) {
        swap16(pk[j - 1][mi][0], pk[j][mi][0]);
        swap16(pk[j - 1][mi][1], pk[j][mi][1]);
        st16<(WT & 8) != 0>(dst0 + ((size_t)(j - 1 + (gq & 1)) * 64 + mi * 16) * 16 + (gq >> 1) * 8,
                            u32x4{pk[j - 1][mi][0], pk[j - 1][mi][1], pk[j][mi][0], pk[j][mi][1]});
      } else if (j == NP - 1) {
        st8<(WT & 8) != 0>(dst0 + ((size_t)j * 64 + mi * 16) * 16 + gq * 4, u32x2{pk[j][mi][0], pk[j][mi][1]});
      }
    }
  };
#pragma unroll
  for (int u = 0; u < NU; ++u) {
    const int j = uj(u), c = uc(u);
    if (u == 1) {
      wait_vm_n(PF4 ? 4 : 0);
      BAR();
      TSTAMP(3, 9);
      rdA(1);
    }
    if (u + 1 < NU) rdB(u + 1);
    const int cj = u == NU - 1 ? NP - 1 : ((u >= 2 && !(u & 1)) ? (u - 2) / 2 : -1);
    if (cj >= 0) {
      DSR128(cg[cj & 1], caddr, cj * 64);
      DSR128(cu[cj & 1], caddr, cj * 64 + 448);
    }
    if (u == 1) {
#pragma unroll
      for (int mi = 0; mi < 2; ++mi) {
        const f32x2 ms = *(const f32x2*)(st_ + (wm * 32 + mi * 16 + l15) * 2);
        nm[mi] = f32x2{-ms[0], -ms[0]};
        const float le = ms[1] * -1.4426950408889634f, iv = 1.0f / (ms[1] * ms[1]);
        le2[mi] = f32x2{le, le};
        iv2[mi] = f32x2{iv, iv};
      }
    }
    SCHED();
#pragma unroll
    for (int q = 0; q < 8; ++q) {
      const int ks = q >> 2, mi = q & 1;
      if (q & 2) au[mi][j] = mfma16(bT[u & 1][ks][1], aT[c * 2 + ks][mi], au[mi][j]);
      else ag[mi][j] = mfma16(bT[u & 1][ks][0], aT[c * 2 + ks][mi], ag[mi][j]);
      SCHED();
      if (u >= 3) epi((u - 3) >> 1, (u - 3) & 1, q);
      if (PF4 && u < 3 && (q == 1 || q == 5) && 2 + u * 2 + (q >> 2) < 7)
        glds(l16, pf + (2 + u * 2 + (q >> 2)) * 8192, lds0 + NST * STAGE + 2048 + wid * 1024);
      if ((CPF & 8) && u == 3 && q == 3) code_warm<false>(wid, lane, lds0 + NST * STAGE + 2048 + wid * 1024, nullptr);
      SCHED();
    }
    if (u + 1 < NU) {
      wtT(false, u == 1, u + 1);
    } else {
      asm volatile("s_waitcnt lgkmcnt(0)" ::: "memory");
    }
    if (cj >= 0) { PIN(cg[cj & 1]); PIN(cu[cj & 1]); }
  }
  TSTAMP(3, 21);
#pragma unroll
  for (int e = NU - 3; e < NU; ++e)
#pragma unroll
    for (int q = 0; q < 8; ++q) epi(e >> 1, e & 1, q);
}

extern "C" __global__ __launch_bounds__(512, 2) void k4_gu(const bf16* __restrict__ Hh, const bf16* __restrict__ Wt,
                                                const float* __restrict__ RS, const float* __restrict__ CS,
                                                bf16* __restrict__ Hm, const bf16* __restrict__ W2t) {
  extern __shared__ __attribute__((aligned(16))) char smem[];
  TDECL
  TSTAMP(3, 0);
  const int tid = threadIdx.x;
  const int wid = __builtin_amdgcn_readfirstlane(tid >> 6);
  const int xg = blockIdx.x & 7, idx = blockIdx.x >> 3;
  const int pm = (xg & 1) * 8 + (idx & 7), pn = (xg >> 1) * 4 + (idx >> 3);
  asm volatile("" : "+s"(RS), "+s"(CS), "+s"(Hm), "+s"(W2t));
  const unsigned lds0 = (unsigned)(uintptr_t)(LDSP(char))smem;
  const char* pf = (const char*)W2t + idx * 57344 + wid * 1024;
  if (wid < 4) gu_body<4, 0>(smem, lds0, tid, wid, pm, pn, Hh, Wt, RS, CS, Hm, pf TPASS);
  else gu_body<3, 4>(smem, lds0, tid, wid, pm, pn, Hh, Wt, RS, CS, Hm, pf TPASS);
  wait_vm<0>();
  TSTAMP(3, 22);
  TFLUSH(3);
  asm volatile("k4_gu_tail:");
}

extern "C" void kernel_launch(void* const* d_in, const int* in_sizes, int n_in, void* d_out, int out_size, void* d_ws,
                              size_t ws_size, hipStream_t stream) {
  const bf16* x = (const bf16*)d_in[0];
  const bf16* wqkv = (const bf16*)d_in[1];
  const bf16* wo = (const bf16*)d_in[2];
  const bf16* w1 = (const bf16*)d_in[4];
  const bf16* w3 = (const bf16*)d_in[5];
  const bf16* w2 = (const bf16*)d_in[6];
  bf16* out = (bf16*)d_out;

  char* ws = (char*)d_ws;
  bf16* QKV = (bf16*)(ws);
  bf16* Oa = (bf16*)(ws + (7u << 20));
  bf16* Hh = (bf16*)(ws + (9u << 20));
  bf16* Hm = (bf16*)(ws + (11u << 20));
  float* RS = (float*)(ws + (18u << 20));
  float* CS = (float*)(ws + (19u << 20));
  bf16* Wt = (bf16*)(ws + (20u << 20));
  bf16* W2t = (bf16*)(ws + (24u << 20));
  bf16* Wot = (bf16*)(ws + (26u << 20));

  {
    constexpr int lds = K1_NST * 28672 + 2560 + 2048;
    (void)hipFuncSetAttribute((const void*)k1_qkv, hipFuncAttributeMaxDynamicSharedMemorySize, lds);
    k1_qkv<<<256, 512, lds, stream>>>(x, wqkv, QKV);
  }
  {
    constexpr int lds = 59392 + 2048;
    (void)hipFuncSetAttribute((const void*)k2_attn, hipFuncAttributeMaxDynamicSharedMemorySize, lds);
    k2_attn<<<256, 512, lds, stream>>>(QKV, Oa, wo, w1, w3, w2, Wt, CS, W2t, Wot);
  }
  {
    auto k = k3_wo;
    constexpr int lds = G_NST * 32768 + 8192;
    (void)hipFuncSetAttribute((const void*)k, hipFuncAttributeMaxDynamicSharedMemorySize, lds);
    k<<<256, 512, lds, stream>>>(Oa, Wot, x, Hh, RS, Wt);
  }
  {
    constexpr int lds = 3 * 45056 + 2048 + 8192;
    (void)hipFuncSetAttribute((const void*)k4_gu, hipFuncAttributeMaxDynamicSharedMemorySize, lds);
    k4_gu<<<256, 512, lds, stream>>>(Hh, Wt, RS, CS, Hm, W2t);
  }
  {
    auto k = k5_down;
    constexpr int lds = G_NST * 32768;
    (void)hipFuncSetAttribute((const void*)k, hipFuncAttributeMaxDynamicSharedMemorySize, lds);
    k<<<256, 512, lds, stream>>>(Hm, W2t, Hh, out);
  }
}
```

```cpp
#include <hip/hip_runtime.h>
#include <stdint.h>

typedef __attribute__((ext_vector_type(8))) __bf16 bf16x8;
typedef __attribute__((ext_vector_type(4))) __bf16 bf16x4;
typedef __attribute__((ext_vector_type(2))) __bf16 bf16x2;
typedef __attribute__((ext_vector_type(2))) float f32x2;
typedef __attribute__((ext_vector_type(4))) float f32x4;
typedef __attribute__((ext_vector_type(16))) float f32x16;
typedef __attribute__((ext_vector_type(4))) unsigned int u32x4;
typedef __attribute__((ext_vector_type(2))) unsigned int u32x2;
typedef __bf16 bf16;

#define LDSP(T) __attribute__((address_space(3))) T*
#define DEV __device__ __forceinline__

#define K1_NST 4
#define G_NST 4
#define G_R 1
#define ROT1 0
#define ROT3 0
#define ROT4 0
#define ROT5 0
#define PF4 1
#define PFT 5
#define LA2 1
#define PF3 0
#define PF3T 4
constexpr int S_ = 2048, D_ = 512, H_ = 32, F_ = 1792;

DEV float bfr(float x) { return (float)(bf16)x; }

template <int N>
DEV void wait_vm() {
  asm volatile("s_waitcnt vmcnt(%0)" ::"n"(N) : "memory");
}
DEV void wait_vm_n(int n) {
  switch (n) {
#define WC(X) case X: wait_vm<X>(); break;
    WC(0) WC(1) WC(2) WC(3) WC(4) WC(5) WC(6) WC(7) WC(8) WC(9) WC(10) WC(11) WC(12) WC(13) WC(14) WC(15)
    WC(16) WC(17) WC(18) WC(19) WC(20) WC(21) WC(22) WC(23) WC(24)
#undef WC
    default: wait_vm<0>(); break;
  }
}

DEV void glds(unsigned voff, const void* sbase, unsigned ldsaddr) {
  ldsaddr = __builtin_amdgcn_readfirstlane(ldsaddr);
  asm volatile("s_mov_b32 m0, %2\n\ts_nop 0\n\tglobal_load_lds_dwordx4 %0, %1" ::"v"(voff), "s"(sbase), "s"(ldsaddr)
               : "memory");
}
#define DSR128(dst, addr, off) asm volatile("ds_read_b128 %0, %1 offset:%2" : "=v"(dst) : "v"(addr), "i"(off))
#define DSRTR(dst, addr, off) asm volatile("ds_read_b64_tr_b16 %0, %1 offset:%2" : "=v"(dst) : "v"(addr), "i"(off))
#define PIN(x) asm volatile("" : "+v"(x))
#define GLD4(dst, voff, sbase, off) \
  asm volatile("global_load_dwordx4 %0, %1, %2 offset:%3" : "=v"(dst) : "v"(voff), "s"(sbase), "i"(off))
#define GLD2(dst, voff, sbase, off) \
  asm volatile("global_load_dwordx2 %0, %1, %2 offset:%3" : "=v"(dst) : "v"(voff), "s"(sbase), "i"(off))
#define GLD1(dst, voff, sbase, off) \
  asm volatile("global_load_dword %0, %1, %2 offset:%3" : "=v"(dst) : "v"(voff), "s"(sbase), "i"(off))
#define WT 31
template <bool W>
DEV void st16(void* p, u32x4 v) {
  if constexpr (W) asm volatile("global_store_dwordx4 %0, %1, off sc1\n\ts_nop 1" ::"v"(p), "v"(v) : "memory");
  else *(u32x4*)p = v;
}
template <bool W>
DEV void st8(void* p, u32x2 v) {
  if constexpr (W) asm volatile("global_store_dwordx2 %0, %1, off sc1\n\ts_nop 1" ::"v"(p), "v"(v) : "memory");
  else *(u32x2*)p = v;
}
template <bool W>
DEV void st4(void* p, unsigned v) {
  if constexpr (W) asm volatile("global_store_dword %0, %1, off sc1" ::"v"(p), "v"(v) : "memory");
  else *(unsigned*)p = v;
}
#define BAR() __builtin_amdgcn_s_barrier()
#define SCHED() __builtin_amdgcn_sched_barrier(0)

#define TDECL
#define TSTAMP(m, i) do { } while (0)
#define TFLUSH(m) do { } while (0)
#define TARG
#define TPASS

#define CPF 15
#define CODE_ADDR(dst, sym)                                                                                  \
  asm volatile("s_getpc_b64 s[30:31]\n\ts_add_u32 s30, s30, " #sym "@rel32@lo+4\n\ts_addc_u32 s31, s31, " #sym \
               "@rel32@hi+12\n\ts_mov_b64 %0, s[30:31]"                                                      \
               : "=s"(dst)::"s30", "s31", "scc")
#define CPW 5
#define XPF 3
constexpr int CW1 = 8, CW3 = 3, CW4 = 14;
template <bool ALL>
DEV void code_warm(int wid, int lane, unsigned ldsaddr, const char* other) {
  const bool sel = wid == CPW && blockIdx.x < 64;
  if (!ALL && !sel) return;
  const char* a;
  int d;
  CODE_ADDR(a, k1_qkv);
  asm volatile("s_mov_b32 %0, k4_gu_tail-k1_qkv" : "=s"(d));
  const int last = d < 0 ? 0 : d >> 7;
  const int ln = sel ? (int)(blockIdx.x >> 3) * 64 + lane : 0;
  unsigned vo = (unsigned)((ln < last ? ln : last) * 128);
  if ((XPF & 1) && (blockIdx.x >> 3) == 6) {
    a = (const char*)((uintptr_t)__builtin_amdgcn_kernarg_segment_ptr() & ~(uintptr_t)4095);
    vo = (unsigned)((lane & 31) * 128);
  }
  if ((XPF & 2) && (blockIdx.x >> 3) == 7) {
    int d2;
    CODE_ADDR(a, k1_qkv.kd);
    asm volatile("s_mov_b32 %0, k4_gu.kd-k1_qkv.kd" : "=s"(d2));
    const int l2 = d2 < 0 ? 0 : d2 >> 6;
    vo = (unsigned)((lane < l2 ? lane : l2) * 64);
  }
  if (ALL && !sel) { a = other; vo = 0; }
  ldsaddr = __builtin_amdgcn_readfirstlane(ldsaddr);
  asm volatile("s_mov_b32 m0, %2\n\ts_nop 0\n\tglobal_load_lds_dword %0, %1" ::"v"(vo), "s"(a), "s"(ldsaddr) : "memory");
}

template <int V> struct IC { static constexpr int value = V; };
template <int I, int N, class F>
DEV void static_for(F&& f) {
  if constexpr (I < N) {
    f(IC<I>{});
    static_for<I + 1, N>(f);
  }
}
template <int P, int KS, int NST, int NK, int PRE, int R>
struct Sched {
  static constexpr int avail(int t) {
    int n = ((t + 1) / KS + NST - 1) * P + ((t + 1) % KS + 1) * (P / KS);
    return n > NK * P ? NK * P : n;
  }
  static constexpr int cum(int t) {
    int n = PRE;
    for (int i = -1; i < t; ++i) { int a = avail(i); n = (n + R < a) ? n + R : a; }
    return n;
  }
};

DEV void swap32(unsigned& a, unsigned& b) {
  const auto r = __builtin_amdgcn_permlane32_swap(a, b, false, false);
  a = r[0]; b = r[1];
}
DEV void swap16(unsigned& a, unsigned& b) {
  const auto r = __builtin_amdgcn_permlane16_swap(a, b, false, false);
  a = r[0]; b = r[1];
}
DEV void swap16f(float& a, float& b) {
  unsigned x = __builtin_bit_cast(unsigned, a), y = __builtin_bit_cast(unsigned, b);
  swap16(x, y);
  a = __builtin_bit_cast(float, x); b = __builtin_bit_cast(float, y);
}
DEV unsigned pk2(float a, float b) { return __builtin_bit_cast(unsigned, bf16x2{(bf16)a, (bf16)b}); }
DEV bf16x8 cat8(bf16x4 a, bf16x4 b) { return __builtin_shufflevector(a, b, 0, 1, 2, 3, 4, 5, 6, 7); }
DEV f32x4 mfma16(bf16x8 a, bf16x8 b, f32x4 c) { return __builtin_amdgcn_mfma_f32_16x16x32_bf16(a, b, c, 0, 0, 0); }
DEV f32x16 mfma32(bf16x8 a, bf16x8 b, f32x16 c) { return __builtin_amdgcn_mfma_f32_32x32x16_bf16(a, b, c, 0, 0, 0); }

DEV f32x2 ln_stats(float sum, float sq) {
  float m = sum * (1.0f / D_);
  float var = fmaxf(sq * (1.0f / D_) - m * m, 0.f);
  float vb = bfr(var);
  float sd = bfr(__builtin_sqrtf(bfr(vb + 1e-5f)));
  f32x2 r;
  r[0] = bfr(m);
  r[1] = 1.0f / sd;
  return r;
}

extern "C" __global__ __launch_bounds__(512, 2) void k1_qkv(const bf16* __restrict__ x, const bf16* __restrict__ w,
                                                            bf16* __restrict__ QKV) {
  constexpr int NST = K1_NST, STAGE = 28672, NK = 8, NT = 16;
  extern __shared__ __attribute__((aligned(16))) char smem[];
  TDECL
  TSTAMP(0, 0);
  const int tid = threadIdx.x, lane = tid & 63;
  const int wid = __builtin_amdgcn_readfirstlane(tid >> 6);
  const int wm = wid & 3, wn = wid >> 2;
  const int l15 = lane & 15, gq = lane >> 4;
  const int xg = blockIdx.x & 7, idx = blockIdx.x >> 3;
  const int pm = (xg & 3) * 4 + (idx & 3), pn = (xg >> 2) * 8 + (idx >> 2);
  const unsigned lds0 = (unsigned)(uintptr_t)(LDSP(char))smem;
  asm volatile("" : "+s"(QKV));

  const unsigned voffA = (unsigned)((wid * 8 + (lane >> 3)) * 1024 + (((lane & 7) ^ ((wid & 1) * 4 + gq)) * 16));
  const int r12 = (lane * 43) >> 9, c12 = lane - r12 * 12;
  const bool validB = lane < 48;
  const unsigned voffB = (unsigned)((wid * 8 + r12) * 3072 + (((c12 >> 1) ^ (wid & 1)) * 16 + (c12 & 1) * 8) * 2);
  const char* baseA = (const char*)x + (size_t)pm * (128 * 1024);
  const char* baseB = (const char*)w + pn * 192;
  const int rot = ROT1 * ((idx >> 2) + 2 * (idx & 3));
  auto part = [&](int kc, int p) {
    const unsigned sb = lds0 + (kc % NST) * STAGE;
    const int kk = (kc + rot) & 7;
    if (p == 0) {
      glds(voffA, baseA + kk * 128, sb + wid * 1024);
      glds(voffA, baseA + kk * 128 + 65536, sb + 8192 + wid * 1024);
    } else if (validB) {
      glds(voffB, baseB + kk * (64 * 3072), sb + 16384 + wid * 1536);
      glds(voffB, baseB + kk * (64 * 3072) + 4 * 3072, sb + 16384 + wid * 1536 + 768);
    }
  };
  auto issue = [&](int kc) { part(kc, 0); part(kc, 1); };
  issue(0);
  BAR();
  issue(1);
  BAR();
  issue(2);
  TSTAMP(0, 1);

  unsigned a_addr[2][2], b_addr[3];
#pragma unroll
  for (int mi = 0; mi < 2; ++mi)
#pragma unroll
    for (int ks = 0; ks < 2; ++ks)
      a_addr[mi][ks] = lds0 + (wm * 32 + ((mi ^ wn) * 16) + l15) * 128 + (((4 * ks + gq) ^ (l15 >> 1)) * 16);
  {
    const int kl = gq * 8 + (l15 >> 2);
#pragma unroll
    for (int j = 0; j < 3; ++j)
      b_addr[j] = lds0 + 16384 + kl * 192 + (((3 * wn + j) ^ (gq & 1)) * 32) + (l15 & 3) * 8;
  }

  f32x4 acc[2][3], s1, s2, cs[3];
  const f32x4 z4 = {0.f, 0.f, 0.f, 0.f};
#pragma unroll
  for (int j = 0; j < 3; ++j) { acc[0][j] = z4; acc[1][j] = z4; cs[j] = z4; }
  s1 = z4; s2 = z4;
  bf16x8 af[2][2];
  bf16x4 bl[2][3], bh[2][3];
  const bf16 one = (bf16)1.0f;
  const bf16x8 ones = {one, one, one, one, one, one, one, one};

  auto rd = [&](int t, int set) {
    const int kc = t >> 1, ks = t & 1, st = kc % NST;
    const unsigned hi = (unsigned)((st & ~1) * STAGE);
    const int so = (st & 1) * STAGE;
#pragma unroll
    for (int j = 0; j < 3; ++j) {
      DSRTR(bl[set][j], b_addr[j] + hi, so + ks * 6144);
      DSRTR(bh[set][j], b_addr[j] + hi, so + ks * 6144 + 768);
      if (j < 2) DSR128(af[set][j], a_addr[j][ks] + hi, so);
    }
  };
  auto wt = [&](int set) {
    asm volatile("s_waitcnt lgkmcnt(0)" ::: "memory");
    PIN(af[set][0]); PIN(af[set][1]);
#pragma unroll
    for (int j = 0; j < 3; ++j) { PIN(bl[set][j]); PIN(bh[set][j]); }
  };

  using SC = Sched<2, 2, NST, NK, 6, 2>;
  wait_vm<(SC::cum(-1) - 2) * 2>();
  BAR();
  TSTAMP(0, 2);
  rd(0, 0);
  static_for<SC::cum(-1), SC::cum(0)>([&](auto n) { part(n.value >> 1, n.value & 1); });
  wt(0);
  static_for<0, NT>([&](auto tc) {
    constexpr int t = tc.value, kc = t >> 1, ks = t & 1, cur = t & 1, nxt = cur ^ 1;
    if constexpr (ks == 1 && kc + 1 < NK) {
      static_assert(SC::cum(t) >= (kc + 2) * 2, "schedule");
      wait_vm<(SC::cum(t) - (kc + 2) * 2) * 2 + (((CPF & 1) && t > CW1) ? 1 : 0)>();
      BAR();
      TSTAMP(0, 3 + kc);
    }
    if constexpr (t + 1 < NT) rd(t + 1, nxt);
    static_for<SC::cum(t), SC::cum(t + 1)>([&](auto n) { part(n.value >> 1, n.value & 1); });
    if constexpr ((CPF & 1) && t == CW1) {
      static_assert(SC::cum(t + 1) == NK * 2, "issued after all operand loads");
      code_warm<true>(wid, lane, lds0 + NST * STAGE + 2560 + wid * 256, baseA);
    }
    SCHED();
    {
      const bf16x8 b0 = cat8(bl[cur][0], bh[cur][0]), b1 = cat8(bl[cur][1], bh[cur][1]),
                   b2 = cat8(bl[cur][2], bh[cur][2]);
#pragma unroll
      for (int mi = 0; mi < 2; ++mi) {
        acc[mi][0] = mfma16(b0, af[cur][mi], acc[mi][0]);
        acc[mi][1] = mfma16(b1, af[cur][mi], acc[mi][1]);
        acc[mi][2] = mfma16(b2, af[cur][mi], acc[mi][2]);
      }
      s1 = mfma16(ones, af[cur][0], s1);
      s2 = mfma16(af[cur][0], af[cur][0], s2);
      if ((t & 3) == wm) {
        cs[0] = mfma16(b0, ones, cs[0]);
        cs[1] = mfma16(b1, ones, cs[1]);
        cs[2] = mfma16(b2, ones, cs[2]);
      }
    }
    SCHED();
    if constexpr (t + 1 < NT) wt(nxt);
  });
  TSTAMP(0, 20);

  float* st_ = (float*)(smem + NST * STAGE);
  float* csum = st_ + 256;
  {
    const float sq = (l15 & 2) ? ((l15 & 1) ? s2[3] : s2[2]) : ((l15 & 1) ? s2[1] : s2[0]);
    const f32x2 ms = ln_stats(s1[0], sq);
    if (gq == (l15 >> 2)) *(f32x2*)(st_ + (wm * 32 + wn * 16 + l15) * 2) = ms;
    if (l15 == 0) {
#pragma unroll
      for (int j = 0; j < 3; ++j) *(f32x4*)(csum + wm * 96 + (3 * wn + j) * 16 + gq * 4) = cs[j];
    }
  }
  __syncthreads();
  TSTAMP(0, 21);
  const int tile = pm * 4 + wm;
  const float qscale = 0.25f * 1.4426950408889634f;
  f32x2 ms[2];
#pragma unroll
  for (int mi = 0; mi < 2; ++mi) ms[mi] = *(const f32x2*)(st_ + (wm * 32 + (mi ^ wn) * 16 + l15) * 2);
  const int loq = (gq >> 1) * 256 + l15 * 8 + (gq & 1) * 4, lov = l15 * 16 + gq * 4;
#pragma unroll
  for (int j = 0; j < 3; ++j) {
    f32x4 c4 = z4;
#pragma unroll
    for (int p = 0; p < 4; ++p) c4 += *(const f32x4*)(csum + p * 96 + (3 * wn + j) * 16 + gq * 4);
    const int blk = pn * 6 + wn * 3 + j;
    const int typ = blk >> 5, h = blk & 31;
    const float sc = typ == 0 ? qscale : 1.0f;
    bf16* db = QKV + ((size_t)typ << 20) + (size_t)(h * 64 + tile) * 512 + (typ == 2 ? lov : loq);
    const int tfs = typ == 2 ? 256 : 128;
#pragma unroll
    for (int mi = 0; mi < 2; ++mi) {
      const float rs = ms[mi][1] * sc;
      bf16x4 o;
#pragma unroll
      for (int r = 0; r < 4; ++r) o[r] = (bf16)((acc[mi][j][r] - ms[mi][0] * c4[r]) * rs);
      st8<(WT & 1) != 0>(db + (mi ^ wn) * tfs, __builtin_bit_cast(u32x2, o));
    }
  }
  TSTAMP(0, 22);
  TFLUSH(0);
}

template <bool FULL, int NPF>
DEV void attn_body(unsigned kaddr, unsigned vaddr, bf16x8 qf, f32x16 cm0, f32x16 cm8, int wid, bf16* dst, unsigned lds0 TARG) {
  const float NEG = -1e30f;
  f32x16 z16;
#pragma unroll
  for (int r = 0; r < 16; ++r) z16[r] = 0.f;
  bf16x8 kf[9];
#pragma unroll
  for (int t = 0; t < 9; ++t)
    if (FULL || wid + t >= 8) DSR128(kf[t], kaddr, t * 1024);
  asm volatile("s_waitcnt lgkmcnt(0)" ::: "memory");
#pragma unroll
  for (int t = 0; t < 9; ++t) PIN(kf[t]);
  TSTAMP(1, 3);
  float m0 = NEG, m1 = NEG;
#pragma unroll
  for (int t = 0; t < 9; ++t)
    if (FULL || wid + t >= 8) {
      const f32x16 s = mfma32(kf[t], qf, t == 0 ? cm0 : (t == 8 ? cm8 : z16));
#pragma unroll
      for (int r = 0; r < 16; r += 4) {
        m0 = fmaxf(fmaxf(m0, s[r]), s[r + 1]);
        m1 = fmaxf(fmaxf(m1, s[r + 2]), s[r + 3]);
      }
    }
  float m = fmaxf(m0, m1);
  m = fmaxf(m, __shfl_xor(m, 32));
  f32x16 nm;
#pragma unroll
  for (int r = 0; r < 16; ++r) { nm[r] = -m; cm0[r] -= m; cm8[r] -= m; }
  TSTAMP(1, 4);
  wait_vm<NPF>();
  BAR();
  TSTAMP(1, 5);
  if (CPF & 2) code_warm<false>(wid, (int)(threadIdx.x & 63), lds0 + 59392 + wid * 256, nullptr);
  f32x16 oa = z16, ob = z16;
  f32x16 sc_, sn_;
  bf16x4 v0l[2], v0h[2], v1l[2], v1h[2];
  auto vrd = [&](int t, int b) {
    DSRTR(v0l[b], vaddr, t * 1024);
    DSRTR(v0h[b], vaddr, t * 1024 + 256);
    DSRTR(v1l[b], vaddr, t * 1024 + 512);
    DSRTR(v1h[b], vaddr, t * 1024 + 768);
  };
  if (FULL) { sc_ = mfma32(kf[0], qf, cm0); vrd(0, 0); }
  else sc_ = z16;
  sn_ = z16;
#pragma unroll
  for (int t = 0; t < 9; ++t) {
    const int b = t & 1;
    const bool cur_ok = FULL || wid + t >= 8, nxt_ok = t < 8 && (FULL || wid + t + 1 >= 8);
    if (cur_ok) {
      asm volatile("s_waitcnt lgkmcnt(0)" ::: "memory");
      PIN(v0l[b]); PIN(v0h[b]); PIN(v1l[b]); PIN(v1h[b]);
    }
    if (nxt_ok) {
      vrd(t + 1, b ^ 1);
      sn_ = mfma32(kf[t + 1], qf, t + 1 == 8 ? cm8 : nm);
    }
    SCHED();
    if (cur_ok) {
      bf16x8 p0, p1;
#pragma unroll
      for (int j = 0; j < 8; ++j) p0[j] = (bf16)__builtin_amdgcn_exp2f(sc_[j]);
      SCHED();
      oa = mfma32(cat8(v0l[b], v0h[b]), p0, oa);
      SCHED();
#pragma unroll
      for (int j = 0; j < 8; ++j) p1[j] = (bf16)__builtin_amdgcn_exp2f(sc_[8 + j]);
      SCHED();
      ob = mfma32(cat8(v1l[b], v1h[b]), p1, ob);
      SCHED();
    }
    sc_ = sn_;
  }
  TSTAMP(1, 6);
  const float inv = 1.0f / (oa[8] + ob[8]);
  unsigned a0 = pk2((oa[0] + ob[0]) * inv, (oa[1] + ob[1]) * inv), a1 = pk2((oa[2] + ob[2]) * inv, (oa[3] + ob[3]) * inv);
  unsigned b0 = pk2((oa[4] + ob[4]) * inv, (oa[5] + ob[5]) * inv), b1 = pk2((oa[6] + ob[6]) * inv, (oa[7] + ob[7]) * inv);
  swap32(a0, b0);
  swap32(a1, b1);
  st16<(WT & 2) != 0>(dst, u32x4{a0, a1, b0, b1});
}

extern "C" __global__ __launch_bounds__(512, 2) void k2_attn(const bf16* __restrict__ QKV, bf16* __restrict__ O,
                                                  const bf16* __restrict__ wo, const bf16* __restrict__ w1,
                                                  const bf16* __restrict__ w3, const bf16* __restrict__ w2,
                                                  bf16* __restrict__ Wt, float* __restrict__ CS,
                                                  bf16* __restrict__ W2t, bf16* __restrict__ Wot) {
  constexpr int LV = 16384, LONE = 32768, LSJ = 43008;
  extern __shared__ __attribute__((aligned(16))) char smem[];
  TDECL
  TSTAMP(1, 0);
  const int tid = threadIdx.x, lane = tid & 63;
  const int wid = __builtin_amdgcn_readfirstlane(tid >> 6);
  const int l15 = lane & 15, gq = lane >> 4;
  const int xg = blockIdx.x >> 5, h = (blockIdx.x & 7) * 4 + ((blockIdx.x >> 3) & 3);
  const unsigned lds0 = (unsigned)(uintptr_t)(LDSP(char))smem;
  const int t0 = xg * 8 - 8;
  const unsigned l16 = lane * 16;
  asm volatile("" : "+s"(O), "+s"(Wt), "+s"(CS), "+s"(W2t), "+s"(Wot));
  asm volatile("" : "+s"(wo), "+s"(w1), "+s"(w3), "+s"(w2));
  const bool ld = xg > 0 || wid >= 4;

  const char* kb = (const char*)QKV + (2u << 20) + (ptrdiff_t)(h * 64 + t0 + 2 * wid) * 1024;
  if (ld) {
    glds(l16, kb, lds0 + wid * 2048);
    glds(l16, kb + 1024, lds0 + wid * 2048 + 1024);
  }
  bf16x8 qf;
  GLD4(qf, l16, (const char*)QKV + (size_t)(h * 64 + xg * 8 + wid) * 1024, 0);
  if (ld) {
    glds(l16, kb + (2u << 20), lds0 + LV + wid * 2048);
    glds(l16, kb + (2u << 20) + 1024, lds0 + LV + wid * 2048 + 1024);
  }
  {
    const u32x4 o4 = {0x3f803f80u, 0x3f803f80u, 0x3f803f80u, 0x3f803f80u};
    *(u32x4*)(smem + LONE + tid * 16) = o4;
    if (tid < 128) *(u32x4*)(smem + LONE + 8192 + tid * 16) = o4;
  }
  f32x16 cm0, cm8;
  {
    const int c = lane & 31, hh = lane >> 5;
#pragma unroll
    for (int r = 0; r < 16; ++r) {
      const int kr = (r & 3) + 8 * (r >> 2) + 4 * hh;
      cm0[r] = (kr > c) ? 0.f : -1e30f;
      cm8[r] = (kr <= c) ? 0.f : -1e30f;
    }
    PIN(cm0); PIN(cm8);
  }
  TSTAMP(1, 1);
  if (ld) asm volatile("s_waitcnt vmcnt(2) lgkmcnt(0)" ::: "memory");
  else asm volatile("s_waitcnt vmcnt(0) lgkmcnt(0)" ::: "memory");
  PIN(qf);
  BAR();
  TSTAMP(1, 2);
  const int skc = blockIdx.x >> 5, smt = (blockIdx.x >> 4) & 1, spn = blockIdx.x & 15;
  u32x4 wr0, wr1;
  {
    const int wj = wid < 7 ? wid : 6;
    const char* sb = (const char*)(smt ? w3 : w1) + (size_t)(skc * 64) * (F_ * 2) + (spn * 112 + wj * 16) * 2;
    const unsigned vo = (unsigned)((lane >> 1) * (F_ * 2) + (lane & 1) * 16);
    glds(vo, sb, lds0 + LSJ + wid * 2048);
    glds(vo, sb + 32 * (F_ * 2), lds0 + LSJ + wid * 2048 + 1024);
    const char* o = (const char*)wo + (size_t)blockIdx.x * 2048;
    const char* r0 = wid < 7 ? (const char*)w2 + (size_t)blockIdx.x * 7168 + wid * 1024 : o;
    GLD4(wr0, l16, r0, 0);
    GLD4(wr1, l16, o, 1024);
  }

  const unsigned kaddr = lds0 + wid * 1024 + l16;
  const unsigned vlane = (unsigned)(((lane >> 5) * 4 + (l15 >> 2)) * 32 + (l15 & 3) * 8);
  const unsigned vaddr = (lane & 16) ? (lds0 + LONE + (vlane ^ 128u)) : (lds0 + LV + wid * 1024 + vlane);
  bf16* dst = O + (size_t)(((xg * 4 + (wid >> 1)) * H_ + h) * 64 + (wid & 1) * 32 + (lane & 31)) * 16 + (lane >> 5) * 8;
  if (xg > 0) attn_body<true, 4>(kaddr, vaddr, qf, cm0, cm8, wid, dst, lds0 TPASS);
  else attn_body<false, 4>(kaddr, vaddr, qf, cm0, cm8, wid, dst, lds0 TPASS);
  TSTAMP(1, 20);
  wait_vm<1>();
  {
    const unsigned co = (unsigned)(lane & 7) * 16;
    PIN(wr0); PIN(wr1);
    if (wid < 7) {
      st16<(WT & 2) != 0>((char*)W2t + (size_t)(lane >> 3) * (F_ * 128) + (blockIdx.x * 7 + wid) * 128 + co, wr0);
    } else {
      char* o = (char*)Wot + (size_t)(lane >> 3) * (D_ * 128) + blockIdx.x * 256 + co;
      st16<(WT & 2) != 0>(o, wr0);
      st16<(WT & 2) != 0>(o + 128, wr1);
    }
  }
  if (wid < 7) {
    const unsigned ra = lds0 + LSJ + wid * 2048 + (gq * 8 + (l15 >> 2)) * 32 + (l15 & 3) * 8;
    bf16x4 f[4];
    DSRTR(f[0], ra, 0);
    DSRTR(f[1], ra, 128);
    DSRTR(f[2], ra, 1024);
    DSRTR(f[3], ra, 1024 + 128);
    asm volatile("s_waitcnt lgkmcnt(0)" ::: "memory");
    float c = 0.f;
#pragma unroll
    for (int i = 0; i < 4; ++i) {
      PIN(f[i]);
#pragma unroll
      for (int e = 0; e < 4; ++e) c += (float)f[i][e];
    }
    char* wd = (char*)Wt + (size_t)(((spn * 8 + skc) * 4 + smt) * 7 + wid) * 1024 + l16;
    st16<(WT & 2) != 0>(wd, __builtin_bit_cast(u32x4, cat8(f[0], f[1])));
    st16<(WT & 2) != 0>(wd + 14 * 1024, __builtin_bit_cast(u32x4, cat8(f[2], f[3])));
    c += __shfl_xor(c, 16);
    c += __shfl_xor(c, 32);
    if (lane < 16) st4<(WT & 2) != 0>(CS + skc * (2 * F_) + smt * F_ + spn * 112 + wid * 16 + lane, __builtin_bit_cast(unsigned, c));
  }
  TSTAMP(1, 22);
  wait_vm<0>();
  TFLUSH(1);
}

template <int MODE, int KTOT>
DEV void gemm_body(const bf16* __restrict__ A, const bf16* __restrict__ B, const bf16* __restrict__ R,
                   bf16* __restrict__ O, float* __restrict__ RS, const bf16* __restrict__ PFW) {
  constexpr int NST = G_NST, STAGE = 32768, NK = KTOT / 128, NT = NK * 4;
  extern __shared__ __attribute__((aligned(16))) char smem[];
  TDECL
  TSTAMP(MODE == 0 ? 2 : 4, 0);
  const int tid = threadIdx.x, lane = tid & 63;
  const int wid = __builtin_amdgcn_readfirstlane(tid >> 6);
  const int wm = wid & 3, wn = wid >> 2;
  const int l15 = lane & 15, gq = lane >> 4;
  const int xg = blockIdx.x & 7, idx = blockIdx.x >> 3;
#define PNX 0
  const int pm = xg * 4 + (idx >> 3), pn = (idx & 7) ^ PNX;
  if (MODE == 0) asm volatile("" : "+s"(RS), "+s"(PFW));
  const unsigned lds0 = (unsigned)(uintptr_t)(LDSP(char))smem;

  u32x4 res;
  const int tok = pm * 64 + wm * 16 + l15;
  const int ncol = pn * 64 + wn * 32 + (gq & 1) * 16 + (gq >> 1) * 8;
  const unsigned hoff = (unsigned)(((((tok >> 7) * 8 + pn) * 128 + (tok & 127)) * 64 + (ncol & 63)) * 2);
  GLD4(res, MODE == 0 ? (unsigned)((tok * D_ + ncol) * 2) : hoff, R, 0);

  const unsigned voffA = (unsigned)(wid * 1024 + lane * 16);
  const int pB = ((lane & 7) >> 1) ^ (((lane >> 4) & 1) | ((wid & 1) << 1));
  const unsigned voffB = (unsigned)((wid * 8 + (lane >> 3)) * 128 + pB * 32 + (lane & 1) * 16);
  const char* baseA = (const char*)A + (size_t)pm * (KTOT * 128);
  const char* baseB = (const char*)B + pn * (KTOT * 128);
  const unsigned sbw = lds0 + wid * 1024;
  const int rot = MODE == 0 ? ROT3 * ((pn + (idx >> 3)) & 3) : ROT5 * ((pn * 2 + (idx >> 3) * 3) % NK);
  auto part = [&](int kc, int p) {
    const unsigned sb = sbw + (kc % NST) * STAGE;
    int kk = kc + rot;
    if (kk >= NK) kk -= NK;
    if (p == 0) glds(voffA, baseA + (size_t)kk * 16384, sb);
    if (p == 1) glds(voffA, baseA + (size_t)kk * 16384 + 8192, sb + 8192);
    if (p == 2) glds(voffB, baseB + (size_t)kk * 16384, sb + 16384);
    if (p == 3) glds(voffB, baseB + (size_t)kk * 16384 + 8192, sb + 16384 + 8192);
  };
  auto issue = [&](int kc) { part(kc, 0); part(kc, 1); part(kc, 2); part(kc, 3); };
  issue(0);
  BAR();
  issue(1);
  BAR();
  issue(2);
  TSTAMP(MODE == 0 ? 2 : 4, 1);

  unsigned a_addr[4], b_addr[2];
#pragma unroll
  for (int ks = 0; ks < 4; ++ks) a_addr[ks] = lds0 + (2 * ks + (gq >> 1)) * 2048 + (wm * 16 + l15) * 32 + (gq & 1) * 16;
  {
    const int kl = gq * 8 + (l15 >> 2);
    const int fr = ((l15 >> 3) & 1) | ((gq & 1) << 1);
#pragma unroll
    for (int j = 0; j < 2; ++j) b_addr[j] = lds0 + 16384 + kl * 128 + (((2 * wn + j) ^ fr) * 32) + (l15 & 3) * 8;
  }

  f32x4 acc[2];
  const f32x4 z4 = {0.f, 0.f, 0.f, 0.f};
  acc[0] = z4; acc[1] = z4;
  constexpr int NS = LA2 ? 3 : 2;
  bf16x8 af[NS];
  bf16x4 bl[NS][2], bh[NS][2];

  auto rd = [&](int t, int set) {
    const int kc = t >> 2, ks = t & 3, st = kc % NST;
    const unsigned hi = (unsigned)((st & ~1) * STAGE);
    const int so = (st & 1) * STAGE;
    DSRTR(bl[set][0], b_addr[0] + hi, so + ks * 4096);
    DSRTR(bh[set][0], b_addr[0] + hi, so + ks * 4096 + 512);
    DSR128(af[set], a_addr[ks] + hi, so);
    DSRTR(bl[set][1], b_addr[1] + hi, so + ks * 4096);
    DSRTR(bh[set][1], b_addr[1] + hi, so + ks * 4096 + 512);
  };
  auto wt = [&](int set) {
    asm volatile("s_waitcnt lgkmcnt(0)" ::: "memory");
    PIN(af[set]);
    PIN(bl[set][0]); PIN(bh[set][0]); PIN(bl[set][1]); PIN(bh[set][1]);
  };

  using SC = Sched<4, 4, NST, NK, 12, G_R>;
  auto pin = [&](int set) {
    PIN(af[set]);
    PIN(bl[set][0]); PIN(bh[set][0]); PIN(bl[set][1]); PIN(bh[set][1]);
  };
  wait_vm<SC::cum(-1) - 4>();
  BAR();
  TSTAMP(MODE == 0 ? 2 : 4, 2);
  rd(0, 0);
  if (LA2) rd(1, 1);
  static_for<SC::cum(-1), SC::cum(0)>([&](auto n) { part(n.value >> 2, n.value & 3); });
  if (!LA2) wt(0);
  static_for<0, NT>([&](auto tc) {
    constexpr int t = tc.value, kc = t >> 2, ks = t & 3, cur = t % NS, nxt = (t + NS - 1) % NS;
    constexpr bool sync = ks == (LA2 ? 2 : 3) && kc + 1 < NK;
    if constexpr (sync) {
      static_assert(SC::cum(t) >= (kc + 2) * 4, "schedule");
      constexpr int npf = (MODE == 0 && PF3 && t > PF3T) ? (t - PF3T > 4 ? 4 : t - PF3T) : 0;
      static_assert(npf == 0 || SC::cum(PF3T) == NK * 4, "warm-up loads come after all operand loads");
      if (LA2) {
        asm volatile("s_waitcnt lgkmcnt(0)" ::: "memory");
        pin(cur); pin((t + 1) % NS);
      }
      wait_vm<SC::cum(t) - (kc + 2) * 4 + npf + ((MODE == 0 && (CPF & 4) && t > CW3) ? 1 : 0)>();
      BAR();
      TSTAMP(MODE == 0 ? 2 : 4, 3 + kc);
    }
    if constexpr (t + NS - 1 < NT) rd(t + NS - 1, nxt);
    static_for<SC::cum(t), SC::cum(t + 1)>([&](auto n) { part(n.value >> 2, n.value & 3); });
    if constexpr (MODE == 0 && (CPF & 4) && t == CW3) {
      static_assert(SC::cum(t + 1) == NK * 4, "issued after all operand loads");
      code_warm<true>(wid, lane, lds0 + NST * STAGE + wid * 1024, baseA);
    }
    if constexpr (MODE == 0 && PF3 && t >= PF3T && t < PF3T + 4) {
      glds((unsigned)(lane * 16),
           (const char*)PFW + (size_t)(xg >> 1) * (4 * 229376) + idx * 28672 + (t - PF3T) * 8192 + (t - PF3T < 3 ? wid : (wid & 3)) * 1024,
           lds0 + NST * STAGE + wid * 1024);
    }
    if constexpr (LA2 && !sync && !(ks == 3 && kc + 1 < NK)) {
      asm volatile("s_waitcnt lgkmcnt(%0)" ::"n"(t + 2 < NT ? 10 : (t + 1 < NT ? 5 : 0)) : "memory");
      pin(cur);
    }
    SCHED();
    acc[0] = mfma16(cat8(bl[cur][0], bh[cur][0]), af[cur], acc[0]);
    acc[1] = mfma16(cat8(bl[cur][1], bh[cur][1]), af[cur], acc[1]);
    SCHED();
    if constexpr (!LA2 && t + 1 < NT) wt(nxt);
  });
  TSTAMP(MODE == 0 ? 2 : 4, 20);
  PIN(res);
  float s = 0.f, q = 0.f;
  {
#pragma unroll
    for (int r = 0; r < 4; ++r) { float a = acc[0][r], b = acc[1][r]; swap16f(a, b); acc[0][r] = a; acc[1][r] = b; }
    const bf16x8 rr = __builtin_bit_cast(bf16x8, res);
    bf16x8 o;
#pragma unroll
    for (int j = 0; j < 2; ++j)
#pragma unroll
      for (int r = 0; r < 4; ++r) {
        o[j * 4 + r] = (bf16)((float)rr[j * 4 + r] + bfr(acc[j][r]));
        const float v = (float)o[j * 4 + r];
        s += v;
        q += v * v;
      }
    st16<(WT & (MODE == 0 ? 4 : 16)) != 0>((char*)O + (MODE == 0 ? hoff : (unsigned)((tok * D_ + ncol) * 2)), __builtin_bit_cast(u32x4, o));
  }
  if (MODE == 0) {
    s += __shfl_xor(s, 16); s += __shfl_xor(s, 32);
    q += __shfl_xor(q, 16); q += __shfl_xor(q, 32);
    if (gq == 0) st8<(WT & 4) != 0>(RS + ((size_t)(pn * 2 + wn) * S_ + tok) * 2, __builtin_bit_cast(u32x2, f32x2{s, q}));
  }
  if (MODE == 0) wait_vm<0>();
  TSTAMP(MODE == 0 ? 2 : 4, 22);
  TFLUSH(MODE == 0 ? 2 : 4);
}
extern "C" __global__ __launch_bounds__(512, 2) void k3_wo(const bf16* __restrict__ A, const bf16* __restrict__ B,
                                                           const bf16* __restrict__ R, bf16* __restrict__ O,
                                                           float* __restrict__ RS, const bf16* __restrict__ PFW) {
  gemm_body<0, 512>(A, B, R, O, RS, PFW);
}
extern "C" __global__ __launch_bounds__(512, 2) void k5_down(const bf16* __restrict__ A, const bf16* __restrict__ B,
                                                             const bf16* __restrict__ R, bf16* __restrict__ O) {
  gemm_body<1, 1792>(A, B, R, O, nullptr, nullptr);
}

template <int NP, int P0>
DEV void gu_body(char* smem, unsigned lds0, int tid, int wid, int pm, int pn, const bf16* __restrict__ Hh,
                 const bf16* __restrict__ Wt, const float* __restrict__ RS, const float* __restrict__ CS,
                 bf16* __restrict__ Hm, const char* pf TARG) {
  constexpr int NST = 3, STAGE = 45056, NK = 8, LPC = 2 + NP, NT = 16;
  const int lane = tid & 63, wm = wid & 3;
  const int l15 = lane & 15, gq = lane >> 4;
  const unsigned l16 = lane * 16;
  const unsigned voffA = (unsigned)((wid * 8 + (lane >> 3)) * 128 + (((lane & 7) ^ ((wid & 1) * 4 + gq)) * 16));
  const char* baseA = (const char*)Hh + (size_t)pm * (128 * 1024);
  const int pc0 = (P0 == 0 ? 0 : 16) + wm * NP;
  const char* baseW = (const char*)Wt + (size_t)pn * (8 * 28672) + pc0 * 1024;
  const int rot = ROT4 * (pm + 2 * pn);
  auto piece = [&](int kc, int i) {
    const unsigned sb = lds0 + (kc % NST) * STAGE;
    const int kk = (kc + rot) & 7;
    if (i == 0) glds(voffA, baseA + kk * 16384, sb + wid * 1024);
    else if (i == 1) glds(voffA, baseA + kk * 16384 + 8192, sb + 8192 + wid * 1024);
    else glds(l16, baseW + kk * 28672 + (i - 2) * 1024, sb + 16384 + (pc0 + i - 2) * 1024);
  };
  auto issue = [&](int kc) {
#pragma unroll
    for (int i = 0; i < LPC; ++i) piece(kc, i);
  };
  float* st_ = (float*)(smem + NST * STAGE);
  float* csum = st_ + 256;
  if (P0 == 0) {
    if (wid < 2) {
      f32x2 sv[16];
      const char* rb = (const char*)RS + pm * 1024;
      const unsigned vo = (unsigned)(tid * 8);
#pragma unroll
      for (int i = 0; i < 16; ++i) GLD2(sv[i], vo, rb + i * (S_ * 8), 0);
      issue(0);
      BAR();
      issue(1);
      wait_vm<2 * LPC>();
#pragma unroll
      for (int i = 0; i < 16; ++i) PIN(sv[i]);
      f32x2 a = sv[0];
#pragma unroll
      for (int i = 1; i < 16; ++i) a += sv[i];
      *(f32x2*)(st_ + tid * 2) = ln_stats(a[0], a[1]);
    } else {
      issue(0);
      BAR();
      issue(1);
    }
  } else {
    float sc[8];
    int c = tid - 256;
    const bool ok = c < 224;
    if (!ok) c = 0;
    const unsigned vo = (unsigned)(((c < 112 ? 0 : F_ - 112) + pn * 112 + c) * 4);
#pragma unroll
    for (int i = 0; i < 8; ++i) GLD1(sc[i], vo, (const char*)CS + i * (2 * F_ * 4), 0);
    issue(0);
    BAR();
    issue(1);
    wait_vm<2 * LPC>();
#pragma unroll
    for (int i = 0; i < 8; ++i) PIN(sc[i]);
    float a = sc[0];
#pragma unroll
    for (int i = 1; i < 8; ++i) a += sc[i];
    if (ok) csum[c] = a;
  }
  TSTAMP(3, 1);

  unsigned a_addr[2];
#pragma unroll
  for (int ks = 0; ks < 2; ++ks) a_addr[ks] = lds0 + (wm * 32 + l15) * 128 + (((4 * ks + gq) ^ (l15 >> 1)) * 16);
  const unsigned b_addr = lds0 + 16384 + P0 * 1024 + l16;
  f32x4 ag[2][NP], au[2][NP];
  const f32x4 z4 = {0.f, 0.f, 0.f, 0.f};
#pragma unroll
  for (int j = 0; j < NP; ++j) { ag[0][j] = z4; ag[1][j] = z4; au[0][j] = z4; au[1][j] = z4; }
  bf16x8 af[2][2], bg[2][NP], bu[2][NP];
  auto rd = [&](int t, int set) {
    const int kc = t >> 1, ks = t & 1, st = kc % NST;
    const unsigned so = st * STAGE;
    DSR128(af[set][0], a_addr[ks] + so, 0);
    DSR128(af[set][1], a_addr[ks] + so, 2048);
#pragma unroll
    for (int j = 0; j < NP; ++j) {
      DSR128(bg[set][j], b_addr + so, (ks * 14 + j) * 1024);
      DSR128(bu[set][j], b_addr + so, (ks * 14 + 7 + j) * 1024);
    }
  };
  auto wt = [&](int set) {
    asm volatile("s_waitcnt lgkmcnt(0)" ::: "memory");
    PIN(af[set][0]); PIN(af[set][1]);
#pragma unroll
    for (int j = 0; j < NP; ++j) { PIN(bg[set][j]); PIN(bu[set][j]); }
  };
  asm volatile("s_waitcnt vmcnt(%0) lgkmcnt(0)" ::"n"(LPC) : "memory");
  BAR();
  TSTAMP(3, 2);
  rd(0, 0);
  piece(NST - 1, 0); piece(NST - 1, 1); piece(NST - 1, 2);
  wt(0);
  constexpr int NTM = NT - 4, NU = 2 * NP;
  auto npf = [&](int lo, int hi) {
    int n = 0;
    if (PF4 == 4)
      for (int i = lo; i <= hi; ++i) n += (i >= PFT && i < PFT + 7) ? 1 : 0;
    return n;
  };
  bf16x8 aT[4][2], bT[2][2][2];
  auto uj = [&](int u) { return u == 0 ? 0 : (u == NU - 1 ? NP - 1 : ((u & 1) ? (u + 1) / 2 : u / 2 - 1)); };
  auto uc = [&](int u) { return (u == 0 || ((u & 1) && u != NU - 1)) ? 0 : 1; };
  auto rdA = [&](int c) {
#pragma unroll
    for (int ks = 0; ks < 2; ++ks) {
      DSR128(aT[c * 2 + ks][0], a_addr[ks] + ((NK - 2 + c) % NST) * STAGE, 0);
      DSR128(aT[c * 2 + ks][1], a_addr[ks] + ((NK - 2 + c) % NST) * STAGE, 2048);
    }
  };
  auto rdB = [&](int u) {
#pragma unroll
    for (int ks = 0; ks < 2; ++ks) {
      DSR128(bT[u & 1][ks][0], b_addr + ((NK - 2 + uc(u)) % NST) * STAGE, (ks * 14 + uj(u)) * 1024);
      DSR128(bT[u & 1][ks][1], b_addr + ((NK - 2 + uc(u)) % NST) * STAGE, (ks * 14 + 7 + uj(u)) * 1024);
    }
  };
#pragma unroll
  for (int t = 0; t < NTM; ++t) {
    const int kc = t >> 1, ks = t & 1, cur = t & 1, nxt = cur ^ 1;
    if (ks == 1) {
      const int last = (kc + NST - 1 < NK - 1) ? (kc + NST - 1) : (NK - 1);
      wait_vm_n((last - (kc + 1)) * LPC + npf(2 * kc - 2, 2 * kc));
      BAR();
      TSTAMP(3, 3 + kc);
    }
    if (t + 1 < NTM) rd(t + 1, nxt);
    else { rdA(0); rdB(0); }
    const int ic = (t + 1) / 2 + NST - 1, ip = ((t + 1) & 1) * 3;
    SCHED();
#pragma unroll
    for (int j = 0; j < NP; ++j) {
      ag[0][j] = mfma16(bg[cur][j], af[cur][0], ag[0][j]);
      ag[1][j] = mfma16(bg[cur][j], af[cur][1], ag[1][j]);
      au[0][j] = mfma16(bu[cur][j], af[cur][0], au[0][j]);
      au[1][j] = mfma16(bu[cur][j], af[cur][1], au[1][j]);
      SCHED();
      if (j < 3 && ic < NK && ip + j < LPC) piece(ic, ip + j);
      if (PF4 && PF4 < 4 && j < (PF4 == 1 ? 2 : 1) && t == NTM - 1) glds(l16, pf + j * 8192, lds0 + NST * STAGE + 2048 + wid * 1024);
      if (j == 2 && npf(t, t)) glds(l16, pf + (t - PFT) * 8192, lds0 + NST * STAGE + 2048 + wid * 1024);
      SCHED();
    }
    if (t + 1 < NTM) wt(nxt);
  }
  auto wtT = [&](bool a0, bool a1, int u) {
    asm volatile("s_waitcnt lgkmcnt(0)" ::: "memory");
    if (a0) { PIN(aT[0][0]); PIN(aT[0][1]); PIN(aT[1][0]); PIN(aT[1][1]); }
    if (a1) { PIN(aT[2][0]); PIN(aT[2][1]); PIN(aT[3][0]); PIN(aT[3][1]); }
    PIN(bT[u & 1][0][0]); PIN(bT[u & 1][0][1]); PIN(bT[u & 1][1][0]); PIN(bT[u & 1][1][1]);
  };
  wtT(true, false, 0);
  TSTAMP(3, 20);
  bf16* dst0 = Hm + ((size_t)((pm * 2 + (wm >> 1)) * 112 + pn * 7 + P0) * 64 + (wm & 1) * 32 + l15) * 16;
  const unsigned caddr = lds0 + NST * STAGE + 1024 + (P0 * 16 + gq * 4) * 4;
  f32x2 nm[2], le2[2], iv2[2];
  f32x4 cg[2], cu[2];
  unsigned pk[NP][2][2];
  f32x2 tg, tu, te, td;
  auto epi = [&](int j, int mi, int q) {
    const int hf = q >> 2;
    if ((q & 3) == 0) {
      tg = __builtin_elementwise_fma(f32x2{cg[j & 1][2 * hf], cg[j & 1][2 * hf + 1]}, nm[mi],
                                     f32x2{ag[mi][j][2 * hf], ag[mi][j][2 * hf + 1]});
      tu = __builtin_elementwise_fma(f32x2{cu[j & 1][2 * hf], cu[j & 1][2 * hf + 1]}, nm[mi],
                                     f32x2{au[mi][j][2 * hf], au[mi][j][2 * hf + 1]});
      te = tg * le2[mi];
    } else if ((q & 3) == 1) {
      te = f32x2{__builtin_amdgcn_exp2f(te[0]), __builtin_amdgcn_exp2f(te[1])};
    } else if ((q & 3) == 2) {
      td = __builtin_elementwise_fma(te, iv2[mi], iv2[mi]);
      tu = tg * tu;
    } else {
      const f32x2 rc = {__builtin_amdgcn_rcpf(td[0]), __builtin_amdgcn_rcpf(td[1])};
      const f32x2 o = tu * rc;
      pk[j][mi][hf] = pk2(o[0], o[1]);
    }
    if (q == 7) {
      if (j & 1) {
        swap16(pk[j - 1][mi][0], pk[j][mi][0]);
        swap16(pk[j - 1][mi][1], pk[j][mi][1]);
        st16<(WT & 8) != 0>(dst0 + ((size_t)(j - 1 + (gq & 1)) * 64 + mi * 16) * 16 + (gq >> 1) * 8,
                            u32x4{pk[j - 1][mi][0], pk[j - 1][mi][1], pk[j][mi][0], pk[j][mi][1]});
      } else if (j == NP - 1) {
        st8<(WT & 8) != 0>(dst0 + ((size_t)j * 64 + mi * 16) * 16 + gq * 4, u32x2{pk[j][mi][0], pk[j][mi][1]});
      }
    }
  };
#pragma unroll
  for (int u = 0; u < NU; ++u) {
    const int j = uj(u), c = uc(u);
    if (u == 1) {
      wait_vm_n(PF4 == 4 ? npf(NTM - 2, NTM - 1) : (PF4 == 1 ? 4 : (PF4 ? 1 : 0)));
      BAR();
      TSTAMP(3, 9);
      rdA(1);
    }
    if (u + 1 < NU) rdB(u + 1);
    const int cj = u == NU - 1 ? NP - 1 : ((u >= 2 && !(u & 1)) ? (u - 2) / 2 : -1);
    if (cj >= 0) {
      DSR128(cg[cj & 1], caddr, cj * 64);
      DSR128(cu[cj & 1], caddr, cj * 64 + 448);
    }
    if (u == 1) {
#pragma unroll
      for (int mi = 0; mi < 2; ++mi) {
        const f32x2 ms = *(const f32x2*)(st_ + (wm * 32 + mi * 16 + l15) * 2);
        nm[mi] = f32x2{-ms[0], -ms[0]};
        const float le = ms[1] * -1.4426950408889634f, iv = 1.0f / (ms[1] * ms[1]);
        le2[mi] = f32x2{le, le};
        iv2[mi] = f32x2{iv, iv};
      }
    }
    SCHED();
#pragma unroll
    for (int q = 0; q < 8; ++q) {
      const int ks = q >> 2, mi = q & 1;
      if (q & 2) au[mi][j] = mfma16(bT[u & 1][ks][1], aT[c * 2 + ks][mi], au[mi][j]);
      else ag[mi][j] = mfma16(bT[u & 1][ks][0], aT[c * 2 + ks][mi], ag[mi][j]);
      SCHED();
      if (u >= 3) epi((u - 3) >> 1, (u - 3) & 1, q);
      if (PF4 == 1 && u < 3 && (q == 1 || q == 5) && 2 + u * 2 + (q >> 2) < 7)
        glds(l16, pf + (2 + u * 2 + (q >> 2)) * 8192, lds0 + NST * STAGE + 2048 + wid * 1024);
      if ((CPF & 8) && u == 3 && q == 3) code_warm<false>(wid, lane, lds0 + NST * STAGE + 2048 + wid * 1024, nullptr);
      SCHED();
    }
    if (u + 1 < NU) {
      wtT(false, u == 1, u + 1);
    } else {
      asm volatile("s_waitcnt lgkmcnt(0)" ::: "memory");
    }
    if (cj >= 0) { PIN(cg[cj & 1]); PIN(cu[cj & 1]); }
  }
  TSTAMP(3, 21);
#pragma unroll
  for (int e = NU - 3; e < NU; ++e)
#pragma unroll
    for (int q = 0; q < 8; ++q) epi(e >> 1, e & 1, q);
}

extern "C" __global__ __launch_bounds__(512, 2) void k4_gu(const bf16* __restrict__ Hh, const bf16* __restrict__ Wt,
                                                const float* __restrict__ RS, const float* __restrict__ CS,
                                                bf16* __restrict__ Hm, const bf16* __restrict__ W2t) {
  extern __shared__ __attribute__((aligned(16))) char smem[];
  TDECL
  TSTAMP(3, 0);
  const int tid = threadIdx.x;
  const int wid = __builtin_amdgcn_readfirstlane(tid >> 6);
  const int xg = blockIdx.x & 7, idx = blockIdx.x >> 3;
  const int pm = (xg & 1) * 8 + (idx & 7), pn = (xg >> 1) * 4 + (idx >> 3);
  asm volatile("" : "+s"(RS), "+s"(CS), "+s"(Hm), "+s"(W2t));
  const unsigned lds0 = (unsigned)(uintptr_t)(LDSP(char))smem;
  const char* pf = (const char*)W2t + (PF4 == 2 ? (blockIdx.x * 7 + (wid < 7 ? wid : 6)) * 1024 : (PF4 == 3 ? idx * 57344 + wid * 7168 : idx * 57344 + wid * 1024));
  if (wid < 4) gu_body<4, 0>(smem, lds0, tid, wid, pm, pn, Hh, Wt, RS, CS, Hm, pf TPASS);
  else gu_body<3, 4>(smem, lds0, tid, wid, pm, pn, Hh, Wt, RS, CS, Hm, pf TPASS);
  wait_vm<0>();
  TSTAMP(3, 22);
  TFLUSH(3);
  asm volatile("k4_gu_tail:");
}

extern "C" void kernel_launch(void* const* d_in, const int* in_sizes, int n_in, void* d_out, int out_size, void* d_ws,
                              size_t ws_size, hipStream_t stream) {
  const bf16* x = (const bf16*)d_in[0];
  const bf16* wqkv = (const bf16*)d_in[1];
  const bf16* wo = (const bf16*)d_in[2];
  const bf16* w1 = (const bf16*)d_in[4];
  const bf16* w3 = (const bf16*)d_in[5];
  const bf16* w2 = (const bf16*)d_in[6];
  bf16* out = (bf16*)d_out;

  char* ws = (char*)d_ws;
  bf16* QKV = (bf16*)(ws);
  bf16* Oa = (bf16*)(ws + (7u << 20));
  bf16* Hh = (bf16*)(ws + (9u << 20));
#define HMO 11
  bf16* Hm = (bf16*)(ws + ((unsigned)HMO << 20));
  float* RS = (float*)(ws + (18u << 20));
  float* CS = (float*)(ws + (19u << 20));
  bf16* Wt = (bf16*)(ws + (20u << 20));
  bf16* W2t = (bf16*)(ws + (24u << 20));
  bf16* Wot = (bf16*)(ws + (26u << 20));

  {
    constexpr int lds = K1_NST * 28672 + 2560 + 2048;
    (void)hipFuncSetAttribute((const void*)k1_qkv, hipFuncAttributeMaxDynamicSharedMemorySize, lds);
    k1_qkv<<<256, 512, lds, stream>>>(x, wqkv, QKV);
  }
  {
    constexpr int lds = 59392 + 2048;
    (void)hipFuncSetAttribute((const void*)k2_attn, hipFuncAttributeMaxDynamicSharedMemorySize, lds);
    k2_attn<<<256, 512, lds, stream>>>(QKV, Oa, wo, w1, w3, w2, Wt, CS, W2t, Wot);
  }
  {
    auto k = k3_wo;
    constexpr int lds = G_NST * 32768 + 8192;
    (void)hipFuncSetAttribute((const void*)k, hipFuncAttributeMaxDynamicSharedMemorySize, lds);
    k<<<256, 512, lds, stream>>>(Oa, Wot, x, Hh, RS, Wt);
  }
  {
    constexpr int lds = 3 * 45056 + 2048 + 8192;
    (void)hipFuncSetAttribute((const void*)k4_gu, hipFuncAttributeMaxDynamicSharedMemorySize, lds);
    k4_gu<<<256, 512, lds, stream>>>(Hh, Wt, RS, CS, Hm, W2t);
  }
  {
    auto k = k5_down;
    constexpr int lds = G_NST * 32768;
    (void)hipFuncSetAttribute((const void*)k, hipFuncAttributeMaxDynamicSharedMemorySize, lds);
    k<<<256, 512, lds, stream>>>(Hm, W2t, Hh, out);
  }
}
```

```cpp
#include <hip/hip_runtime.h>
#include <stdint.h>

typedef __attribute__((ext_vector_type(8))) __bf16 bf16x8;
typedef __attribute__((ext_vector_type(4))) __bf16 bf16x4;
typedef __attribute__((ext_vector_type(2))) __bf16 bf16x2;
typedef __attribute__((ext_vector_type(2))) float f32x2;
typedef __attribute__((ext_vector_type(4))) float f32x4;
typedef __attribute__((ext_vector_type(16))) float f32x16;
typedef __attribute__((ext_vector_type(4))) unsigned int u32x4;
typedef __attribute__((ext_vector_type(2))) unsigned int u32x2;
typedef __bf16 bf16;

#define LDSP(T) __attribute__((address_space(3))) T*
#define DEV __device__ __forceinline__

#define K1_NST 4
#define G_NST 4
#define G_R 1
#define ROT1 0
#define ROT3 0
#define ROT4 0
#define ROT5 0
#define PF4 1
#define PFT 5
#define LA2 1
#define PF3 0
#define K1DOT 0
#define K1T 1
#define ILV1 1
#define ILV4 1
#define GQ 1
#define PF5 0
#define PF3T 4
constexpr int S_ = 2048, D_ = 512, H_ = 32, F_ = 1792;

DEV float bfr(float x) { return (float)(bf16)x; }

template <int N>
DEV void wait_vm() {
  asm volatile("s_waitcnt vmcnt(%0)" ::"n"(N) : "memory");
}
DEV void wait_vm_n(int n) {
  switch (n) {
#define WC(X) case X: wait_vm<X>(); break;
    WC(0) WC(1) WC(2) WC(3) WC(4) WC(5) WC(6) WC(7) WC(8) WC(9) WC(10) WC(11) WC(12) WC(13) WC(14) WC(15)
    WC(16) WC(17) WC(18) WC(19) WC(20) WC(21) WC(22) WC(23) WC(24)
#undef WC
    default: wait_vm<0>(); break;
  }
}

DEV void glds(unsigned voff, const void* sbase, unsigned ldsaddr) {
  ldsaddr = __builtin_amdgcn_readfirstlane(ldsaddr);
  asm volatile("s_mov_b32 m0, %2\n\ts_nop 0\n\tglobal_load_lds_dwordx4 %0, %1" ::"v"(voff), "s"(sbase), "s"(ldsaddr)
               : "memory");
}
#define DSR128(dst, addr, off) asm volatile("ds_read_b128 %0, %1 offset:%2" : "=v"(dst) : "v"(addr), "i"(off))
#define DSRTR(dst, addr, off) asm volatile("ds_read_b64_tr_b16 %0, %1 offset:%2" : "=v"(dst) : "v"(addr), "i"(off))
#define PIN(x) asm volatile("" : "+v"(x))
#define GLD4(dst, voff, sbase, off) \
  asm volatile("global_load_dwordx4 %0, %1, %2 offset:%3" : "=v"(dst) : "v"(voff), "s"(sbase), "i"(off))
#define GLD2(dst, voff, sbase, off) \
  asm volatile("global_load_dwordx2 %0, %1, %2 offset:%3" : "=v"(dst) : "v"(voff), "s"(sbase), "i"(off))
#define GLD1(dst, voff, sbase, off) \
  asm volatile("global_load_dword %0, %1, %2 offset:%3" : "=v"(dst) : "v"(voff), "s"(sbase), "i"(off))
#define WT 31
template <bool W>
DEV void st16(void* p, u32x4 v) {
  if constexpr (W) asm volatile("global_store_dwordx4 %0, %1, off sc1\n\ts_nop 1" ::"v"(p), "v"(v) : "memory");
  else *(u32x4*)p = v;
}
template <bool W>
DEV void st8(void* p, u32x2 v) {
  if constexpr (W) asm volatile("global_store_dwordx2 %0, %1, off sc1\n\ts_nop 1" ::"v"(p), "v"(v) : "memory");
  else *(u32x2*)p = v;
}
template <bool W>
DEV void st4(void* p, unsigned v) {
  if constexpr (W) asm volatile("global_store_dword %0, %1, off sc1" ::"v"(p), "v"(v) : "memory");
  else *(unsigned*)p = v;
}
#define BAR() __builtin_amdgcn_s_barrier()
#define SCHED() __builtin_amdgcn_sched_barrier(0)

#define TDECL
#define TSTAMP(m, i) do { } while (0)
#define TFLUSH(m) do { } while (0)
#define TARG
#define TPASS

#define CPF 15
#define CODE_ADDR(dst, sym)                                                                                  \
  asm volatile("s_getpc_b64 s[30:31]\n\ts_add_u32 s30, s30, " #sym "@rel32@lo+4\n\ts_addc_u32 s31, s31, " #sym \
               "@rel32@hi+12\n\ts_mov_b64 %0, s[30:31]"                                                      \
               : "=s"(dst)::"s30", "s31", "scc")
#define CPW 5
#define XPF 3
constexpr int CW1 = 8, CW3 = 3, CW4 = 14;
template <bool ALL>
DEV void code_warm(int wid, int lane, unsigned ldsaddr, const char* other) {
  const bool sel = wid == CPW && blockIdx.x < 64;
  if (!ALL && !sel) return;
  const char* a;
  int d;
  CODE_ADDR(a, k1_qkv);
  asm volatile("s_mov_b32 %0, k4_gu_tail-k1_qkv" : "=s"(d));
  const int last = d < 0 ? 0 : d >> 7;
  const int ln = sel ? (int)(blockIdx.x >> 3) * 64 + lane : 0;
  unsigned vo = (unsigned)((ln < last ? ln : last) * 128);
  if ((XPF & 1) && (blockIdx.x >> 3) == 6) {
    a = (const char*)((uintptr_t)__builtin_amdgcn_kernarg_segment_ptr() & ~(uintptr_t)4095);
    vo = (unsigned)((lane & 31) * 128);
  }
  if ((XPF & 2) && (blockIdx.x >> 3) == 7) {
    int d2;
    CODE_ADDR(a, k1_qkv.kd);
    asm volatile("s_mov_b32 %0, k4_gu.kd-k1_qkv.kd" : "=s"(d2));
    const int l2 = d2 < 0 ? 0 : d2 >> 6;
    vo = (unsigned)((lane < l2 ? lane : l2) * 64);
  }
  if (ALL && !sel) { a = other; vo = 0; }
  ldsaddr = __builtin_amdgcn_readfirstlane(ldsaddr);
  asm volatile("s_mov_b32 m0, %2\n\ts_nop 0\n\tglobal_load_lds_dword %0, %1" ::"v"(vo), "s"(a), "s"(ldsaddr) : "memory");
}

template <int V> struct IC { static constexpr int value = V; };
template <int I, int N, class F>
DEV void static_for(F&& f) {
  if constexpr (I < N) {
    f(IC<I>{});
    static_for<I + 1, N>(f);
  }
}
template <int P, int KS, int NST, int NK, int PRE, int R>
struct Sched {
  static constexpr int avail(int t) {
    int n = ((t + 1) / KS + NST - 1) * P + ((t + 1) % KS + 1) * (P / KS);
    return n > NK * P ? NK * P : n;
  }
  static constexpr int cum(int t) {
    int n = PRE;
    for (int i = -1; i < t; ++i) { int a = avail(i); n = (n + R < a) ? n + R : a; }
    return n;
  }
};

DEV void swap32(unsigned& a, unsigned& b) {
  const auto r = __builtin_amdgcn_permlane32_swap(a, b, false, false);
  a = r[0]; b = r[1];
}
DEV void swap16(unsigned& a, unsigned& b) {
  const auto r = __builtin_amdgcn_permlane16_swap(a, b, false, false);
  a = r[0]; b = r[1];
}
DEV void swap16f(float& a, float& b) {
  unsigned x = __builtin_bit_cast(unsigned, a), y = __builtin_bit_cast(unsigned, b);
  swap16(x, y);
  a = __builtin_bit_cast(float, x); b = __builtin_bit_cast(float, y);
}
DEV unsigned pk2(float a, float b) { return __builtin_bit_cast(unsigned, bf16x2{(bf16)a, (bf16)b}); }
DEV bf16x8 cat8(bf16x4 a, bf16x4 b) { return __builtin_shufflevector(a, b, 0, 1, 2, 3, 4, 5, 6, 7); }
DEV f32x4 mfma16(bf16x8 a, bf16x8 b, f32x4 c) { return __builtin_amdgcn_mfma_f32_16x16x32_bf16(a, b, c, 0, 0, 0); }
DEV f32x16 mfma32(bf16x8 a, bf16x8 b, f32x16 c) { return __builtin_amdgcn_mfma_f32_32x32x16_bf16(a, b, c, 0, 0, 0); }

DEV f32x2 ln_stats(float sum, float sq) {
  float m = sum * (1.0f / D_);
  float var = fmaxf(sq * (1.0f / D_) - m * m, 0.f);
  float vb = bfr(var);
  float sd = bfr(__builtin_sqrtf(bfr(vb + 1e-5f)));
  f32x2 r;
  r[0] = bfr(m);
  r[1] = 1.0f / sd;
  return r;
}

DEV f32x2 ln_stats_hw(float sum, float sq) {
  float m = sum * (1.0f / D_);
  float var = fmaxf(sq * (1.0f / D_) - m * m, 0.f);
  float vb = bfr(var);
  float sd = bfr(__builtin_amdgcn_sqrtf(bfr(vb + 1e-5f)));
  f32x2 r;
  r[0] = bfr(m);
  r[1] = __builtin_amdgcn_rcpf(sd);
  return r;
}

extern "C" __global__ __launch_bounds__(512, 2) void k1_qkv(const bf16* __restrict__ x, const bf16* __restrict__ w,
                                                            bf16* __restrict__ QKV) {
  constexpr int NST = K1_NST, STAGE = 28672, NK = 8, NT = 16;
  extern __shared__ __attribute__((aligned(16))) char smem[];
  TDECL
  TSTAMP(0, 0);
  const int tid = threadIdx.x, lane = tid & 63;
  const int wid = __builtin_amdgcn_readfirstlane(tid >> 6);
  const int wm = wid & 3, wn = wid >> 2;
  const int l15 = lane & 15, gq = lane >> 4;
  const int xg = blockIdx.x & 7, idx = blockIdx.x >> 3;
  const int pm = (xg & 3) * 4 + (idx & 3), pn = (xg >> 2) * 8 + (idx >> 2);
  const unsigned lds0 = (unsigned)(uintptr_t)(LDSP(char))smem;
  asm volatile("" : "+s"(QKV));

  const unsigned voffA = (unsigned)((wid * 8 + (lane >> 3)) * 1024 + (((lane & 7) ^ ((wid & 1) * 4 + gq)) * 16));
  const int r12 = (lane * 43) >> 9, c12 = lane - r12 * 12;
  const bool validB = lane < 48;
  const unsigned voffB = (unsigned)((wid * 8 + r12) * 3072 + (((c12 >> 1) ^ (wid & 1)) * 16 + (c12 & 1) * 8) * 2);
  const char* baseA = (const char*)x + (size_t)pm * (128 * 1024);
  const char* baseB = (const char*)w + pn * 192;
  const int rot = ROT1 * ((idx >> 2) + 2 * (idx & 3));
  auto part = [&](int kc, int p) {
    const unsigned sb = lds0 + (kc % NST) * STAGE;
    const int kk = (kc + rot) & 7;
    if (p == 0) {
      glds(voffA, baseA + kk * 128, sb + wid * 1024);
      glds(voffA, baseA + kk * 128 + 65536, sb + 8192 + wid * 1024);
    } else if (validB) {
      glds(voffB, baseB + kk * (64 * 3072), sb + 16384 + wid * 1536);
      glds(voffB, baseB + kk * (64 * 3072) + 4 * 3072, sb + 16384 + wid * 1536 + 768);
    }
  };
  auto issue = [&](int kc) { part(kc, 0); part(kc, 1); };
  issue(0);
  BAR();
  issue(1);
  BAR();
  issue(2);
  TSTAMP(0, 1);

  unsigned a_addr[2][2], b_addr[3];
#pragma unroll
  for (int mi = 0; mi < 2; ++mi)
#pragma unroll
    for (int ks = 0; ks < 2; ++ks)
      a_addr[mi][ks] = lds0 + (wm * 32 + ((mi ^ wn) * 16) + l15) * 128 + (((4 * ks + gq) ^ (l15 >> 1)) * 16);
  {
    const int kl = gq * 8 + (l15 >> 2);
#pragma unroll
    for (int j = 0; j < 3; ++j)
      b_addr[j] = lds0 + 16384 + kl * 192 + (((3 * wn + j) ^ (gq & 1)) * 32) + (l15 & 3) * 8;
  }

  f32x4 acc[2][3], s1, s2, cs[3];
  const f32x4 z4 = {0.f, 0.f, 0.f, 0.f};
#pragma unroll
  for (int j = 0; j < 3; ++j) { acc[0][j] = z4; acc[1][j] = z4; cs[j] = z4; }
  s1 = z4; s2 = z4;
  float rs_ = 0.f, rq_ = 0.f;
  bf16x8 af[2][2];
  bf16x4 bl[2][3], bh[2][3];
  const bf16 one = (bf16)1.0f;
  const bf16x8 ones = {one, one, one, one, one, one, one, one};

  auto rd = [&](int t, int set) {
    const int kc = t >> 1, ks = t & 1, st = kc % NST;
    const unsigned hi = (unsigned)((st & ~1) * STAGE);
    const int so = (st & 1) * STAGE;
#pragma unroll
    for (int j = 0; j < 3; ++j) {
      DSRTR(bl[set][j], b_addr[j] + hi, so + ks * 6144);
      DSRTR(bh[set][j], b_addr[j] + hi, so + ks * 6144 + 768);
      if (j < 2) DSR128(af[set][j], a_addr[j][ks] + hi, so);
    }
  };
  auto rdp = [&](int t, int set, int i) {
    const int kc = t >> 1, ks = t & 1, st = kc % NST, j = i / 3;
    const unsigned hi = (unsigned)((st & ~1) * STAGE);
    const int so = (st & 1) * STAGE;
    if (i % 3 == 0) DSRTR(bl[set][j], b_addr[j] + hi, so + ks * 6144);
    else if (i % 3 == 1) DSRTR(bh[set][j], b_addr[j] + hi, so + ks * 6144 + 768);
    else DSR128(af[set][j], a_addr[j][ks] + hi, so);
  };
  auto wt = [&](int set) {
    asm volatile("s_waitcnt lgkmcnt(0)" ::: "memory");
    PIN(af[set][0]); PIN(af[set][1]);
#pragma unroll
    for (int j = 0; j < 3; ++j) { PIN(bl[set][j]); PIN(bh[set][j]); }
  };

  using SC = Sched<2, 2, NST, NK, 6, 2>;
  wait_vm<(SC::cum(-1) - 2) * 2>();
  BAR();
  TSTAMP(0, 2);
  rd(0, 0);
  static_for<SC::cum(-1), SC::cum(0)>([&](auto n) { part(n.value >> 1, n.value & 1); });
  wt(0);
  static_for<0, NT>([&](auto tc) {
    constexpr int t = tc.value, kc = t >> 1, ks = t & 1, cur = t & 1, nxt = cur ^ 1;
    constexpr bool sync = ks == 1 && kc + 1 < NK;
    const bf16x8 b0 = cat8(bl[cur][0], bh[cur][0]), b1 = cat8(bl[cur][1], bh[cur][1]), b2 = cat8(bl[cur][2], bh[cur][2]);
    auto mf = [&](int i) {
      if (i < 6) acc[i / 3][i % 3] = mfma16(i % 3 == 0 ? b0 : (i % 3 == 1 ? b1 : b2), af[cur][i / 3], acc[i / 3][i % 3]);
      else if (i == 6) s1 = mfma16(ones, af[cur][0], s1);
      else s2 = mfma16(af[cur][0], af[cur][0], s2);
      SCHED();
    };
    constexpr int PRE = (ILV1 == 2 && sync) ? 2 : 0;
    static_for<0, PRE>([&](auto i) { mf(i.value); });
    if constexpr (sync) {
      static_assert(SC::cum(t) >= (kc + 2) * 2, "schedule");
      wait_vm<(SC::cum(t) - (kc + 2) * 2) * 2 + (((CPF & 1) && t > CW1) ? 1 : 0)>();
      BAR();
      TSTAMP(0, 3 + kc);
    }
    SCHED();
    static_for<PRE, 8>([&](auto ic) {
      constexpr int i = ic.value, r = i - PRE + (PRE ? 0 : 0);
      mf(i);
      if constexpr (t + 1 < NT && r < 4) { rdp(t + 1, nxt, 2 * r); rdp(t + 1, nxt, 2 * r + 1); }
      if constexpr (r == 4) static_for<SC::cum(t), SC::cum(t + 1)>([&](auto n) { part(n.value >> 1, n.value & 1); });
      if constexpr (r == 5 && (CPF & 1) && t == CW1) {
        static_assert(SC::cum(t + 1) == NK * 2, "issued after all operand loads");
        code_warm<true>(wid, lane, lds0 + NST * STAGE + 2560 + wid * 256, baseA);
      }
      SCHED();
    });
    if ((t & 3) == wm) {
      cs[0] = mfma16(b0, ones, cs[0]);
      cs[1] = mfma16(b1, ones, cs[1]);
      cs[2] = mfma16(b2, ones, cs[2]);
    }
    SCHED();
    if constexpr (t + 1 < NT) wt(nxt);
  });
  TSTAMP(0, 20);

  float* st_ = (float*)(smem + NST * STAGE);
  float* csum = st_ + 256;
  {
    float sm, sq;
    if (K1DOT) {
      float a = rs_, b = rq_;
      swap16f(a, b);
      a += b;
      unsigned x = __builtin_bit_cast(unsigned, a), y = x;
      swap32(x, y);
      a = __builtin_bit_cast(float, x) + __builtin_bit_cast(float, y);
      b = a;
      swap16f(a, b);
      sm = a; sq = b;
    } else {
      sm = s1[0];
      sq = (l15 & 2) ? ((l15 & 1) ? s2[3] : s2[2]) : ((l15 & 1) ? s2[1] : s2[0]);
    }
    const f32x2 ms = ln_stats_hw(sm, sq);
    const bool mine = K1DOT || gq == (l15 >> 2);
    *(f32x2*)(mine ? st_ + (wm * 32 + wn * 16 + l15) * 2 : csum + 384 + lane * 2) = ms;
#pragma unroll
    for (int j = 0; j < 3; ++j) *(f32x4*)(csum + wm * 96 + (3 * wn + j) * 16 + gq * 4) = cs[j];
  }
  __syncthreads();
  TSTAMP(0, 21);
  const int tile = pm * 4 + wm;
  const float qscale = 0.25f * 1.4426950408889634f;
  f32x2 ms[2];
#pragma unroll
  for (int mi = 0; mi < 2; ++mi) ms[mi] = *(const f32x2*)(st_ + (wm * 32 + (mi ^ wn) * 16 + l15) * 2);
  f32x4 c4[3];
#pragma unroll
  for (int j = 0; j < 3; ++j) {
    f32x4 cp[4];
#pragma unroll
    for (int p = 0; p < 4; ++p) cp[p] = *(const f32x4*)(csum + p * 96 + (3 * wn + j) * 16 + gq * 4);
    c4[j] = (cp[0] + cp[1]) + (cp[2] + cp[3]);
  }
  const int msel = (gq & 1) ^ wn;
  const int loq = msel * 128 + (gq >> 1) * 256 + l15 * 8, lov = msel * 256 + l15 * 16 + (gq >> 1) * 8;
  u32x4 ov[3];
#pragma unroll
  for (int j = 0; j < 3; ++j) {
    const int blk = pn * 6 + wn * 3 + j;
    const float sc = (blk >> 5) == 0 ? qscale : 1.0f;
    unsigned o[2][2];
#pragma unroll
    for (int mi = 0; mi < 2; ++mi) {
      const float rs = ms[mi][1] * sc;
#pragma unroll
      for (int r = 0; r < 2; ++r)
        o[mi][r] = pk2((acc[mi][j][2 * r] - ms[mi][0] * c4[j][2 * r]) * rs, (acc[mi][j][2 * r + 1] - ms[mi][0] * c4[j][2 * r + 1]) * rs);
    }
    swap16(o[0][0], o[1][0]);
    swap16(o[0][1], o[1][1]);
    ov[j] = u32x4{o[0][0], o[0][1], o[1][0], o[1][1]};
  }
#pragma unroll
  for (int j = 0; j < 3; ++j) {
    const int blk = pn * 6 + wn * 3 + j;
    const int typ = blk >> 5, h = blk & 31;
    bf16* db = QKV + ((size_t)typ << 20) + (size_t)(h * 64 + tile) * 512 + (typ == 2 ? lov : loq);
    st16<(WT & 1) != 0>(db, ov[j]);
  }
  TSTAMP(0, 22);
  TFLUSH(0);
}

template <bool FULL, int NPF>
DEV void attn_body(unsigned kaddr, unsigned vaddr, bf16x8 qf, f32x16 cm0, f32x16 cm8, int wid, bf16* dst, unsigned lds0 TARG) {
  const float NEG = -1e30f;
  f32x16 z16;
#pragma unroll
  for (int r = 0; r < 16; ++r) z16[r] = 0.f;
  bf16x8 kf[9];
#pragma unroll
  for (int t = 0; t < 9; ++t)
    if (FULL || wid + t >= 8) DSR128(kf[t], kaddr, t * 1024);
  asm volatile("s_waitcnt lgkmcnt(0)" ::: "memory");
#pragma unroll
  for (int t = 0; t < 9; ++t) PIN(kf[t]);
  TSTAMP(1, 3);
  float m0 = NEG, m1 = NEG;
#pragma unroll
  for (int t = 0; t < 9; ++t)
    if (FULL || wid + t >= 8) {
      const f32x16 s = mfma32(kf[t], qf, t == 0 ? cm0 : (t == 8 ? cm8 : z16));
#pragma unroll
      for (int r = 0; r < 16; r += 4) {
        m0 = fmaxf(fmaxf(m0, s[r]), s[r + 1]);
        m1 = fmaxf(fmaxf(m1, s[r + 2]), s[r + 3]);
      }
    }
  float m = fmaxf(m0, m1);
  m = fmaxf(m, __shfl_xor(m, 32));
  f32x16 nm;
#pragma unroll
  for (int r = 0; r < 16; ++r) { nm[r] = -m; cm0[r] -= m; cm8[r] -= m; }
  TSTAMP(1, 4);
  wait_vm<NPF>();
  BAR();
  TSTAMP(1, 5);
  if (CPF & 2) code_warm<false>(wid, (int)(threadIdx.x & 63), lds0 + 59392 + wid * 256, nullptr);
  f32x16 oa = z16, ob = z16;
  f32x16 sc_, sn_;
  bf16x4 v0l[2], v0h[2], v1l[2], v1h[2];
  auto vrd = [&](int t, int b) {
    DSRTR(v0l[b], vaddr, t * 1024);
    DSRTR(v0h[b], vaddr, t * 1024 + 256);
    DSRTR(v1l[b], vaddr, t * 1024 + 512);
    DSRTR(v1h[b], vaddr, t * 1024 + 768);
  };
  if (FULL) { sc_ = mfma32(kf[0], qf, cm0); vrd(0, 0); }
  else sc_ = z16;
  sn_ = z16;
#pragma unroll
  for (int t = 0; t < 9; ++t) {
    const int b = t & 1;
    const bool cur_ok = FULL || wid + t >= 8, nxt_ok = t < 8 && (FULL || wid + t + 1 >= 8);
    if (cur_ok) {
      asm volatile("s_waitcnt lgkmcnt(0)" ::: "memory");
      PIN(v0l[b]); PIN(v0h[b]); PIN(v1l[b]); PIN(v1h[b]);
    }
    if (nxt_ok) {
      vrd(t + 1, b ^ 1);
      sn_ = mfma32(kf[t + 1], qf, t + 1 == 8 ? cm8 : nm);
    }
    SCHED();
    if (cur_ok) {
      bf16x8 p0, p1;
#pragma unroll
      for (int j = 0; j < 8; ++j) p0[j] = (bf16)__builtin_amdgcn_exp2f(sc_[j]);
      SCHED();
      oa = mfma32(cat8(v0l[b], v0h[b]), p0, oa);
      SCHED();
#pragma unroll
      for (int j = 0; j < 8; ++j) p1[j] = (bf16)__builtin_amdgcn_exp2f(sc_[8 + j]);
      SCHED();
      ob = mfma32(cat8(v1l[b], v1h[b]), p1, ob);
      SCHED();
    }
    sc_ = sn_;
  }
  TSTAMP(1, 6);
  const float inv = 1.0f / (oa[8] + ob[8]);
  unsigned a0 = pk2((oa[0] + ob[0]) * inv, (oa[1] + ob[1]) * inv), a1 = pk2((oa[2] + ob[2]) * inv, (oa[3] + ob[3]) * inv);
  unsigned b0 = pk2((oa[4] + ob[4]) * inv, (oa[5] + ob[5]) * inv), b1 = pk2((oa[6] + ob[6]) * inv, (oa[7] + ob[7]) * inv);
  swap32(a0, b0);
  swap32(a1, b1);
  st16<(WT & 2) != 0>(dst, u32x4{a0, a1, b0, b1});
}

extern "C" __global__ __launch_bounds__(512, 2) void k2_attn(const bf16* __restrict__ QKV, bf16* __restrict__ O,
                                                  const bf16* __restrict__ wo, const bf16* __restrict__ w1,
                                                  const bf16* __restrict__ w3, const bf16* __restrict__ w2,
                                                  bf16* __restrict__ Wt, float* __restrict__ CS,
                                                  bf16* __restrict__ W2t, bf16* __restrict__ Wot) {
  constexpr int LV = 16384, LONE = 32768, LSJ = 43008;
  extern __shared__ __attribute__((aligned(16))) char smem[];
  TDECL
  TSTAMP(1, 0);
  const int tid = threadIdx.x, lane = tid & 63;
  const int wid = __builtin_amdgcn_readfirstlane(tid >> 6);
  const int l15 = lane & 15, gq = lane >> 4;
  const int xg = blockIdx.x >> 5, h = (blockIdx.x & 7) * 4 + ((blockIdx.x >> 3) & 3);
  const unsigned lds0 = (unsigned)(uintptr_t)(LDSP(char))smem;
  const int t0 = xg * 8 - 8;
  const unsigned l16 = lane * 16;
  asm volatile("" : "+s"(O), "+s"(Wt), "+s"(CS), "+s"(W2t), "+s"(Wot));
  asm volatile("" : "+s"(wo), "+s"(w1), "+s"(w3), "+s"(w2));
  const bool ld = xg > 0 || wid >= 4;

  const char* kb = (const char*)QKV + (2u << 20) + (ptrdiff_t)(h * 64 + t0 + 2 * wid) * 1024;
  if (ld) {
    glds(l16, kb, lds0 + wid * 2048);
    glds(l16, kb + 1024, lds0 + wid * 2048 + 1024);
  }
  bf16x8 qf;
  GLD4(qf, l16, (const char*)QKV + (size_t)(h * 64 + xg * 8 + wid) * 1024, 0);
  if (ld) {
    glds(l16, kb + (2u << 20), lds0 + LV + wid * 2048);
    glds(l16, kb + (2u << 20) + 1024, lds0 + LV + wid * 2048 + 1024);
  }
  {
    const u32x4 o4 = {0x3f803f80u, 0x3f803f80u, 0x3f803f80u, 0x3f803f80u};
    *(u32x4*)(smem + LONE + tid * 16) = o4;
    if (tid < 128) *(u32x4*)(smem + LONE + 8192 + tid * 16) = o4;
  }
  f32x16 cm0, cm8;
  {
    const int c = lane & 31, hh = lane >> 5;
#pragma unroll
    for (int r = 0; r < 16; ++r) {
      const int kr = (r & 3) + 8 * (r >> 2) + 4 * hh;
      cm0[r] = (kr > c) ? 0.f : -1e30f;
      cm8[r] = (kr <= c) ? 0.f : -1e30f;
    }
    PIN(cm0); PIN(cm8);
  }
  TSTAMP(1, 1);
  if (ld) asm volatile("s_waitcnt vmcnt(2) lgkmcnt(0)" ::: "memory");
  else asm volatile("s_waitcnt vmcnt(0) lgkmcnt(0)" ::: "memory");
  PIN(qf);
  BAR();
  TSTAMP(1, 2);
  const int skc = blockIdx.x >> 5, smt = (blockIdx.x >> 4) & 1, spn = blockIdx.x & 15;
  u32x4 wr0, wr1;
  {
    const int wj = wid < 7 ? wid : 6;
    const char* sb = (const char*)(smt ? w3 : w1) + (size_t)(skc * 64) * (F_ * 2) + (spn * 112 + wj * 16) * 2;
    const unsigned vo = (unsigned)((lane >> 1) * (F_ * 2) + (lane & 1) * 16);
    glds(vo, sb, lds0 + LSJ + wid * 2048);
    glds(vo, sb + 32 * (F_ * 2), lds0 + LSJ + wid * 2048 + 1024);
    const char* o = (const char*)wo + (size_t)blockIdx.x * 2048;
    const char* r0 = wid < 7 ? (const char*)w2 + (size_t)blockIdx.x * 7168 + wid * 1024 : o;
    GLD4(wr0, l16, r0, 0);
    GLD4(wr1, l16, o, 1024);
  }

  const unsigned kaddr = lds0 + wid * 1024 + l16;
  const unsigned vlane = (unsigned)(((lane >> 5) * 4 + (l15 >> 2)) * 32 + (l15 & 3) * 8);
  const unsigned vaddr = (lane & 16) ? (lds0 + LONE + (vlane ^ 128u)) : (lds0 + LV + wid * 1024 + vlane);
  bf16* dst = O + (size_t)(((xg * 4 + (wid >> 1)) * H_ + h) * 64 + (wid & 1) * 32 + (lane & 31)) * 16 + (lane >> 5) * 8;
  if (xg > 0) attn_body<true, 4>(kaddr, vaddr, qf, cm0, cm8, wid, dst, lds0 TPASS);
  else attn_body<false, 4>(kaddr, vaddr, qf, cm0, cm8, wid, dst, lds0 TPASS);
  TSTAMP(1, 20);
  wait_vm<1>();
  {
    const unsigned co = (unsigned)(lane & 7) * 16;
    PIN(wr0); PIN(wr1);
    if (wid < 7) {
      st16<(WT & 2) != 0>((char*)W2t + (size_t)(lane >> 3) * (F_ * 128) + (blockIdx.x * 7 + wid) * 128 + co, wr0);
    } else {
      char* o = (char*)Wot + (size_t)(lane >> 3) * (D_ * 128) + blockIdx.x * 256 + co;
      st16<(WT & 2) != 0>(o, wr0);
      st16<(WT & 2) != 0>(o + 128, wr1);
    }
  }
  if (wid < 7) {
    const unsigned ra = lds0 + LSJ + wid * 2048 + (gq * 8 + (l15 >> 2)) * 32 + (l15 & 3) * 8;
    bf16x4 f[4];
    DSRTR(f[0], ra, 0);
    DSRTR(f[1], ra, 128);
    DSRTR(f[2], ra, 1024);
    DSRTR(f[3], ra, 1024 + 128);
    asm volatile("s_waitcnt lgkmcnt(0)" ::: "memory");
    float c = 0.f;
#pragma unroll
    for (int i = 0; i < 4; ++i) {
      PIN(f[i]);
#pragma unroll
      for (int e = 0; e < 4; ++e) c += (float)f[i][e];
    }
    char* wd = (char*)Wt + (size_t)(((spn * 8 + skc) * 4 + smt) * 7 + wid) * 1024 + l16;
    st16<(WT & 2) != 0>(wd, __builtin_bit_cast(u32x4, cat8(f[0], f[1])));
    st16<(WT & 2) != 0>(wd + 14 * 1024, __builtin_bit_cast(u32x4, cat8(f[2], f[3])));
    c += __shfl_xor(c, 16);
    c += __shfl_xor(c, 32);
    if (lane < 16) st4<(WT & 2) != 0>(CS + skc * (2 * F_) + smt * F_ + spn * 112 + wid * 16 + lane, __builtin_bit_cast(unsigned, c));
  }
  TSTAMP(1, 22);
  wait_vm<0>();
  TFLUSH(1);
}

template <int MODE, int KTOT>
DEV void gemm_body(const bf16* __restrict__ A, const bf16* __restrict__ B, const bf16* __restrict__ R,
                   bf16* __restrict__ O, float* __restrict__ RS, const bf16* __restrict__ PFW) {
  constexpr int NST = G_NST, STAGE = 32768, NK = KTOT / 128, NT = NK * 4;
  extern __shared__ __attribute__((aligned(16))) char smem[];
  TDECL
  TSTAMP(MODE == 0 ? 2 : 4, 0);
  const int tid = threadIdx.x, lane = tid & 63;
  const int wid = __builtin_amdgcn_readfirstlane(tid >> 6);
  const int wm = wid & 3, wn = wid >> 2;
  const int l15 = lane & 15, gq = lane >> 4;
  const int xg = blockIdx.x & 7, idx = blockIdx.x >> 3;
#define PNX 0
  const int pm = xg * 4 + (idx >> 3), pn = (idx & 7) ^ PNX;
  if (MODE == 0) asm volatile("" : "+s"(RS), "+s"(PFW));
  const unsigned lds0 = (unsigned)(uintptr_t)(LDSP(char))smem;

  u32x4 res;
  const int tok = pm * 64 + wm * 16 + l15;
  const int ncol = pn * 64 + wn * 32 + (gq & 1) * 16 + (gq >> 1) * 8;
  const unsigned hoff = (unsigned)(((((tok >> 7) * 8 + pn) * 128 + (tok & 127)) * 64 + (ncol & 63)) * 2);
  GLD4(res, MODE == 0 ? (unsigned)((tok * D_ + ncol) * 2) : hoff, R, 0);

  const unsigned voffA = (unsigned)(wid * 1024 + lane * 16);
  const int pB = ((lane & 7) >> 1) ^ (((lane >> 4) & 1) | ((wid & 1) << 1));
  const unsigned voffB = (unsigned)((wid * 8 + (lane >> 3)) * 128 + pB * 32 + (lane & 1) * 16);
  const char* baseA = (const char*)A + (size_t)pm * (KTOT * 128);
  const char* baseB = (const char*)B + pn * (KTOT * 128);
  const bool isA = wid < 4;
  const unsigned voff = isA ? (unsigned)((wid & 3) * 1024 + lane * 16)
                            : (unsigned)(((wid & 3) * 8 + (lane >> 3)) * 128 + pB * 32 + (lane & 1) * 16);
  const char* base = isA ? baseA : baseB;
  const unsigned sbw = lds0 + (isA ? 0 : 16384) + (wid & 3) * 1024;
  auto part = [&](int kc, int p) {
    glds(voff, base + (size_t)kc * 16384 + p * 4096, sbw + (kc % NST) * STAGE + p * 4096);
  };
  auto issue = [&](int kc) { part(kc, 0); part(kc, 1); part(kc, 2); part(kc, 3); };
  TSTAMP(MODE == 0 ? 2 : 4, 16);
  issue(0);
  TSTAMP(MODE == 0 ? 2 : 4, 17);
  BAR();
  issue(1);
  BAR();
  TSTAMP(MODE == 0 ? 2 : 4, 21);
  issue(2);
  if (MODE == 1 && PF5) {
    const int g = wid * 60 + (lane < 60 ? lane : 59);
    const unsigned dB = (unsigned)(baseB - baseA);
    const unsigned vo = g < 160 ? (unsigned)((512 + pn * 160 + g) * 128) : dB + (unsigned)((512 + (idx >> 3) * 320 + g - 160) * 128);
    const unsigned la = __builtin_amdgcn_readfirstlane(lds0 + NST * STAGE + wid * 256);
    asm volatile("s_mov_b32 m0, %2\n\ts_nop 0\n\tglobal_load_lds_dword %0, %1" ::"v"(vo), "s"(baseA), "s"(la) : "memory");
  }
  TSTAMP(MODE == 0 ? 2 : 4, 1);

  unsigned a_addr[4], b_addr[2];
#pragma unroll
  for (int ks = 0; ks < 4; ++ks) a_addr[ks] = lds0 + (2 * ks + (gq >> 1)) * 2048 + (wm * 16 + l15) * 32 + (gq & 1) * 16;
  {
    const int kl = gq * 8 + (l15 >> 2);
    const int fr = ((l15 >> 3) & 1) | ((gq & 1) << 1);
#pragma unroll
    for (int j = 0; j < 2; ++j) b_addr[j] = lds0 + 16384 + kl * 128 + (((2 * wn + j) ^ fr) * 32) + (l15 & 3) * 8;
  }

  f32x4 acc[2];
  const f32x4 z4 = {0.f, 0.f, 0.f, 0.f};
  acc[0] = z4; acc[1] = z4;
  constexpr int NS = LA2 ? 3 : 2;
  bf16x8 af[NS];
  bf16x4 bl[NS][2], bh[NS][2];

  auto rd = [&](int t, int set) {
    const int kc = t >> 2, ks = t & 3, st = kc % NST;
    const unsigned hi = (unsigned)((st & ~1) * STAGE);
    const int so = (st & 1) * STAGE;
    DSRTR(bl[set][0], b_addr[0] + hi, so + ks * 4096);
    DSRTR(bh[set][0], b_addr[0] + hi, so + ks * 4096 + 512);
    DSR128(af[set], a_addr[ks] + hi, so);
    DSRTR(bl[set][1], b_addr[1] + hi, so + ks * 4096);
    DSRTR(bh[set][1], b_addr[1] + hi, so + ks * 4096 + 512);
  };
  auto wt = [&](int set) {
    asm volatile("s_waitcnt lgkmcnt(0)" ::: "memory");
    PIN(af[set]);
    PIN(bl[set][0]); PIN(bh[set][0]); PIN(bl[set][1]); PIN(bh[set][1]);
  };

  using SC = Sched<4, 4, NST, NK, 12, G_R>;
  auto pin = [&](int set) {
    PIN(af[set]);
    PIN(bl[set][0]); PIN(bh[set][0]); PIN(bl[set][1]); PIN(bh[set][1]);
  };
  wait_vm<SC::cum(-1) - 4 + ((MODE == 1 && PF5) ? 1 : 0)>();
  BAR();
  TSTAMP(MODE == 0 ? 2 : 4, 2);
  rd(0, 0);
  if (LA2) rd(1, 1);
  static_for<SC::cum(-1), SC::cum(0)>([&](auto n) { part(n.value >> 2, n.value & 3); });
  if (!LA2) wt(0);
  static_for<0, NT>([&](auto tc) {
    constexpr int t = tc.value, kc = t >> 2, ks = t & 3, cur = t % NS, nxt = (t + NS - 1) % NS;
    constexpr bool sync = ks == (LA2 ? 2 : 3) && kc + 1 < NK;
    if constexpr (sync) {
      static_assert(SC::cum(t) >= (kc + 2) * 4, "schedule");
      constexpr int npf = (MODE == 0 && PF3 && t > PF3T) ? (t - PF3T > 4 ? 4 : t - PF3T) : 0;
      static_assert(npf == 0 || SC::cum(PF3T) == NK * 4, "warm-up loads come after all operand loads");
      if (LA2 && GQ) {
        asm volatile("s_waitcnt lgkmcnt(5)" ::: "memory");
        pin(cur);
      } else if (LA2) {
        asm volatile("s_waitcnt lgkmcnt(0)" ::: "memory");
        pin(cur); pin((t + 1) % NS);
      }
      wait_vm<SC::cum(t) - (kc + 2) * 4 + npf + ((MODE == 0 && (CPF & 4) && t > CW3) ? 1 : 0) + ((MODE == 1 && PF5 && kc + 1 <= 2) ? 1 : 0)>();
      BAR();
      TSTAMP(MODE == 0 ? 2 : 4, 3 + kc);
    }
    if constexpr (t + NS - 1 < NT) rd(t + NS - 1, nxt);
    static_for<SC::cum(t), SC::cum(t + 1)>([&](auto n) { part(n.value >> 2, n.value & 3); });
    if constexpr (MODE == 0 && (CPF & 4) && t == CW3) {
      static_assert(SC::cum(t + 1) == NK * 4, "issued after all operand loads");
      code_warm<true>(wid, lane, lds0 + NST * STAGE + wid * 1024, baseA);
    }
    if constexpr (MODE == 0 && PF3 && t >= PF3T && t < PF3T + 4) {
      glds((unsigned)(lane * 16),
           (const char*)PFW + (size_t)(xg >> 1) * (4 * 229376) + idx * 28672 + (t - PF3T) * 8192 + (t - PF3T < 3 ? wid : (wid & 3)) * 1024,
           lds0 + NST * STAGE + wid * 1024);
    }
    if constexpr (LA2 && !sync && (GQ || !(ks == 3 && kc + 1 < NK))) {
      asm volatile("s_waitcnt lgkmcnt(%0)" ::"n"(t + 2 < NT ? 10 : (t + 1 < NT ? 5 : 0)) : "memory");
      pin(cur);
    }
    SCHED();
    acc[0] = mfma16(cat8(bl[cur][0], bh[cur][0]), af[cur], acc[0]);
    acc[1] = mfma16(cat8(bl[cur][1], bh[cur][1]), af[cur], acc[1]);
    SCHED();
    if constexpr (!LA2 && t + 1 < NT) wt(nxt);
  });
  TSTAMP(MODE == 0 ? 2 : 4, 20);
  PIN(res);
  float s = 0.f, q = 0.f;
  {
#pragma unroll
    for (int r = 0; r < 4; ++r) { float a = acc[0][r], b = acc[1][r]; swap16f(a, b); acc[0][r] = a; acc[1][r] = b; }
    const bf16x8 rr = __builtin_bit_cast(bf16x8, res);
    bf16x8 o;
#pragma unroll
    for (int j = 0; j < 2; ++j)
#pragma unroll
      for (int r = 0; r < 4; ++r) {
        o[j * 4 + r] = (bf16)((float)rr[j * 4 + r] + bfr(acc[j][r]));
        const float v = (float)o[j * 4 + r];
        s += v;
        q += v * v;
      }
    st16<(WT & (MODE == 0 ? 4 : 16)) != 0>((char*)O + (MODE == 0 ? hoff : (unsigned)((tok * D_ + ncol) * 2)), __builtin_bit_cast(u32x4, o));
  }
  if (MODE == 0) {
    s += __shfl_xor(s, 16); s += __shfl_xor(s, 32);
    q += __shfl_xor(q, 16); q += __shfl_xor(q, 32);
    if (gq == 0) st8<(WT & 4) != 0>(RS + ((size_t)(pn * 2 + wn) * S_ + tok) * 2, __builtin_bit_cast(u32x2, f32x2{s, q}));
  }
  if (MODE == 0) wait_vm<0>();
  TSTAMP(MODE == 0 ? 2 : 4, 22);
  TFLUSH(MODE == 0 ? 2 : 4);
}
extern "C" __global__ __launch_bounds__(512, 2) void k3_wo(const bf16* __restrict__ A, const bf16* __restrict__ B,
                                                           const bf16* __restrict__ R, bf16* __restrict__ O,
                                                           float* __restrict__ RS, const bf16* __restrict__ PFW) {
  gemm_body<0, 512>(A, B, R, O, RS, PFW);
}
extern "C" __global__ __launch_bounds__(512, 2) void k5_down(const bf16* __restrict__ A, const bf16* __restrict__ B,
                                                             const bf16* __restrict__ R, bf16* __restrict__ O) {
  gemm_body<1, 1792>(A, B, R, O, nullptr, nullptr);
}

template <int NP, int P0>
DEV void gu_body(char* smem, unsigned lds0, int tid, int wid, int pm, int pn, const bf16* __restrict__ Hh,
                 const bf16* __restrict__ Wt, const float* __restrict__ RS, const float* __restrict__ CS,
                 bf16* __restrict__ Hm, const char* pf TARG) {
  constexpr int NST = 3, STAGE = 45056, NK = 8, LPC = 2 + NP, NT = 16;
  const int lane = tid & 63, wm = wid & 3;
  const int l15 = lane & 15, gq = lane >> 4;
  const unsigned l16 = lane * 16;
  const unsigned voffA = (unsigned)((wid * 8 + (lane >> 3)) * 128 + (((lane & 7) ^ ((wid & 1) * 4 + gq)) * 16));
  const char* baseA = (const char*)Hh + (size_t)pm * (128 * 1024);
  const int pc0 = (P0 == 0 ? 0 : 16) + wm * NP;
  const char* baseW = (const char*)Wt + (size_t)pn * (8 * 28672) + pc0 * 1024;
  const int rot = ROT4 * (pm + 2 * pn);
  auto piece = [&](int kc, int i) {
    const unsigned sb = lds0 + (kc % NST) * STAGE;
    const int kk = (kc + rot) & 7;
    if (i == 0) glds(voffA, baseA + kk * 16384, sb + wid * 1024);
    else if (i == 1) glds(voffA, baseA + kk * 16384 + 8192, sb + 8192 + wid * 1024);
    else glds(l16, baseW + kk * 28672 + (i - 2) * 1024, sb + 16384 + (pc0 + i - 2) * 1024);
  };
  auto issue = [&](int kc) {
#pragma unroll
    for (int i = 0; i < LPC; ++i) piece(kc, i);
  };
  float* st_ = (float*)(smem + NST * STAGE);
  float* csum = st_ + 256;
  if (P0 == 0) {
    if (wid < 2) {
      f32x2 sv[16];
      const char* rb = (const char*)RS + pm * 1024;
      const unsigned vo = (unsigned)(tid * 8);
#pragma unroll
      for (int i = 0; i < 16; ++i) GLD2(sv[i], vo, rb + i * (S_ * 8), 0);
      issue(0);
      BAR();
      issue(1);
      wait_vm<2 * LPC>();
#pragma unroll
      for (int i = 0; i < 16; ++i) PIN(sv[i]);
      f32x2 a = sv[0];
#pragma unroll
      for (int i = 1; i < 16; ++i) a += sv[i];
      *(f32x2*)(st_ + tid * 2) = ln_stats(a[0], a[1]);
    } else {
      issue(0);
      BAR();
      issue(1);
    }
  } else {
    float sc[8];
    int c = tid - 256;
    const bool ok = c < 224;
    if (!ok) c = 0;
    const unsigned vo = (unsigned)(((c < 112 ? 0 : F_ - 112) + pn * 112 + c) * 4);
#pragma unroll
    for (int i = 0; i < 8; ++i) GLD1(sc[i], vo, (const char*)CS + i * (2 * F_ * 4), 0);
    issue(0);
    BAR();
    issue(1);
    wait_vm<2 * LPC>();
#pragma unroll
    for (int i = 0; i < 8; ++i) PIN(sc[i]);
    float a = sc[0];
#pragma unroll
    for (int i = 1; i < 8; ++i) a += sc[i];
    if (ok) csum[c] = a;
  }
  TSTAMP(3, 1);

  unsigned a_addr[2];
#pragma unroll
  for (int ks = 0; ks < 2; ++ks) a_addr[ks] = lds0 + (wm * 32 + l15) * 128 + (((4 * ks + gq) ^ (l15 >> 1)) * 16);
  const unsigned b_addr = lds0 + 16384 + P0 * 1024 + l16;
  f32x4 ag[2][NP], au[2][NP];
  const f32x4 z4 = {0.f, 0.f, 0.f, 0.f};
#pragma unroll
  for (int j = 0; j < NP; ++j) { ag[0][j] = z4; ag[1][j] = z4; au[0][j] = z4; au[1][j] = z4; }
  bf16x8 af[2][2], bg[2][NP], bu[2][NP];
  auto rd = [&](int t, int set) {
    const int kc = t >> 1, ks = t & 1, st = kc % NST;
    const unsigned so = st * STAGE;
    DSR128(af[set][0], a_addr[ks] + so, 0);
    DSR128(af[set][1], a_addr[ks] + so, 2048);
#pragma unroll
    for (int j = 0; j < NP; ++j) {
      DSR128(bg[set][j], b_addr + so, (ks * 14 + j) * 1024);
      DSR128(bu[set][j], b_addr + so, (ks * 14 + 7 + j) * 1024);
    }
  };
  auto wt = [&](int set) {
    asm volatile("s_waitcnt lgkmcnt(0)" ::: "memory");
    PIN(af[set][0]); PIN(af[set][1]);
#pragma unroll
    for (int j = 0; j < NP; ++j) { PIN(bg[set][j]); PIN(bu[set][j]); }
  };
  asm volatile("s_waitcnt vmcnt(%0) lgkmcnt(0)" ::"n"(LPC) : "memory");
  BAR();
  TSTAMP(3, 2);
  rd(0, 0);
  piece(NST - 1, 0); piece(NST - 1, 1); piece(NST - 1, 2);
  wt(0);
  constexpr int NTM = NT - 4, NU = 2 * NP;
  auto npf = [&](int lo, int hi) {
    int n = 0;
    if (PF4 == 4)
      for (int i = lo; i <= hi; ++i) n += (i >= PFT && i < PFT + 7) ? 1 : 0;
    return n;
  };
  bf16x8 aT[4][2], bT[2][2][2];
  auto uj = [&](int u) { return u == 0 ? 0 : (u == NU - 1 ? NP - 1 : ((u & 1) ? (u + 1) / 2 : u / 2 - 1)); };
  auto uc = [&](int u) { return (u == 0 || ((u & 1) && u != NU - 1)) ? 0 : 1; };
  auto rdA = [&](int c) {
#pragma unroll
    for (int ks = 0; ks < 2; ++ks) {
      DSR128(aT[c * 2 + ks][0], a_addr[ks] + ((NK - 2 + c) % NST) * STAGE, 0);
      DSR128(aT[c * 2 + ks][1], a_addr[ks] + ((NK - 2 + c) % NST) * STAGE, 2048);
    }
  };
  auto rdB = [&](int u) {
#pragma unroll
    for (int ks = 0; ks < 2; ++ks) {
      DSR128(bT[u & 1][ks][0], b_addr + ((NK - 2 + uc(u)) % NST) * STAGE, (ks * 14 + uj(u)) * 1024);
      DSR128(bT[u & 1][ks][1], b_addr + ((NK - 2 + uc(u)) % NST) * STAGE, (ks * 14 + 7 + uj(u)) * 1024);
    }
  };
  auto rdp = [&](int t, int set, int i) {
    const int kc = t >> 1, ks = t & 1, st = kc % NST;
    const unsigned so = st * STAGE;
    if (i < 2) DSR128(af[set][i], a_addr[ks] + so, i * 2048);
    else if (i & 1) DSR128(bu[set][(i - 2) >> 1], b_addr + so, (ks * 14 + 7 + ((i - 2) >> 1)) * 1024);
    else DSR128(bg[set][(i - 2) >> 1], b_addr + so, (ks * 14 + ((i - 2) >> 1)) * 1024);
  };
  auto rdT = [&](int i) {
    if (i < 4) DSR128(aT[i >> 1][i & 1], a_addr[i >> 1] + ((NK - 2) % NST) * STAGE, (i & 1) * 2048);
    else DSR128(bT[0][(i - 4) >> 1][i & 1], b_addr + ((NK - 2 + uc(0)) % NST) * STAGE, (((i - 4) >> 1) * 14 + (i & 1) * 7 + uj(0)) * 1024);
  };
#pragma unroll
  for (int t = 0; t < NTM; ++t) {
    const int kc = t >> 1, ks = t & 1, cur = t & 1, nxt = cur ^ 1;
    if (ks == 1) {
      const int last = (kc + NST - 1 < NK - 1) ? (kc + NST - 1) : (NK - 1);
      wait_vm_n((last - (kc + 1)) * LPC + npf(2 * kc - 2, 2 * kc));
      BAR();
      TSTAMP(3, 3 + kc);
    }
    const int ic = (t + 1) / 2 + NST - 1, ip = ((t + 1) & 1) * 3;
    SCHED();
#pragma unroll
    for (int j = 0; j < NP; ++j) {
#pragma unroll
      for (int q = 0; q < 4; ++q) {
        if (q == 0) ag[0][j] = mfma16(bg[cur][j], af[cur][0], ag[0][j]);
        if (q == 1) ag[1][j] = mfma16(bg[cur][j], af[cur][1], ag[1][j]);
        if (q == 2) au[0][j] = mfma16(bu[cur][j], af[cur][0], au[0][j]);
        if (q == 3) au[1][j] = mfma16(bu[cur][j], af[cur][1], au[1][j]);
        SCHED();
#pragma unroll
        for (int r = (j * 4 + q) * ILV4; r < (j * 4 + q + 1) * ILV4; ++r) {
          if (t + 1 < NTM) { if (r < 2 + 2 * NP) rdp(t + 1, nxt, r); }
          else if (r < 8) rdT(r);
        }
        SCHED();
      }
      if (j < 3 && ic < NK && ip + j < LPC) piece(ic, ip + j);
      if (PF4 && PF4 < 4 && j < (PF4 == 1 ? 2 : 1) && t == NTM - 1) glds(l16, pf + j * 8192, lds0 + NST * STAGE + 2048 + wid * 1024);
      if (j == 2 && npf(t, t)) glds(l16, pf + (t - PFT) * 8192, lds0 + NST * STAGE + 2048 + wid * 1024);
      SCHED();
    }
    if (t + 1 < NTM) wt(nxt);
  }
  auto wtT = [&](bool a0, bool a1, int u) {
    asm volatile("s_waitcnt lgkmcnt(0)" ::: "memory");
    if (a0) { PIN(aT[0][0]); PIN(aT[0][1]); PIN(aT[1][0]); PIN(aT[1][1]); }
    if (a1) { PIN(aT[2][0]); PIN(aT[2][1]); PIN(aT[3][0]); PIN(aT[3][1]); }
    PIN(bT[u & 1][0][0]); PIN(bT[u & 1][0][1]); PIN(bT[u & 1][1][0]); PIN(bT[u & 1][1][1]);
  };
  wtT(true, false, 0);
  TSTAMP(3, 20);
  bf16* dst0 = Hm + ((size_t)((pm * 2 + (wm >> 1)) * 112 + pn * 7 + P0) * 64 + (wm & 1) * 32 + l15) * 16;
  const unsigned caddr = lds0 + NST * STAGE + 1024 + (P0 * 16 + gq * 4) * 4;
  f32x2 nm[2], le2[2], iv2[2];
  f32x4 cg[2], cu[2];
  unsigned pk[NP][2][2];
  f32x2 tg, tu, te, td;
  auto epi = [&](int j, int mi, int q) {
    const int hf = q >> 2;
    if ((q & 3) == 0) {
      tg = __builtin_elementwise_fma(f32x2{cg[j & 1][2 * hf], cg[j & 1][2 * hf + 1]}, nm[mi],
                                     f32x2{ag[mi][j][2 * hf], ag[mi][j][2 * hf + 1]});
      tu = __builtin_elementwise_fma(f32x2{cu[j & 1][2 * hf], cu[j & 1][2 * hf + 1]}, nm[mi],
                                     f32x2{au[mi][j][2 * hf], au[mi][j][2 * hf + 1]});
      te = tg * le2[mi];
    } else if ((q & 3) == 1) {
      te = f32x2{__builtin_amdgcn_exp2f(te[0]), __builtin_amdgcn_exp2f(te[1])};
    } else if ((q & 3) == 2) {
      td = __builtin_elementwise_fma(te, iv2[mi], iv2[mi]);
      tu = tg * tu;
    } else {
      const f32x2 rc = {__builtin_amdgcn_rcpf(td[0]), __builtin_amdgcn_rcpf(td[1])};
      const f32x2 o = tu * rc;
      pk[j][mi][hf] = pk2(o[0], o[1]);
    }
    if (q == 7) {
      if (j & 1) {
        swap16(pk[j - 1][mi][0], pk[j][mi][0]);
        swap16(pk[j - 1][mi][1], pk[j][mi][1]);
        st16<(WT & 8) != 0>(dst0 + ((size_t)(j - 1 + (gq & 1)) * 64 + mi * 16) * 16 + (gq >> 1) * 8,
                            u32x4{pk[j - 1][mi][0], pk[j - 1][mi][1], pk[j][mi][0], pk[j][mi][1]});
      } else if (j == NP - 1) {
        st8<(WT & 8) != 0>(dst0 + ((size_t)j * 64 + mi * 16) * 16 + gq * 4, u32x2{pk[j][mi][0], pk[j][mi][1]});
      }
    }
  };
#pragma unroll
  for (int u = 0; u < NU; ++u) {
    const int j = uj(u), c = uc(u);
    if (u == 1) {
      wait_vm_n(PF4 == 4 ? npf(NTM - 2, NTM - 1) : (PF4 == 1 ? 4 : (PF4 ? 1 : 0)));
      BAR();
      TSTAMP(3, 9);
      rdA(1);
    }
    if (u + 1 < NU) rdB(u + 1);
    const int cj = u == NU - 1 ? NP - 1 : ((u >= 2 && !(u & 1)) ? (u - 2) / 2 : -1);
    if (cj >= 0) {
      DSR128(cg[cj & 1], caddr, cj * 64);
      DSR128(cu[cj & 1], caddr, cj * 64 + 448);
    }
    if (u == 1) {
#pragma unroll
      for (int mi = 0; mi < 2; ++mi) {
        const f32x2 ms = *(const f32x2*)(st_ + (wm * 32 + mi * 16 + l15) * 2);
        nm[mi] = f32x2{-ms[0], -ms[0]};
        const float le = ms[1] * -1.4426950408889634f, iv = 1.0f / (ms[1] * ms[1]);
        le2[mi] = f32x2{le, le};
        iv2[mi] = f32x2{iv, iv};
      }
    }
    SCHED();
#pragma unroll
    for (int q = 0; q < 8; ++q) {
      const int ks = q >> 2, mi = q & 1;
      if (q & 2) au[mi][j] = mfma16(bT[u & 1][ks][1], aT[c * 2 + ks][mi], au[mi][j]);
      else ag[mi][j] = mfma16(bT[u & 1][ks][0], aT[c * 2 + ks][mi], ag[mi][j]);
      SCHED();
      if (u >= 3) epi((u - 3) >> 1, (u - 3) & 1, q);
      if (PF4 == 1 && u < 3 && (q == 1 || q == 5) && 2 + u * 2 + (q >> 2) < 7)
        glds(l16, pf + (2 + u * 2 + (q >> 2)) * 8192, lds0 + NST * STAGE + 2048 + wid * 1024);
      if ((CPF & 8) && u == 3 && q == 3) code_warm<false>(wid, lane, lds0 + NST * STAGE + 2048 + wid * 1024, nullptr);
      SCHED();
    }
    if (u + 1 < NU) {
      wtT(false, u == 1, u + 1);
    } else {
      asm volatile("s_waitcnt lgkmcnt(0)" ::: "memory");
    }
    if (cj >= 0) { PIN(cg[cj & 1]); PIN(cu[cj & 1]); }
  }
  TSTAMP(3, 21);
#pragma unroll
  for (int e = NU - 3; e < NU; ++e)
#pragma unroll
    for (int q = 0; q < 8; ++q) epi(e >> 1, e & 1, q);
}

extern "C" __global__ __launch_bounds__(512, 2) void k4_gu(const bf16* __restrict__ Hh, const bf16* __restrict__ Wt,
                                                const float* __restrict__ RS, const float* __restrict__ CS,
                                                bf16* __restrict__ Hm, const bf16* __restrict__ W2t) {
  extern __shared__ __attribute__((aligned(16))) char smem[];
  TDECL
  TSTAMP(3, 0);
  const int tid = threadIdx.x;
  const int wid = __builtin_amdgcn_readfirstlane(tid >> 6);
  const int xg = blockIdx.x & 7, idx = blockIdx.x >> 3;
  const int pm = (xg & 1) * 8 + (idx & 7), pn = (xg >> 1) * 4 + (idx >> 3);
  asm volatile("" : "+s"(RS), "+s"(CS), "+s"(Hm), "+s"(W2t));
  const unsigned lds0 = (unsigned)(uintptr_t)(LDSP(char))smem;
  const char* pf = (const char*)W2t + (PF4 == 2 ? (blockIdx.x * 7 + (wid < 7 ? wid : 6)) * 1024 : (PF4 == 3 ? idx * 57344 + wid * 7168 : idx * 57344 + wid * 1024));
  if (wid < 4) gu_body<4, 0>(smem, lds0, tid, wid, pm, pn, Hh, Wt, RS, CS, Hm, pf TPASS);
  else gu_body<3, 4>(smem, lds0, tid, wid, pm, pn, Hh, Wt, RS, CS, Hm, pf TPASS);
  wait_vm<0>();
  TSTAMP(3, 22);
  TFLUSH(3);
  asm volatile("k4_gu_tail:");
}

extern "C" void kernel_launch(void* const* d_in, const int* in_sizes, int n_in, void* d_out, int out_size, void* d_ws,
                              size_t ws_size, hipStream_t stream) {
  const bf16* x = (const bf16*)d_in[0];
  const bf16* wqkv = (const bf16*)d_in[1];
  const bf16* wo = (const bf16*)d_in[2];
  const bf16* w1 = (const bf16*)d_in[4];
  const bf16* w3 = (const bf16*)d_in[5];
  const bf16* w2 = (const bf16*)d_in[6];
  bf16* out = (bf16*)d_out;

  char* ws = (char*)d_ws;
  bf16* QKV = (bf16*)(ws);
  bf16* Oa = (bf16*)(ws + (7u << 20));
  bf16* Hh = (bf16*)(ws + (9u << 20));
#define HMO 11
  bf16* Hm = (bf16*)(ws + ((unsigned)HMO << 20));
  float* RS = (float*)(ws + (18u << 20));
  float* CS = (float*)(ws + (19u << 20));
  bf16* Wt = (bf16*)(ws + (20u << 20));
  bf16* W2t = (bf16*)(ws + (24u << 20));
  bf16* Wot = (bf16*)(ws + (26u << 20));

  {
    constexpr int lds = K1_NST * 28672 + 2560 + 2048;
    (void)hipFuncSetAttribute((const void*)k1_qkv, hipFuncAttributeMaxDynamicSharedMemorySize, lds);
    k1_qkv<<<256, 512, lds, stream>>>(x, wqkv, QKV);
  }
  {
    constexpr int lds = 59392 + 2048;
    (void)hipFuncSetAttribute((const void*)k2_attn, hipFuncAttributeMaxDynamicSharedMemorySize, lds);
    k2_attn<<<256, 512, lds, stream>>>(QKV, Oa, wo, w1, w3, w2, Wt, CS, W2t, Wot);
  }
  {
    auto k = k3_wo;
    constexpr int lds = G_NST * 32768 + 8192;
    (void)hipFuncSetAttribute((const void*)k, hipFuncAttributeMaxDynamicSharedMemorySize, lds);
    k<<<256, 512, lds, stream>>>(Oa, Wot, x, Hh, RS, Wt);
  }
  {
    constexpr int lds = 3 * 45056 + 2048 + 8192;
    (void)hipFuncSetAttribute((const void*)k4_gu, hipFuncAttributeMaxDynamicSharedMemorySize, lds);
    k4_gu<<<256, 512, lds, stream>>>(Hh, Wt, RS, CS, Hm, W2t);
  }
  {
    auto k = k5_down;
    constexpr int lds = G_NST * 32768 + 2048;
    (void)hipFuncSetAttribute((const void*)k, hipFuncAttributeMaxDynamicSharedMemorySize, lds);
    k<<<256, 512, lds, stream>>>(Hm, W2t, Hh, out);
  }
}
```

```cpp
#include <hip/hip_runtime.h>
#include <stdint.h>

typedef __attribute__((ext_vector_type(8))) __bf16 bf16x8;
typedef __attribute__((ext_vector_type(4))) __bf16 bf16x4;
typedef __attribute__((ext_vector_type(2))) __bf16 bf16x2;
typedef __attribute__((ext_vector_type(2))) float f32x2;
typedef __attribute__((ext_vector_type(4))) float f32x4;
typedef __attribute__((ext_vector_type(16))) float f32x16;
typedef __attribute__((ext_vector_type(4))) unsigned int u32x4;
typedef __attribute__((ext_vector_type(2))) unsigned int u32x2;
typedef __bf16 bf16;

#define LDSP(T) __attribute__((address_space(3))) T*
#define DEV __device__ __forceinline__

#define K1_NST 4
#define G_NST 4
#define G_R 1
#define ROT1 0
#define ROT3 0
#define ROT4 0
#define ROT5 0
#define PF4 1
#define PFT 5
#define LA2 1
#define PF3 0
#define K1DOT 0
#define K1T 1
#define ILV1 1
#define ILV4 1
#define GQ 1
#define PF5 0
#define MAP5 0
#define PFE 1
#define PF3W 2
#define PFH 1
#define PF3T 4
constexpr int S_ = 2048, D_ = 512, H_ = 32, F_ = 1792;

DEV float bfr(float x) { return (float)(bf16)x; }

template <int N>
DEV void wait_vm() {
  asm volatile("s_waitcnt vmcnt(%0)" ::"n"(N) : "memory");
}
DEV void wait_vm_n(int n) {
  switch (n) {
#define WC(X) case X: wait_vm<X>(); break;
    WC(0) WC(1) WC(2) WC(3) WC(4) WC(5) WC(6) WC(7) WC(8) WC(9) WC(10) WC(11) WC(12) WC(13) WC(14) WC(15)
    WC(16) WC(17) WC(18) WC(19) WC(20) WC(21) WC(22) WC(23) WC(24)
#undef WC
    default: wait_vm<0>(); break;
  }
}

DEV void glds(unsigned voff, const void* sbase, unsigned ldsaddr) {
  ldsaddr = __builtin_amdgcn_readfirstlane(ldsaddr);
  asm volatile("s_mov_b32 m0, %2\n\ts_nop 0\n\tglobal_load_lds_dwordx4 %0, %1" ::"v"(voff), "s"(sbase), "s"(ldsaddr)
               : "memory");
}
#define DSR128(dst, addr, off) asm volatile("ds_read_b128 %0, %1 offset:%2" : "=v"(dst) : "v"(addr), "i"(off))
#define DSRTR(dst, addr, off) asm volatile("ds_read_b64_tr_b16 %0, %1 offset:%2" : "=v"(dst) : "v"(addr), "i"(off))
#define PIN(x) asm volatile("" : "+v"(x))
#define GLD4(dst, voff, sbase, off) \
  asm volatile("global_load_dwordx4 %0, %1, %2 offset:%3" : "=v"(dst) : "v"(voff), "s"(sbase), "i"(off))
#define GLD2(dst, voff, sbase, off) \
  asm volatile("global_load_dwordx2 %0, %1, %2 offset:%3" : "=v"(dst) : "v"(voff), "s"(sbase), "i"(off))
#define GLD1(dst, voff, sbase, off) \
  asm volatile("global_load_dword %0, %1, %2 offset:%3" : "=v"(dst) : "v"(voff), "s"(sbase), "i"(off))
#define WT 31
template <bool W>
DEV void st16(void* p, u32x4 v) {
  if constexpr (W) asm volatile("global_store_dwordx4 %0, %1, off sc1\n\ts_nop 1" ::"v"(p), "v"(v) : "memory");
  else *(u32x4*)p = v;
}
template <bool W>
DEV void st8(void* p, u32x2 v) {
  if constexpr (W) asm volatile("global_store_dwordx2 %0, %1, off sc1\n\ts_nop 1" ::"v"(p), "v"(v) : "memory");
  else *(u32x2*)p = v;
}
template <bool W>
DEV void st4(void* p, unsigned v) {
  if constexpr (W) asm volatile("global_store_dword %0, %1, off sc1" ::"v"(p), "v"(v) : "memory");
  else *(unsigned*)p = v;
}
#define BAR() __builtin_amdgcn_s_barrier()
#define SCHED() __builtin_amdgcn_sched_barrier(0)

#define TDECL
#define TSTAMP(m, i) do { } while (0)
#define TFLUSH(m) do { } while (0)
#define TARG
#define TPASS

#define CPF 15
#define CODE_ADDR(dst, sym)                                                                                  \
  asm volatile("s_getpc_b64 s[30:31]\n\ts_add_u32 s30, s30, " #sym "@rel32@lo+4\n\ts_addc_u32 s31, s31, " #sym \
               "@rel32@hi+12\n\ts_mov_b64 %0, s[30:31]"                                                      \
               : "=s"(dst)::"s30", "s31", "scc")
#define CPW 5
#define XPF 3
constexpr int CW1 = 8, CW3 = 3, CW4 = 14;
template <bool ALL>
DEV void code_warm(int wid, int lane, unsigned ldsaddr, const char* other, unsigned ovo = 0) {
  const bool sel = wid == CPW && blockIdx.x < 64;
  if (!ALL && !sel) return;
  const char* a;
  int d;
  CODE_ADDR(a, k1_qkv);
  asm volatile("s_mov_b32 %0, k4_gu_tail-k1_qkv" : "=s"(d));
  const int last = d < 0 ? 0 : d >> 7;
  const int ln = sel ? (int)(blockIdx.x >> 3) * 64 + lane : 0;
  unsigned vo = (unsigned)((ln < last ? ln : last) * 128);
  if ((XPF & 1) && (blockIdx.x >> 3) == 6) {
    a = (const char*)((uintptr_t)__builtin_amdgcn_kernarg_segment_ptr() & ~(uintptr_t)4095);
    vo = (unsigned)((lane & 31) * 128);
  }
  if ((XPF & 2) && (blockIdx.x >> 3) == 7) {
    int d2;
    CODE_ADDR(a, k1_qkv.kd);
    asm volatile("s_mov_b32 %0, k4_gu.kd-k1_qkv.kd" : "=s"(d2));
    const int l2 = d2 < 0 ? 0 : d2 >> 6;
    vo = (unsigned)((lane < l2 ? lane : l2) * 64);
  }
  if (ALL && !sel) { a = other; vo = ovo; }
  ldsaddr = __builtin_amdgcn_readfirstlane(ldsaddr);
  asm volatile("s_mov_b32 m0, %2\n\ts_nop 0\n\tglobal_load_lds_dword %0, %1" ::"v"(vo), "s"(a), "s"(ldsaddr) : "memory");
}

template <int V> struct IC { static constexpr int value = V; };
template <int I, int N, class F>
DEV void static_for(F&& f) {
  if constexpr (I < N) {
    f(IC<I>{});
    static_for<I + 1, N>(f);
  }
}
template <int P, int KS, int NST, int NK, int PRE, int R>
struct Sched {
  static constexpr int avail(int t) {
    int n = ((t + 1) / KS + NST - 1) * P + ((t + 1) % KS + 1) * (P / KS);
    return n > NK * P ? NK * P : n;
  }
  static constexpr int cum(int t) {
    int n = PRE;
    for (int i = -1; i < t; ++i) { int a = avail(i); n = (n + R < a) ? n + R : a; }
    return n;
  }
};

DEV void swap32(unsigned& a, unsigned& b) {
  const auto r = __builtin_amdgcn_permlane32_swap(a, b, false, false);
  a = r[0]; b = r[1];
}
DEV void swap16(unsigned& a, unsigned& b) {
  const auto r = __builtin_amdgcn_permlane16_swap(a, b, false, false);
  a = r[0]; b = r[1];
}
DEV void swap16f(float& a, float& b) {
  unsigned x = __builtin_bit_cast(unsigned, a), y = __builtin_bit_cast(unsigned, b);
  swap16(x, y);
  a = __builtin_bit_cast(float, x); b = __builtin_bit_cast(float, y);
}
DEV unsigned pk2(float a, float b) { return __builtin_bit_cast(unsigned, bf16x2{(bf16)a, (bf16)b}); }
DEV bf16x8 cat8(bf16x4 a, bf16x4 b) { return __builtin_shufflevector(a, b, 0, 1, 2, 3, 4, 5, 6, 7); }
DEV f32x4 mfma16(bf16x8 a, bf16x8 b, f32x4 c) { return __builtin_amdgcn_mfma_f32_16x16x32_bf16(a, b, c, 0, 0, 0); }
DEV f32x16 mfma32(bf16x8 a, bf16x8 b, f32x16 c) { return __builtin_amdgcn_mfma_f32_32x32x16_bf16(a, b, c, 0, 0, 0); }

DEV f32x2 ln_stats(float sum, float sq) {
  float m = sum * (1.0f / D_);
  float var = fmaxf(sq * (1.0f / D_) - m * m, 0.f);
  float vb = bfr(var);
  float sd = bfr(__builtin_sqrtf(bfr(vb + 1e-5f)));
  f32x2 r;
  r[0] = bfr(m);
  r[1] = 1.0f / sd;
  return r;
}

DEV f32x2 ln_stats_hw(float sum, float sq) {
  float m = sum * (1.0f / D_);
  float var = fmaxf(sq * (1.0f / D_) - m * m, 0.f);
  float vb = bfr(var);
  float sd = bfr(__builtin_amdgcn_sqrtf(bfr(vb + 1e-5f)));
  f32x2 r;
  r[0] = bfr(m);
  r[1] = __builtin_amdgcn_rcpf(sd);
  return r;
}

extern "C" __global__ __launch_bounds__(512, 2) void k1_qkv(const bf16* __restrict__ x, const bf16* __restrict__ w,
                                                            bf16* __restrict__ QKV) {
  constexpr int NST = K1_NST, STAGE = 28672, NK = 8, NT = 16;
  extern __shared__ __attribute__((aligned(16))) char smem[];
  TDECL
  TSTAMP(0, 0);
  const int tid = threadIdx.x, lane = tid & 63;
  const int wid = __builtin_amdgcn_readfirstlane(tid >> 6);
  const int wm = wid & 3, wn = wid >> 2;
  const int l15 = lane & 15, gq = lane >> 4;
  const int xg = blockIdx.x & 7, idx = blockIdx.x >> 3;
  const int pm = (xg & 3) * 4 + (idx & 3), pn = (xg >> 2) * 8 + (idx >> 2);
  const unsigned lds0 = (unsigned)(uintptr_t)(LDSP(char))smem;
  asm volatile("" : "+s"(QKV));

  const unsigned voffA = (unsigned)((wid * 8 + (lane >> 3)) * 1024 + (((lane & 7) ^ ((wid & 1) * 4 + gq)) * 16));
  const int r12 = (lane * 43) >> 9, c12 = lane - r12 * 12;
  const bool validB = lane < 48;
  const unsigned voffB = (unsigned)((wid * 8 + r12) * 3072 + (((c12 >> 1) ^ (wid & 1)) * 16 + (c12 & 1) * 8) * 2);
  const char* baseA = (const char*)x + (size_t)pm * (128 * 1024);
  const char* baseB = (const char*)w + pn * 192;
  const int rot = ROT1 * ((idx >> 2) + 2 * (idx & 3));
  auto part = [&](int kc, int p) {
    const unsigned sb = lds0 + (kc % NST) * STAGE;
    const int kk = (kc + rot) & 7;
    if (p == 0) {
      glds(voffA, baseA + kk * 128, sb + wid * 1024);
      glds(voffA, baseA + kk * 128 + 65536, sb + 8192 + wid * 1024);
    } else if (validB) {
      glds(voffB, baseB + kk * (64 * 3072), sb + 16384 + wid * 1536);
      glds(voffB, baseB + kk * (64 * 3072) + 4 * 3072, sb + 16384 + wid * 1536 + 768);
    }
  };
  auto issue = [&](int kc) { part(kc, 0); part(kc, 1); };
  issue(0);
  BAR();
  issue(1);
  BAR();
  issue(2);
  TSTAMP(0, 1);

  unsigned a_addr[2][2], b_addr[3];
#pragma unroll
  for (int mi = 0; mi < 2; ++mi)
#pragma unroll
    for (int ks = 0; ks < 2; ++ks)
      a_addr[mi][ks] = lds0 + (wm * 32 + ((mi ^ wn) * 16) + l15) * 128 + (((4 * ks + gq) ^ (l15 >> 1)) * 16);
  {
    const int kl = gq * 8 + (l15 >> 2);
#pragma unroll
    for (int j = 0; j < 3; ++j)
      b_addr[j] = lds0 + 16384 + kl * 192 + (((3 * wn + j) ^ (gq & 1)) * 32) + (l15 & 3) * 8;
  }

  f32x4 acc[2][3], s1, s2, cs[3];
  const f32x4 z4 = {0.f, 0.f, 0.f, 0.f};
#pragma unroll
  for (int j = 0; j < 3; ++j) { acc[0][j] = z4; acc[1][j] = z4; cs[j] = z4; }
  s1 = z4; s2 = z4;
  float rs_ = 0.f, rq_ = 0.f;
  bf16x8 af[2][2];
  bf16x4 bl[2][3], bh[2][3];
  const bf16 one = (bf16)1.0f;
  const bf16x8 ones = {one, one, one, one, one, one, one, one};

  auto rd = [&](int t, int set) {
    const int kc = t >> 1, ks = t & 1, st = kc % NST;
    const unsigned hi = (unsigned)((st & ~1) * STAGE);
    const int so = (st & 1) * STAGE;
#pragma unroll
    for (int j = 0; j < 3; ++j) {
      DSRTR(bl[set][j], b_addr[j] + hi, so + ks * 6144);
      DSRTR(bh[set][j], b_addr[j] + hi, so + ks * 6144 + 768);
      if (j < 2) DSR128(af[set][j], a_addr[j][ks] + hi, so);
    }
  };
  auto rdp = [&](int t, int set, int i) {
    const int kc = t >> 1, ks = t & 1, st = kc % NST, j = i / 3;
    const unsigned hi = (unsigned)((st & ~1) * STAGE);
    const int so = (st & 1) * STAGE;
    if (i % 3 == 0) DSRTR(bl[set][j], b_addr[j] + hi, so + ks * 6144);
    else if (i % 3 == 1) DSRTR(bh[set][j], b_addr[j] + hi, so + ks * 6144 + 768);
    else DSR128(af[set][j], a_addr[j][ks] + hi, so);
  };
  auto wt = [&](int set) {
    asm volatile("s_waitcnt lgkmcnt(0)" ::: "memory");
    PIN(af[set][0]); PIN(af[set][1]);
#pragma unroll
    for (int j = 0; j < 3; ++j) { PIN(bl[set][j]); PIN(bh[set][j]); }
  };

  using SC = Sched<2, 2, NST, NK, 6, 2>;
  wait_vm<(SC::cum(-1) - 2) * 2>();
  BAR();
  TSTAMP(0, 2);
  rd(0, 0);
  static_for<SC::cum(-1), SC::cum(0)>([&](auto n) { part(n.value >> 1, n.value & 1); });
  wt(0);
  static_for<0, NT>([&](auto tc) {
    constexpr int t = tc.value, kc = t >> 1, ks = t & 1, cur = t & 1, nxt = cur ^ 1;
    constexpr bool sync = ks == 1 && kc + 1 < NK;
    const bf16x8 b0 = cat8(bl[cur][0], bh[cur][0]), b1 = cat8(bl[cur][1], bh[cur][1]), b2 = cat8(bl[cur][2], bh[cur][2]);
    auto mf = [&](int i) {
      if (i < 6) acc[i / 3][i % 3] = mfma16(i % 3 == 0 ? b0 : (i % 3 == 1 ? b1 : b2), af[cur][i / 3], acc[i / 3][i % 3]);
      else if (i == 6) s1 = mfma16(ones, af[cur][0], s1);
      else s2 = mfma16(af[cur][0], af[cur][0], s2);
      SCHED();
    };
    constexpr int PRE = (ILV1 == 2 && sync) ? 2 : 0;
    static_for<0, PRE>([&](auto i) { mf(i.value); });
    if constexpr (sync) {
      static_assert(SC::cum(t) >= (kc + 2) * 2, "schedule");
      wait_vm<(SC::cum(t) - (kc + 2) * 2) * 2 + (((CPF & 1) && t > CW1) ? 1 : 0)>();
      BAR();
      TSTAMP(0, 3 + kc);
    }
    SCHED();
    static_for<PRE, 8>([&](auto ic) {
      constexpr int i = ic.value, r = i - PRE + (PRE ? 0 : 0);
      mf(i);
      if constexpr (t + 1 < NT && r < 4) { rdp(t + 1, nxt, 2 * r); rdp(t + 1, nxt, 2 * r + 1); }
      if constexpr (r == 4) static_for<SC::cum(t), SC::cum(t + 1)>([&](auto n) { part(n.value >> 1, n.value & 1); });
      if constexpr (r == 5 && (CPF & 1) && t == CW1) {
        static_assert(SC::cum(t + 1) == NK * 2, "issued after all operand loads");
        code_warm<true>(wid, lane, lds0 + NST * STAGE + 2560 + wid * 256, baseA);
      }
      SCHED();
    });
    if ((t & 3) == wm) {
      cs[0] = mfma16(b0, ones, cs[0]);
      cs[1] = mfma16(b1, ones, cs[1]);
      cs[2] = mfma16(b2, ones, cs[2]);
    }
    SCHED();
    if constexpr (t + 1 < NT) wt(nxt);
  });
  TSTAMP(0, 20);

  float* st_ = (float*)(smem + NST * STAGE);
  float* csum = st_ + 256;
  {
    float sm, sq;
    if (K1DOT) {
      float a = rs_, b = rq_;
      swap16f(a, b);
      a += b;
      unsigned x = __builtin_bit_cast(unsigned, a), y = x;
      swap32(x, y);
      a = __builtin_bit_cast(float, x) + __builtin_bit_cast(float, y);
      b = a;
      swap16f(a, b);
      sm = a; sq = b;
    } else {
      sm = s1[0];
      sq = (l15 & 2) ? ((l15 & 1) ? s2[3] : s2[2]) : ((l15 & 1) ? s2[1] : s2[0]);
    }
    const f32x2 ms = ln_stats_hw(sm, sq);
    const bool mine = K1DOT || gq == (l15 >> 2);
    *(f32x2*)(mine ? st_ + (wm * 32 + wn * 16 + l15) * 2 : csum + 384 + lane * 2) = ms;
#pragma unroll
    for (int j = 0; j < 3; ++j) *(f32x4*)(csum + wm * 96 + (3 * wn + j) * 16 + gq * 4) = cs[j];
  }
  __syncthreads();
  TSTAMP(0, 21);
  const int tile = pm * 4 + wm;
  const float qscale = 0.25f * 1.4426950408889634f;
  f32x2 ms[2];
#pragma unroll
  for (int mi = 0; mi < 2; ++mi) ms[mi] = *(const f32x2*)(st_ + (wm * 32 + (mi ^ wn) * 16 + l15) * 2);
  f32x4 c4[3];
#pragma unroll
  for (int j = 0; j < 3; ++j) {
    f32x4 cp[4];
#pragma unroll
    for (int p = 0; p < 4; ++p) cp[p] = *(const f32x4*)(csum + p * 96 + (3 * wn + j) * 16 + gq * 4);
    c4[j] = (cp[0] + cp[1]) + (cp[2] + cp[3]);
  }
  const int msel = (gq & 1) ^ wn;
  const int loq = msel * 128 + (gq >> 1) * 256 + l15 * 8, lov = msel * 256 + l15 * 16 + (gq >> 1) * 8;
  u32x4 ov[3];
#pragma unroll
  for (int j = 0; j < 3; ++j) {
    const int blk = pn * 6 + wn * 3 + j;
    const float sc = (blk >> 5) == 0 ? qscale : 1.0f;
    unsigned o[2][2];
#pragma unroll
    for (int mi = 0; mi < 2; ++mi) {
      const float rs = ms[mi][1] * sc;
#pragma unroll
      for (int r = 0; r < 2; ++r)
        o[mi][r] = pk2((acc[mi][j][2 * r] - ms[mi][0] * c4[j][2 * r]) * rs, (acc[mi][j][2 * r + 1] - ms[mi][0] * c4[j][2 * r + 1]) * rs);
    }
    swap16(o[0][0], o[1][0]);
    swap16(o[0][1], o[1][1]);
    ov[j] = u32x4{o[0][0], o[0][1], o[1][0], o[1][1]};
  }
#pragma unroll
  for (int j = 0; j < 3; ++j) {
    const int blk = pn * 6 + wn * 3 + j;
    const int typ = blk >> 5, h = blk & 31;
    bf16* db = QKV + ((size_t)typ << 20) + (size_t)(h * 64 + tile) * 512 + (typ == 2 ? lov : loq);
    st16<(WT & 1) != 0>(db, ov[j]);
  }
  TSTAMP(0, 22);
  TFLUSH(0);
}

template <bool FULL, int NPF>
DEV void attn_body(unsigned kaddr, unsigned vaddr, bf16x8 qf, f32x16 cm0, f32x16 cm8, int wid, bf16* dst, unsigned lds0 TARG) {
  const float NEG = -1e30f;
  f32x16 z16;
#pragma unroll
  for (int r = 0; r < 16; ++r) z16[r] = 0.f;
  bf16x8 kf[9];
#pragma unroll
  for (int t = 0; t < 9; ++t)
    if (FULL || wid + t >= 8) DSR128(kf[t], kaddr, t * 1024);
  asm volatile("s_waitcnt lgkmcnt(0)" ::: "memory");
#pragma unroll
  for (int t = 0; t < 9; ++t) PIN(kf[t]);
  TSTAMP(1, 3);
  float m0 = NEG, m1 = NEG;
#pragma unroll
  for (int t = 0; t < 9; ++t)
    if (FULL || wid + t >= 8) {
      const f32x16 s = mfma32(kf[t], qf, t == 0 ? cm0 : (t == 8 ? cm8 : z16));
#pragma unroll
      for (int r = 0; r < 16; r += 4) {
        m0 = fmaxf(fmaxf(m0, s[r]), s[r + 1]);
        m1 = fmaxf(fmaxf(m1, s[r + 2]), s[r + 3]);
      }
    }
  float m = fmaxf(m0, m1);
  m = fmaxf(m, __shfl_xor(m, 32));
  f32x16 nm;
#pragma unroll
  for (int r = 0; r < 16; ++r) { nm[r] = -m; cm0[r] -= m; cm8[r] -= m; }
  TSTAMP(1, 4);
  wait_vm<NPF>();
  BAR();
  TSTAMP(1, 5);
  if (CPF & 2) code_warm<false>(wid, (int)(threadIdx.x & 63), lds0 + 59392 + wid * 256, nullptr);
  f32x16 oa = z16, ob = z16;
  f32x16 sc_, sn_;
  bf16x4 v0l[2], v0h[2], v1l[2], v1h[2];
  auto vrd = [&](int t, int b) {
    DSRTR(v0l[b], vaddr, t * 1024);
    DSRTR(v0h[b], vaddr, t * 1024 + 256);
    DSRTR(v1l[b], vaddr, t * 1024 + 512);
    DSRTR(v1h[b], vaddr, t * 1024 + 768);
  };
  if (FULL) { sc_ = mfma32(kf[0], qf, cm0); vrd(0, 0); }
  else sc_ = z16;
  sn_ = z16;
#pragma unroll
  for (int t = 0; t < 9; ++t) {
    const int b = t & 1;
    const bool cur_ok = FULL || wid + t >= 8, nxt_ok = t < 8 && (FULL || wid + t + 1 >= 8);
    if (cur_ok) {
      asm volatile("s_waitcnt lgkmcnt(0)" ::: "memory");
      PIN(v0l[b]); PIN(v0h[b]); PIN(v1l[b]); PIN(v1h[b]);
    }
    if (nxt_ok) {
      vrd(t + 1, b ^ 1);
      sn_ = mfma32(kf[t + 1], qf, t + 1 == 8 ? cm8 : nm);
    }
    SCHED();
    if (cur_ok) {
      bf16x8 p0, p1;
#pragma unroll
      for (int j = 0; j < 8; ++j) p0[j] = (bf16)__builtin_amdgcn_exp2f(sc_[j]);
      SCHED();
      oa = mfma32(cat8(v0l[b], v0h[b]), p0, oa);
      SCHED();
#pragma unroll
      for (int j = 0; j < 8; ++j) p1[j] = (bf16)__builtin_amdgcn_exp2f(sc_[8 + j]);
      SCHED();
      ob = mfma32(cat8(v1l[b], v1h[b]), p1, ob);
      SCHED();
    }
    sc_ = sn_;
  }
  TSTAMP(1, 6);
  const float inv = 1.0f / (oa[8] + ob[8]);
  unsigned a0 = pk2((oa[0] + ob[0]) * inv, (oa[1] + ob[1]) * inv), a1 = pk2((oa[2] + ob[2]) * inv, (oa[3] + ob[3]) * inv);
  unsigned b0 = pk2((oa[4] + ob[4]) * inv, (oa[5] + ob[5]) * inv), b1 = pk2((oa[6] + ob[6]) * inv, (oa[7] + ob[7]) * inv);
  swap32(a0, b0);
  swap32(a1, b1);
  st16<(WT & 2) != 0>(dst, u32x4{a0, a1, b0, b1});
}

extern "C" __global__ __launch_bounds__(512, 2) void k2_attn(const bf16* __restrict__ QKV, bf16* __restrict__ O,
                                                  const bf16* __restrict__ wo, const bf16* __restrict__ w1,
                                                  const bf16* __restrict__ w3, const bf16* __restrict__ w2,
                                                  bf16* __restrict__ Wt, float* __restrict__ CS,
                                                  bf16* __restrict__ W2t, bf16* __restrict__ Wot) {
  constexpr int LV = 16384, LONE = 32768, LSJ = 43008;
  extern __shared__ __attribute__((aligned(16))) char smem[];
  TDECL
  TSTAMP(1, 0);
  const int tid = threadIdx.x, lane = tid & 63;
  const int wid = __builtin_amdgcn_readfirstlane(tid >> 6);
  const int l15 = lane & 15, gq = lane >> 4;
  const int xg = blockIdx.x >> 5, h = (blockIdx.x & 7) * 4 + ((blockIdx.x >> 3) & 3);
  const unsigned lds0 = (unsigned)(uintptr_t)(LDSP(char))smem;
  const int t0 = xg * 8 - 8;
  const unsigned l16 = lane * 16;
  asm volatile("" : "+s"(O), "+s"(Wt), "+s"(CS), "+s"(W2t), "+s"(Wot));
  asm volatile("" : "+s"(wo), "+s"(w1), "+s"(w3), "+s"(w2));
  const bool ld = xg > 0 || wid >= 4;

  const char* kb = (const char*)QKV + (2u << 20) + (ptrdiff_t)(h * 64 + t0 + 2 * wid) * 1024;
  if (ld) {
    glds(l16, kb, lds0 + wid * 2048);
    glds(l16, kb + 1024, lds0 + wid * 2048 + 1024);
  }
  bf16x8 qf;
  GLD4(qf, l16, (const char*)QKV + (size_t)(h * 64 + xg * 8 + wid) * 1024, 0);
  if (ld) {
    glds(l16, kb + (2u << 20), lds0 + LV + wid * 2048);
    glds(l16, kb + (2u << 20) + 1024, lds0 + LV + wid * 2048 + 1024);
  }
  {
    const u32x4 o4 = {0x3f803f80u, 0x3f803f80u, 0x3f803f80u, 0x3f803f80u};
    *(u32x4*)(smem + LONE + tid * 16) = o4;
    if (tid < 128) *(u32x4*)(smem + LONE + 8192 + tid * 16) = o4;
  }
  f32x16 cm0, cm8;
  {
    const int c = lane & 31, hh = lane >> 5;
#pragma unroll
    for (int r = 0; r < 16; ++r) {
      const int kr = (r & 3) + 8 * (r >> 2) + 4 * hh;
      cm0[r] = (kr > c) ? 0.f : -1e30f;
      cm8[r] = (kr <= c) ? 0.f : -1e30f;
    }
    PIN(cm0); PIN(cm8);
  }
  TSTAMP(1, 1);
  if (ld) asm volatile("s_waitcnt vmcnt(2) lgkmcnt(0)" ::: "memory");
  else asm volatile("s_waitcnt vmcnt(0) lgkmcnt(0)" ::: "memory");
  PIN(qf);
  BAR();
  TSTAMP(1, 2);
  const int skc = blockIdx.x >> 5, smt = (blockIdx.x >> 4) & 1, spn = blockIdx.x & 15;
  u32x4 wr0, wr1;
  {
    const int wj = wid < 7 ? wid : 6;
    const char* sb = (const char*)(smt ? w3 : w1) + (size_t)(skc * 64) * (F_ * 2) + (spn * 112 + wj * 16) * 2;
    const unsigned vo = (unsigned)((lane >> 1) * (F_ * 2) + (lane & 1) * 16);
    glds(vo, sb, lds0 + LSJ + wid * 2048);
    glds(vo, sb + 32 * (F_ * 2), lds0 + LSJ + wid * 2048 + 1024);
    const char* o = (const char*)wo + (size_t)blockIdx.x * 2048;
    const char* r0 = wid < 7 ? (const char*)w2 + (size_t)blockIdx.x * 7168 + wid * 1024 : o;
    GLD4(wr0, l16, r0, 0);
    GLD4(wr1, l16, o, 1024);
  }

  const unsigned kaddr = lds0 + wid * 1024 + l16;
  const unsigned vlane = (unsigned)(((lane >> 5) * 4 + (l15 >> 2)) * 32 + (l15 & 3) * 8);
  const unsigned vaddr = (lane & 16) ? (lds0 + LONE + (vlane ^ 128u)) : (lds0 + LV + wid * 1024 + vlane);
  bf16* dst = O + (size_t)(((xg * 4 + (wid >> 1)) * H_ + h) * 64 + (wid & 1) * 32 + (lane & 31)) * 16 + (lane >> 5) * 8;
  if (xg > 0) attn_body<true, 4>(kaddr, vaddr, qf, cm0, cm8, wid, dst, lds0 TPASS);
  else attn_body<false, 4>(kaddr, vaddr, qf, cm0, cm8, wid, dst, lds0 TPASS);
  TSTAMP(1, 20);
  wait_vm<1>();
  {
    const unsigned co = (unsigned)(lane & 7) * 16;
    PIN(wr0); PIN(wr1);
    if (wid < 7) {
      st16<(WT & 2) != 0>((char*)W2t + (size_t)(lane >> 3) * (F_ * 128) + (blockIdx.x * 7 + wid) * 128 + co, wr0);
    } else {
      char* o = (char*)Wot + (size_t)(lane >> 3) * (D_ * 128) + blockIdx.x * 256 + co;
      st16<(WT & 2) != 0>(o, wr0);
      st16<(WT & 2) != 0>(o + 128, wr1);
    }
  }
  if (wid < 7) {
    const unsigned ra = lds0 + LSJ + wid * 2048 + (gq * 8 + (l15 >> 2)) * 32 + (l15 & 3) * 8;
    bf16x4 f[4];
    DSRTR(f[0], ra, 0);
    DSRTR(f[1], ra, 128);
    DSRTR(f[2], ra, 1024);
    DSRTR(f[3], ra, 1024 + 128);
    asm volatile("s_waitcnt lgkmcnt(0)" ::: "memory");
    float c = 0.f;
#pragma unroll
    for (int i = 0; i < 4; ++i) {
      PIN(f[i]);
#pragma unroll
      for (int e = 0; e < 4; ++e) c += (float)f[i][e];
    }
    char* wd = (char*)Wt + (size_t)(((spn * 8 + skc) * 4 + smt) * 7 + wid) * 1024 + l16;
    st16<(WT & 2) != 0>(wd, __builtin_bit_cast(u32x4, cat8(f[0], f[1])));
    st16<(WT & 2) != 0>(wd + 14 * 1024, __builtin_bit_cast(u32x4, cat8(f[2], f[3])));
    c += __shfl_xor(c, 16);
    c += __shfl_xor(c, 32);
    if (lane < 16) st4<(WT & 2) != 0>(CS + skc * (2 * F_) + smt * F_ + spn * 112 + wid * 16 + lane, __builtin_bit_cast(unsigned, c));
  }
  TSTAMP(1, 22);
  wait_vm<0>();
  TFLUSH(1);
}

template <int MODE, int KTOT>
DEV void gemm_body(const bf16* __restrict__ A, const bf16* __restrict__ B, const bf16* __restrict__ R,
                   bf16* __restrict__ O, float* __restrict__ RS, const bf16* __restrict__ PFW) {
  constexpr int NST = G_NST, STAGE = 32768, NK = KTOT / 128, NT = NK * 4;
  extern __shared__ __attribute__((aligned(16))) char smem[];
  TDECL
  TSTAMP(MODE == 0 ? 2 : 4, 0);
  const int tid = threadIdx.x, lane = tid & 63;
  const int wid = __builtin_amdgcn_readfirstlane(tid >> 6);
  const int wm = wid & 3, wn = wid >> 2;
  const int l15 = lane & 15, gq = lane >> 4;
  const int xg = blockIdx.x & 7, idx = blockIdx.x >> 3;
#define PNX 0
  const int pm = (MODE == 1 && MAP5) ? (xg >> 1) * 8 + (idx >> 2) : xg * 4 + (idx >> 3);
  const int pn = (MODE == 1 && MAP5) ? (xg & 1) * 4 + (idx & 3) : (idx & 7) ^ PNX;
  if (MODE == 0) asm volatile("" : "+s"(RS), "+s"(PFW));
  const unsigned lds0 = (unsigned)(uintptr_t)(LDSP(char))smem;

  u32x4 res;
  const int tok = pm * 64 + wm * 16 + l15;
  const int ncol = pn * 64 + wn * 32 + (gq & 1) * 16 + (gq >> 1) * 8;
  const unsigned hoff = (unsigned)(((((tok >> 7) * 8 + pn) * 128 + (tok & 127)) * 64 + (ncol & 63)) * 2);
  GLD4(res, MODE == 0 ? (unsigned)((tok * D_ + ncol) * 2) : hoff, R, 0);

  const unsigned voffA = (unsigned)(wid * 1024 + lane * 16);
  const int pB = ((lane & 7) >> 1) ^ (((lane >> 4) & 1) | ((wid & 1) << 1));
  const unsigned voffB = (unsigned)((wid * 8 + (lane >> 3)) * 128 + pB * 32 + (lane & 1) * 16);
  const char* baseA = (const char*)A + (size_t)pm * (KTOT * 128);
  const char* baseB = (const char*)B + pn * (KTOT * 128);
  const bool isA = wid < 4;
  const unsigned voff = isA ? (unsigned)((wid & 3) * 1024 + lane * 16)
                            : (unsigned)(((wid & 3) * 8 + (lane >> 3)) * 128 + pB * 32 + (lane & 1) * 16);
  const char* base = isA ? baseA : baseB;
  const unsigned sbw = lds0 + (isA ? 0 : 16384) + (wid & 3) * 1024;
  auto part = [&](int kc, int p) {
    glds(voff, base + (size_t)kc * 16384 + p * 4096, sbw + (kc % NST) * STAGE + p * 4096);
  };
  auto issue = [&](int kc) { part(kc, 0); part(kc, 1); part(kc, 2); part(kc, 3); };
  TSTAMP(MODE == 0 ? 2 : 4, 16);
  issue(0);
  TSTAMP(MODE == 0 ? 2 : 4, 17);
  BAR();
  issue(1);
  BAR();
  TSTAMP(MODE == 0 ? 2 : 4, 21);
  issue(2);
  if (MODE == 1 && PF5) {
    const int g = wid * 60 + (lane < 60 ? lane : 59);
    const unsigned dB = (unsigned)(baseB - baseA);
    const unsigned vo = g < 160 ? (unsigned)((512 + pn * 160 + g) * 128) : dB + (unsigned)((512 + (idx >> 3) * 320 + g - 160) * 128);
    const unsigned la = __builtin_amdgcn_readfirstlane(lds0 + NST * STAGE + wid * 256);
    asm volatile("s_mov_b32 m0, %2\n\ts_nop 0\n\tglobal_load_lds_dword %0, %1" ::"v"(vo), "s"(baseA), "s"(la) : "memory");
  }
  TSTAMP(MODE == 0 ? 2 : 4, 1);

  unsigned a_addr[4], b_addr[2];
#pragma unroll
  for (int ks = 0; ks < 4; ++ks) a_addr[ks] = lds0 + (2 * ks + (gq >> 1)) * 2048 + (wm * 16 + l15) * 32 + (gq & 1) * 16;
  {
    const int kl = gq * 8 + (l15 >> 2);
    const int fr = ((l15 >> 3) & 1) | ((gq & 1) << 1);
#pragma unroll
    for (int j = 0; j < 2; ++j) b_addr[j] = lds0 + 16384 + kl * 128 + (((2 * wn + j) ^ fr) * 32) + (l15 & 3) * 8;
  }

  f32x4 acc[2];
  const f32x4 z4 = {0.f, 0.f, 0.f, 0.f};
  acc[0] = z4; acc[1] = z4;
  constexpr int NS = LA2 ? 3 : 2;
  bf16x8 af[NS];
  bf16x4 bl[NS][2], bh[NS][2];

  auto rd = [&](int t, int set) {
    const int kc = t >> 2, ks = t & 3, st = kc % NST;
    const unsigned hi = (unsigned)((st & ~1) * STAGE);
    const int so = (st & 1) * STAGE;
    DSRTR(bl[set][0], b_addr[0] + hi, so + ks * 4096);
    DSRTR(bh[set][0], b_addr[0] + hi, so + ks * 4096 + 512);
    DSR128(af[set], a_addr[ks] + hi, so);
    DSRTR(bl[set][1], b_addr[1] + hi, so + ks * 4096);
    DSRTR(bh[set][1], b_addr[1] + hi, so + ks * 4096 + 512);
  };
  auto wt = [&](int set) {
    asm volatile("s_waitcnt lgkmcnt(0)" ::: "memory");
    PIN(af[set]);
    PIN(bl[set][0]); PIN(bh[set][0]); PIN(bl[set][1]); PIN(bh[set][1]);
  };

  using SC = Sched<4, 4, NST, NK, 12, G_R>;
  auto pin = [&](int set) {
    PIN(af[set]);
    PIN(bl[set][0]); PIN(bh[set][0]); PIN(bl[set][1]); PIN(bh[set][1]);
  };
  wait_vm<SC::cum(-1) - 4 + ((MODE == 1 && PF5) ? 1 : 0)>();
  BAR();
  TSTAMP(MODE == 0 ? 2 : 4, 2);
  rd(0, 0);
  if (LA2) rd(1, 1);
  static_for<SC::cum(-1), SC::cum(0)>([&](auto n) { part(n.value >> 2, n.value & 3); });
  if (!LA2) wt(0);
  static_for<0, NT>([&](auto tc) {
    constexpr int t = tc.value, kc = t >> 2, ks = t & 3, cur = t % NS, nxt = (t + NS - 1) % NS;
    constexpr bool sync = ks == (LA2 ? 2 : 3) && kc + 1 < NK;
    if constexpr (sync) {
      static_assert(SC::cum(t) >= (kc + 2) * 4, "schedule");
      constexpr int npf = (MODE == 0 && PF3 && t > PF3T) ? (t - PF3T > 4 ? 4 : t - PF3T) : 0;
      static_assert(npf == 0 || SC::cum(PF3T) == NK * 4, "warm-up loads come after all operand loads");
      if (LA2 && GQ) {
        asm volatile("s_waitcnt lgkmcnt(5)" ::: "memory");
        pin(cur);
      } else if (LA2) {
        asm volatile("s_waitcnt lgkmcnt(0)" ::: "memory");
        pin(cur); pin((t + 1) % NS);
      }
      wait_vm<SC::cum(t) - (kc + 2) * 4 + npf + ((MODE == 0 && (CPF & 4) && t > CW3) ? 1 : 0) + ((MODE == 1 && PF5 && kc + 1 <= 2) ? 1 : 0)>();
      BAR();
      TSTAMP(MODE == 0 ? 2 : 4, 3 + kc);
    }
    if constexpr (t + NS - 1 < NT) rd(t + NS - 1, nxt);
    static_for<SC::cum(t), SC::cum(t + 1)>([&](auto n) { part(n.value >> 2, n.value & 3); });
    if constexpr (MODE == 0 && (CPF & 4) && t == CW3) {
      static_assert(SC::cum(t + 1) == NK * 4, "issued after all operand loads");
      if constexpr (PF3W == 0) code_warm<true>(wid, lane, lds0 + NST * STAGE + wid * 1024, baseA);
      else if constexpr (PF3W == 1) {
        const int g = wid * 64 + lane;
        code_warm<true>(wid, lane, lds0 + NST * STAGE + wid * 1024, (const char*)PFW + (4u << 20) + blockIdx.x * 7168, (unsigned)((g < 112 ? g : 0) * 64));
      } else {
        const int w7 = wid > CPW ? wid - 1 : wid;
        const int g = w7 * 64 + lane;
        code_warm<true>(wid, lane, lds0 + NST * STAGE + wid * 1024, (const char*)PFW + (4u << 20) + (idx >> 2) * 229376 + (idx & 3) * 28672, (unsigned)(g * 64));
      }
    }
    if constexpr (MODE == 0 && PF3 && t >= PF3T && t < PF3T + 4) {
      glds((unsigned)(lane * 16),
           (const char*)PFW + (size_t)(xg >> 1) * (4 * 229376) + idx * 28672 + (t - PF3T) * 8192 + (t - PF3T < 3 ? wid : (wid & 3)) * 1024,
           lds0 + NST * STAGE + wid * 1024);
    }
    if constexpr (LA2 && !sync && (GQ || !(ks == 3 && kc + 1 < NK))) {
      asm volatile("s_waitcnt lgkmcnt(%0)" ::"n"(t + 2 < NT ? 10 : (t + 1 < NT ? 5 : 0)) : "memory");
      pin(cur);
    }
    SCHED();
    acc[0] = mfma16(cat8(bl[cur][0], bh[cur][0]), af[cur], acc[0]);
    acc[1] = mfma16(cat8(bl[cur][1], bh[cur][1]), af[cur], acc[1]);
    SCHED();
    if constexpr (!LA2 && t + 1 < NT) wt(nxt);
  });
  TSTAMP(MODE == 0 ? 2 : 4, 20);
  PIN(res);
  float s = 0.f, q = 0.f;
  {
#pragma unroll
    for (int r = 0; r < 4; ++r) { float a = acc[0][r], b = acc[1][r]; swap16f(a, b); acc[0][r] = a; acc[1][r] = b; }
    const bf16x8 rr = __builtin_bit_cast(bf16x8, res);
    bf16x8 o;
#pragma unroll
    for (int j = 0; j < 2; ++j)
#pragma unroll
      for (int r = 0; r < 4; ++r) {
        o[j * 4 + r] = (bf16)((float)rr[j * 4 + r] + bfr(acc[j][r]));
        const float v = (float)o[j * 4 + r];
        s += v;
        q += v * v;
      }
    st16<(WT & (MODE == 0 ? 4 : 16)) != 0>((char*)O + (MODE == 0 ? hoff : (unsigned)((tok * D_ + ncol) * 2)), __builtin_bit_cast(u32x4, o));
  }
  if (MODE == 0) {
    s += __shfl_xor(s, 16); s += __shfl_xor(s, 32);
    q += __shfl_xor(q, 16); q += __shfl_xor(q, 32);
    if (gq == 0) st8<(WT & 4) != 0>(RS + ((size_t)(pn * 2 + wn) * S_ + tok) * 2, __builtin_bit_cast(u32x2, f32x2{s, q}));
  }
  if (MODE == 0) wait_vm<0>();
  TSTAMP(MODE == 0 ? 2 : 4, 22);
  TFLUSH(MODE == 0 ? 2 : 4);
}
extern "C" __global__ __launch_bounds__(512, 2) void k3_wo(const bf16* __restrict__ A, const bf16* __restrict__ B,
                                                           const bf16* __restrict__ R, bf16* __restrict__ O,
                                                           float* __restrict__ RS, const bf16* __restrict__ PFW) {
  gemm_body<0, 512>(A, B, R, O, RS, PFW);
}
extern "C" __global__ __launch_bounds__(512, 2) void k5_down(const bf16* __restrict__ A, const bf16* __restrict__ B,
                                                             const bf16* __restrict__ R, bf16* __restrict__ O) {
  gemm_body<1, 1792>(A, B, R, O, nullptr, nullptr);
}

template <int NP, int P0>
DEV void gu_body(char* smem, unsigned lds0, int tid, int wid, int pm, int pn, const bf16* __restrict__ Hh,
                 const bf16* __restrict__ Wt, const float* __restrict__ RS, const float* __restrict__ CS,
                 bf16* __restrict__ Hm, const char* pf TARG) {
  constexpr int NST = 3, STAGE = 45056, NK = 8, LPC = 2 + NP, NT = 16;
  const int lane = tid & 63, wm = wid & 3;
  const int l15 = lane & 15, gq = lane >> 4;
  const unsigned l16 = lane * 16;
  const unsigned voffA = (unsigned)((wid * 8 + (lane >> 3)) * 128 + (((lane & 7) ^ ((wid & 1) * 4 + gq)) * 16));
  const char* baseA = (const char*)Hh + (size_t)pm * (128 * 1024);
  const int pc0 = (P0 == 0 ? 0 : 16) + wm * NP;
  const char* baseW = (const char*)Wt + (size_t)pn * (8 * 28672) + pc0 * 1024;
  const int rot = ROT4 * (pm + 2 * pn);
  auto piece = [&](int kc, int i) {
    const unsigned sb = lds0 + (kc % NST) * STAGE;
    const int kk = (kc + rot) & 7;
    if (i == 0) glds(voffA, baseA + kk * 16384, sb + wid * 1024);
    else if (i == 1) glds(voffA, baseA + kk * 16384 + 8192, sb + 8192 + wid * 1024);
    else glds(l16, baseW + kk * 28672 + (i - 2) * 1024, sb + 16384 + (pc0 + i - 2) * 1024);
  };
  auto issue = [&](int kc) {
#pragma unroll
    for (int i = 0; i < LPC; ++i) piece(kc, i);
  };
  float* st_ = (float*)(smem + NST * STAGE);
  float* csum = st_ + 256;
  if (P0 == 0) {
    if (wid < 2) {
      f32x2 sv[16];
      const char* rb = (const char*)RS + pm * 1024;
      const unsigned vo = (unsigned)(tid * 8);
#pragma unroll
      for (int i = 0; i < 16; ++i) GLD2(sv[i], vo, rb + i * (S_ * 8), 0);
      issue(0);
      BAR();
      issue(1);
      wait_vm<2 * LPC>();
#pragma unroll
      for (int i = 0; i < 16; ++i) PIN(sv[i]);
      f32x2 a = sv[0];
#pragma unroll
      for (int i = 1; i < 16; ++i) a += sv[i];
      *(f32x2*)(st_ + tid * 2) = ln_stats(a[0], a[1]);
    } else {
      issue(0);
      BAR();
      issue(1);
    }
  } else {
    float sc[8];
    int c = tid - 256;
    const bool ok = c < 224;
    if (!ok) c = 0;
    const unsigned vo = (unsigned)(((c < 112 ? 0 : F_ - 112) + pn * 112 + c) * 4);
#pragma unroll
    for (int i = 0; i < 8; ++i) GLD1(sc[i], vo, (const char*)CS + i * (2 * F_ * 4), 0);
    issue(0);
    BAR();
    issue(1);
    wait_vm<2 * LPC>();
#pragma unroll
    for (int i = 0; i < 8; ++i) PIN(sc[i]);
    float a = sc[0];
#pragma unroll
    for (int i = 1; i < 8; ++i) a += sc[i];
    if (ok) csum[c] = a;
  }
  TSTAMP(3, 1);

  unsigned a_addr[2];
#pragma unroll
  for (int ks = 0; ks < 2; ++ks) a_addr[ks] = lds0 + (wm * 32 + l15) * 128 + (((4 * ks + gq) ^ (l15 >> 1)) * 16);
  const unsigned b_addr = lds0 + 16384 + P0 * 1024 + l16;
  f32x4 ag[2][NP], au[2][NP];
  const f32x4 z4 = {0.f, 0.f, 0.f, 0.f};
#pragma unroll
  for (int j = 0; j < NP; ++j) { ag[0][j] = z4; ag[1][j] = z4; au[0][j] = z4; au[1][j] = z4; }
  bf16x8 af[2][2], bg[2][NP], bu[2][NP];
  auto rd = [&](int t, int set) {
    const int kc = t >> 1, ks = t & 1, st = kc % NST;
    const unsigned so = st * STAGE;
    DSR128(af[set][0], a_addr[ks] + so, 0);
    DSR128(af[set][1], a_addr[ks] + so, 2048);
#pragma unroll
    for (int j = 0; j < NP; ++j) {
      DSR128(bg[set][j], b_addr + so, (ks * 14 + j) * 1024);
      DSR128(bu[set][j], b_addr + so, (ks * 14 + 7 + j) * 1024);
    }
  };
  auto wt = [&](int set) {
    asm volatile("s_waitcnt lgkmcnt(0)" ::: "memory");
    PIN(af[set][0]); PIN(af[set][1]);
#pragma unroll
    for (int j = 0; j < NP; ++j) { PIN(bg[set][j]); PIN(bu[set][j]); }
  };
  asm volatile("s_waitcnt vmcnt(%0) lgkmcnt(0)" ::"n"(LPC) : "memory");
  BAR();
  TSTAMP(3, 2);
  rd(0, 0);
  piece(NST - 1, 0); piece(NST - 1, 1); piece(NST - 1, 2);
  wt(0);
  constexpr int NTM = NT - 4, NU = 2 * NP;
  auto npf = [&](int lo, int hi) {
    int n = 0;
    if (PF4 == 4)
      for (int i = lo; i <= hi; ++i) n += (i >= PFT && i < PFT + 7) ? 1 : 0;
    return n;
  };
  bf16x8 aT[4][2], bT[2][2][2];
  auto uj = [&](int u) { return u == 0 ? 0 : (u == NU - 1 ? NP - 1 : ((u & 1) ? (u + 1) / 2 : u / 2 - 1)); };
  auto uc = [&](int u) { return (u == 0 || ((u & 1) && u != NU - 1)) ? 0 : 1; };
  auto rdA = [&](int c) {
#pragma unroll
    for (int ks = 0; ks < 2; ++ks) {
      DSR128(aT[c * 2 + ks][0], a_addr[ks] + ((NK - 2 + c) % NST) * STAGE, 0);
      DSR128(aT[c * 2 + ks][1], a_addr[ks] + ((NK - 2 + c) % NST) * STAGE, 2048);
    }
  };
  auto rdB = [&](int u) {
#pragma unroll
    for (int ks = 0; ks < 2; ++ks) {
      DSR128(bT[u & 1][ks][0], b_addr + ((NK - 2 + uc(u)) % NST) * STAGE, (ks * 14 + uj(u)) * 1024);
      DSR128(bT[u & 1][ks][1], b_addr + ((NK - 2 + uc(u)) % NST) * STAGE, (ks * 14 + 7 + uj(u)) * 1024);
    }
  };
  auto rdp = [&](int t, int set, int i) {
    const int kc = t >> 1, ks = t & 1, st = kc % NST;
    const unsigned so = st * STAGE;
    if (i < 2) DSR128(af[set][i], a_addr[ks] + so, i * 2048);
    else if (i & 1) DSR128(bu[set][(i - 2) >> 1], b_addr + so, (ks * 14 + 7 + ((i - 2) >> 1)) * 1024);
    else DSR128(bg[set][(i - 2) >> 1], b_addr + so, (ks * 14 + ((i - 2) >> 1)) * 1024);
  };
  auto rdT = [&](int i) {
    if (i < 4) DSR128(aT[i >> 1][i & 1], a_addr[i >> 1] + ((NK - 2) % NST) * STAGE, (i & 1) * 2048);
    else DSR128(bT[0][(i - 4) >> 1][i & 1], b_addr + ((NK - 2 + uc(0)) % NST) * STAGE, (((i - 4) >> 1) * 14 + (i & 1) * 7 + uj(0)) * 1024);
  };
#pragma unroll
  for (int t = 0; t < NTM; ++t) {
    const int kc = t >> 1, ks = t & 1, cur = t & 1, nxt = cur ^ 1;
    if (ks == 1) {
      const int last = (kc + NST - 1 < NK - 1) ? (kc + NST - 1) : (NK - 1);
      wait_vm_n((last - (kc + 1)) * LPC + npf(2 * kc - 2, 2 * kc));
      BAR();
      TSTAMP(3, 3 + kc);
    }
    const int ic = (t + 1) / 2 + NST - 1, ip = ((t + 1) & 1) * 3;
    SCHED();
#pragma unroll
    for (int j = 0; j < NP; ++j) {
#pragma unroll
      for (int q = 0; q < 4; ++q) {
        if (q == 0) ag[0][j] = mfma16(bg[cur][j], af[cur][0], ag[0][j]);
        if (q == 1) ag[1][j] = mfma16(bg[cur][j], af[cur][1], ag[1][j]);
        if (q == 2) au[0][j] = mfma16(bu[cur][j], af[cur][0], au[0][j]);
        if (q == 3) au[1][j] = mfma16(bu[cur][j], af[cur][1], au[1][j]);
        SCHED();
#pragma unroll
        for (int r = (j * 4 + q) * ILV4; r < (j * 4 + q + 1) * ILV4; ++r) {
          if (t + 1 < NTM) { if (r < 2 + 2 * NP) rdp(t + 1, nxt, r); }
          else if (r < 8) rdT(r);
        }
        SCHED();
      }
      if (j < 3 && ic < NK && ip + j < LPC) piece(ic, ip + j);
      if (PF4 && PF4 < 4 && j < (PF4 == 1 ? 2 : 1) && t == NTM - 1 && (!(MAP5 || PFH) || PFE)) glds(l16, pf + j * ((MAP5 || PFH) ? 7168 : 8192), lds0 + NST * STAGE + 2048 + wid * 1024);
      if (j == 2 && npf(t, t)) glds(l16, pf + (t - PFT) * 8192, lds0 + NST * STAGE + 2048 + wid * 1024);
      SCHED();
    }
    if (t + 1 < NTM) wt(nxt);
  }
  auto wtT = [&](bool a0, bool a1, int u) {
    asm volatile("s_waitcnt lgkmcnt(0)" ::: "memory");
    if (a0) { PIN(aT[0][0]); PIN(aT[0][1]); PIN(aT[1][0]); PIN(aT[1][1]); }
    if (a1) { PIN(aT[2][0]); PIN(aT[2][1]); PIN(aT[3][0]); PIN(aT[3][1]); }
    PIN(bT[u & 1][0][0]); PIN(bT[u & 1][0][1]); PIN(bT[u & 1][1][0]); PIN(bT[u & 1][1][1]);
  };
  wtT(true, false, 0);
  TSTAMP(3, 20);
  bf16* dst0 = Hm + ((size_t)((pm * 2 + (wm >> 1)) * 112 + pn * 7 + P0) * 64 + (wm & 1) * 32 + l15) * 16;
  const unsigned caddr = lds0 + NST * STAGE + 1024 + (P0 * 16 + gq * 4) * 4;
  f32x2 nm[2], le2[2], iv2[2];
  f32x4 cg[2], cu[2];
  unsigned pk[NP][2][2];
  f32x2 tg, tu, te, td;
  auto epi = [&](int j, int mi, int q) {
    const int hf = q >> 2;
    if ((q & 3) == 0) {
      tg = __builtin_elementwise_fma(f32x2{cg[j & 1][2 * hf], cg[j & 1][2 * hf + 1]}, nm[mi],
                                     f32x2{ag[mi][j][2 * hf], ag[mi][j][2 * hf + 1]});
      tu = __builtin_elementwise_fma(f32x2{cu[j & 1][2 * hf], cu[j & 1][2 * hf + 1]}, nm[mi],
                                     f32x2{au[mi][j][2 * hf], au[mi][j][2 * hf + 1]});
      te = tg * le2[mi];
    } else if ((q & 3) == 1) {
      te = f32x2{__builtin_amdgcn_exp2f(te[0]), __builtin_amdgcn_exp2f(te[1])};
    } else if ((q & 3) == 2) {
      td = __builtin_elementwise_fma(te, iv2[mi], iv2[mi]);
      tu = tg * tu;
    } else {
      const f32x2 rc = {__builtin_amdgcn_rcpf(td[0]), __builtin_amdgcn_rcpf(td[1])};
      const f32x2 o = tu * rc;
      pk[j][mi][hf] = pk2(o[0], o[1]);
    }
    if (q == 7) {
      if (j & 1) {
        swap16(pk[j - 1][mi][0], pk[j][mi][0]);
        swap16(pk[j - 1][mi][1], pk[j][mi][1]);
        st16<(WT & 8) != 0>(dst0 + ((size_t)(j - 1 + (gq & 1)) * 64 + mi * 16) * 16 + (gq >> 1) * 8,
                            u32x4{pk[j - 1][mi][0], pk[j - 1][mi][1], pk[j][mi][0], pk[j][mi][1]});
      } else if (j == NP - 1) {
        st8<(WT & 8) != 0>(dst0 + ((size_t)j * 64 + mi * 16) * 16 + gq * 4, u32x2{pk[j][mi][0], pk[j][mi][1]});
      }
    }
  };
#pragma unroll
  for (int u = 0; u < NU; ++u) {
    const int j = uj(u), c = uc(u);
    if (u == 1) {
      wait_vm_n(PF4 == 4 ? npf(NTM - 2, NTM - 1) : (PF4 == 1 ? ((MAP5 || PFH) ? (PFE ? 4 : 2) : 4) : (PF4 ? 1 : 0)));
      BAR();
      TSTAMP(3, 9);
      rdA(1);
    }
    if (u + 1 < NU) rdB(u + 1);
    const int cj = u == NU - 1 ? NP - 1 : ((u >= 2 && !(u & 1)) ? (u - 2) / 2 : -1);
    if (cj >= 0) {
      DSR128(cg[cj & 1], caddr, cj * 64);
      DSR128(cu[cj & 1], caddr, cj * 64 + 448);
    }
    if (u == 1) {
#pragma unroll
      for (int mi = 0; mi < 2; ++mi) {
        const f32x2 ms = *(const f32x2*)(st_ + (wm * 32 + mi * 16 + l15) * 2);
        nm[mi] = f32x2{-ms[0], -ms[0]};
        const float le = ms[1] * -1.4426950408889634f, iv = 1.0f / (ms[1] * ms[1]);
        le2[mi] = f32x2{le, le};
        iv2[mi] = f32x2{iv, iv};
      }
    }
    SCHED();
#pragma unroll
    for (int q = 0; q < 8; ++q) {
      const int ks = q >> 2, mi = q & 1;
      if (q & 2) au[mi][j] = mfma16(bT[u & 1][ks][1], aT[c * 2 + ks][mi], au[mi][j]);
      else ag[mi][j] = mfma16(bT[u & 1][ks][0], aT[c * 2 + ks][mi], ag[mi][j]);
      SCHED();
      if (u >= 3) epi((u - 3) >> 1, (u - 3) & 1, q);
      if (PF4 == 1 && !(MAP5 || PFH) && u < 3 && (q == 1 || q == 5) && 2 + u * 2 + (q >> 2) < 7)
        glds(l16, pf + (2 + u * 2 + (q >> 2)) * 8192, lds0 + NST * STAGE + 2048 + wid * 1024);
      if (PF4 == 1 && (MAP5 || PFH) && u < 2 && (q == 1 || q == 5) && (PFE ? 2 : 0) + u * 2 + (q >> 2) < 4)
        glds(l16, pf + ((PFE ? 2 : 0) + u * 2 + (q >> 2)) * 7168, lds0 + NST * STAGE + 2048 + wid * 1024);
      if ((CPF & 8) && u == 3 && q == 3) code_warm<false>(wid, lane, lds0 + NST * STAGE + 2048 + wid * 1024, nullptr);
      SCHED();
    }
    if (u + 1 < NU) {
      wtT(false, u == 1, u + 1);
    } else {
      asm volatile("s_waitcnt lgkmcnt(0)" ::: "memory");
    }
    if (cj >= 0) { PIN(cg[cj & 1]); PIN(cu[cj & 1]); }
  }
  TSTAMP(3, 21);
#pragma unroll
  for (int e = NU - 3; e < NU; ++e)
#pragma unroll
    for (int q = 0; q < 8; ++q) epi(e >> 1, e & 1, q);
}

extern "C" __global__ __launch_bounds__(512, 2) void k4_gu(const bf16* __restrict__ Hh, const bf16* __restrict__ Wt,
                                                const float* __restrict__ RS, const float* __restrict__ CS,
                                                bf16* __restrict__ Hm, const bf16* __restrict__ W2t) {
  extern __shared__ __attribute__((aligned(16))) char smem[];
  TDECL
  TSTAMP(3, 0);
  const int tid = threadIdx.x;
  const int wid = __builtin_amdgcn_readfirstlane(tid >> 6);
  const int xg = blockIdx.x & 7, idx = blockIdx.x >> 3;
#define MAP4 0
  const int pm = MAP4 == 0 ? (xg & 1) * 8 + (idx & 7) : (MAP4 == 1 ? xg * 2 + (idx & 1) : (MAP4 == 2 ? (xg >> 1) * 4 + (idx & 3) : xg * 2 + (idx >> 4)));
  const int pn = MAP4 == 0 ? (xg >> 1) * 4 + (idx >> 3) : (MAP4 == 1 ? idx >> 1 : (MAP4 == 2 ? (xg & 1) * 8 + (idx >> 2) : (idx & 15)));
  asm volatile("" : "+s"(RS), "+s"(CS), "+s"(Hm), "+s"(W2t));
  const unsigned lds0 = (unsigned)(uintptr_t)(LDSP(char))smem;
  const char* pf = PFH ? (const char*)W2t + (idx >> 2) * 229376 + 114688 + (idx & 3) * 28672 + (wid < 7 ? wid : 6) * 1024 : MAP5 ? (const char*)W2t + (xg & 1) * 917504 + idx * 28672 + (wid < 7 ? wid : 6) * 1024 : (const char*)W2t + (PF4 == 2 ? (blockIdx.x * 7 + (wid < 7 ? wid : 6)) * 1024 : (PF4 == 3 ? idx * 57344 + wid * 7168 : idx * 57344 + wid * 1024));
  if (wid < 4) gu_body<4, 0>(smem, lds0, tid, wid, pm, pn, Hh, Wt, RS, CS, Hm, pf TPASS);
  else gu_body<3, 4>(smem, lds0, tid, wid, pm, pn, Hh, Wt, RS, CS, Hm, pf TPASS);
  wait_vm<0>();
  TSTAMP(3, 22);
  TFLUSH(3);
  asm volatile("k4_gu_tail:");
}

extern "C" void kernel_launch(void* const* d_in, const int* in_sizes, int n_in, void* d_out, int out_size, void* d_ws,
                              size_t ws_size, hipStream_t stream) {
  const bf16* x = (const bf16*)d_in[0];
  const bf16* wqkv = (const bf16*)d_in[1];
  const bf16* wo = (const bf16*)d_in[2];
  const bf16* w1 = (const bf16*)d_in[4];
  const bf16* w3 = (const bf16*)d_in[5];
  const bf16* w2 = (const bf16*)d_in[6];
  bf16* out = (bf16*)d_out;

  char* ws = (char*)d_ws;
  bf16* QKV = (bf16*)(ws);
  bf16* Oa = (bf16*)(ws + (7u << 20));
  bf16* Hh = (bf16*)(ws + (9u << 20));
#define HMO 11
  bf16* Hm = (bf16*)(ws + ((unsigned)HMO << 20));
  float* RS = (float*)(ws + (18u << 20));
  float* CS = (float*)(ws + (19u << 20));
  bf16* Wt = (bf16*)(ws + (20u << 20));
  bf16* W2t = (bf16*)(ws + (24u << 20));
  bf16* Wot = (bf16*)(ws + (26u << 20));

  {
    constexpr int lds = K1_NST * 28672 + 2560 + 2048;
    (void)hipFuncSetAttribute((const void*)k1_qkv, hipFuncAttributeMaxDynamicSharedMemorySize, lds);
    k1_qkv<<<256, 512, lds, stream>>>(x, wqkv, QKV);
  }
  {
    constexpr int lds = 59392 + 2048;
    (void)hipFuncSetAttribute((const void*)k2_attn, hipFuncAttributeMaxDynamicSharedMemorySize, lds);
    k2_attn<<<256, 512, lds, stream>>>(QKV, Oa, wo, w1, w3, w2, Wt, CS, W2t, Wot);
  }
  {
    auto k = k3_wo;
    constexpr int lds = G_NST * 32768 + 8192;
    (void)hipFuncSetAttribute((const void*)k, hipFuncAttributeMaxDynamicSharedMemorySize, lds);
    k<<<256, 512, lds, stream>>>(Oa, Wot, x, Hh, RS, Wt);
  }
  {
    constexpr int lds = 3 * 45056 + 2048 + 8192;
    (void)hipFuncSetAttribute((const void*)k4_gu, hipFuncAttributeMaxDynamicSharedMemorySize, lds);
    k4_gu<<<256, 512, lds, stream>>>(Hh, Wt, RS, CS, Hm, W2t);
  }
  {
    auto k = k5_down;
    constexpr int lds = G_NST * 32768 + 2048;
    (void)hipFuncSetAttribute((const void*)k, hipFuncAttributeMaxDynamicSharedMemorySize, lds);
    k<<<256, 512, lds, stream>>>(Hm, W2t, Hh, out);
  }
}
```

```cpp
#include <hip/hip_runtime.h>
#include <stdint.h>

typedef __attribute__((ext_vector_type(8))) __bf16 bf16x8;
typedef __attribute__((ext_vector_type(4))) __bf16 bf16x4;
typedef __attribute__((ext_vector_type(2))) __bf16 bf16x2;
typedef __attribute__((ext_vector_type(2))) float f32x2;
typedef __attribute__((ext_vector_type(4))) float f32x4;
typedef __attribute__((ext_vector_type(16))) float f32x16;
typedef __attribute__((ext_vector_type(4))) unsigned int u32x4;
typedef __attribute__((ext_vector_type(2))) unsigned int u32x2;
typedef __bf16 bf16;

#define LDSP(T) __attribute__((address_space(3))) T*
#define DEV __device__ __forceinline__

#define K1_NST 4
#define G_NST 4
#define G_R 1
#define ROT1 0
#define ROT3 0
#define ROT4 0
#define ROT5 0
#define PF4 1
#define PFT 5
#define LA2 1
#define PF3 0
#define K1DOT 0
#define K1T 1
#define ILV1 1
#define ILV4 1
#define GQ 1
#define PF5 0
#define MAP5 0
#define PFE 1
#define PF3W 2
#define PFH 1
#define PF3T 4
#define SJP 0
#define PF1 1
constexpr int S_ = 2048, D_ = 512, H_ = 32, F_ = 1792;

DEV float bfr(float x) { return (float)(bf16)x; }

template <int N>
DEV void wait_vm() {
  asm volatile("s_waitcnt vmcnt(%0)" ::"n"(N) : "memory");
}
DEV void wait_vm_n(int n) {
  switch (n) {
#define WC(X) case X: wait_vm<X>(); break;
    WC(0) WC(1) WC(2) WC(3) WC(4) WC(5) WC(6) WC(7) WC(8) WC(9) WC(10) WC(11) WC(12) WC(13) WC(14) WC(15)
    WC(16) WC(17) WC(18) WC(19) WC(20) WC(21) WC(22) WC(23) WC(24)
#undef WC
    default: wait_vm<0>(); break;
  }
}

DEV void glds(unsigned voff, const void* sbase, unsigned ldsaddr) {
  ldsaddr = __builtin_amdgcn_readfirstlane(ldsaddr);
  asm volatile("s_mov_b32 m0, %2\n\ts_nop 0\n\tglobal_load_lds_dwordx4 %0, %1" ::"v"(voff), "s"(sbase), "s"(ldsaddr)
               : "memory");
}
#define DSR128(dst, addr, off) asm volatile("ds_read_b128 %0, %1 offset:%2" : "=v"(dst) : "v"(addr), "i"(off))
#define DSRTR(dst, addr, off) asm volatile("ds_read_b64_tr_b16 %0, %1 offset:%2" : "=v"(dst) : "v"(addr), "i"(off))
#define PIN(x) asm volatile("" : "+v"(x))
#define GLD4(dst, voff, sbase, off) \
  asm volatile("global_load_dwordx4 %0, %1, %2 offset:%3" : "=v"(dst) : "v"(voff), "s"(sbase), "i"(off))
#define GLD2(dst, voff, sbase, off) \
  asm volatile("global_load_dwordx2 %0, %1, %2 offset:%3" : "=v"(dst) : "v"(voff), "s"(sbase), "i"(off))
#define GLD1(dst, voff, sbase, off) \
  asm volatile("global_load_dword %0, %1, %2 offset:%3" : "=v"(dst) : "v"(voff), "s"(sbase), "i"(off))
#define WT 31
template <bool W>
DEV void st16(void* p, u32x4 v) {
  if constexpr (W) asm volatile("global_store_dwordx4 %0, %1, off sc1\n\ts_nop 1" ::"v"(p), "v"(v) : "memory");
  else *(u32x4*)p = v;
}
template <bool W>
DEV void st8(void* p, u32x2 v) {
  if constexpr (W) asm volatile("global_store_dwordx2 %0, %1, off sc1\n\ts_nop 1" ::"v"(p), "v"(v) : "memory");
  else *(u32x2*)p = v;
}
template <bool W>
DEV void st4(void* p, unsigned v) {
  if constexpr (W) asm volatile("global_store_dword %0, %1, off sc1" ::"v"(p), "v"(v) : "memory");
  else *(unsigned*)p = v;
}
#define BAR() __builtin_amdgcn_s_barrier()
#define SCHED() __builtin_amdgcn_sched_barrier(0)

#define TDECL
#define TSTAMP(m, i) do { } while (0)
#define TFLUSH(m) do { } while (0)
#define TARG
#define TPASS

#define CPF 15
#define CODE_ADDR(dst, sym)                                                                                  \
  asm volatile("s_getpc_b64 s[30:31]\n\ts_add_u32 s30, s30, " #sym "@rel32@lo+4\n\ts_addc_u32 s31, s31, " #sym \
               "@rel32@hi+12\n\ts_mov_b64 %0, s[30:31]"                                                      \
               : "=s"(dst)::"s30", "s31", "scc")
#define CPW 5
#define XPF 3
constexpr int CW1 = 8, CW3 = 3, CW4 = 14;
template <bool ALL>
DEV void code_warm(int wid, int lane, unsigned ldsaddr, const char* other, unsigned ovo = 0) {
  const bool sel = wid == CPW && blockIdx.x < 64;
  if (!ALL && !sel) return;
  const char* a;
  int d;
  CODE_ADDR(a, k1_qkv);
  asm volatile("s_mov_b32 %0, k4_gu_tail-k1_qkv" : "=s"(d));
  const int last = d < 0 ? 0 : d >> 7;
  const int ln = sel ? (int)(blockIdx.x >> 3) * 64 + lane : 0;
  unsigned vo = (unsigned)((ln < last ? ln : last) * 128);
  if ((XPF & 1) && (blockIdx.x >> 3) == 6) {
    a = (const char*)((uintptr_t)__builtin_amdgcn_kernarg_segment_ptr() & ~(uintptr_t)4095);
    vo = (unsigned)((lane & 31) * 128);
  }
  if ((XPF & 2) && (blockIdx.x >> 3) == 7) {
    int d2;
    CODE_ADDR(a, k1_qkv.kd);
    asm volatile("s_mov_b32 %0, k4_gu.kd-k1_qkv.kd" : "=s"(d2));
    const int l2 = d2 < 0 ? 0 : d2 >> 6;
    vo = (unsigned)((lane < l2 ? lane : l2) * 64);
  }
  if (ALL && !sel) { a = other; vo = ovo; }
  ldsaddr = __builtin_amdgcn_readfirstlane(ldsaddr);
  asm volatile("s_mov_b32 m0, %2\n\ts_nop 0\n\tglobal_load_lds_dword %0, %1" ::"v"(vo), "s"(a), "s"(ldsaddr) : "memory");
}

template <int V> struct IC { static constexpr int value = V; };
template <int I, int N, class F>
DEV void static_for(F&& f) {
  if constexpr (I < N) {
    f(IC<I>{});
    static_for<I + 1, N>(f);
  }
}
template <int P, int KS, int NST, int NK, int PRE, int R>
struct Sched {
  static constexpr int avail(int t) {
    int n = ((t + 1) / KS + NST - 1) * P + ((t + 1) % KS + 1) * (P / KS);
    return n > NK * P ? NK * P : n;
  }
  static constexpr int cum(int t) {
    int n = PRE;
    for (int i = -1; i < t; ++i) { int a = avail(i); n = (n + R < a) ? n + R : a; }
    return n;
  }
};

DEV void swap32(unsigned& a, unsigned& b) {
  const auto r = __builtin_amdgcn_permlane32_swap(a, b, false, false);
  a = r[0]; b = r[1];
}
DEV void swap16(unsigned& a, unsigned& b) {
  const auto r = __builtin_amdgcn_permlane16_swap(a, b, false, false);
  a = r[0]; b = r[1];
}
DEV void swap16f(float& a, float& b) {
  unsigned x = __builtin_bit_cast(unsigned, a), y = __builtin_bit_cast(unsigned, b);
  swap16(x, y);
  a = __builtin_bit_cast(float, x); b = __builtin_bit_cast(float, y);
}
DEV unsigned pk2(float a, float b) { return __builtin_bit_cast(unsigned, bf16x2{(bf16)a, (bf16)b}); }
DEV bf16x8 cat8(bf16x4 a, bf16x4 b) { return __builtin_shufflevector(a, b, 0, 1, 2, 3, 4, 5, 6, 7); }
DEV f32x4 mfma16(bf16x8 a, bf16x8 b, f32x4 c) { return __builtin_amdgcn_mfma_f32_16x16x32_bf16(a, b, c, 0, 0, 0); }
DEV f32x16 mfma32(bf16x8 a, bf16x8 b, f32x16 c) { return __builtin_amdgcn_mfma_f32_32x32x16_bf16(a, b, c, 0, 0, 0); }

DEV f32x2 ln_stats(float sum, float sq) {
  float m = sum * (1.0f / D_);
  float var = fmaxf(sq * (1.0f / D_) - m * m, 0.f);
  float vb = bfr(var);
  float sd = bfr(__builtin_sqrtf(bfr(vb + 1e-5f)));
  f32x2 r;
  r[0] = bfr(m);
  r[1] = 1.0f / sd;
  return r;
}

DEV f32x2 ln_stats_hw(float sum, float sq) {
  float m = sum * (1.0f / D_);
  float var = fmaxf(sq * (1.0f / D_) - m * m, 0.f);
  float vb = bfr(var);
  float sd = bfr(__builtin_amdgcn_sqrtf(bfr(vb + 1e-5f)));
  f32x2 r;
  r[0] = bfr(m);
  r[1] = __builtin_amdgcn_rcpf(sd);
  return r;
}

extern "C" __global__ __launch_bounds__(512, 2) void k1_qkv(const bf16* __restrict__ x, const bf16* __restrict__ w,
                                                            bf16* __restrict__ QKV, const bf16* __restrict__ wo,
                                                            const bf16* __restrict__ w1, const bf16* __restrict__ w3,
                                                            const bf16* __restrict__ w2) {
  constexpr int NST = K1_NST, STAGE = 28672, NK = 8, NT = 16;
  extern __shared__ __attribute__((aligned(16))) char smem[];
  TDECL
  TSTAMP(0, 0);
  const int tid = threadIdx.x, lane = tid & 63;
  const int wid = __builtin_amdgcn_readfirstlane(tid >> 6);
  const int wm = wid & 3, wn = wid >> 2;
  const int l15 = lane & 15, gq = lane >> 4;
  const int xg = blockIdx.x & 7, idx = blockIdx.x >> 3;
  const int pm = (xg & 3) * 4 + (idx & 3), pn = (xg >> 2) * 8 + (idx >> 2);
  const unsigned lds0 = (unsigned)(uintptr_t)(LDSP(char))smem;
  asm volatile("" : "+s"(QKV));
  const char* pfb;
  unsigned pfo;
  {
    const int skc = blockIdx.x >> 5, smt = (blockIdx.x >> 4) & 1, spn = blockIdx.x & 15;
    const char* sl = (const char*)(smt ? w3 : w1) + (size_t)(skc * 64) * (F_ * 2) + spn * 224;
    pfb = wid < 3 ? sl : (wid == 3 ? (const char*)w2 + (size_t)blockIdx.x * 7168 : (wid == 4 ? (const char*)wo + (size_t)blockIdx.x * 2048 : (const char*)x));
    pfo = wid < 3 ? (unsigned)(lane * (F_ * 2) + (wid == 2 ? 220 : wid * 128)) : (wid == 3 ? (unsigned)((lane < 56 ? lane : 55) * 128) : (wid == 4 ? (unsigned)((lane & 15) * 128) : 0u));
    if (!PF1) { pfb = (const char*)x; pfo = 0; }
    asm volatile("" : "+s"(pfb));
  }

  const unsigned voffA = (unsigned)((wid * 8 + (lane >> 3)) * 1024 + (((lane & 7) ^ ((wid & 1) * 4 + gq)) * 16));
  const int r12 = (lane * 43) >> 9, c12 = lane - r12 * 12;
  const bool validB = lane < 48;
  const unsigned voffB = (unsigned)((wid * 8 + r12) * 3072 + (((c12 >> 1) ^ (wid & 1)) * 16 + (c12 & 1) * 8) * 2);
  const char* baseA = (const char*)x + (size_t)pm * (128 * 1024);
  const char* baseB = (const char*)w + pn * 192;
  const int rot = ROT1 * ((idx >> 2) + 2 * (idx & 3));
  auto part = [&](int kc, int p) {
    const unsigned sb = lds0 + (kc % NST) * STAGE;
    const int kk = (kc + rot) & 7;
    if (p == 0) {
      glds(voffA, baseA + kk * 128, sb + wid * 1024);
      glds(voffA, baseA + kk * 128 + 65536, sb + 8192 + wid * 1024);
    } else if (validB) {
      glds(voffB, baseB + kk * (64 * 3072), sb + 16384 + wid * 1536);
      glds(voffB, baseB + kk * (64 * 3072) + 4 * 3072, sb + 16384 + wid * 1536 + 768);
    }
  };
  auto issue = [&](int kc) { part(kc, 0); part(kc, 1); };
  issue(0);
  BAR();
  issue(1);
  BAR();
  issue(2);
  TSTAMP(0, 1);

  unsigned a_addr[2][2], b_addr[3];
#pragma unroll
  for (int mi = 0; mi < 2; ++mi)
#pragma unroll
    for (int ks = 0; ks < 2; ++ks)
      a_addr[mi][ks] = lds0 + (wm * 32 + ((mi ^ wn) * 16) + l15) * 128 + (((4 * ks + gq) ^ (l15 >> 1)) * 16);
  {
    const int kl = gq * 8 + (l15 >> 2);
#pragma unroll
    for (int j = 0; j < 3; ++j)
      b_addr[j] = lds0 + 16384 + kl * 192 + (((3 * wn + j) ^ (gq & 1)) * 32) + (l15 & 3) * 8;
  }

  f32x4 acc[2][3], s1, s2, cs[3];
  const f32x4 z4 = {0.f, 0.f, 0.f, 0.f};
#pragma unroll
  for (int j = 0; j < 3; ++j) { acc[0][j] = z4; acc[1][j] = z4; cs[j] = z4; }
  s1 = z4; s2 = z4;
  float rs_ = 0.f, rq_ = 0.f;
  bf16x8 af[2][2];
  bf16x4 bl[2][3], bh[2][3];
  const bf16 one = (bf16)1.0f;
  const bf16x8 ones = {one, one, one, one, one, one, one, one};

  auto rd = [&](int t, int set) {
    const int kc = t >> 1, ks = t & 1, st = kc % NST;
    const unsigned hi = (unsigned)((st & ~1) * STAGE);
    const int so = (st & 1) * STAGE;
#pragma unroll
    for (int j = 0; j < 3; ++j) {
      DSRTR(bl[set][j], b_addr[j] + hi, so + ks * 6144);
      DSRTR(bh[set][j], b_addr[j] + hi, so + ks * 6144 + 768);
      if (j < 2) DSR128(af[set][j], a_addr[j][ks] + hi, so);
    }
  };
  auto rdp = [&](int t, int set, int i) {
    const int kc = t >> 1, ks = t & 1, st = kc % NST, j = i / 3;
    const unsigned hi = (unsigned)((st & ~1) * STAGE);
    const int so = (st & 1) * STAGE;
    if (i % 3 == 0) DSRTR(bl[set][j], b_addr[j] + hi, so + ks * 6144);
    else if (i % 3 == 1) DSRTR(bh[set][j], b_addr[j] + hi, so + ks * 6144 + 768);
    else DSR128(af[set][j], a_addr[j][ks] + hi, so);
  };
  auto wt = [&](int set) {
    asm volatile("s_waitcnt lgkmcnt(0)" ::: "memory");
    PIN(af[set][0]); PIN(af[set][1]);
#pragma unroll
    for (int j = 0; j < 3; ++j) { PIN(bl[set][j]); PIN(bh[set][j]); }
  };

  using SC = Sched<2, 2, NST, NK, 6, 2>;
  wait_vm<(SC::cum(-1) - 2) * 2>();
  BAR();
  TSTAMP(0, 2);
  rd(0, 0);
  static_for<SC::cum(-1), SC::cum(0)>([&](auto n) { part(n.value >> 1, n.value & 1); });
  wt(0);
  static_for<0, NT>([&](auto tc) {
    constexpr int t = tc.value, kc = t >> 1, ks = t & 1, cur = t & 1, nxt = cur ^ 1;
    constexpr bool sync = ks == 1 && kc + 1 < NK;
    const bf16x8 b0 = cat8(bl[cur][0], bh[cur][0]), b1 = cat8(bl[cur][1], bh[cur][1]), b2 = cat8(bl[cur][2], bh[cur][2]);
    auto mf = [&](int i) {
      if (i < 6) acc[i / 3][i % 3] = mfma16(i % 3 == 0 ? b0 : (i % 3 == 1 ? b1 : b2), af[cur][i / 3], acc[i / 3][i % 3]);
      else if (i == 6) s1 = mfma16(ones, af[cur][0], s1);
      else s2 = mfma16(af[cur][0], af[cur][0], s2);
      SCHED();
    };
    constexpr int PRE = (ILV1 == 2 && sync) ? 2 : 0;
    static_for<0, PRE>([&](auto i) { mf(i.value); });
    if constexpr (sync) {
      static_assert(SC::cum(t) >= (kc + 2) * 2, "schedule");
      wait_vm<(SC::cum(t) - (kc + 2) * 2) * 2 + (((CPF & 1) && t > CW1) ? 1 : 0)>();
      BAR();
      TSTAMP(0, 3 + kc);
    }
    SCHED();
    static_for<PRE, 8>([&](auto ic) {
      constexpr int i = ic.value, r = i - PRE + (PRE ? 0 : 0);
      mf(i);
      if constexpr (t + 1 < NT && r < 4) { rdp(t + 1, nxt, 2 * r); rdp(t + 1, nxt, 2 * r + 1); }
      if constexpr (r == 4) static_for<SC::cum(t), SC::cum(t + 1)>([&](auto n) { part(n.value >> 1, n.value & 1); });
      if constexpr (r == 5 && (CPF & 1) && t == CW1) {
        static_assert(SC::cum(t + 1) == NK * 2, "issued after all operand loads");
        code_warm<true>(wid, lane, lds0 + NST * STAGE + 2560 + wid * 256, pfb, pfo);
      }
      SCHED();
    });
    if ((t & 3) == wm) {
      cs[0] = mfma16(b0, ones, cs[0]);
      cs[1] = mfma16(b1, ones, cs[1]);
      cs[2] = mfma16(b2, ones, cs[2]);
    }
    SCHED();
    if constexpr (t + 1 < NT) wt(nxt);
  });
  TSTAMP(0, 20);

  float* st_ = (float*)(smem + NST * STAGE);
  float* csum = st_ + 256;
  {
    float sm, sq;
    if (K1DOT) {
      float a = rs_, b = rq_;
      swap16f(a, b);
      a += b;
      unsigned x = __builtin_bit_cast(unsigned, a), y = x;
      swap32(x, y);
      a = __builtin_bit_cast(float, x) + __builtin_bit_cast(float, y);
      b = a;
      swap16f(a, b);
      sm = a; sq = b;
    } else {
      sm = s1[0];
      sq = (l15 & 2) ? ((l15 & 1) ? s2[3] : s2[2]) : ((l15 & 1) ? s2[1] : s2[0]);
    }
    const f32x2 ms = ln_stats_hw(sm, sq);
    const bool mine = K1DOT || gq == (l15 >> 2);
    *(f32x2*)(mine ? st_ + (wm * 32 + wn * 16 + l15) * 2 : csum + 384 + lane * 2) = ms;
#pragma unroll
    for (int j = 0; j < 3; ++j) *(f32x4*)(csum + wm * 96 + (3 * wn + j) * 16 + gq * 4) = cs[j];
  }
  __syncthreads();
  TSTAMP(0, 21);
  const int tile = pm * 4 + wm;
  const float qscale = 0.25f * 1.4426950408889634f;
  f32x2 ms[2];
#pragma unroll
  for (int mi = 0; mi < 2; ++mi) ms[mi] = *(const f32x2*)(st_ + (wm * 32 + (mi ^ wn) * 16 + l15) * 2);
  f32x4 c4[3];
#pragma unroll
  for (int j = 0; j < 3; ++j) {
    f32x4 cp[4];
#pragma unroll
    for (int p = 0; p < 4; ++p) cp[p] = *(const f32x4*)(csum + p * 96 + (3 * wn + j) * 16 + gq * 4);
    c4[j] = (cp[0] + cp[1]) + (cp[2] + cp[3]);
  }
  const int msel = (gq & 1) ^ wn;
  const int loq = msel * 128 + (gq >> 1) * 256 + l15 * 8, lov = msel * 256 + l15 * 16 + (gq >> 1) * 8;
  u32x4 ov[3];
#pragma unroll
  for (int j = 0; j < 3; ++j) {
    const int blk = pn * 6 + wn * 3 + j;
    const float sc = (blk >> 5) == 0 ? qscale : 1.0f;
    unsigned o[2][2];
#pragma unroll
    for (int mi = 0; mi < 2; ++mi) {
      const float rs = ms[mi][1] * sc;
#pragma unroll
      for (int r = 0; r < 2; ++r)
        o[mi][r] = pk2((acc[mi][j][2 * r] - ms[mi][0] * c4[j][2 * r]) * rs, (acc[mi][j][2 * r + 1] - ms[mi][0] * c4[j][2 * r + 1]) * rs);
    }
    swap16(o[0][0], o[1][0]);
    swap16(o[0][1], o[1][1]);
    ov[j] = u32x4{o[0][0], o[0][1], o[1][0], o[1][1]};
  }
#pragma unroll
  for (int j = 0; j < 3; ++j) {
    const int blk = pn * 6 + wn * 3 + j;
    const int typ = blk >> 5, h = blk & 31;
    bf16* db = QKV + ((size_t)typ << 20) + (size_t)(h * 64 + tile) * 512 + (typ == 2 ? lov : loq);
    st16<(WT & 1) != 0>(db, ov[j]);
  }
  TSTAMP(0, 22);
  TFLUSH(0);
}

template <bool FULL, int NPF, class F>
DEV void attn_body(unsigned kaddr, unsigned vaddr, bf16x8 qf, f32x16 cm0, f32x16 cm8, int wid, bf16* dst, unsigned lds0, F&& side TARG) {
  const float NEG = -1e30f;
  f32x16 z16;
#pragma unroll
  for (int r = 0; r < 16; ++r) z16[r] = 0.f;
  bf16x8 kf[9];
#pragma unroll
  for (int t = 0; t < 9; ++t)
    if (FULL || wid + t >= 8) DSR128(kf[t], kaddr, t * 1024);
  asm volatile("s_waitcnt lgkmcnt(0)" ::: "memory");
#pragma unroll
  for (int t = 0; t < 9; ++t) PIN(kf[t]);
  TSTAMP(1, 3);
  float m0 = NEG, m1 = NEG;
#pragma unroll
  for (int t = 0; t < 9; ++t)
    if (FULL || wid + t >= 8) {
      const f32x16 s = mfma32(kf[t], qf, t == 0 ? cm0 : (t == 8 ? cm8 : z16));
#pragma unroll
      for (int r = 0; r < 16; r += 4) {
        m0 = fmaxf(fmaxf(m0, s[r]), s[r + 1]);
        m1 = fmaxf(fmaxf(m1, s[r + 2]), s[r + 3]);
      }
    }
  float m = fmaxf(m0, m1);
  m = fmaxf(m, __shfl_xor(m, 32));
  f32x16 nm;
#pragma unroll
  for (int r = 0; r < 16; ++r) { nm[r] = -m; cm0[r] -= m; cm8[r] -= m; }
  TSTAMP(1, 4);
  if (NPF > 0 && wid < 4) wait_vm<NPF>();
  else wait_vm<0>();
  BAR();
  TSTAMP(1, 5);
  if (CPF & 2) code_warm<false>(wid, (int)(threadIdx.x & 63), lds0 + 59392 + wid * 256, nullptr);
  if (SJP == 1) side();
  f32x16 oa = z16, ob = z16;
  f32x16 sc_, sn_;
  bf16x4 v0l[2], v0h[2], v1l[2], v1h[2];
  auto vrd = [&](int t, int b) {
    DSRTR(v0l[b], vaddr, t * 1024);
    DSRTR(v0h[b], vaddr, t * 1024 + 256);
    DSRTR(v1l[b], vaddr, t * 1024 + 512);
    DSRTR(v1h[b], vaddr, t * 1024 + 768);
  };
  if (FULL) { sc_ = mfma32(kf[0], qf, cm0); vrd(0, 0); }
  else sc_ = z16;
  sn_ = z16;
#pragma unroll
  for (int t = 0; t < 9; ++t) {
    const int b = t & 1;
    const bool cur_ok = FULL || wid + t >= 8, nxt_ok = t < 8 && (FULL || wid + t + 1 >= 8);
    if (cur_ok) {
      asm volatile("s_waitcnt lgkmcnt(0)" ::: "memory");
      PIN(v0l[b]); PIN(v0h[b]); PIN(v1l[b]); PIN(v1h[b]);
    }
    if (nxt_ok) {
      vrd(t + 1, b ^ 1);
      sn_ = mfma32(kf[t + 1], qf, t + 1 == 8 ? cm8 : nm);
    }
    if (SJP >= 2 && t == SJP - 1) side();
    SCHED();
    if (cur_ok) {
      bf16x8 p0, p1;
#pragma unroll
      for (int j = 0; j < 8; ++j) p0[j] = (bf16)__builtin_amdgcn_exp2f(sc_[j]);
      SCHED();
      oa = mfma32(cat8(v0l[b], v0h[b]), p0, oa);
      SCHED();
#pragma unroll
      for (int j = 0; j < 8; ++j) p1[j] = (bf16)__builtin_amdgcn_exp2f(sc_[8 + j]);
      SCHED();
      ob = mfma32(cat8(v1l[b], v1h[b]), p1, ob);
      SCHED();
    }
    sc_ = sn_;
  }
  TSTAMP(1, 6);
  const float inv = 1.0f / (oa[8] + ob[8]);
  unsigned a0 = pk2((oa[0] + ob[0]) * inv, (oa[1] + ob[1]) * inv), a1 = pk2((oa[2] + ob[2]) * inv, (oa[3] + ob[3]) * inv);
  unsigned b0 = pk2((oa[4] + ob[4]) * inv, (oa[5] + ob[5]) * inv), b1 = pk2((oa[6] + ob[6]) * inv, (oa[7] + ob[7]) * inv);
  swap32(a0, b0);
  swap32(a1, b1);
  st16<(WT & 2) != 0>(dst, u32x4{a0, a1, b0, b1});
}

extern "C" __global__ __launch_bounds__(512, 2) void k2_attn(const bf16* __restrict__ QKV, bf16* __restrict__ O,
                                                  const bf16* __restrict__ wo, const bf16* __restrict__ w1,
                                                  const bf16* __restrict__ w3, const bf16* __restrict__ w2,
                                                  bf16* __restrict__ Wt, float* __restrict__ CS,
                                                  bf16* __restrict__ W2t, bf16* __restrict__ Wot) {
  constexpr int LV = 16384, LONE = 32768, LSJ = 43008;
  extern __shared__ __attribute__((aligned(16))) char smem[];
  TDECL
  TSTAMP(1, 0);
  const int tid = threadIdx.x, lane = tid & 63;
  const int wid = __builtin_amdgcn_readfirstlane(tid >> 6);
  const int l15 = lane & 15, gq = lane >> 4;
  const int xg = blockIdx.x >> 5, h = (blockIdx.x & 7) * 4 + ((blockIdx.x >> 3) & 3);
  const unsigned lds0 = (unsigned)(uintptr_t)(LDSP(char))smem;
  const int t0 = xg * 8 - 8;
  const unsigned l16 = lane * 16;
  asm volatile("" : "+s"(O), "+s"(Wt), "+s"(CS), "+s"(W2t), "+s"(Wot));
  asm volatile("" : "+s"(wo), "+s"(w1), "+s"(w3), "+s"(w2));
  const bool ld = xg > 0 || wid >= 4;

  const char* kb = (const char*)QKV + (2u << 20) + (ptrdiff_t)(h * 64 + t0 + 2 * wid) * 1024;
  if (ld) {
    glds(l16, kb, lds0 + wid * 2048);
    glds(l16, kb + 1024, lds0 + wid * 2048 + 1024);
  }
  bf16x8 qf;
  GLD4(qf, l16, (const char*)QKV + (size_t)(h * 64 + xg * 8 + wid) * 1024, 0);
  if (ld) {
    glds(l16, kb + (2u << 20), lds0 + LV + wid * 2048);
    glds(l16, kb + (2u << 20) + 1024, lds0 + LV + wid * 2048 + 1024);
  }
  const int skc = blockIdx.x >> 5, smt = (blockIdx.x >> 4) & 1, spn = blockIdx.x & 15;
  u32x4 wr0, wr1, wr2;
  auto side = [&]() {
    if (wid < 4) {
      const char* sb = (const char*)(smt ? w3 : w1) + (size_t)(skc * 64) * (F_ * 2) + (spn * 112 + wid * 16) * 2;
      const unsigned vo = (unsigned)((lane >> 1) * (F_ * 2) + (lane & 1) * 16);
      const char* sb2 = sb + (wid < 3 ? 128 : 96);
      glds(vo, sb, lds0 + LSJ + wid * 2048);
      glds(vo, sb + 32 * (F_ * 2), lds0 + LSJ + wid * 2048 + 1024);
      glds(vo, sb2, lds0 + LSJ + 8192 + wid * 2048);
      glds(vo, sb2 + 32 * (F_ * 2), lds0 + LSJ + 8192 + wid * 2048 + 1024);
      const char* o = (const char*)wo + (size_t)blockIdx.x * 2048;
      const char* r0 = (const char*)w2 + (size_t)blockIdx.x * 7168 + wid * 1024;
      const char* r1 = wid < 3 ? r0 + 4096 : o;
      GLD4(wr0, l16, r0, 0);
      GLD4(wr1, l16, r1, 0);
      GLD4(wr2, l16, o, 1024);
    }
  };
  if (SJP < 0) side();
  {
    const u32x4 o4 = {0x3f803f80u, 0x3f803f80u, 0x3f803f80u, 0x3f803f80u};
    *(u32x4*)(smem + LONE + tid * 16) = o4;
    if (tid < 128) *(u32x4*)(smem + LONE + 8192 + tid * 16) = o4;
  }
  f32x16 cm0, cm8;
  {
    const int c = lane & 31, hh = lane >> 5;
#pragma unroll
    for (int r = 0; r < 16; ++r) {
      const int kr = (r & 3) + 8 * (r >> 2) + 4 * hh;
      cm0[r] = (kr > c) ? 0.f : -1e30f;
      cm8[r] = (kr <= c) ? 0.f : -1e30f;
    }
    PIN(cm0); PIN(cm8);
  }
  TSTAMP(1, 1);
  if (SJP < 0 && wid < 4) {
    if (ld) asm volatile("s_waitcnt vmcnt(9) lgkmcnt(0)" ::: "memory");
    else asm volatile("s_waitcnt vmcnt(7) lgkmcnt(0)" ::: "memory");
  } else {
    if (ld) asm volatile("s_waitcnt vmcnt(2) lgkmcnt(0)" ::: "memory");
    else asm volatile("s_waitcnt vmcnt(0) lgkmcnt(0)" ::: "memory");
  }
  PIN(qf);
  BAR();
  TSTAMP(1, 2);
  if (SJP == 0) side();

  const unsigned kaddr = lds0 + wid * 1024 + l16;
  const unsigned vlane = (unsigned)(((lane >> 5) * 4 + (l15 >> 2)) * 32 + (l15 & 3) * 8);
  const unsigned vaddr = (lane & 16) ? (lds0 + LONE + (vlane ^ 128u)) : (lds0 + LV + wid * 1024 + vlane);
  bf16* dst = O + (size_t)(((xg * 4 + (wid >> 1)) * H_ + h) * 64 + (wid & 1) * 32 + (lane & 31)) * 16 + (lane >> 5) * 8;
  if (xg > 0) attn_body<true, SJP <= 0 ? 7 : 0>(kaddr, vaddr, qf, cm0, cm8, wid, dst, lds0, side TPASS);
  else attn_body<false, SJP <= 0 ? 7 : 0>(kaddr, vaddr, qf, cm0, cm8, wid, dst, lds0, side TPASS);
  TSTAMP(1, 20);
  if (wid < 4) {
    wait_vm<1>();
    const unsigned co = (unsigned)(lane & 7) * 16;
    PIN(wr0); PIN(wr1); PIN(wr2);
    char* w2d = (char*)W2t + (size_t)(lane >> 3) * (F_ * 128) + (blockIdx.x * 7 + wid) * 128 + co;
    st16<(WT & 2) != 0>(w2d, wr0);
    if (wid < 3) {
      st16<(WT & 2) != 0>(w2d + 512, wr1);
    } else {
      char* o = (char*)Wot + (size_t)(lane >> 3) * (D_ * 128) + blockIdx.x * 256 + co;
      st16<(WT & 2) != 0>(o, wr1);
      st16<(WT & 2) != 0>(o + 128, wr2);
    }
    const unsigned ra = lds0 + LSJ + wid * 2048 + (gq * 8 + (l15 >> 2)) * 32 + (l15 & 3) * 8;
#pragma unroll
    for (int hf = 0; hf < 2; ++hf) {
      if (hf == 1 && wid == 3) break;
      const int wj = wid + 4 * hf;
      bf16x4 f[4];
      DSRTR(f[0], ra, hf * 8192);
      DSRTR(f[1], ra, hf * 8192 + 128);
      DSRTR(f[2], ra, hf * 8192 + 1024);
      DSRTR(f[3], ra, hf * 8192 + 1024 + 128);
      asm volatile("s_waitcnt lgkmcnt(0)" ::: "memory");
      float c = 0.f;
#pragma unroll
      for (int i = 0; i < 4; ++i) {
        PIN(f[i]);
#pragma unroll
        for (int e = 0; e < 4; ++e) c += (float)f[i][e];
      }
      char* wd = (char*)Wt + (size_t)(((spn * 8 + skc) * 4 + smt) * 7 + wj) * 1024 + l16;
      st16<(WT & 2) != 0>(wd, __builtin_bit_cast(u32x4, cat8(f[0], f[1])));
      st16<(WT & 2) != 0>(wd + 14 * 1024, __builtin_bit_cast(u32x4, cat8(f[2], f[3])));
      c += __shfl_xor(c, 16);
      c += __shfl_xor(c, 32);
      if (lane < 16) st4<(WT & 2) != 0>(CS + skc * (2 * F_) + smt * F_ + spn * 112 + wj * 16 + lane, __builtin_bit_cast(unsigned, c));
    }
  }
  TSTAMP(1, 22);
  wait_vm<0>();
  TFLUSH(1);
}

template <int MODE, int KTOT>
DEV void gemm_body(const bf16* __restrict__ A, const bf16* __restrict__ B, const bf16* __restrict__ R,
                   bf16* __restrict__ O, float* __restrict__ RS, const bf16* __restrict__ PFW) {
  constexpr int NST = G_NST, STAGE = 32768, NK = KTOT / 128, NT = NK * 4;
  extern __shared__ __attribute__((aligned(16))) char smem[];
  TDECL
  TSTAMP(MODE == 0 ? 2 : 4, 0);
  const int tid = threadIdx.x, lane = tid & 63;
  const int wid = __builtin_amdgcn_readfirstlane(tid >> 6);
  const int wm = wid & 3, wn = wid >> 2;
  const int l15 = lane & 15, gq = lane >> 4;
  const int xg = blockIdx.x & 7, idx = blockIdx.x >> 3;
#define PNX 0
  const int pm = (MODE == 1 && MAP5) ? (xg >> 1) * 8 + (idx >> 2) : xg * 4 + (idx >> 3);
  const int pn = (MODE == 1 && MAP5) ? (xg & 1) * 4 + (idx & 3) : (idx & 7) ^ PNX;
  if (MODE == 0) asm volatile("" : "+s"(RS), "+s"(PFW));
  const unsigned lds0 = (unsigned)(uintptr_t)(LDSP(char))smem;

  u32x4 res;
  const int tok = pm * 64 + wm * 16 + l15;
  const int ncol = pn * 64 + wn * 32 + (gq & 1) * 16 + (gq >> 1) * 8;
  const unsigned hoff = (unsigned)(((((tok >> 7) * 8 + pn) * 128 + (tok & 127)) * 64 + (ncol & 63)) * 2);
  GLD4(res, MODE == 0 ? (unsigned)((tok * D_ + ncol) * 2) : hoff, R, 0);

  const unsigned voffA = (unsigned)(wid * 1024 + lane * 16);
  const int pB = ((lane & 7) >> 1) ^ (((lane >> 4) & 1) | ((wid & 1) << 1));
  const unsigned voffB = (unsigned)((wid * 8 + (lane >> 3)) * 128 + pB * 32 + (lane & 1) * 16);
  const char* baseA = (const char*)A + (size_t)pm * (KTOT * 128);
  const char* baseB = (const char*)B + pn * (KTOT * 128);
  const bool isA = wid < 4;
  const unsigned voff = isA ? (unsigned)((wid & 3) * 1024 + lane * 16)
                            : (unsigned)(((wid & 3) * 8 + (lane >> 3)) * 128 + pB * 32 + (lane & 1) * 16);
  const char* base = isA ? baseA : baseB;
  const unsigned sbw = lds0 + (isA ? 0 : 16384) + (wid & 3) * 1024;
  auto part = [&](int kc, int p) {
    glds(voff, base + (size_t)kc * 16384 + p * 4096, sbw + (kc % NST) * STAGE + p * 4096);
  };
  auto issue = [&](int kc) { part(kc, 0); part(kc, 1); part(kc, 2); part(kc, 3); };
  TSTAMP(MODE == 0 ? 2 : 4, 16);
  issue(0);
  TSTAMP(MODE == 0 ? 2 : 4, 17);
  BAR();
  issue(1);
  BAR();
  TSTAMP(MODE == 0 ? 2 : 4, 21);
  issue(2);
  if (MODE == 1 && PF5) {
    const int g = wid * 60 + (lane < 60 ? lane : 59);
    const unsigned dB = (unsigned)(baseB - baseA);
    const unsigned vo = g < 160 ? (unsigned)((512 + pn * 160 + g) * 128) : dB + (unsigned)((512 + (idx >> 3) * 320 + g - 160) * 128);
    const unsigned la = __builtin_amdgcn_readfirstlane(lds0 + NST * STAGE + wid * 256);
    asm volatile("s_mov_b32 m0, %2\n\ts_nop 0\n\tglobal_load_lds_dword %0, %1" ::"v"(vo), "s"(baseA), "s"(la) : "memory");
  }
  TSTAMP(MODE == 0 ? 2 : 4, 1);

  unsigned a_addr[4], b_addr[2];
#pragma unroll
  for (int ks = 0; ks < 4; ++ks) a_addr[ks] = lds0 + (2 * ks + (gq >> 1)) * 2048 + (wm * 16 + l15) * 32 + (gq & 1) * 16;
  {
    const int kl = gq * 8 + (l15 >> 2);
    const int fr = ((l15 >> 3) & 1) | ((gq & 1) << 1);
#pragma unroll
    for (int j = 0; j < 2; ++j) b_addr[j] = lds0 + 16384 + kl * 128 + (((2 * wn + j) ^ fr) * 32) + (l15 & 3) * 8;
  }

  f32x4 acc[2];
  const f32x4 z4 = {0.f, 0.f, 0.f, 0.f};
  acc[0] = z4; acc[1] = z4;
  constexpr int NS = LA2 ? 3 : 2;
  bf16x8 af[NS];
  bf16x4 bl[NS][2], bh[NS][2];

  auto rd = [&](int t, int set) {
    const int kc = t >> 2, ks = t & 3, st = kc % NST;
    const unsigned hi = (unsigned)((st & ~1) * STAGE);
    const int so = (st & 1) * STAGE;
    DSRTR(bl[set][0], b_addr[0] + hi, so + ks * 4096);
    DSRTR(bh[set][0], b_addr[0] + hi, so + ks * 4096 + 512);
    DSR128(af[set], a_addr[ks] + hi, so);
    DSRTR(bl[set][1], b_addr[1] + hi, so + ks * 4096);
    DSRTR(bh[set][1], b_addr[1] + hi, so + ks * 4096 + 512);
  };
  auto wt = [&](int set) {
    asm volatile("s_waitcnt lgkmcnt(0)" ::: "memory");
    PIN(af[set]);
    PIN(bl[set][0]); PIN(bh[set][0]); PIN(bl[set][1]); PIN(bh[set][1]);
  };

  using SC = Sched<4, 4, NST, NK, 12, G_R>;
  auto pin = [&](int set) {
    PIN(af[set]);
    PIN(bl[set][0]); PIN(bh[set][0]); PIN(bl[set][1]); PIN(bh[set][1]);
  };
  wait_vm<SC::cum(-1) - 4 + ((MODE == 1 && PF5) ? 1 : 0)>();
  BAR();
  TSTAMP(MODE == 0 ? 2 : 4, 2);
  rd(0, 0);
  if (LA2) rd(1, 1);
  static_for<SC::cum(-1), SC::cum(0)>([&](auto n) { part(n.value >> 2, n.value & 3); });
  if (!LA2) wt(0);
  static_for<0, NT>([&](auto tc) {
    constexpr int t = tc.value, kc = t >> 2, ks = t & 3, cur = t % NS, nxt = (t + NS - 1) % NS;
    constexpr bool sync = ks == (LA2 ? 2 : 3) && kc + 1 < NK;
    if constexpr (sync) {
      static_assert(SC::cum(t) >= (kc + 2) * 4, "schedule");
      constexpr int npf = (MODE == 0 && PF3 && t > PF3T) ? (t - PF3T > 4 ? 4 : t - PF3T) : 0;
      static_assert(npf == 0 || SC::cum(PF3T) == NK * 4, "warm-up loads come after all operand loads");
      if (LA2 && GQ) {
        asm volatile("s_waitcnt lgkmcnt(5)" ::: "memory");
        pin(cur);
      } else if (LA2) {
        asm volatile("s_waitcnt lgkmcnt(0)" ::: "memory");
        pin(cur); pin((t + 1) % NS);
      }
      wait_vm<SC::cum(t) - (kc + 2) * 4 + npf + ((MODE == 0 && (CPF & 4) && t > CW3) ? 1 : 0) + ((MODE == 1 && PF5 && kc + 1 <= 2) ? 1 : 0)>();
      BAR();
      TSTAMP(MODE == 0 ? 2 : 4, 3 + kc);
    }
    if constexpr (t + NS - 1 < NT) rd(t + NS - 1, nxt);
    static_for<SC::cum(t), SC::cum(t + 1)>([&](auto n) { part(n.value >> 2, n.value & 3); });
    if constexpr (MODE == 0 && (CPF & 4) && t == CW3) {
      static_assert(SC::cum(t + 1) == NK * 4, "issued after all operand loads");
      if constexpr (PF3W == 0) code_warm<true>(wid, lane, lds0 + NST * STAGE + wid * 1024, baseA);
      else if constexpr (PF3W == 1) {
        const int g = wid * 64 + lane;
        code_warm<true>(wid, lane, lds0 + NST * STAGE + wid * 1024, (const char*)PFW + (4u << 20) + blockIdx.x * 7168, (unsigned)((g < 112 ? g : 0) * 64));
      } else {
        const int w7 = wid > CPW ? wid - 1 : wid;
        const int g = w7 * 64 + lane;
        code_warm<true>(wid, lane, lds0 + NST * STAGE + wid * 1024, (const char*)PFW + (4u << 20) + (idx >> 2) * 229376 + (idx & 3) * 28672, (unsigned)(g * 64));
      }
    }
    if constexpr (MODE == 0 && PF3 && t >= PF3T && t < PF3T + 4) {
      glds((unsigned)(lane * 16),
           (const char*)PFW + (size_t)(xg >> 1) * (4 * 229376) + idx * 28672 + (t - PF3T) * 8192 + (t - PF3T < 3 ? wid : (wid & 3)) * 1024,
           lds0 + NST * STAGE + wid * 1024);
    }
    if constexpr (LA2 && !sync && (GQ || !(ks == 3 && kc + 1 < NK))) {
      asm volatile("s_waitcnt lgkmcnt(%0)" ::"n"(t + 2 < NT ? 10 : (t + 1 < NT ? 5 : 0)) : "memory");
      pin(cur);
    }
    SCHED();
    acc[0] = mfma16(cat8(bl[cur][0], bh[cur][0]), af[cur], acc[0]);
    acc[1] = mfma16(cat8(bl[cur][1], bh[cur][1]), af[cur], acc[1]);
    SCHED();
    if constexpr (!LA2 && t + 1 < NT) wt(nxt);
  });
  TSTAMP(MODE == 0 ? 2 : 4, 20);
  PIN(res);
  float s = 0.f, q = 0.f;
  {
#pragma unroll
    for (int r = 0; r < 4; ++r) { float a = acc[0][r], b = acc[1][r]; swap16f(a, b); acc[0][r] = a; acc[1][r] = b; }
    const bf16x8 rr = __builtin_bit_cast(bf16x8, res);
    bf16x8 o;
#pragma unroll
    for (int j = 0; j < 2; ++j)
#pragma unroll
      for (int r = 0; r < 4; ++r) {
        o[j * 4 + r] = (bf16)((float)rr[j * 4 + r] + bfr(acc[j][r]));
        const float v = (float)o[j * 4 + r];
        s += v;
        q += v * v;
      }
    st16<(WT & (MODE == 0 ? 4 : 16)) != 0>((char*)O + (MODE == 0 ? hoff : (unsigned)((tok * D_ + ncol) * 2)), __builtin_bit_cast(u32x4, o));
  }
  if (MODE == 0) {
    s += __shfl_xor(s, 16); s += __shfl_xor(s, 32);
    q += __shfl_xor(q, 16); q += __shfl_xor(q, 32);
    if (gq == 0) st8<(WT & 4) != 0>(RS + ((size_t)(pn * 2 + wn) * S_ + tok) * 2, __builtin_bit_cast(u32x2, f32x2{s, q}));
  }
  if (MODE == 0) wait_vm<0>();
  TSTAMP(MODE == 0 ? 2 : 4, 22);
  TFLUSH(MODE == 0 ? 2 : 4);
}
extern "C" __global__ __launch_bounds__(512, 2) void k3_wo(const bf16* __restrict__ A, const bf16* __restrict__ B,
                                                           const bf16* __restrict__ R, bf16* __restrict__ O,
                                                           float* __restrict__ RS, const bf16* __restrict__ PFW) {
  gemm_body<0, 512>(A, B, R, O, RS, PFW);
}
extern "C" __global__ __launch_bounds__(512, 2) void k5_down(const bf16* __restrict__ A, const bf16* __restrict__ B,
                                                             const bf16* __restrict__ R, bf16* __restrict__ O) {
  gemm_body<1, 1792>(A, B, R, O, nullptr, nullptr);
}

template <int NP, int P0>
DEV void gu_body(char* smem, unsigned lds0, int tid, int wid, int pm, int pn, const bf16* __restrict__ Hh,
                 const bf16* __restrict__ Wt, const float* __restrict__ RS, const float* __restrict__ CS,
                 bf16* __restrict__ Hm, const char* pf TARG) {
  constexpr int NST = 3, STAGE = 45056, NK = 8, LPC = 2 + NP, NT = 16;
  const int lane = tid & 63, wm = wid & 3;
  const int l15 = lane & 15, gq = lane >> 4;
  const unsigned l16 = lane * 16;
  const unsigned voffA = (unsigned)((wid * 8 + (lane >> 3)) * 128 + (((lane & 7) ^ ((wid & 1) * 4 + gq)) * 16));
  const char* baseA = (const char*)Hh + (size_t)pm * (128 * 1024);
  const int pc0 = (P0 == 0 ? 0 : 16) + wm * NP;
  const char* baseW = (const char*)Wt + (size_t)pn * (8 * 28672) + pc0 * 1024;
  const int rot = ROT4 * (pm + 2 * pn);
  auto piece = [&](int kc, int i) {
    const unsigned sb = lds0 + (kc % NST) * STAGE;
    const int kk = (kc + rot) & 7;
    if (i == 0) glds(voffA, baseA + kk * 16384, sb + wid * 1024);
    else if (i == 1) glds(voffA, baseA + kk * 16384 + 8192, sb + 8192 + wid * 1024);
    else glds(l16, baseW + kk * 28672 + (i - 2) * 1024, sb + 16384 + (pc0 + i - 2) * 1024);
  };
  auto issue = [&](int kc) {
#pragma unroll
    for (int i = 0; i < LPC; ++i) piece(kc, i);
  };
  float* st_ = (float*)(smem + NST * STAGE);
  float* csum = st_ + 256;
  if (P0 == 0) {
    if (wid < 2) {
      f32x2 sv[16];
      const char* rb = (const char*)RS + pm * 1024;
      const unsigned vo = (unsigned)(tid * 8);
#pragma unroll
      for (int i = 0; i < 16; ++i) GLD2(sv[i], vo, rb + i * (S_ * 8), 0);
      issue(0);
      BAR();
      issue(1);
      wait_vm<2 * LPC>();
#pragma unroll
      for (int i = 0; i < 16; ++i) PIN(sv[i]);
      f32x2 a = sv[0];
#pragma unroll
      for (int i = 1; i < 16; ++i) a += sv[i];
      *(f32x2*)(st_ + tid * 2) = ln_stats(a[0], a[1]);
    } else {
      issue(0);
      BAR();
      issue(1);
    }
  } else {
    float sc[8];
    int c = tid - 256;
    const bool ok = c < 224;
    if (!ok) c = 0;
    const unsigned vo = (unsigned)(((c < 112 ? 0 : F_ - 112) + pn * 112 + c) * 4);
#pragma unroll
    for (int i = 0; i < 8; ++i) GLD1(sc[i], vo, (const char*)CS + i * (2 * F_ * 4), 0);
    issue(0);
    BAR();
    issue(1);
    wait_vm<2 * LPC>();
#pragma unroll
    for (int i = 0; i < 8; ++i) PIN(sc[i]);
    float a = sc[0];
#pragma unroll
    for (int i = 1; i < 8; ++i) a += sc[i];
    if (ok) csum[c] = a;
  }
  TSTAMP(3, 1);

  unsigned a_addr[2];
#pragma unroll
  for (int ks = 0; ks < 2; ++ks) a_addr[ks] = lds0 + (wm * 32 + l15) * 128 + (((4 * ks + gq) ^ (l15 >> 1)) * 16);
  const unsigned b_addr = lds0 + 16384 + P0 * 1024 + l16;
  f32x4 ag[2][NP], au[2][NP];
  const f32x4 z4 = {0.f, 0.f, 0.f, 0.f};
#pragma unroll
  for (int j = 0; j < NP; ++j) { ag[0][j] = z4; ag[1][j] = z4; au[0][j] = z4; au[1][j] = z4; }
  bf16x8 af[2][2], bg[2][NP], bu[2][NP];
  auto rd = [&](int t, int set) {
    const int kc = t >> 1, ks = t & 1, st = kc % NST;
    const unsigned so = st * STAGE;
    DSR128(af[set][0], a_addr[ks] + so, 0);
    DSR128(af[set][1], a_addr[ks] + so, 2048);
#pragma unroll
    for (int j = 0; j < NP; ++j) {
      DSR128(bg[set][j], b_addr + so, (ks * 14 + j) * 1024);
      DSR128(bu[set][j], b_addr + so, (ks * 14 + 7 + j) * 1024);
    }
  };
  auto wt = [&](int set) {
    asm volatile("s_waitcnt lgkmcnt(0)" ::: "memory");
    PIN(af[set][0]); PIN(af[set][1]);
#pragma unroll
    for (int j = 0; j < NP; ++j) { PIN(bg[set][j]); PIN(bu[set][j]); }
  };
  asm volatile("s_waitcnt vmcnt(%0) lgkmcnt(0)" ::"n"(LPC) : "memory");
  BAR();
  TSTAMP(3, 2);
  rd(0, 0);
  piece(NST - 1, 0); piece(NST - 1, 1); piece(NST - 1, 2);
  wt(0);
  constexpr int NTM = NT - 4, NU = 2 * NP;
  auto npf = [&](int lo, int hi) {
    int n = 0;
    if (PF4 == 4)
      for (int i = lo; i <= hi; ++i) n += (i >= PFT && i < PFT + 7) ? 1 : 0;
    return n;
  };
  bf16x8 aT[4][2], bT[2][2][2];
  auto uj = [&](int u) { return u == 0 ? 0 : (u == NU - 1 ? NP - 1 : ((u & 1) ? (u + 1) / 2 : u / 2 - 1)); };
  auto uc = [&](int u) { return (u == 0 || ((u & 1) && u != NU - 1)) ? 0 : 1; };
  auto rdA = [&](int c) {
#pragma unroll
    for (int ks = 0; ks < 2; ++ks) {
      DSR128(aT[c * 2 + ks][0], a_addr[ks] + ((NK - 2 + c) % NST) * STAGE, 0);
      DSR128(aT[c * 2 + ks][1], a_addr[ks] + ((NK - 2 + c) % NST) * STAGE, 2048);
    }
  };
  auto rdB = [&](int u) {
#pragma unroll
    for (int ks = 0; ks < 2; ++ks) {
      DSR128(bT[u & 1][ks][0], b_addr + ((NK - 2 + uc(u)) % NST) * STAGE, (ks * 14 + uj(u)) * 1024);
      DSR128(bT[u & 1][ks][1], b_addr + ((NK - 2 + uc(u)) % NST) * STAGE, (ks * 14 + 7 + uj(u)) * 1024);
    }
  };
  auto rdp = [&](int t, int set, int i) {
    const int kc = t >> 1, ks = t & 1, st = kc % NST;
    const unsigned so = st * STAGE;
    if (i < 2) DSR128(af[set][i], a_addr[ks] + so, i * 2048);
    else if (i & 1) DSR128(bu[set][(i - 2) >> 1], b_addr + so, (ks * 14 + 7 + ((i - 2) >> 1)) * 1024);
    else DSR128(bg[set][(i - 2) >> 1], b_addr + so, (ks * 14 + ((i - 2) >> 1)) * 1024);
  };
  auto rdT = [&](int i) {
    if (i < 4) DSR128(aT[i >> 1][i & 1], a_addr[i >> 1] + ((NK - 2) % NST) * STAGE, (i & 1) * 2048);
    else DSR128(bT[0][(i - 4) >> 1][i & 1], b_addr + ((NK - 2 + uc(0)) % NST) * STAGE, (((i - 4) >> 1) * 14 + (i & 1) * 7 + uj(0)) * 1024);
  };
#pragma unroll
  for (int t = 0; t < NTM; ++t) {
    const int kc = t >> 1, ks = t & 1, cur = t & 1, nxt = cur ^ 1;
    if (ks == 1) {
      const int last = (kc + NST - 1 < NK - 1) ? (kc + NST - 1) : (NK - 1);
      wait_vm_n((last - (kc + 1)) * LPC + npf(2 * kc - 2, 2 * kc));
      BAR();
      TSTAMP(3, 3 + kc);
    }
    const int ic = (t + 1) / 2 + NST - 1, ip = ((t + 1) & 1) * 3;
    SCHED();
#pragma unroll
    for (int j = 0; j < NP; ++j) {
#pragma unroll
      for (int q = 0; q < 4; ++q) {
        if (q == 0) ag[0][j] = mfma16(bg[cur][j], af[cur][0], ag[0][j]);
        if (q == 1) ag[1][j] = mfma16(bg[cur][j], af[cur][1], ag[1][j]);
        if (q == 2) au[0][j] = mfma16(bu[cur][j], af[cur][0], au[0][j]);
        if (q == 3) au[1][j] = mfma16(bu[cur][j], af[cur][1], au[1][j]);
        SCHED();
#pragma unroll
        for (int r = (j * 4 + q) * ILV4; r < (j * 4 + q + 1) * ILV4; ++r) {
          if (t + 1 < NTM) { if (r < 2 + 2 * NP) rdp(t + 1, nxt, r); }
          else if (r < 8) rdT(r);
        }
        SCHED();
      }
      if (j < 3 && ic < NK && ip + j < LPC) piece(ic, ip + j);
      if (PF4 && PF4 < 4 && j < (PF4 == 1 ? 2 : 1) && t == NTM - 1 && (!(MAP5 || PFH) || PFE)) glds(l16, pf + j * ((MAP5 || PFH) ? 7168 : 8192), lds0 + NST * STAGE + 2048 + wid * 1024);
      if (j == 2 && npf(t, t)) glds(l16, pf + (t - PFT) * 8192, lds0 + NST * STAGE + 2048 + wid * 1024);
      SCHED();
    }
    if (t + 1 < NTM) wt(nxt);
  }
  auto wtT = [&](bool a0, bool a1, int u) {
    asm volatile("s_waitcnt lgkmcnt(0)" ::: "memory");
    if (a0) { PIN(aT[0][0]); PIN(aT[0][1]); PIN(aT[1][0]); PIN(aT[1][1]); }
    if (a1) { PIN(aT[2][0]); PIN(aT[2][1]); PIN(aT[3][0]); PIN(aT[3][1]); }
    PIN(bT[u & 1][0][0]); PIN(bT[u & 1][0][1]); PIN(bT[u & 1][1][0]); PIN(bT[u & 1][1][1]);
  };
  wtT(true, false, 0);
  TSTAMP(3, 20);
  bf16* dst0 = Hm + ((size_t)((pm * 2 + (wm >> 1)) * 112 + pn * 7 + P0) * 64 + (wm & 1) * 32 + l15) * 16;
  const unsigned caddr = lds0 + NST * STAGE + 1024 + (P0 * 16 + gq * 4) * 4;
  f32x2 nm[2], le2[2], iv2[2];
  f32x4 cg[2], cu[2];
  unsigned pk[NP][2][2];
  f32x2 tg, tu, te, td;
  auto epi = [&](int j, int mi, int q) {
    const int hf = q >> 2;
    if ((q & 3) == 0) {
      tg = __builtin_elementwise_fma(f32x2{cg[j & 1][2 * hf], cg[j & 1][2 * hf + 1]}, nm[mi],
                                     f32x2{ag[mi][j][2 * hf], ag[mi][j][2 * hf + 1]});
      tu = __builtin_elementwise_fma(f32x2{cu[j & 1][2 * hf], cu[j & 1][2 * hf + 1]}, nm[mi],
                                     f32x2{au[mi][j][2 * hf], au[mi][j][2 * hf + 1]});
      te = tg * le2[mi];
    } else if ((q & 3) == 1) {
      te = f32x2{__builtin_amdgcn_exp2f(te[0]), __builtin_amdgcn_exp2f(te[1])};
    } else if ((q & 3) == 2) {
      td = __builtin_elementwise_fma(te, iv2[mi], iv2[mi]);
      tu = tg * tu;
    } else {
      const f32x2 rc = {__builtin_amdgcn_rcpf(td[0]), __builtin_amdgcn_rcpf(td[1])};
      const f32x2 o = tu * rc;
      pk[j][mi][hf] = pk2(o[0], o[1]);
    }
    if (q == 7) {
      if (j & 1) {
        swap16(pk[j - 1][mi][0], pk[j][mi][0]);
        swap16(pk[j - 1][mi][1], pk[j][mi][1]);
        st16<(WT & 8) != 0>(dst0 + ((size_t)(j - 1 + (gq & 1)) * 64 + mi * 16) * 16 + (gq >> 1) * 8,
                            u32x4{pk[j - 1][mi][0], pk[j - 1][mi][1], pk[j][mi][0], pk[j][mi][1]});
      } else if (j == NP - 1) {
        st8<(WT & 8) != 0>(dst0 + ((size_t)j * 64 + mi * 16) * 16 + gq * 4, u32x2{pk[j][mi][0], pk[j][mi][1]});
      }
    }
  };
#pragma unroll
  for (int u = 0; u < NU; ++u) {
    const int j = uj(u), c = uc(u);
    if (u == 1) {
      wait_vm_n(PF4 == 4 ? npf(NTM - 2, NTM - 1) : (PF4 == 1 ? ((MAP5 || PFH) ? (PFE ? 4 : 2) : 4) : (PF4 ? 1 : 0)));
      BAR();
      TSTAMP(3, 9);
      rdA(1);
    }
    if (u + 1 < NU) rdB(u + 1);
    const int cj = u == NU - 1 ? NP - 1 : ((u >= 2 && !(u & 1)) ? (u - 2) / 2 : -1);
    if (cj >= 0) {
      DSR128(cg[cj & 1], caddr, cj * 64);
      DSR128(cu[cj & 1], caddr, cj * 64 + 448);
    }
    if (u == 1) {
#pragma unroll
      for (int mi = 0; mi < 2; ++mi) {
        const f32x2 ms = *(const f32x2*)(st_ + (wm * 32 + mi * 16 + l15) * 2);
        nm[mi] = f32x2{-ms[0], -ms[0]};
        const float le = ms[1] * -1.4426950408889634f, iv = 1.0f / (ms[1] * ms[1]);
        le2[mi] = f32x2{le, le};
        iv2[mi] = f32x2{iv, iv};
      }
    }
    SCHED();
#pragma unroll
    for (int q = 0; q < 8; ++q) {
      const int ks = q >> 2, mi = q & 1;
      if (q & 2) au[mi][j] = mfma16(bT[u & 1][ks][1], aT[c * 2 + ks][mi], au[mi][j]);
      else ag[mi][j] = mfma16(bT[u & 1][ks][0], aT[c * 2 + ks][mi], ag[mi][j]);
      SCHED();
      if (u >= 3) epi((u - 3) >> 1, (u - 3) & 1, q);
      if (PF4 == 1 && !(MAP5 || PFH) && u < 3 && (q == 1 || q == 5) && 2 + u * 2 + (q >> 2) < 7)
        glds(l16, pf + (2 + u * 2 + (q >> 2)) * 8192, lds0 + NST * STAGE + 2048 + wid * 1024);
      if (PF4 == 1 && (MAP5 || PFH) && u < 2 && (q == 1 || q == 5) && (PFE ? 2 : 0) + u * 2 + (q >> 2) < 4)
        glds(l16, pf + ((PFE ? 2 : 0) + u * 2 + (q >> 2)) * 7168, lds0 + NST * STAGE + 2048 + wid * 1024);
      if ((CPF & 8) && u == 3 && q == 3) code_warm<false>(wid, lane, lds0 + NST * STAGE + 2048 + wid * 1024, nullptr);
      SCHED();
    }
    if (u + 1 < NU) {
      wtT(false, u == 1, u + 1);
    } else {
      asm volatile("s_waitcnt lgkmcnt(0)" ::: "memory");
    }
    if (cj >= 0) { PIN(cg[cj & 1]); PIN(cu[cj & 1]); }
  }
  TSTAMP(3, 21);
#pragma unroll
  for (int e = NU - 3; e < NU; ++e)
#pragma unroll
    for (int q = 0; q < 8; ++q) epi(e >> 1, e & 1, q);
}

extern "C" __global__ __launch_bounds__(512, 2) void k4_gu(const bf16* __restrict__ Hh, const bf16* __restrict__ Wt,
                                                const float* __restrict__ RS, const float* __restrict__ CS,
                                                bf16* __restrict__ Hm, const bf16* __restrict__ W2t) {
  extern __shared__ __attribute__((aligned(16))) char smem[];
  TDECL
  TSTAMP(3, 0);
  const int tid = threadIdx.x;
  const int wid = __builtin_amdgcn_readfirstlane(tid >> 6);
  const int xg = blockIdx.x & 7, idx = blockIdx.x >> 3;
#define MAP4 0
  const int pm = MAP4 == 0 ? (xg & 1) * 8 + (idx & 7) : (MAP4 == 1 ? xg * 2 + (idx & 1) : (MAP4 == 2 ? (xg >> 1) * 4 + (idx & 3) : xg * 2 + (idx >> 4)));
  const int pn = MAP4 == 0 ? (xg >> 1) * 4 + (idx >> 3) : (MAP4 == 1 ? idx >> 1 : (MAP4 == 2 ? (xg & 1) * 8 + (idx >> 2) : (idx & 15)));
  asm volatile("" : "+s"(RS), "+s"(CS), "+s"(Hm), "+s"(W2t));
  const unsigned lds0 = (unsigned)(uintptr_t)(LDSP(char))smem;
  const char* pf = PFH ? (const char*)W2t + (idx >> 2) * 229376 + 114688 + (idx & 3) * 28672 + (wid < 7 ? wid : 6) * 1024 : MAP5 ? (const char*)W2t + (xg & 1) * 917504 + idx * 28672 + (wid < 7 ? wid : 6) * 1024 : (const char*)W2t + (PF4 == 2 ? (blockIdx.x * 7 + (wid < 7 ? wid : 6)) * 1024 : (PF4 == 3 ? idx * 57344 + wid * 7168 : idx * 57344 + wid * 1024));
  if (wid < 4) gu_body<4, 0>(smem, lds0, tid, wid, pm, pn, Hh, Wt, RS, CS, Hm, pf TPASS);
  else gu_body<3, 4>(smem, lds0, tid, wid, pm, pn, Hh, Wt, RS, CS, Hm, pf TPASS);
  wait_vm<0>();
  TSTAMP(3, 22);
  TFLUSH(3);
  asm volatile("k4_gu_tail:");
}

extern "C" void kernel_launch(void* const* d_in, const int* in_sizes, int n_in, void* d_out, int out_size, void* d_ws,
                              size_t ws_size, hipStream_t stream) {
  const bf16* x = (const bf16*)d_in[0];
  const bf16* wqkv = (const bf16*)d_in[1];
  const bf16* wo = (const bf16*)d_in[2];
  const bf16* w1 = (const bf16*)d_in[4];
  const bf16* w3 = (const bf16*)d_in[5];
  const bf16* w2 = (const bf16*)d_in[6];
  bf16* out = (bf16*)d_out;

  char* ws = (char*)d_ws;
  bf16* QKV = (bf16*)(ws);
  bf16* Oa = (bf16*)(ws + (7u << 20));
  bf16* Hh = (bf16*)(ws + (9u << 20));
#define HMO 11
  bf16* Hm = (bf16*)(ws + ((unsigned)HMO << 20));
  float* RS = (float*)(ws + (18u << 20));
  float* CS = (float*)(ws + (19u << 20));
  bf16* Wt = (bf16*)(ws + (20u << 20));
  bf16* W2t = (bf16*)(ws + (24u << 20));
  bf16* Wot = (bf16*)(ws + (26u << 20));

  {
    constexpr int lds = K1_NST * 28672 + 2560 + 2048;
    (void)hipFuncSetAttribute((const void*)k1_qkv, hipFuncAttributeMaxDynamicSharedMemorySize, lds);
    k1_qkv<<<256, 512, lds, stream>>>(x, wqkv, QKV, wo, w1, w3, w2);
  }
  {
    constexpr int lds = 59392 + 2048;
    (void)hipFuncSetAttribute((const void*)k2_attn, hipFuncAttributeMaxDynamicSharedMemorySize, lds);
    k2_attn<<<256, 512, lds, stream>>>(QKV, Oa, wo, w1, w3, w2, Wt, CS, W2t, Wot);
  }
  {
    auto k = k3_wo;
    constexpr int lds = G_NST * 32768 + 8192;
    (void)hipFuncSetAttribute((const void*)k, hipFuncAttributeMaxDynamicSharedMemorySize, lds);
    k<<<256, 512, lds, stream>>>(Oa, Wot, x, Hh, RS, Wt);
  }
  {
    constexpr int lds = 3 * 45056 + 2048 + 8192;
    (void)hipFuncSetAttribute((const void*)k4_gu, hipFuncAttributeMaxDynamicSharedMemorySize, lds);
    k4_gu<<<256, 512, lds, stream>>>(Hh, Wt, RS, CS, Hm, W2t);
  }
  {
    auto k = k5_down;
    constexpr int lds = G_NST * 32768 + 2048;
    (void)hipFuncSetAttribute((const void*)k, hipFuncAttributeMaxDynamicSharedMemorySize, lds);
    k<<<256, 512, lds, stream>>>(Hm, W2t, Hh, out);
  }
}
```

```cpp
#include <hip/hip_runtime.h>
#include <stdint.h>

typedef __attribute__((ext_vector_type(8))) __bf16 bf16x8;
typedef __attribute__((ext_vector_type(4))) __bf16 bf16x4;
typedef __attribute__((ext_vector_type(2))) __bf16 bf16x2;
typedef __attribute__((ext_vector_type(2))) float f32x2;
typedef __attribute__((ext_vector_type(4))) float f32x4;
typedef __attribute__((ext_vector_type(16))) float f32x16;
typedef __attribute__((ext_vector_type(4))) unsigned int u32x4;
typedef __attribute__((ext_vector_type(2))) unsigned int u32x2;
typedef __bf16 bf16;

#define LDSP(T) __attribute__((address_space(3))) T*
#define DEV __device__ __forceinline__

#define K1_NST 4
#define G_NST 4
#define G_R 1
#define ROT1 0
#define ROT3 0
#define ROT4 0
#define ROT5 0
#define PF4 1
#define PFT 5
#define LA2 1
#define PF3 0
#define K1DOT 0
#define K1T 1
#define ILV1 1
#define ILV4 1
#define GQ 1
#define PF5 0
#define MAP5 0
#define PFE 1
#define PF3W 2
#define PFH 1
#define PF3T 4
#define EPS 1
#define EAT 10
constexpr int S_ = 2048, D_ = 512, H_ = 32, F_ = 1792;

DEV float bfr(float x) { return (float)(bf16)x; }

template <int N>
DEV void wait_vm() {
  asm volatile("s_waitcnt vmcnt(%0)" ::"n"(N) : "memory");
}
DEV void wait_vm_n(int n) {
  switch (n) {
#define WC(X) case X: wait_vm<X>(); break;
    WC(0) WC(1) WC(2) WC(3) WC(4) WC(5) WC(6) WC(7) WC(8) WC(9) WC(10) WC(11) WC(12) WC(13) WC(14) WC(15)
    WC(16) WC(17) WC(18) WC(19) WC(20) WC(21) WC(22) WC(23) WC(24)
#undef WC
    default: wait_vm<0>(); break;
  }
}

DEV void glds(unsigned voff, const void* sbase, unsigned ldsaddr) {
  ldsaddr = __builtin_amdgcn_readfirstlane(ldsaddr);
  asm volatile("s_mov_b32 m0, %2\n\ts_nop 0\n\tglobal_load_lds_dwordx4 %0, %1" ::"v"(voff), "s"(sbase), "s"(ldsaddr)
               : "memory");
}
#define DSR128(dst, addr, off) asm volatile("ds_read_b128 %0, %1 offset:%2" : "=v"(dst) : "v"(addr), "i"(off))
#define DSRTR(dst, addr, off) asm volatile("ds_read_b64_tr_b16 %0, %1 offset:%2" : "=v"(dst) : "v"(addr), "i"(off))
#define PIN(x) asm volatile("" : "+v"(x))
#define GLD4(dst, voff, sbase, off) \
  asm volatile("global_load_dwordx4 %0, %1, %2 offset:%3" : "=v"(dst) : "v"(voff), "s"(sbase), "i"(off))
#define GLD2(dst, voff, sbase, off) \
  asm volatile("global_load_dwordx2 %0, %1, %2 offset:%3" : "=v"(dst) : "v"(voff), "s"(sbase), "i"(off))
#define GLD1(dst, voff, sbase, off) \
  asm volatile("global_load_dword %0, %1, %2 offset:%3" : "=v"(dst) : "v"(voff), "s"(sbase), "i"(off))
#define WT 31
template <bool W>
DEV void st16(void* p, u32x4 v) {
  if constexpr (W) asm volatile("global_store_dwordx4 %0, %1, off sc1\n\ts_nop 1" ::"v"(p), "v"(v) : "memory");
  else *(u32x4*)p = v;
}
template <bool W>
DEV void st8(void* p, u32x2 v) {
  if constexpr (W) asm volatile("global_store_dwordx2 %0, %1, off sc1\n\ts_nop 1" ::"v"(p), "v"(v) : "memory");
  else *(u32x2*)p = v;
}
template <bool W>
DEV void st4(void* p, unsigned v) {
  if constexpr (W) asm volatile("global_store_dword %0, %1, off sc1" ::"v"(p), "v"(v) : "memory");
  else *(unsigned*)p = v;
}
#define BAR() __builtin_amdgcn_s_barrier()
#define SCHED() __builtin_amdgcn_sched_barrier(0)

#define TDECL
#define TSTAMP(m, i) do { } while (0)
#define TFLUSH(m) do { } while (0)
#define TARG
#define TPASS

#define CPF 15
#define CODE_ADDR(dst, sym)                                                                                  \
  asm volatile("s_getpc_b64 s[30:31]\n\ts_add_u32 s30, s30, " #sym "@rel32@lo+4\n\ts_addc_u32 s31, s31, " #sym \
               "@rel32@hi+12\n\ts_mov_b64 %0, s[30:31]"                                                      \
               : "=s"(dst)::"s30", "s31", "scc")
#define CPW 5
#define XPF 3
constexpr int CW1 = 8, CW3 = 3, CW4 = 14;
template <bool ALL>
DEV void code_warm(int wid, int lane, unsigned ldsaddr, const char* other, unsigned ovo = 0) {
  const bool sel = wid == CPW && blockIdx.x < 64;
  if (!ALL && !sel) return;
  const char* a;
  int d;
  CODE_ADDR(a, k1_qkv);
  asm volatile("s_mov_b32 %0, k4_gu_tail-k1_qkv" : "=s"(d));
  const int last = d < 0 ? 0 : d >> 7;
  const int ln = sel ? (int)(blockIdx.x >> 3) * 64 + lane : 0;
  unsigned vo = (unsigned)((ln < last ? ln : last) * 128);
  if ((XPF & 1) && (blockIdx.x >> 3) == 6) {
    a = (const char*)((uintptr_t)__builtin_amdgcn_kernarg_segment_ptr() & ~(uintptr_t)4095);
    vo = (unsigned)((lane & 31) * 128);
  }
  if ((XPF & 2) && (blockIdx.x >> 3) == 7) {
    int d2;
    CODE_ADDR(a, k1_qkv.kd);
    asm volatile("s_mov_b32 %0, k4_gu.kd-k1_qkv.kd" : "=s"(d2));
    const int l2 = d2 < 0 ? 0 : d2 >> 6;
    vo = (unsigned)((lane < l2 ? lane : l2) * 64);
  }
  if (ALL && !sel) { a = other; vo = ovo; }
  ldsaddr = __builtin_amdgcn_readfirstlane(ldsaddr);
  asm volatile("s_mov_b32 m0, %2\n\ts_nop 0\n\tglobal_load_lds_dword %0, %1" ::"v"(vo), "s"(a), "s"(ldsaddr) : "memory");
}

template <int V> struct IC { static constexpr int value = V; };
template <int I, int N, class F>
DEV void static_for(F&& f) {
  if constexpr (I < N) {
    f(IC<I>{});
    static_for<I + 1, N>(f);
  }
}
template <int P, int KS, int NST, int NK, int PRE, int R>
struct Sched {
  static constexpr int avail(int t) {
    int n = ((t + 1) / KS + NST - 1) * P + ((t + 1) % KS + 1) * (P / KS);
    return n > NK * P ? NK * P : n;
  }
  static constexpr int cum(int t) {
    int n = PRE;
    for (int i = -1; i < t; ++i) { int a = avail(i); n = (n + R < a) ? n + R : a; }
    return n;
  }
};

DEV void swap32(unsigned& a, unsigned& b) {
  const auto r = __builtin_amdgcn_permlane32_swap(a, b, false, false);
  a = r[0]; b = r[1];
}
DEV void swap16(unsigned& a, unsigned& b) {
  const auto r = __builtin_amdgcn_permlane16_swap(a, b, false, false);
  a = r[0]; b = r[1];
}
DEV void swap16f(float& a, float& b) {
  unsigned x = __builtin_bit_cast(unsigned, a), y = __builtin_bit_cast(unsigned, b);
  swap16(x, y);
  a = __builtin_bit_cast(float, x); b = __builtin_bit_cast(float, y);
}
DEV unsigned pk2(float a, float b) { return __builtin_bit_cast(unsigned, bf16x2{(bf16)a, (bf16)b}); }
DEV bf16x8 cat8(bf16x4 a, bf16x4 b) { return __builtin_shufflevector(a, b, 0, 1, 2, 3, 4, 5, 6, 7); }
DEV f32x4 mfma16(bf16x8 a, bf16x8 b, f32x4 c) { return __builtin_amdgcn_mfma_f32_16x16x32_bf16(a, b, c, 0, 0, 0); }
DEV f32x16 mfma32(bf16x8 a, bf16x8 b, f32x16 c) { return __builtin_amdgcn_mfma_f32_32x32x16_bf16(a, b, c, 0, 0, 0); }

DEV f32x2 ln_stats(float sum, float sq) {
  float m = sum * (1.0f / D_);
  float var = fmaxf(sq * (1.0f / D_) - m * m, 0.f);
  float vb = bfr(var);
  float sd = bfr(__builtin_sqrtf(bfr(vb + 1e-5f)));
  f32x2 r;
  r[0] = bfr(m);
  r[1] = 1.0f / sd;
  return r;
}

DEV f32x2 ln_stats_hw(float sum, float sq) {
  float m = sum * (1.0f / D_);
  float var = fmaxf(sq * (1.0f / D_) - m * m, 0.f);
  float vb = bfr(var);
  float sd = bfr(__builtin_amdgcn_sqrtf(bfr(vb + 1e-5f)));
  f32x2 r;
  r[0] = bfr(m);
  r[1] = __builtin_amdgcn_rcpf(sd);
  return r;
}

extern "C" __global__ __launch_bounds__(512, 2) void k1_qkv(const bf16* __restrict__ x, const bf16* __restrict__ w,
                                                            bf16* __restrict__ QKV) {
  constexpr int NST = K1_NST, STAGE = 28672, NK = 8, NT = 16;
  extern __shared__ __attribute__((aligned(16))) char smem[];
  TDECL
  TSTAMP(0, 0);
  const int tid = threadIdx.x, lane = tid & 63;
  const int wid = __builtin_amdgcn_readfirstlane(tid >> 6);
  const int wm = wid & 3, wn = wid >> 2;
  const int l15 = lane & 15, gq = lane >> 4;
  const int xg = blockIdx.x & 7, idx = blockIdx.x >> 3;
  const int pm = (xg & 3) * 4 + (idx & 3), pn = (xg >> 2) * 8 + (idx >> 2);
  const unsigned lds0 = (unsigned)(uintptr_t)(LDSP(char))smem;
  asm volatile("" : "+s"(QKV));

  const unsigned voffA = (unsigned)((wid * 8 + (lane >> 3)) * 1024 + (((lane & 7) ^ ((wid & 1) * 4 + gq)) * 16));
  const int r12 = (lane * 43) >> 9, c12 = lane - r12 * 12;
  const bool validB = lane < 48;
  const unsigned voffB = (unsigned)((wid * 8 + r12) * 3072 + (((c12 >> 1) ^ (wid & 1)) * 16 + (c12 & 1) * 8) * 2);
  const char* baseA = (const char*)x + (size_t)pm * (128 * 1024);
  const char* baseB = (const char*)w + pn * 192;
  const int rot = ROT1 * ((idx >> 2) + 2 * (idx & 3));
  auto part = [&](int kc, int p) {
    const unsigned sb = lds0 + (kc % NST) * STAGE;
    const int kk = (kc + rot) & 7;
    if (p == 0) {
      glds(voffA, baseA + kk * 128, sb + wid * 1024);
      glds(voffA, baseA + kk * 128 + 65536, sb + 8192 + wid * 1024);
    } else if (validB) {
      glds(voffB, baseB + kk * (64 * 3072), sb + 16384 + wid * 1536);
      glds(voffB, baseB + kk * (64 * 3072) + 4 * 3072, sb + 16384 + wid * 1536 + 768);
    }
  };
  auto issue = [&](int kc) { part(kc, 0); part(kc, 1); };
  issue(0);
  BAR();
  issue(1);
  BAR();
  issue(2);
  TSTAMP(0, 1);

  unsigned a_addr[2][2], b_addr[3], c_addr;
#pragma unroll
  for (int mi = 0; mi < 2; ++mi)
#pragma unroll
    for (int ks = 0; ks < 2; ++ks)
      a_addr[mi][ks] = lds0 + (wm * 32 + ((mi ^ wn) * 16) + l15) * 128 + (((4 * ks + gq) ^ (l15 >> 1)) * 16);
  {
    const int kl = gq * 8 + (l15 >> 2);
#pragma unroll
    for (int j = 0; j < 3; ++j)
      b_addr[j] = lds0 + 16384 + kl * 192 + (((3 * wn + j) ^ (gq & 1)) * 32) + (l15 & 3) * 8;
    c_addr = lds0 + 16384 + kl * 192 + (((3 * wn + (wm < 3 ? wm : 0)) ^ (gq & 1)) * 32) + (l15 & 3) * 8;
  }

  f32x4 acc[2][3], s1, s2, cs;
  const f32x4 z4 = {0.f, 0.f, 0.f, 0.f};
#pragma unroll
  for (int j = 0; j < 3; ++j) { acc[0][j] = z4; acc[1][j] = z4; }
  cs = z4; s1 = z4; s2 = z4;
  bf16x8 af[2][2];
  bf16x4 bl[2][3], bh[2][3];
  const bf16 one = (bf16)1.0f;
  bf16x8 ones = {one, one, one, one, one, one, one, one};
  PIN(ones);
  bf16x4 cl[2], ch[2];
  const unsigned st_ = lds0 + NST * STAGE;
  const unsigned csum = st_ + 1024;
  unsigned e_stw, e_csw, e_ms0, e_ms1, e_csr, e_vo[3], e_sc[3];
  const char* e_db;
  auto eaddr = [&](int i) {
    const int blk0 = pn * 6 + wn * 3;
    if (i == 0) {
      e_ms0 = st_ + (wm * 32 + wn * 16 + l15) * 8;
      e_ms1 = e_ms0 ^ 128u;
      PIN(e_ms0); PIN(e_ms1);
    } else if (i == 1) {
      unsigned dmy = csum + 1024 + lane * 8;
      PIN(dmy);
      e_stw = gq == (l15 >> 2) ? e_ms0 : dmy;
      e_csr = csum + wn * 256 + gq * 16;
      e_csw = e_csr + wm * 64;
      PIN(e_stw); PIN(e_csw); PIN(e_csr);
    } else if (i == 2) {
      e_db = (const char*)QKV + ((size_t)(unsigned)(blk0 * 64 + pm * 4 + wm) << 10);
      asm volatile("" : "+s"(e_db));
#pragma unroll
      for (int j = 0; j < 3; ++j) {
        e_sc[j] = blk0 + j < 32 ? __builtin_bit_cast(unsigned, 0.25f * 1.4426950408889634f) : 0x3f800000u;
        asm volatile("" : "+s"(e_sc[j]));
      }
    } else {
      const int msel = (gq & 1) ^ wn;
      unsigned loq = (unsigned)(msel * 128 + (gq >> 1) * 256 + l15 * 8) * 2, lov = (unsigned)(msel * 256 + l15 * 16 + (gq >> 1) * 8) * 2;
      PIN(loq); PIN(lov);
#pragma unroll
      for (int j = 0; j < 3; ++j) {
        e_vo[j] = (blk0 + j >= 64 ? lov : loq) + j * 65536;
        PIN(e_vo[j]);
      }
    }
    SCHED();
  };

  auto rd = [&](int t, int set) {
    const int kc = t >> 1, ks = t & 1, st = kc % NST;
    const unsigned hi = (unsigned)((st & ~1) * STAGE);
    const int so = (st & 1) * STAGE;
#pragma unroll
    for (int j = 0; j < 3; ++j) {
      DSRTR(bl[set][j], b_addr[j] + hi, so + ks * 6144);
      DSRTR(bh[set][j], b_addr[j] + hi, so + ks * 6144 + 768);
      if (j < 2) DSR128(af[set][j], a_addr[j][ks] + hi, so);
    }
  };
  auto rdp = [&](int t, int set, int i) {
    const int kc = t >> 1, ks = t & 1, st = kc % NST, j = i / 3;
    const unsigned hi = (unsigned)((st & ~1) * STAGE);
    const int so = (st & 1) * STAGE;
    if (i % 3 == 0) DSRTR(bl[set][j], b_addr[j] + hi, so + ks * 6144);
    else if (i % 3 == 1) DSRTR(bh[set][j], b_addr[j] + hi, so + ks * 6144 + 768);
    else DSR128(af[set][j], a_addr[j][ks] + hi, so);
  };
  auto rdc = [&](int t, int set) {
    const int kc = t >> 1, ks = t & 1, st = kc % NST;
    const unsigned hi = (unsigned)((st & ~1) * STAGE);
    const int so = (st & 1) * STAGE;
    DSRTR(cl[set], c_addr + hi, so + ks * 6144);
    DSRTR(ch[set], c_addr + hi, so + ks * 6144 + 768);
  };
  auto wt = [&](int set) {
    asm volatile("s_waitcnt lgkmcnt(0)" ::: "memory");
    PIN(af[set][0]); PIN(af[set][1]); PIN(cl[set]); PIN(ch[set]);
#pragma unroll
    for (int j = 0; j < 3; ++j) { PIN(bl[set][j]); PIN(bh[set][j]); }
  };

  using SC = Sched<2, 2, NST, NK, 6, 2>;
  wait_vm<(SC::cum(-1) - 2) * 2>();
  BAR();
  TSTAMP(0, 2);
  rd(0, 0);
  rdc(0, 0);
  static_for<SC::cum(-1), SC::cum(0)>([&](auto n) { part(n.value >> 1, n.value & 1); });
  wt(0);
  static_for<0, NT>([&](auto tc) {
    constexpr int t = tc.value, kc = t >> 1, ks = t & 1, cur = t & 1, nxt = cur ^ 1;
    constexpr bool sync = ks == 1 && kc + 1 < NK;
    const bf16x8 b0 = cat8(bl[cur][0], bh[cur][0]), b1 = cat8(bl[cur][1], bh[cur][1]), b2 = cat8(bl[cur][2], bh[cur][2]);
    auto mf = [&](int i) {
      if (i < 6) acc[i / 3][i % 3] = mfma16(i % 3 == 0 ? b0 : (i % 3 == 1 ? b1 : b2), af[cur][i / 3], acc[i / 3][i % 3]);
      else if (i == 6) s1 = mfma16(ones, af[cur][0], s1);
      else s2 = mfma16(af[cur][0], af[cur][0], s2);
      SCHED();
    };
    auto mcs = [&]() {
      cs = mfma16(cat8(cl[cur], ch[cur]), ones, cs);
      SCHED();
    };
    if constexpr (sync) {
      static_assert(SC::cum(t) >= (kc + 2) * 2, "schedule");
      wait_vm<(SC::cum(t) - (kc + 2) * 2) * 2 + (((CPF & 1) && t > CW1) ? 1 : 0)>();
      BAR();
      TSTAMP(0, 3 + kc);
    }
    SCHED();
    if constexpr (t + 1 < NT) {
      static_for<0, 8>([&](auto ic) {
        constexpr int i = ic.value, r = i;
        mf(i);
        if constexpr (r < 4) { rdp(t + 1, nxt, 2 * r); rdp(t + 1, nxt, 2 * r + 1); }
        if constexpr (r == 4) rdc(t + 1, nxt);
        if constexpr (r == 5) static_for<SC::cum(t), SC::cum(t + 1)>([&](auto n) { part(n.value >> 1, n.value & 1); });
        if constexpr (r == 5 && t >= EAT && t < EAT + 4) eaddr(t - EAT);
        if constexpr (r == 6 && (CPF & 1) && t == CW1) {
          static_assert(SC::cum(t + 1) == NK * 2, "issued after all operand loads");
          code_warm<true>(wid, lane, lds0 + NST * STAGE + 2560 + wid * 256, baseA);
        }
        SCHED();
      });
      mcs();
      wt(nxt);
    } else {
      mf(6); mf(7); mcs();
      mf(0); mf(1);
      float q0 = s2[0], q1 = s2[1], q2 = s2[2], q3 = s2[3];
      asm volatile("" : "+v"(q0), "+v"(q1), "+v"(q2), "+v"(q3));
      const bool m1 = __builtin_amdgcn_inverse_ballot_w64(0xAAAAAAAAAAAAAAAAull), m2 = __builtin_amdgcn_inverse_ballot_w64(0xCCCCCCCCCCCCCCCCull);
      const float qa = m1 ? q1 : q0, qb = m1 ? q3 : q2;
      const float sq = m2 ? qb : qa;
      const float m = s1[0] * (1.0f / D_);
      float var = fmaxf(sq * (1.0f / D_) - m * m, 0.f);
      var = bfr(bfr(var) + 1e-5f);
      PIN(var);
      SCHED();
      mf(2);
      asm volatile("ds_write_b128 %0, %1" ::"v"(e_csw), "v"(cs) : "memory");
      float sd = bfr(__builtin_amdgcn_sqrtf(var));
      PIN(sd);
      SCHED();
      mf(3);
      const f32x2 ms = {bfr(m), __builtin_amdgcn_rcpf(sd)};
      asm volatile("ds_write_b64 %0, %1" ::"v"(e_stw), "v"(ms) : "memory");
      SCHED();
      mf(4); mf(5);
    }
  });
  TSTAMP(0, 20);
  asm volatile("s_waitcnt lgkmcnt(0)" ::: "memory");
  BAR();
  TSTAMP(0, 21);
  f32x2 ms[2];
  f32x4 c4[3];
  asm volatile("ds_read_b64 %0, %1" : "=v"(ms[0]) : "v"(e_ms0));
  asm volatile("ds_read_b64 %0, %1" : "=v"(ms[1]) : "v"(e_ms1));
  DSR128(c4[0], e_csr, 0); DSR128(c4[1], e_csr, 64); DSR128(c4[2], e_csr, 128);
  asm volatile("s_waitcnt lgkmcnt(0)" ::: "memory");
  PIN(ms[0]); PIN(ms[1]); PIN(c4[0]); PIN(c4[1]); PIN(c4[2]);
#pragma unroll
  for (int j = 0; j < 3; ++j) {
    unsigned o[2][2];
#pragma unroll
    for (int mi = 0; mi < 2; ++mi) {
      const float rs = ms[mi][1] * __builtin_bit_cast(float, e_sc[j]);
#pragma unroll
      for (int r = 0; r < 2; ++r)
        o[mi][r] = pk2((acc[mi][j][2 * r] - ms[mi][0] * c4[j][2 * r]) * rs, (acc[mi][j][2 * r + 1] - ms[mi][0] * c4[j][2 * r + 1]) * rs);
    }
    swap16(o[0][0], o[1][0]);
    swap16(o[0][1], o[1][1]);
    const u32x4 ov = {o[0][0], o[0][1], o[1][0], o[1][1]};
    if constexpr ((WT & 1) != 0) asm volatile("global_store_dwordx4 %0, %1, %2 sc1\n\ts_nop 1" ::"v"(e_vo[j]), "v"(ov), "s"(e_db) : "memory");
    else asm volatile("global_store_dwordx4 %0, %1, %2\n\ts_nop 1" ::"v"(e_vo[j]), "v"(ov), "s"(e_db) : "memory");
  }
  TSTAMP(0, 22);
  TFLUSH(0);
}

template <bool FULL, int NPF>
DEV void attn_body(unsigned kaddr, unsigned vaddr, bf16x8 qf, f32x16 cm0, f32x16 cm8, int wid, bf16* dst, unsigned lds0 TARG) {
  const float NEG = -1e30f;
  f32x16 z16;
#pragma unroll
  for (int r = 0; r < 16; ++r) z16[r] = 0.f;
  bf16x8 kf[9];
#pragma unroll
  for (int t = 0; t < 9; ++t)
    if (FULL || wid + t >= 8) DSR128(kf[t], kaddr, t * 1024);
  asm volatile("s_waitcnt lgkmcnt(0)" ::: "memory");
#pragma unroll
  for (int t = 0; t < 9; ++t) PIN(kf[t]);
  TSTAMP(1, 3);
  float m0 = NEG, m1 = NEG;
#pragma unroll
  for (int t = 0; t < 9; ++t)
    if (FULL || wid + t >= 8) {
      const f32x16 s = mfma32(kf[t], qf, t == 0 ? cm0 : (t == 8 ? cm8 : z16));
#pragma unroll
      for (int r = 0; r < 16; r += 4) {
        m0 = fmaxf(fmaxf(m0, s[r]), s[r + 1]);
        m1 = fmaxf(fmaxf(m1, s[r + 2]), s[r + 3]);
      }
    }
  float m = fmaxf(m0, m1);
  m = fmaxf(m, __shfl_xor(m, 32));
  f32x16 nm;
#pragma unroll
  for (int r = 0; r < 16; ++r) { nm[r] = -m; cm0[r] -= m; cm8[r] -= m; }
  TSTAMP(1, 4);
  wait_vm<NPF>();
  BAR();
  TSTAMP(1, 5);
  if (CPF & 2) code_warm<false>(wid, (int)(threadIdx.x & 63), lds0 + 59392 + wid * 256, nullptr);
  f32x16 oa = z16, ob = z16;
  f32x16 sc_, sn_;
  bf16x4 v0l[2], v0h[2], v1l[2], v1h[2];
  auto vrd = [&](int t, int b) {
    DSRTR(v0l[b], vaddr, t * 1024);
    DSRTR(v0h[b], vaddr, t * 1024 + 256);
    DSRTR(v1l[b], vaddr, t * 1024 + 512);
    DSRTR(v1h[b], vaddr, t * 1024 + 768);
  };
  if (FULL) { sc_ = mfma32(kf[0], qf, cm0); vrd(0, 0); }
  else sc_ = z16;
  sn_ = z16;
#pragma unroll
  for (int t = 0; t < 9; ++t) {
    const int b = t & 1;
    const bool cur_ok = FULL || wid + t >= 8, nxt_ok = t < 8 && (FULL || wid + t + 1 >= 8);
    if (cur_ok) {
      asm volatile("s_waitcnt lgkmcnt(0)" ::: "memory");
      PIN(v0l[b]); PIN(v0h[b]); PIN(v1l[b]); PIN(v1h[b]);
    }
    if (nxt_ok) {
      vrd(t + 1, b ^ 1);
      sn_ = mfma32(kf[t + 1], qf, t + 1 == 8 ? cm8 : nm);
    }
    SCHED();
    if (cur_ok) {
      bf16x8 p0, p1;
#pragma unroll
      for (int j = 0; j < 8; ++j) p0[j] = (bf16)__builtin_amdgcn_exp2f(sc_[j]);
      SCHED();
      oa = mfma32(cat8(v0l[b], v0h[b]), p0, oa);
      SCHED();
#pragma unroll
      for (int j = 0; j < 8; ++j) p1[j] = (bf16)__builtin_amdgcn_exp2f(sc_[8 + j]);
      SCHED();
      ob = mfma32(cat8(v1l[b], v1h[b]), p1, ob);
      SCHED();
    }
    sc_ = sn_;
  }
  TSTAMP(1, 6);
  const float inv = 1.0f / (oa[8] + ob[8]);
  unsigned a0 = pk2((oa[0] + ob[0]) * inv, (oa[1] + ob[1]) * inv), a1 = pk2((oa[2] + ob[2]) * inv, (oa[3] + ob[3]) * inv);
  unsigned b0 = pk2((oa[4] + ob[4]) * inv, (oa[5] + ob[5]) * inv), b1 = pk2((oa[6] + ob[6]) * inv, (oa[7] + ob[7]) * inv);
  swap32(a0, b0);
  swap32(a1, b1);
  st16<(WT & 2) != 0>(dst, u32x4{a0, a1, b0, b1});
}

extern "C" __global__ __launch_bounds__(512, 2) void k2_attn(const bf16* __restrict__ QKV, bf16* __restrict__ O,
                                                  const bf16* __restrict__ wo, const bf16* __restrict__ w1,
                                                  const bf16* __restrict__ w3, const bf16* __restrict__ w2,
                                                  bf16* __restrict__ Wt, float* __restrict__ CS,
                                                  bf16* __restrict__ W2t, bf16* __restrict__ Wot) {
  constexpr int LV = 16384, LONE = 32768, LSJ = 43008;
  extern __shared__ __attribute__((aligned(16))) char smem[];
  TDECL
  TSTAMP(1, 0);
  const int tid = threadIdx.x, lane = tid & 63;
  const int wid = __builtin_amdgcn_readfirstlane(tid >> 6);
  const int l15 = lane & 15, gq = lane >> 4;
  const int xg = blockIdx.x >> 5, h = (blockIdx.x & 7) * 4 + ((blockIdx.x >> 3) & 3);
  const unsigned lds0 = (unsigned)(uintptr_t)(LDSP(char))smem;
  const int t0 = xg * 8 - 8;
  const unsigned l16 = lane * 16;
  asm volatile("" : "+s"(O), "+s"(Wt), "+s"(CS), "+s"(W2t), "+s"(Wot));
  asm volatile("" : "+s"(wo), "+s"(w1), "+s"(w3), "+s"(w2));
  const bool ld = xg > 0 || wid >= 4;

  const char* kb = (const char*)QKV + (2u << 20) + (ptrdiff_t)(h * 64 + t0 + 2 * wid) * 1024;
  if (ld) {
    glds(l16, kb, lds0 + wid * 2048);
    glds(l16, kb + 1024, lds0 + wid * 2048 + 1024);
  }
  bf16x8 qf;
  GLD4(qf, l16, (const char*)QKV + (size_t)(h * 64 + xg * 8 + wid) * 1024, 0);
  if (ld) {
    glds(l16, kb + (2u << 20), lds0 + LV + wid * 2048);
    glds(l16, kb + (2u << 20) + 1024, lds0 + LV + wid * 2048 + 1024);
  }
  {
    const u32x4 o4 = {0x3f803f80u, 0x3f803f80u, 0x3f803f80u, 0x3f803f80u};
    *(u32x4*)(smem + LONE + tid * 16) = o4;
    if (tid < 128) *(u32x4*)(smem + LONE + 8192 + tid * 16) = o4;
  }
  f32x16 cm0, cm8;
  {
    const int c = lane & 31, hh = lane >> 5;
#pragma unroll
    for (int r = 0; r < 16; ++r) {
      const int kr = (r & 3) + 8 * (r >> 2) + 4 * hh;
      cm0[r] = (kr > c) ? 0.f : -1e30f;
      cm8[r] = (kr <= c) ? 0.f : -1e30f;
    }
    PIN(cm0); PIN(cm8);
  }
  TSTAMP(1, 1);
  if (ld) asm volatile("s_waitcnt vmcnt(2) lgkmcnt(0)" ::: "memory");
  else asm volatile("s_waitcnt vmcnt(0) lgkmcnt(0)" ::: "memory");
  PIN(qf);
  BAR();
  TSTAMP(1, 2);
  const int skc = blockIdx.x >> 5, smt = (blockIdx.x >> 4) & 1, spn = blockIdx.x & 15;
  u32x4 wr0, wr1;
  {
    const int wj = wid < 7 ? wid : 6;
    const char* sb = (const char*)(smt ? w3 : w1) + (size_t)(skc * 64) * (F_ * 2) + (spn * 112 + wj * 16) * 2;
    const unsigned vo = (unsigned)((lane >> 1) * (F_ * 2) + (lane & 1) * 16);
    glds(vo, sb, lds0 + LSJ + wid * 2048);
    glds(vo, sb + 32 * (F_ * 2), lds0 + LSJ + wid * 2048 + 1024);
    const char* o = (const char*)wo + (size_t)blockIdx.x * 2048;
    const char* r0 = wid < 7 ? (const char*)w2 + (size_t)blockIdx.x * 7168 + wid * 1024 : o;
    GLD4(wr0, l16, r0, 0);
    GLD4(wr1, l16, o, 1024);
  }

  const unsigned kaddr = lds0 + wid * 1024 + l16;
  const unsigned vlane = (unsigned)(((lane >> 5) * 4 + (l15 >> 2)) * 32 + (l15 & 3) * 8);
  const unsigned vaddr = (lane & 16) ? (lds0 + LONE + (vlane ^ 128u)) : (lds0 + LV + wid * 1024 + vlane);
  bf16* dst = O + (size_t)(((xg * 4 + (wid >> 1)) * H_ + h) * 64 + (wid & 1) * 32 + (lane & 31)) * 16 + (lane >> 5) * 8;
  if (xg > 0) attn_body<true, 4>(kaddr, vaddr, qf, cm0, cm8, wid, dst, lds0 TPASS);
  else attn_body<false, 4>(kaddr, vaddr, qf, cm0, cm8, wid, dst, lds0 TPASS);
  TSTAMP(1, 20);
  wait_vm<1>();
  {
    const unsigned co = (unsigned)(lane & 7) * 16;
    PIN(wr0); PIN(wr1);
    if (wid < 7) {
      st16<(WT & 2) != 0>((char*)W2t + (size_t)(lane >> 3) * (F_ * 128) + (blockIdx.x * 7 + wid) * 128 + co, wr0);
    } else {
      char* o = (char*)Wot + (size_t)(lane >> 3) * (D_ * 128) + blockIdx.x * 256 + co;
      st16<(WT & 2) != 0>(o, wr0);
      st16<(WT & 2) != 0>(o + 128, wr1);
    }
  }
  if (wid < 7) {
    const unsigned ra = lds0 + LSJ + wid * 2048 + (gq * 8 + (l15 >> 2)) * 32 + (l15 & 3) * 8;
    bf16x4 f[4];
    DSRTR(f[0], ra, 0);
    DSRTR(f[1], ra, 128);
    DSRTR(f[2], ra, 1024);
    DSRTR(f[3], ra, 1024 + 128);
    asm volatile("s_waitcnt lgkmcnt(0)" ::: "memory");
    float c = 0.f;
#pragma unroll
    for (int i = 0; i < 4; ++i) {
      PIN(f[i]);
#pragma unroll
      for (int e = 0; e < 4; ++e) c += (float)f[i][e];
    }
    char* wd = (char*)Wt + (size_t)(((spn * 8 + skc) * 4 + smt) * 7 + wid) * 1024 + l16;
    st16<(WT & 2) != 0>(wd, __builtin_bit_cast(u32x4, cat8(f[0], f[1])));
    st16<(WT & 2) != 0>(wd + 14 * 1024, __builtin_bit_cast(u32x4, cat8(f[2], f[3])));
    c += __shfl_xor(c, 16);
    c += __shfl_xor(c, 32);
    if (lane < 16) st4<(WT & 2) != 0>(CS + skc * (2 * F_) + smt * F_ + spn * 112 + wid * 16 + lane, __builtin_bit_cast(unsigned, c));
  }
  TSTAMP(1, 22);
  wait_vm<0>();
  TFLUSH(1);
}

template <int MODE, int KTOT>
DEV void gemm_body(const bf16* __restrict__ A, const bf16* __restrict__ B, const bf16* __restrict__ R,
                   bf16* __restrict__ O, float* __restrict__ RS, const bf16* __restrict__ PFW) {
  constexpr int NST = G_NST, STAGE = 32768, NK = KTOT / 128, NT = NK * 4;
  extern __shared__ __attribute__((aligned(16))) char smem[];
  TDECL
  TSTAMP(MODE == 0 ? 2 : 4, 0);
  const int tid = threadIdx.x, lane = tid & 63;
  const int wid = __builtin_amdgcn_readfirstlane(tid >> 6);
  const int wm = wid & 3, wn = wid >> 2;
  const int l15 = lane & 15, gq = lane >> 4;
  const int xg = blockIdx.x & 7, idx = blockIdx.x >> 3;
#define PNX 0
  const int pm = (MODE == 1 && MAP5) ? (xg >> 1) * 8 + (idx >> 2) : xg * 4 + (idx >> 3);
  const int pn = (MODE == 1 && MAP5) ? (xg & 1) * 4 + (idx & 3) : (idx & 7) ^ PNX;
  if (MODE == 0) asm volatile("" : "+s"(RS), "+s"(PFW));
  const unsigned lds0 = (unsigned)(uintptr_t)(LDSP(char))smem;

  u32x4 res;
  const int tok = pm * 64 + wm * 16 + l15;
  const int ncol = pn * 64 + wn * 32 + (gq & 1) * 16 + (gq >> 1) * 8;
  const unsigned hoff = (unsigned)(((((tok >> 7) * 8 + pn) * 128 + (tok & 127)) * 64 + (ncol & 63)) * 2);
  GLD4(res, MODE == 0 ? (unsigned)((tok * D_ + ncol) * 2) : hoff, R, 0);

  const unsigned voffA = (unsigned)(wid * 1024 + lane * 16);
  const int pB = ((lane & 7) >> 1) ^ (((lane >> 4) & 1) | ((wid & 1) << 1));
  const unsigned voffB = (unsigned)((wid * 8 + (lane >> 3)) * 128 + pB * 32 + (lane & 1) * 16);
  const char* baseA = (const char*)A + (size_t)pm * (KTOT * 128);
  const char* baseB = (const char*)B + pn * (KTOT * 128);
  const bool isA = wid < 4;
  const unsigned voff = isA ? (unsigned)((wid & 3) * 1024 + lane * 16)
                            : (unsigned)(((wid & 3) * 8 + (lane >> 3)) * 128 + pB * 32 + (lane & 1) * 16);
  const char* base = isA ? baseA : baseB;
  const unsigned sbw = lds0 + (isA ? 0 : 16384) + (wid & 3) * 1024;
  auto part = [&](int kc, int p) {
    glds(voff, base + (size_t)kc * 16384 + p * 4096, sbw + (kc % NST) * STAGE + p * 4096);
  };
  auto issue = [&](int kc) { part(kc, 0); part(kc, 1); part(kc, 2); part(kc, 3); };
  TSTAMP(MODE == 0 ? 2 : 4, 16);
  issue(0);
  TSTAMP(MODE == 0 ? 2 : 4, 17);
  BAR();
  issue(1);
  BAR();
  TSTAMP(MODE == 0 ? 2 : 4, 21);
  issue(2);
  if (MODE == 1 && PF5) {
    const int g = wid * 60 + (lane < 60 ? lane : 59);
    const unsigned dB = (unsigned)(baseB - baseA);
    const unsigned vo = g < 160 ? (unsigned)((512 + pn * 160 + g) * 128) : dB + (unsigned)((512 + (idx >> 3) * 320 + g - 160) * 128);
    const unsigned la = __builtin_amdgcn_readfirstlane(lds0 + NST * STAGE + wid * 256);
    asm volatile("s_mov_b32 m0, %2\n\ts_nop 0\n\tglobal_load_lds_dword %0, %1" ::"v"(vo), "s"(baseA), "s"(la) : "memory");
  }
  TSTAMP(MODE == 0 ? 2 : 4, 1);

  unsigned a_addr[4], b_addr[2];
#pragma unroll
  for (int ks = 0; ks < 4; ++ks) a_addr[ks] = lds0 + (2 * ks + (gq >> 1)) * 2048 + (wm * 16 + l15) * 32 + (gq & 1) * 16;
  {
    const int kl = gq * 8 + (l15 >> 2);
    const int fr = ((l15 >> 3) & 1) | ((gq & 1) << 1);
#pragma unroll
    for (int j = 0; j < 2; ++j) b_addr[j] = lds0 + 16384 + kl * 128 + (((2 * wn + j) ^ fr) * 32) + (l15 & 3) * 8;
  }

  f32x4 acc[2];
  const f32x4 z4 = {0.f, 0.f, 0.f, 0.f};
  acc[0] = z4; acc[1] = z4;
  constexpr int NS = LA2 ? 3 : 2;
  bf16x8 af[NS];
  bf16x4 bl[NS][2], bh[NS][2];

  auto rd = [&](int t, int set) {
    const int kc = t >> 2, ks = t & 3, st = kc % NST;
    const unsigned hi = (unsigned)((st & ~1) * STAGE);
    const int so = (st & 1) * STAGE;
    DSRTR(bl[set][0], b_addr[0] + hi, so + ks * 4096);
    DSRTR(bh[set][0], b_addr[0] + hi, so + ks * 4096 + 512);
    DSR128(af[set], a_addr[ks] + hi, so);
    DSRTR(bl[set][1], b_addr[1] + hi, so + ks * 4096);
    DSRTR(bh[set][1], b_addr[1] + hi, so + ks * 4096 + 512);
  };
  auto wt = [&](int set) {
    asm volatile("s_waitcnt lgkmcnt(0)" ::: "memory");
    PIN(af[set]);
    PIN(bl[set][0]); PIN(bh[set][0]); PIN(bl[set][1]); PIN(bh[set][1]);
  };

  using SC = Sched<4, 4, NST, NK, 12, G_R>;
  auto pin = [&](int set) {
    PIN(af[set]);
    PIN(bl[set][0]); PIN(bh[set][0]); PIN(bl[set][1]); PIN(bh[set][1]);
  };
  wait_vm<SC::cum(-1) - 4 + ((MODE == 1 && PF5) ? 1 : 0)>();
  BAR();
  TSTAMP(MODE == 0 ? 2 : 4, 2);
  rd(0, 0);
  if (LA2) rd(1, 1);
  static_for<SC::cum(-1), SC::cum(0)>([&](auto n) { part(n.value >> 2, n.value & 3); });
  if (!LA2) wt(0);
  static_for<0, NT>([&](auto tc) {
    constexpr int t = tc.value, kc = t >> 2, ks = t & 3, cur = t % NS, nxt = (t + NS - 1) % NS;
    constexpr bool sync = ks == (LA2 ? 2 : 3) && kc + 1 < NK;
    if constexpr (sync) {
      static_assert(SC::cum(t) >= (kc + 2) * 4, "schedule");
      constexpr int npf = (MODE == 0 && PF3 && t > PF3T) ? (t - PF3T > 4 ? 4 : t - PF3T) : 0;
      static_assert(npf == 0 || SC::cum(PF3T) == NK * 4, "warm-up loads come after all operand loads");
      if (LA2 && GQ) {
        asm volatile("s_waitcnt lgkmcnt(5)" ::: "memory");
        pin(cur);
      } else if (LA2) {
        asm volatile("s_waitcnt lgkmcnt(0)" ::: "memory");
        pin(cur); pin((t + 1) % NS);
      }
      wait_vm<SC::cum(t) - (kc + 2) * 4 + npf + ((MODE == 0 && (CPF & 4) && t > CW3) ? 1 : 0) + ((MODE == 1 && PF5 && kc + 1 <= 2) ? 1 : 0)>();
      BAR();
      TSTAMP(MODE == 0 ? 2 : 4, 3 + kc);
    }
    if constexpr (t + NS - 1 < NT) rd(t + NS - 1, nxt);
    static_for<SC::cum(t), SC::cum(t + 1)>([&](auto n) { part(n.value >> 2, n.value & 3); });
    if constexpr (MODE == 0 && (CPF & 4) && t == CW3) {
      static_assert(SC::cum(t + 1) == NK * 4, "issued after all operand loads");
      if constexpr (PF3W == 0) code_warm<true>(wid, lane, lds0 + NST * STAGE + wid * 1024, baseA);
      else if constexpr (PF3W == 1) {
        const int g = wid * 64 + lane;
        code_warm<true>(wid, lane, lds0 + NST * STAGE + wid * 1024, (const char*)PFW + (4u << 20) + blockIdx.x * 7168, (unsigned)((g < 112 ? g : 0) * 64));
      } else {
        const int w7 = wid > CPW ? wid - 1 : wid;
        const int g = w7 * 64 + lane;
        code_warm<true>(wid, lane, lds0 + NST * STAGE + wid * 1024, (const char*)PFW + (4u << 20) + (idx >> 2) * 229376 + (idx & 3) * 28672, (unsigned)(g * 64));
      }
    }
    if constexpr (MODE == 0 && PF3 && t >= PF3T && t < PF3T + 4) {
      glds((unsigned)(lane * 16),
           (const char*)PFW + (size_t)(xg >> 1) * (4 * 229376) + idx * 28672 + (t - PF3T) * 8192 + (t - PF3T < 3 ? wid : (wid & 3)) * 1024,
           lds0 + NST * STAGE + wid * 1024);
    }
    if constexpr (LA2 && !sync && (GQ || !(ks == 3 && kc + 1 < NK))) {
      asm volatile("s_waitcnt lgkmcnt(%0)" ::"n"(t + 2 < NT ? 10 : (t + 1 < NT ? 5 : 0)) : "memory");
      pin(cur);
    }
    SCHED();
    acc[0] = mfma16(cat8(bl[cur][0], bh[cur][0]), af[cur], acc[0]);
    acc[1] = mfma16(cat8(bl[cur][1], bh[cur][1]), af[cur], acc[1]);
    SCHED();
    if constexpr (!LA2 && t + 1 < NT) wt(nxt);
  });
  TSTAMP(MODE == 0 ? 2 : 4, 20);
  PIN(res);
  float s = 0.f, q = 0.f;
  {
#pragma unroll
    for (int r = 0; r < 4; ++r) { float a = acc[0][r], b = acc[1][r]; swap16f(a, b); acc[0][r] = a; acc[1][r] = b; }
    const bf16x8 rr = __builtin_bit_cast(bf16x8, res);
    bf16x8 o;
#pragma unroll
    for (int j = 0; j < 2; ++j)
#pragma unroll
      for (int r = 0; r < 4; ++r) {
        o[j * 4 + r] = (bf16)((float)rr[j * 4 + r] + bfr(acc[j][r]));
        const float v = (float)o[j * 4 + r];
        s += v;
        q += v * v;
      }
    st16<(WT & (MODE == 0 ? 4 : 16)) != 0>((char*)O + (MODE == 0 ? hoff : (unsigned)((tok * D_ + ncol) * 2)), __builtin_bit_cast(u32x4, o));
  }
  if (MODE == 0) {
    s += __shfl_xor(s, 16); s += __shfl_xor(s, 32);
    q += __shfl_xor(q, 16); q += __shfl_xor(q, 32);
    if (gq == 0) st8<(WT & 4) != 0>(RS + ((size_t)(pn * 2 + wn) * S_ + tok) * 2, __builtin_bit_cast(u32x2, f32x2{s, q}));
  }
  if (MODE == 0) wait_vm<0>();
  TSTAMP(MODE == 0 ? 2 : 4, 22);
  TFLUSH(MODE == 0 ? 2 : 4);
}
extern "C" __global__ __launch_bounds__(512, 2) void k3_wo(const bf16* __restrict__ A, const bf16* __restrict__ B,
                                                           const bf16* __restrict__ R, bf16* __restrict__ O,
                                                           float* __restrict__ RS, const bf16* __restrict__ PFW) {
  gemm_body<0, 512>(A, B, R, O, RS, PFW);
}
extern "C" __global__ __launch_bounds__(512, 2) void k5_down(const bf16* __restrict__ A, const bf16* __restrict__ B,
                                                             const bf16* __restrict__ R, bf16* __restrict__ O) {
  gemm_body<1, 1792>(A, B, R, O, nullptr, nullptr);
}

template <int NP, int P0>
DEV void gu_body(char* smem, unsigned lds0, int tid, int wid, int pm, int pn, const bf16* __restrict__ Hh,
                 const bf16* __restrict__ Wt, const float* __restrict__ RS, const float* __restrict__ CS,
                 bf16* __restrict__ Hm, const char* pf TARG) {
  constexpr int NST = 3, STAGE = 45056, NK = 8, LPC = 2 + NP, NT = 16;
  const int lane = tid & 63, wm = wid & 3;
  const int l15 = lane & 15, gq = lane >> 4;
  const unsigned l16 = lane * 16;
  const unsigned voffA = (unsigned)((wid * 8 + (lane >> 3)) * 128 + (((lane & 7) ^ ((wid & 1) * 4 + gq)) * 16));
  const char* baseA = (const char*)Hh + (size_t)pm * (128 * 1024);
  const int pc0 = (P0 == 0 ? 0 : 16) + wm * NP;
  const char* baseW = (const char*)Wt + (size_t)pn * (8 * 28672) + pc0 * 1024;
  const int rot = ROT4 * (pm + 2 * pn);
  auto piece = [&](int kc, int i) {
    const unsigned sb = lds0 + (kc % NST) * STAGE;
    const int kk = (kc + rot) & 7;
    if (i == 0) glds(voffA, baseA + kk * 16384, sb + wid * 1024);
    else if (i == 1) glds(voffA, baseA + kk * 16384 + 8192, sb + 8192 + wid * 1024);
    else glds(l16, baseW + kk * 28672 + (i - 2) * 1024, sb + 16384 + (pc0 + i - 2) * 1024);
  };
  auto issue = [&](int kc) {
#pragma unroll
    for (int i = 0; i < LPC; ++i) piece(kc, i);
  };
  float* st_ = (float*)(smem + NST * STAGE);
  float* csum = st_ + 256;
  if (P0 == 0) {
    if (wid < 2) {
      f32x2 sv[16];
      const char* rb = (const char*)RS + pm * 1024;
      const unsigned vo = (unsigned)(tid * 8);
#pragma unroll
      for (int i = 0; i < 16; ++i) GLD2(sv[i], vo, rb + i * (S_ * 8), 0);
      issue(0);
      BAR();
      issue(1);
      wait_vm<2 * LPC>();
#pragma unroll
      for (int i = 0; i < 16; ++i) PIN(sv[i]);
      f32x2 a = sv[0];
#pragma unroll
      for (int i = 1; i < 16; ++i) a += sv[i];
      *(f32x2*)(st_ + tid * 2) = ln_stats(a[0], a[1]);
    } else {
      issue(0);
      BAR();
      issue(1);
    }
  } else {
    float sc[8];
    int c = tid - 256;
    const bool ok = c < 224;
    if (!ok) c = 0;
    const unsigned vo = (unsigned)(((c < 112 ? 0 : F_ - 112) + pn * 112 + c) * 4);
#pragma unroll
    for (int i = 0; i < 8; ++i) GLD1(sc[i], vo, (const char*)CS + i * (2 * F_ * 4), 0);
    issue(0);
    BAR();
    issue(1);
    wait_vm<2 * LPC>();
#pragma unroll
    for (int i = 0; i < 8; ++i) PIN(sc[i]);
    float a = sc[0];
#pragma unroll
    for (int i = 1; i < 8; ++i) a += sc[i];
    if (ok) csum[c] = a;
  }
  TSTAMP(3, 1);

  unsigned a_addr[2];
#pragma unroll
  for (int ks = 0; ks < 2; ++ks) a_addr[ks] = lds0 + (wm * 32 + l15) * 128 + (((4 * ks + gq) ^ (l15 >> 1)) * 16);
  const unsigned b_addr = lds0 + 16384 + P0 * 1024 + l16;
  f32x4 ag[2][NP], au[2][NP];
  const f32x4 z4 = {0.f, 0.f, 0.f, 0.f};
#pragma unroll
  for (int j = 0; j < NP; ++j) { ag[0][j] = z4; ag[1][j] = z4; au[0][j] = z4; au[1][j] = z4; }
  bf16x8 af[2][2], bg[2][NP], bu[2][NP];
  auto rd = [&](int t, int set) {
    const int kc = t >> 1, ks = t & 1, st = kc % NST;
    const unsigned so = st * STAGE;
    DSR128(af[set][0], a_addr[ks] + so, 0);
    DSR128(af[set][1], a_addr[ks] + so, 2048);
#pragma unroll
    for (int j = 0; j < NP; ++j) {
      DSR128(bg[set][j], b_addr + so, (ks * 14 + j) * 1024);
      DSR128(bu[set][j], b_addr + so, (ks * 14 + 7 + j) * 1024);
    }
  };
  auto wt = [&](int set) {
    asm volatile("s_waitcnt lgkmcnt(0)" ::: "memory");
    PIN(af[set][0]); PIN(af[set][1]);
#pragma unroll
    for (int j = 0; j < NP; ++j) { PIN(bg[set][j]); PIN(bu[set][j]); }
  };
  asm volatile("s_waitcnt vmcnt(%0) lgkmcnt(0)" ::"n"(LPC) : "memory");
  BAR();
  TSTAMP(3, 2);
  rd(0, 0);
  piece(NST - 1, 0); piece(NST - 1, 1); piece(NST - 1, 2);
  wt(0);
  constexpr int NTM = NT - 4, NU = 2 * NP;
  auto npf = [&](int lo, int hi) {
    int n = 0;
    if (PF4 == 4)
      for (int i = lo; i <= hi; ++i) n += (i >= PFT && i < PFT + 7) ? 1 : 0;
    return n;
  };
  bf16x8 aT[4][2], bT[2][2][2];
  auto uj = [&](int u) { return u == 0 ? 0 : (u == NU - 1 ? NP - 1 : ((u & 1) ? (u + 1) / 2 : u / 2 - 1)); };
  auto uc = [&](int u) { return (u == 0 || ((u & 1) && u != NU - 1)) ? 0 : 1; };
  auto rdA = [&](int c) {
#pragma unroll
    for (int ks = 0; ks < 2; ++ks) {
      DSR128(aT[c * 2 + ks][0], a_addr[ks] + ((NK - 2 + c) % NST) * STAGE, 0);
      DSR128(aT[c * 2 + ks][1], a_addr[ks] + ((NK - 2 + c) % NST) * STAGE, 2048);
    }
  };
  auto rdB = [&](int u) {
#pragma unroll
    for (int ks = 0; ks < 2; ++ks) {
      DSR128(bT[u & 1][ks][0], b_addr + ((NK - 2 + uc(u)) % NST) * STAGE, (ks * 14 + uj(u)) * 1024);
      DSR128(bT[u & 1][ks][1], b_addr + ((NK - 2 + uc(u)) % NST) * STAGE, (ks * 14 + 7 + uj(u)) * 1024);
    }
  };
  auto rdp = [&](int t, int set, int i) {
    const int kc = t >> 1, ks = t & 1, st = kc % NST;
    const unsigned so = st * STAGE;
    if (i < 2) DSR128(af[set][i], a_addr[ks] + so, i * 2048);
    else if (i & 1) DSR128(bu[set][(i - 2) >> 1], b_addr + so, (ks * 14 + 7 + ((i - 2) >> 1)) * 1024);
    else DSR128(bg[set][(i - 2) >> 1], b_addr + so, (ks * 14 + ((i - 2) >> 1)) * 1024);
  };
  auto rdT = [&](int i) {
    if (i < 4) DSR128(aT[i >> 1][i & 1], a_addr[i >> 1] + ((NK - 2) % NST) * STAGE, (i & 1) * 2048);
    else DSR128(bT[0][(i - 4) >> 1][i & 1], b_addr + ((NK - 2 + uc(0)) % NST) * STAGE, (((i - 4) >> 1) * 14 + (i & 1) * 7 + uj(0)) * 1024);
  };
#pragma unroll
  for (int t = 0; t < NTM; ++t) {
    const int kc = t >> 1, ks = t & 1, cur = t & 1, nxt = cur ^ 1;
    if (ks == 1) {
      const int last = (kc + NST - 1 < NK - 1) ? (kc + NST - 1) : (NK - 1);
      wait_vm_n((last - (kc + 1)) * LPC + npf(2 * kc - 2, 2 * kc));
      BAR();
      TSTAMP(3, 3 + kc);
    }
    const int ic = (t + 1) / 2 + NST - 1, ip = ((t + 1) & 1) * 3;
    SCHED();
#pragma unroll
    for (int j = 0; j < NP; ++j) {
#pragma unroll
      for (int q = 0; q < 4; ++q) {
        if (q == 0) ag[0][j] = mfma16(bg[cur][j], af[cur][0], ag[0][j]);
        if (q == 1) ag[1][j] = mfma16(bg[cur][j], af[cur][1], ag[1][j]);
        if (q == 2) au[0][j] = mfma16(bu[cur][j], af[cur][0], au[0][j]);
        if (q == 3) au[1][j] = mfma16(bu[cur][j], af[cur][1], au[1][j]);
        SCHED();
#pragma unroll
        for (int r = (j * 4 + q) * ILV4; r < (j * 4 + q + 1) * ILV4; ++r) {
          if (t + 1 < NTM) { if (r < 2 + 2 * NP) rdp(t + 1, nxt, r); }
          else if (r < 8) rdT(r);
        }
        SCHED();
      }
      if (j < 3 && ic < NK && ip + j < LPC) piece(ic, ip + j);
      if (PF4 && PF4 < 4 && j < (PF4 == 1 ? 2 : 1) && t == NTM - 1 && (!(MAP5 || PFH) || PFE)) glds(l16, pf + j * ((MAP5 || PFH) ? 7168 : 8192), lds0 + NST * STAGE + 2048 + wid * 1024);
      if (j == 2 && npf(t, t)) glds(l16, pf + (t - PFT) * 8192, lds0 + NST * STAGE + 2048 + wid * 1024);
      SCHED();
    }
    if (t + 1 < NTM) wt(nxt);
  }
  auto wtT = [&](bool a0, bool a1, int u) {
    asm volatile("s_waitcnt lgkmcnt(0)" ::: "memory");
    if (a0) { PIN(aT[0][0]); PIN(aT[0][1]); PIN(aT[1][0]); PIN(aT[1][1]); }
    if (a1) { PIN(aT[2][0]); PIN(aT[2][1]); PIN(aT[3][0]); PIN(aT[3][1]); }
    PIN(bT[u & 1][0][0]); PIN(bT[u & 1][0][1]); PIN(bT[u & 1][1][0]); PIN(bT[u & 1][1][1]);
  };
  wtT(true, false, 0);
  TSTAMP(3, 20);
  bf16* dst0 = Hm + ((size_t)((pm * 2 + (wm >> 1)) * 112 + pn * 7 + P0) * 64 + (wm & 1) * 32 + l15) * 16;
  const unsigned caddr = lds0 + NST * STAGE + 1024 + (P0 * 16 + gq * 4) * 4;
  f32x2 nm[2], le2[2], iv2[2];
  f32x4 cg[2], cu[2];
  unsigned pk[NP][2][2];
  float tg[2], tu[2], te[2], td[2];
#define PINS(x) asm volatile("" : "+v"(x[0]), "+v"(x[1]))
  auto epi = [&](int j, int mi, int q) {
    const int hf = q >> 2;
    if ((q & 3) == 0) {
      tg[0] = __builtin_fmaf(cg[j & 1][2 * hf], nm[mi][0], ag[mi][j][2 * hf]);
      tg[1] = __builtin_fmaf(cg[j & 1][2 * hf + 1], nm[mi][0], ag[mi][j][2 * hf + 1]);
      tu[0] = __builtin_fmaf(cu[j & 1][2 * hf], nm[mi][0], au[mi][j][2 * hf]);
      tu[1] = __builtin_fmaf(cu[j & 1][2 * hf + 1], nm[mi][0], au[mi][j][2 * hf + 1]);
      te[0] = tg[0] * le2[mi][0];
      te[1] = tg[1] * le2[mi][0];
      PINS(tg); PINS(tu); PINS(te);
    } else if ((q & 3) == 1) {
      te[0] = __builtin_amdgcn_exp2f(te[0]); te[1] = __builtin_amdgcn_exp2f(te[1]);
      PINS(te);
    } else if ((q & 3) == 2) {
      td[0] = __builtin_fmaf(te[0], iv2[mi][0], iv2[mi][0]);
      td[1] = __builtin_fmaf(te[1], iv2[mi][0], iv2[mi][0]);
      tu[0] = tg[0] * tu[0]; tu[1] = tg[1] * tu[1];
      PINS(td); PINS(tu);
    } else {
      float o0 = tu[0] * __builtin_amdgcn_rcpf(td[0]), o1 = tu[1] * __builtin_amdgcn_rcpf(td[1]);
      PIN(o0); PIN(o1);
      pk[j][mi][hf] = pk2(o0, o1);
    }
    if (q == 7) {
      if (j & 1) {
        swap16(pk[j - 1][mi][0], pk[j][mi][0]);
        swap16(pk[j - 1][mi][1], pk[j][mi][1]);
        st16<(WT & 8) != 0>(dst0 + ((size_t)(j - 1 + (gq & 1)) * 64 + mi * 16) * 16 + (gq >> 1) * 8,
                            u32x4{pk[j - 1][mi][0], pk[j - 1][mi][1], pk[j][mi][0], pk[j][mi][1]});
      } else if (j == NP - 1) {
        st8<(WT & 8) != 0>(dst0 + ((size_t)j * 64 + mi * 16) * 16 + gq * 4, u32x2{pk[j][mi][0], pk[j][mi][1]});
      }
    }
  };
#pragma unroll
  for (int u = 0; u < NU; ++u) {
    const int j = uj(u), c = uc(u);
    if (u == 1) {
      wait_vm_n(PF4 == 4 ? npf(NTM - 2, NTM - 1) : (PF4 == 1 ? ((MAP5 || PFH) ? (PFE ? 4 : 2) : 4) : (PF4 ? 1 : 0)));
      BAR();
      TSTAMP(3, 9);
      rdA(1);
    }
    if (u + 1 < NU) rdB(u + 1);
    const int cj = u == NU - 1 ? NP - 1 : ((u >= 2 && !(u & 1)) ? (u - 2) / 2 : -1);
    if (cj >= 0) {
      DSR128(cg[cj & 1], caddr, cj * 64);
      DSR128(cu[cj & 1], caddr, cj * 64 + 448);
    }
    if (u == 1) {
#pragma unroll
      for (int mi = 0; mi < 2; ++mi) {
        const f32x2 ms = *(const f32x2*)(st_ + (wm * 32 + mi * 16 + l15) * 2);
        nm[mi] = f32x2{-ms[0], -ms[0]};
        const float le = ms[1] * -1.4426950408889634f, iv = 1.0f / (ms[1] * ms[1]);
        le2[mi] = f32x2{le, le};
        iv2[mi] = f32x2{iv, iv};
      }
    }
    SCHED();
#pragma unroll
    for (int q = 0; q < 8; ++q) {
      const int ks = q >> 2, mi = q & 1;
      if (q & 2) au[mi][j] = mfma16(bT[u & 1][ks][1], aT[c * 2 + ks][mi], au[mi][j]);
      else ag[mi][j] = mfma16(bT[u & 1][ks][0], aT[c * 2 + ks][mi], ag[mi][j]);
      SCHED();
      if (u >= 3) epi((u - 3) >> 1, (u - 3) & 1, q);
      if (PF4 == 1 && !(MAP5 || PFH) && u < 3 && (q == 1 || q == 5) && 2 + u * 2 + (q >> 2) < 7)
        glds(l16, pf + (2 + u * 2 + (q >> 2)) * 8192, lds0 + NST * STAGE + 2048 + wid * 1024);
      if (PF4 == 1 && (MAP5 || PFH) && u < 2 && (q == 1 || q == 5) && (PFE ? 2 : 0) + u * 2 + (q >> 2) < 4)
        glds(l16, pf + ((PFE ? 2 : 0) + u * 2 + (q >> 2)) * 7168, lds0 + NST * STAGE + 2048 + wid * 1024);
      if ((CPF & 8) && u == 3 && q == 3) code_warm<false>(wid, lane, lds0 + NST * STAGE + 2048 + wid * 1024, nullptr);
      SCHED();
    }
    if (u + 1 < NU) {
      wtT(false, u == 1, u + 1);
    } else {
      asm volatile("s_waitcnt lgkmcnt(0)" ::: "memory");
    }
    if (cj >= 0) { PIN(cg[cj & 1]); PIN(cu[cj & 1]); }
  }
  TSTAMP(3, 21);
#pragma unroll
  for (int e = NU - 3; e < NU; ++e)
#pragma unroll
    for (int q = 0; q < 8; ++q) epi(e >> 1, e & 1, q);
}

extern "C" __global__ __launch_bounds__(512, 2) void k4_gu(const bf16* __restrict__ Hh, const bf16* __restrict__ Wt,
                                                const float* __restrict__ RS, const float* __restrict__ CS,
                                                bf16* __restrict__ Hm, const bf16* __restrict__ W2t) {
  extern __shared__ __attribute__((aligned(16))) char smem[];
  TDECL
  TSTAMP(3, 0);
  const int tid = threadIdx.x;
  const int wid = __builtin_amdgcn_readfirstlane(tid >> 6);
  const int xg = blockIdx.x & 7, idx = blockIdx.x >> 3;
#define MAP4 0
  const int pm = MAP4 == 0 ? (xg & 1) * 8 + (idx & 7) : (MAP4 == 1 ? xg * 2 + (idx & 1) : (MAP4 == 2 ? (xg >> 1) * 4 + (idx & 3) : xg * 2 + (idx >> 4)));
  const int pn = MAP4 == 0 ? (xg >> 1) * 4 + (idx >> 3) : (MAP4 == 1 ? idx >> 1 : (MAP4 == 2 ? (xg & 1) * 8 + (idx >> 2) : (idx & 15)));
  asm volatile("" : "+s"(RS), "+s"(CS), "+s"(Hm), "+s"(W2t));
  const unsigned lds0 = (unsigned)(uintptr_t)(LDSP(char))smem;
  const char* pf = PFH ? (const char*)W2t + (idx >> 2) * 229376 + 114688 + (idx & 3) * 28672 + (wid < 7 ? wid : 6) * 1024 : MAP5 ? (const char*)W2t + (xg & 1) * 917504 + idx * 28672 + (wid < 7 ? wid : 6) * 1024 : (const char*)W2t + (PF4 == 2 ? (blockIdx.x * 7 + (wid < 7 ? wid : 6)) * 1024 : (PF4 == 3 ? idx * 57344 + wid * 7168 : idx * 57344 + wid * 1024));
  if (wid < 4) gu_body<4, 0>(smem, lds0, tid, wid, pm, pn, Hh, Wt, RS, CS, Hm, pf TPASS);
  else gu_body<3, 4>(smem, lds0, tid, wid, pm, pn, Hh, Wt, RS, CS, Hm, pf TPASS);
  wait_vm<0>();
  TSTAMP(3, 22);
  TFLUSH(3);
  asm volatile("k4_gu_tail:");
}

extern "C" void kernel_launch(void* const* d_in, const int* in_sizes, int n_in, void* d_out, int out_size, void* d_ws,
                              size_t ws_size, hipStream_t stream) {
  const bf16* x = (const bf16*)d_in[0];
  const bf16* wqkv = (const bf16*)d_in[1];
  const bf16* wo = (const bf16*)d_in[2];
  const bf16* w1 = (const bf16*)d_in[4];
  const bf16* w3 = (const bf16*)d_in[5];
  const bf16* w2 = (const bf16*)d_in[6];
  bf16* out = (bf16*)d_out;

  char* ws = (char*)d_ws;
  bf16* QKV = (bf16*)(ws);
  bf16* Oa = (bf16*)(ws + (7u << 20));
  bf16* Hh = (bf16*)(ws + (9u << 20));
#define HMO 11
  bf16* Hm = (bf16*)(ws + ((unsigned)HMO << 20));
  float* RS = (float*)(ws + (18u << 20));
  float* CS = (float*)(ws + (19u << 20));
  bf16* Wt = (bf16*)(ws + (20u << 20));
  bf16* W2t = (bf16*)(ws + (24u << 20));
  bf16* Wot = (bf16*)(ws + (26u << 20));

  {
    constexpr int lds = K1_NST * 28672 + 2560 + 2048;
    (void)hipFuncSetAttribute((const void*)k1_qkv, hipFuncAttributeMaxDynamicSharedMemorySize, lds);
    k1_qkv<<<256, 512, lds, stream>>>(x, wqkv, QKV);
  }
  {
    constexpr int lds = 59392 + 2048;
    (void)hipFuncSetAttribute((const void*)k2_attn, hipFuncAttributeMaxDynamicSharedMemorySize, lds);
    k2_attn<<<256, 512, lds, stream>>>(QKV, Oa, wo, w1, w3, w2, Wt, CS, W2t, Wot);
  }
  {
    auto k = k3_wo;
    constexpr int lds = G_NST * 32768 + 8192;
    (void)hipFuncSetAttribute((const void*)k, hipFuncAttributeMaxDynamicSharedMemorySize, lds);
    k<<<256, 512, lds, stream>>>(Oa, Wot, x, Hh, RS, Wt);
  }
  {
    constexpr int lds = 3 * 45056 + 2048 + 8192;
    (void)hipFuncSetAttribute((const void*)k4_gu, hipFuncAttributeMaxDynamicSharedMemorySize, lds);
    k4_gu<<<256, 512, lds, stream>>>(Hh, Wt, RS, CS, Hm, W2t);
  }
  {
    auto k = k5_down;
    constexpr int lds = G_NST * 32768 + 2048;
    (void)hipFuncSetAttribute((const void*)k, hipFuncAttributeMaxDynamicSharedMemorySize, lds);
    k<<<256, 512, lds, stream>>>(Hm, W2t, Hh, out);
  }
}
```
